```python
import torch

def _softmax(x, axis=-1):
    return torch.softmax(x, dim=axis)
def _gelu(x):
    return torch.nn.functional.gelu(x, approximate="tanh")
def _silu(x):
    return torch.nn.functional.silu(x)
def _sigmoid(x):
    return torch.sigmoid(x)
def _layernorm(x, axis=-1, eps=1e-5):
    m = x.mean(dim=axis, keepdim=True)
    v = x.var(dim=axis, keepdim=True, unbiased=False)
    return (x - m) / torch.sqrt(v + eps)
def _scatter_add(tgt, idx, src, axis=0):
    return tgt.clone().index_add_(axis, idx, src)
def _segment_sum(src, seg, n):
    out = torch.zeros((n,) + src.shape[1:], dtype=src.dtype)
    return out.index_add_(0, seg, src)
def _pad(x, pw):
    flat = tuple(v for pair in reversed(pw) for v in pair)
    return torch.nn.functional.pad(x, flat)

kernel_name = 'gated_mix__ssm_conv_h3__glu_gated_swiglu__prompt'

def setup_inputs():

    _w = (lambda a: (lambda t: t.to(torch.bfloat16) if t.is_floating_point() else t)(torch.as_tensor(a)))
    def _A_setup():
        B, S, D = 1, 2048, 576
        ks = 4
        return {
            "q": _w((lambda *s: torch.randn(*s, dtype=torch.bfloat16) * 0.05)(B, S, D)),
            "k": _w((lambda *s: torch.randn(*s, dtype=torch.bfloat16) * 0.05)(B, S, D)),
            "v": _w((lambda *s: torch.randn(*s, dtype=torch.bfloat16) * 0.05)(B, S, D)),
            "shift_k": _w((lambda *s: torch.randn(*s, dtype=torch.bfloat16) * 0.05)(D, ks) * 0.5),
            "ssm_k": _w((lambda *s: torch.randn(*s, dtype=torch.bfloat16) * 0.05)(D, S) * (1.0 / S ** 0.5)),
            "ssm_D": _w((lambda *s: torch.randn(*s, dtype=torch.bfloat16) * 0.05)(D)),
        }
    _A_in = _A_setup()

    def _B_setup():
        B, S, D, F = 1, 2048, 576, 4608
        return {
            "x": _w((lambda *s: torch.randn(*s, dtype=torch.bfloat16) * 0.05)(B, S, D)),
            "w1": _w((lambda *s: torch.randn(*s, dtype=torch.bfloat16) * 0.05)(D, F) * (2.0 / D) ** 0.5),
            "w3": _w((lambda *s: torch.randn(*s, dtype=torch.bfloat16) * 0.05)(D, F) * (2.0 / D) ** 0.5),
            "w2": _w((lambda *s: torch.randn(*s, dtype=torch.bfloat16) * 0.05)(F, D) * (2.0 / F) ** 0.5),
        }
    _B_in = _B_setup()

    return {
        "A_q": _A_in["q"],
        "A_k": _A_in["k"],
        "A_v": _A_in["v"],
        "A_shift_k": _A_in["shift_k"],
        "A_ssm_k": _A_in["ssm_k"],
        "A_ssm_D": _A_in["ssm_D"],
        "B_x": _B_in["x"],
        "B_w1": _B_in["w1"],
        "B_w3": _B_in["w3"],
        "B_w2": _B_in["w2"],
        "gate_w": _w((lambda *s: torch.randn(*s, dtype=torch.bfloat16) * 0.05)(576) * 16.0),
    }

def reference(A_q, A_k, A_v, A_shift_k, A_ssm_k, A_ssm_D, B_x, B_w1, B_w3, B_w2, gate_w):
    def _A_ref(q, k, v, shift_k, ssm_k, ssm_D):
        B, S, D = q.shape
        ks = shift_k.shape[-1]
        k_pad = _pad(k, ((0, 0), (ks - 1, 0), (0, 0)))
        k_shift = torch.zeros_like(k)
        for t in range(ks):
            k_shift = k_shift + k_pad[:, t:t + S, :] * shift_k[:, ks - 1 - t][None, None, :]
        kv = k_shift * v
        L = 2 * S
        kv_pad = _pad(kv, ((0, 0), (0, S), (0, 0)))
        kf_pad = _pad(ssm_k, ((0, 0), (0, S)))
        kv_f = (lambda x, axes=(-2,-1): torch.fft.rfft2(x.float(), dim=axes))(kv_pad[:, None, :, :], axes=(1, 2))
        kf_f = (lambda x, axes=(-2,-1): torch.fft.rfft2(x.float(), dim=axes))(kf_pad[:, None, :], axes=(1, 2))
        kf_f = (lambda x, *d: x.permute(*d))(kf_f, 1, 2, 0)
        y_f = kv_f * kf_f[None, :, :, :]
        y = (lambda x, s, axes=(-2,-1): torch.fft.irfft2(x, s=s, dim=axes).to(torch.bfloat16))(y_f, (1, L), axes=(1, 2))[:, 0, :S, :]
        y = y + kv * ssm_D[None, None, :]
        return q * y

    def _B_ref(x, w1, w3, w2):
        gate = torch.einsum("bsd,df->bsf", x, w1)
        up = torch.einsum("bsd,df->bsf", x, w3)
        h = _silu(gate) * up
        out = torch.einsum("bsf,fd->bsd", h, w2)
        return out

    x = A_q
    g = _sigmoid((x * gate_w).sum(-1, keepdims=True))
    a = _A_ref(x, A_k, A_v, A_shift_k, A_ssm_k, A_ssm_D)
    b = _B_ref(x, B_w1, B_w3, B_w2)
    return g * a + (1 - g) * b
```

```cpp
#include <hip/hip_runtime.h>
#include <hip/hip_bf16.h>

#define LAS __attribute__((address_space(3)))
typedef unsigned short u16;
typedef __bf16 bf16x2 __attribute__((ext_vector_type(2)));
typedef __bf16 bf16x4 __attribute__((ext_vector_type(4)));
typedef __bf16 bf16x8 __attribute__((ext_vector_type(8)));
typedef float f32x2 __attribute__((ext_vector_type(2)));
typedef float f32x4 __attribute__((ext_vector_type(4)));
typedef float f32x16 __attribute__((ext_vector_type(16)));
typedef unsigned u32x2 __attribute__((ext_vector_type(2)));
typedef unsigned u32x4 __attribute__((ext_vector_type(4)));

constexpr int S_ = 2048, D_ = 576, F_ = 4608;
constexpr size_t WS_H = 0;
constexpr size_t WS_PART = WS_H + (size_t)64 * 288 * 1024;
constexpr size_t WS_UR = WS_PART + (size_t)4 * S_ * D_ * 4;
constexpr size_t WS_Z = WS_UR + (size_t)D_ * 2 * S_ * 2;
constexpr size_t WS_G = WS_Z + (size_t)D_ * S_ * 2;
constexpr size_t WS_END = WS_G + S_ * 4;

__device__ __forceinline__ float bf2f(u16 v) { return __builtin_bit_cast(float, (unsigned)v << 16); }
__device__ __forceinline__ float bflo(unsigned v) { return __builtin_bit_cast(float, v << 16); }
__device__ __forceinline__ float bfhi(unsigned v) { return __builtin_bit_cast(float, v & 0xffff0000u); }
__device__ __forceinline__ float rbf(float x) { return (float)(__bf16)x; }
__device__ __forceinline__ unsigned pk2(float lo, float hi) {
  f32x2 v = {lo, hi};
  return __builtin_bit_cast(unsigned, __builtin_convertvector(v, bf16x2));
}

__device__ __forceinline__ void dma16(unsigned voff, const void* sbase, unsigned lds_dst) {
  unsigned keep;
  asm volatile("s_mov_b32 %0, m0\n\ts_mov_b32 m0, %3\n\ts_nop 0\n\tglobal_load_lds_dwordx4 %1, %2\n\ts_mov_b32 m0, %0"
               : "=&s"(keep) : "v"(voff), "s"(sbase), "s"(lds_dst) : "memory");
}
__device__ __forceinline__ void dma16_48(unsigned voff, const void* sbase, unsigned lds_dst) {
  unsigned keep;
  asm volatile("s_mov_b32 %0, m0\n\ts_mov_b32 m0, %3\n\ts_mov_b32 exec_hi, 0xffff\n\tglobal_load_lds_dwordx4 %1, %2\n\ts_mov_b32 exec_hi, -1\n\ts_mov_b32 m0, %0"
               : "=&s"(keep) : "v"(voff), "s"(sbase), "s"(lds_dst) : "memory");
}
template <int N> __device__ __forceinline__ void wait_vm_barrier() {
  asm volatile("s_waitcnt vmcnt(%0)\n\ts_barrier" ::"n"(N) : "memory");
}
template <int N> __device__ __forceinline__ void wait_vm() { asm volatile("s_waitcnt vmcnt(%0)" ::"n"(N) : "memory"); }
__device__ __forceinline__ void wait_lgkm0() { asm volatile("s_waitcnt lgkmcnt(0)" ::: "memory"); }

__device__ __forceinline__ bf16x8 tr_frag(LAS char* p0, LAS char* p1) {
  bf16x4 a = __builtin_amdgcn_ds_read_tr16_b64_v4bf16((LAS bf16x4*)p0);
  bf16x4 b = __builtin_amdgcn_ds_read_tr16_b64_v4bf16((LAS bf16x4*)p1);
  return __builtin_shufflevector(a, b, 0, 1, 2, 3, 4, 5, 6, 7);
}
__device__ __forceinline__ float silu_mul(float g, float u) {
  float e = __builtin_amdgcn_exp2f(g * -1.44269504088896f);
  return g * __builtin_amdgcn_rcpf(1.0f + e) * u;
}

constexpr int K1_SLOT = 12416, K1_UPOFF = 6272, K1_NSLOT = 5;
constexpr int K1_XOFF = K1_SLOT * K1_NSLOT;
constexpr int K1_LDS = K1_XOFF + 8 * 3 * 4096;

struct K1Ctx {
  const char* w1; const char* w3; const char* xb;
  unsigned voff_w, voff_x0, voff_x1;
  unsigned lds0;
  int w;
};
template <int s> __device__ __forceinline__ void k1_issue_w(const K1Ctx& c) {
  constexpr int ch = s / 9, ks = s % 9;
  constexpr size_t goff = ((size_t)(64 * ks) * F_ + 48 * ch) * 2;
  unsigned dst = c.lds0 + (s % K1_NSLOT) * K1_SLOT + c.w * 768;
  dma16_48(c.voff_w, c.w1 + goff, dst);
  dma16_48(c.voff_w, c.w3 + goff, dst + K1_UPOFF);
}
template <int s> __device__ __forceinline__ void k1_issue_x(const K1Ctx& c) {
  unsigned dst = c.lds0 + K1_XOFF + c.w * (3 * 4096) + (s % 3) * 4096;
#pragma unroll
  for (int i = 0; i < 4; ++i)
    dma16((i & 1) ? c.voff_x1 : c.voff_x0, c.xb + (size_t)(8 * i) * (D_ * 2) + s * 128, dst + i * 1024);
}
constexpr int k1_nw(int s) { return (s >= 0 && s < 27) ? 2 : 0; }
constexpr int k1_nx(int s) { return (s >= 0 && s < 9) ? 4 : 0; }
constexpr int k1_nst_between(int after_step, int upto_barrier) {
  int n = 0;
  for (int c = 0; c < 2; ++c) if (9 * c + 13 >= after_step && 9 * c + 9 < upto_barrier) n += 3;
  return n;
}
constexpr int k1_wait(int s) {
  return k1_nw(s + 2) + k1_nx(s + 2) + k1_nw(s + 3) + k1_nx(s + 3) + k1_nst_between(s + 1, s);
}

__device__ __forceinline__ void k1_store_tile(const f32x16& a, char* hp) {
  u32x4 o;
#pragma unroll
  for (int i = 0; i < 4; ++i) o[i] = pk2(silu_mul(a[2 * i], a[2 * i + 8]), silu_mul(a[2 * i + 1], a[2 * i + 9]));
  *(u32x4*)hp = o;
}

template <int s>
__device__ __forceinline__ void k1_slice(const K1Ctx& c, LAS char* wl, LAS char* xl0, bf16x8 (&xf)[36], bf16x8 (&wf)[3], f32x16 (&acc)[3],
                                         char* hp) {
  constexpr int ch = s / 9, ks = s % 9;
  if constexpr (s + 1 < 9) {
#pragma unroll
    for (int j = 0; j < 4; ++j)
      xf[4 * (s + 1) + j] = *(LAS bf16x8*)((LAS char*)((unsigned)(size_t)xl0 ^ (32 * j)) + ((s + 1) % 3) * 4096);
  }
  if constexpr (s + 4 < 27) k1_issue_w<s + 4>(c);
  if constexpr (s + 4 < 9) {
    wait_lgkm0();
    k1_issue_x<s + 4>(c);
  }
  if constexpr (ks == 0 && ch > 0) {
#pragma unroll
    for (int t = 0; t < 3; ++t) {
      k1_store_tile(acc[t], hp + (size_t)(3 * (ch - 1) + t) * 1024);
      acc[t] = (f32x16){};
    }
    asm volatile("" ::: "memory");
  }
#pragma unroll
  for (int j = 0; j < 4; ++j) {
    bf16x8 nf[3];
    if (j < 3 || s + 1 < 27) {
      constexpr int dummy = 0; (void)dummy;
      const int sn = (j < 3) ? s : s + 1, jn = (j < 3) ? j + 1 : 0;
      LAS char* p = wl + (sn % K1_NSLOT) * K1_SLOT + (16 * jn) * 96;
#pragma unroll
      for (int t = 0; t < 3; ++t) nf[t] = tr_frag(p + t * 32, p + t * 32 + 4 * 96);
    }
#pragma unroll
    for (int t = 0; t < 3; ++t) acc[t] = __builtin_amdgcn_mfma_f32_32x32x16_bf16(wf[t], xf[4 * ks + j], acc[t], 0, 0, 0);
    if (j < 3 || s + 1 < 27) {
#pragma unroll
      for (int t = 0; t < 3; ++t) wf[t] = nf[t];
    }
  }
  if constexpr (s + 1 < 27) {
    wait_lgkm0();
    wait_vm_barrier<k1_wait(s + 1)>();
  }
}
template <int s>
__device__ __forceinline__ void k1_run(const K1Ctx& c, LAS char* wl, LAS char* xl0, bf16x8 (&xf)[36], bf16x8 (&wf)[3], f32x16 (&acc)[3], char* hp) {
  k1_slice<s>(c, wl, xl0, xf, wf, acc, hp);
  if constexpr (s + 1 < 27) k1_run<s + 1>(c, wl, xl0, xf, wf, acc, hp);
}

__global__ __launch_bounds__(512, 2) void k1_kernel(const u16* __restrict__ x, const u16* __restrict__ kk, const u16* __restrict__ vv,
                                                    const u16* __restrict__ shift_k, const u16* __restrict__ w1, const u16* __restrict__ w3,
                                                    const u16* __restrict__ gate_w, char* __restrict__ ws) {
  extern __shared__ __attribute__((aligned(16))) char smem[];
  LAS char* lds = (LAS char*)smem;
  const int b = blockIdx.x, xcd = b & 7, r = b >> 3;
  const int mt = 4 * (xcd & 1) + (r & 3), nt = 8 * (xcd >> 1) + (r >> 2);
  const int tid = threadIdx.x, lane = tid & 63;
  const int w = __builtin_amdgcn_readfirstlane(tid >> 6);
  K1Ctx c;
  c.w = w;
  c.lds0 = (unsigned)(size_t)lds;
  c.w1 = (const char*)w1 + ((size_t)(8 * w) * F_ + 144 * nt) * 2;
  c.w3 = (const char*)w3 + ((size_t)(8 * w) * F_ + 144 * nt) * 2;
  c.xb = (const char*)x + (size_t)(256 * mt + 32 * w) * (D_ * 2);
  {
    int l48 = lane < 48 ? lane : 47;
    c.voff_w = (l48 / 6) * (F_ * 2) + (l48 % 6) * 16;
    int row = lane >> 3, sw = (lane >> 4);
    c.voff_x0 = row * (D_ * 2) + (((lane & 7) ^ sw) * 16);
    c.voff_x1 = row * (D_ * 2) + (((lane & 7) ^ (sw + 4)) * 16);
  }
  k1_issue_w<0>(c); k1_issue_x<0>(c);
  k1_issue_w<1>(c); k1_issue_x<1>(c);
  k1_issue_w<2>(c); k1_issue_x<2>(c);

  LAS char* wl;
  {
    int gq = lane >> 4, mat = gq & 1, h = gq >> 1, q = (lane & 15) >> 2, p = lane & 3;
    wl = lds + mat * K1_UPOFF + (8 * h + q) * 96 + p * 8;
  }
  LAS char* xl0;
  {
    int cc = lane & 31, h = lane >> 5;
    xl0 = lds + K1_XOFF + w * (3 * 4096) + cc * 128 + ((h ^ ((cc >> 1) & 7)) * 16);
  }
  char* hp = ws + WS_H + ((size_t)((8 * mt + w) * 288 + 9 * nt) * 64 + lane) * 16;
  bf16x8 xf[36];
  bf16x8 wf[3];
  f32x16 acc[3] = {};

  wait_vm_barrier<12>();
#pragma unroll
  for (int j = 0; j < 4; ++j) xf[j] = *(LAS bf16x8*)((LAS char*)((unsigned)(size_t)xl0 ^ (32 * j)));
#pragma unroll
  for (int t = 0; t < 3; ++t) wf[t] = tr_frag(wl + t * 32, wl + t * 32 + 4 * 96);
  k1_issue_w<3>(c);
  wait_lgkm0();
  k1_issue_x<3>(c);
  wait_vm_barrier<k1_wait(0)>();
  k1_run<0>(c, wl, xl0, xf, wf, acc, hp);
#pragma unroll
  for (int t = 0; t < 3; ++t) k1_store_tile(acc[t], hp + (size_t)(6 + t) * 1024);

  if (tid < 288) {
    const int tt = b >> 3, dg = b & 7;
    const int dp = tid % 36, tg = tid / 36;
    const int d = 72 * dg + 2 * dp, t0 = 64 * tt + 8 * tg;
    unsigned kr[12], vr[9];
#pragma unroll
    for (int i = 0; i < 12; ++i) {
      int t = t0 - 4 + i;
      int tc = t < 0 ? 0 : t;
      unsigned v = *(const unsigned*)(kk + (size_t)tc * D_ + d);
      kr[i] = t < 0 ? 0u : v;
    }
#pragma unroll
    for (int i = 0; i < 9; ++i) {
      int t = t0 - 1 + i;
      int tc = t < 0 ? 0 : t;
      unsigned v = *(const unsigned*)(vv + (size_t)tc * D_ + d);
      vr[i] = t < 0 ? 0u : v;
    }
    u32x4 sk = *(const u32x4*)(shift_k + d * 4);
#pragma unroll
    for (int ch = 0; ch < 2; ++ch) {
      float s0 = bflo(sk[2 * ch]), s1 = bfhi(sk[2 * ch]), s2 = bflo(sk[2 * ch + 1]), s3 = bfhi(sk[2 * ch + 1]);
      float u[9];
#pragma unroll
      for (int i = 0; i < 9; ++i) {
        float k0 = ch ? bfhi(kr[i]) : bflo(kr[i]), k1 = ch ? bfhi(kr[i + 1]) : bflo(kr[i + 1]);
        float k2 = ch ? bfhi(kr[i + 2]) : bflo(kr[i + 2]), k3 = ch ? bfhi(kr[i + 3]) : bflo(kr[i + 3]);
        float a = rbf(k0 * s3);
        a = rbf(a + rbf(k1 * s2));
        a = rbf(a + rbf(k2 * s1));
        a = rbf(a + rbf(k3 * s0));
        u[i] = a * (ch ? bfhi(vr[i]) : bflo(vr[i]));
      }
      u32x4 c1 = {pk2(u[8], u[7]), pk2(u[6], u[5]), pk2(u[4], u[3]), pk2(u[2], u[1])};
      u32x4 c0 = {pk2(u[7], u[6]), pk2(u[5], u[4]), pk2(u[3], u[2]), pk2(u[1], u[0])};
      char* up = ws + WS_UR + ((size_t)(d + ch) * 2 * S_ + (2040 - t0)) * 2;
      *(u32x4*)up = c0;
      *(u32x4*)(up + S_ * 2) = c1;
    }
  }
  {
    const int t = 8 * b + w;
    float sum = 0.f;
#pragma unroll
    for (int it = 0; it < 2; ++it) {
      int v = lane + 64 * it;
      if (v < 72) {
        u32x4 a = *(const u32x4*)(x + (size_t)t * D_ + v * 8);
        u32x4 g = *(const u32x4*)(gate_w + v * 8);
#pragma unroll
        for (int i = 0; i < 4; ++i) sum += rbf(bflo(a[i]) * bflo(g[i])) + rbf(bfhi(a[i]) * bfhi(g[i]));
      }
    }
#pragma unroll
    for (int o = 32; o > 0; o >>= 1) sum += __shfl_xor(sum, o);
    if (lane == 0) {
      float z = rbf(sum);
      float g = rbf(1.0f / (1.0f + __expf(-z)));
      *(float*)(ws + WS_G + t * 4) = g;
    }
  }
}

constexpr int G2_SLOT = 24576, G2_NSLOT = 6, G2_WIN = 16;
constexpr int CV_K = 0, CV_U0 = 4160, CV_U1 = CV_U0 + 4640, CV_CH = 13440;
constexpr int K2_LDS = G2_SLOT * G2_NSLOT;

struct G2Ctx {
  const char* w2; unsigned voff[3]; unsigned lds0; int w;
};
template <int s> __device__ __forceinline__ void g2_issue_w(const G2Ctx& c) {
  unsigned dst = c.lds0 + (s % G2_NSLOT) * G2_SLOT + c.w * 3072;
#pragma unroll
  for (int i = 0; i < 3; ++i) dma16(c.voff[i], c.w2 + (size_t)(128 * s) * (D_ * 2), dst + i * 1024);
}
template <int s>
__device__ __forceinline__ void g2_slice(const G2Ctx& c, LAS char* wl, const u32x4* __restrict__ hsrc, u32x4 (&hw)[G2_WIN], bf16x8 (&wf)[3],
                                         f32x16 (&acc)[3]) {
  if constexpr (s >= 1 && s + 5 < 9) g2_issue_w<s + 5>(c);
#pragma unroll
  for (int j = 0; j < 8; ++j) {
    const int i = 8 * s + j;
    bf16x8 nf[3];
    if (i + 1 < 72) {
      const int sn = (i + 1) / 8, jn = (i + 1) % 8;
      LAS char* p = wl + (sn % G2_NSLOT) * G2_SLOT + (16 * jn) * 192;
#pragma unroll
      for (int t = 0; t < 3; ++t) nf[t] = tr_frag(p + t * 64, p + t * 64 + 8 * 192);
    }
    bf16x8 hf = __builtin_bit_cast(bf16x8, hw[i % G2_WIN]);
#pragma unroll
    for (int t = 0; t < 3; ++t) acc[t] = __builtin_amdgcn_mfma_f32_32x32x16_bf16(wf[t], hf, acc[t], 0, 0, 0);
    if (i + G2_WIN < 72) hw[i % G2_WIN] = hsrc[(size_t)(i + G2_WIN) * 64];
    if (i + 1 < 72) {
#pragma unroll
      for (int t = 0; t < 3; ++t) wf[t] = nf[t];
    }
  }
  if constexpr (s + 1 < 9) {
    wait_lgkm0();
    constexpr int nh = (72 - G2_WIN - 8 * s) > 8 ? 8 : ((72 - G2_WIN - 8 * s) > 0 ? (72 - G2_WIN - 8 * s) : 0);
    wait_vm_barrier<(s + 2 < 9) ? nh : 63>();
  }
}
template <int s>
__device__ __forceinline__ void g2_run(const G2Ctx& c, LAS char* wl, const u32x4* __restrict__ hsrc, u32x4 (&hw)[G2_WIN], bf16x8 (&wf)[3],
                                       f32x16 (&acc)[3]) {
  g2_slice<s>(c, wl, hsrc, hw, wf, acc);
  if constexpr (s + 1 < 9) g2_run<s + 1>(c, wl, hsrc, hw, wf, acc);
}

typedef unsigned u32x2a4 __attribute__((ext_vector_type(2), aligned(4)));

__device__ __forceinline__ void conv_channel(LAS char* cl, int d, int lane, const u16* __restrict__ ssm_D, char* __restrict__ ws) {
  const int rho = lane & 15, q = lane >> 4, pi = (lane >> 3) & 1, ii = lane & 7;
  LAS char* ap = cl + CV_K + 4 + 4 * rho + 16 * q;
  LAS char* bp = cl + CV_U0 + pi * 4640 + 2 * (2016 - 32 * ii + 8 * q) - 64 * 56;
  f32x4 acc[8] = {};
#pragma unroll 1
  for (int r = 0; r < 8; ++r) {
    bf16x8 af[8], bfr[8];
#pragma unroll
    for (int p = 0; p < 8; ++p) {
      u32x2 lo = *(LAS u32x2a4*)(ap + 64 * (8 * p) + 64 * r);
      u32x2 hi = *(LAS u32x2a4*)(ap + 64 * (8 * p) + 64 * r + 8);
      u32x4 v = {lo[0], lo[1], hi[0], hi[1]};
      af[p] = __builtin_bit_cast(bf16x8, v);
      bfr[p] = *(LAS bf16x8*)(bp + 64 * (56 - 8 * p) + 64 * r);
    }
#pragma unroll
    for (int p = 0; p < 8; ++p)
#pragma unroll
      for (int dl = 0; dl + p < 8; ++dl) acc[p + dl] = __builtin_amdgcn_mfma_f32_16x16x32_bf16(af[p], bfr[dl], acc[p + dl], 0, 0, 0);
  }
  const float Dv = bf2f(ssm_D[d]);
#pragma unroll
  for (int g = 0; g < 8; ++g) {
    const int T = 256 * g + 32 * ii + 8 * q;
    u32x4 uu = *(LAS u32x4*)(cl + CV_U1 + 2 * (2040 - T));
    float z[4];
#pragma unroll
    for (int k = 0; k < 4; ++k) {
      float u = pi ? bflo(uu[3 - k]) : bfhi(uu[3 - k]);
      z[k] = rbf(acc[g][k]) + rbf(u * Dv);
    }
    unsigned p01 = pk2(z[0], z[1]), p23 = pk2(z[2], z[3]);
    unsigned snd = pi ? p01 : p23;
    unsigned rcv = __builtin_amdgcn_update_dpp(0u, snd, 0x128, 0xf, 0xf, false);
    unsigned E = pi ? rcv : p01, O = pi ? p23 : rcv;
    u32x2 o = {(E & 0xffffu) | (O << 16), (E >> 16) | (O & 0xffff0000u)};
    *(u32x2*)(ws + WS_Z + ((size_t)d * S_ + T + 4 * pi) * 2) = o;
  }
}

__global__ __launch_bounds__(512, 2) void k2_kernel(const u16* __restrict__ w2, const u16* __restrict__ ssm_k, const u16* __restrict__ ssm_D,
                                                    char* __restrict__ ws) {
  extern __shared__ __attribute__((aligned(16))) char smem[];
  LAS char* lds = (LAS char*)smem;
  const int b = blockIdx.x, xcd = b & 7, r = b >> 3;
  const int tid = threadIdx.x, lane = tid & 63;
  const int w = __builtin_amdgcn_readfirstlane(tid >> 6);
  if (r >= 8) {
    const int rr = r - 8;
    const int mt = 4 * (xcd & 1) + (rr & 3), ks = xcd >> 1, nt = rr >> 2;
    G2Ctx c;
    c.w = w;
    c.lds0 = (unsigned)(size_t)lds;
    c.w2 = (const char*)w2 + ((size_t)(1152 * ks + 16 * w) * D_ + 96 * nt) * 2;
#pragma unroll
    for (int i = 0; i < 3; ++i) {
      int pos = 1024 * i + 16 * lane;
      c.voff[i] = (pos / 192) * (D_ * 2) + (pos % 192);
    }
    const u32x4* hsrc = (const u32x4*)(ws + WS_H) + ((size_t)((8 * mt + w) * 288 + 72 * ks) * 64 + lane);
    u32x4 hw[G2_WIN];
    g2_issue_w<0>(c);
    g2_issue_w<1>(c);
#pragma unroll
    for (int i = 0; i < G2_WIN; ++i) hw[i] = hsrc[(size_t)i * 64];
    asm volatile("" ::: "memory");
    g2_issue_w<2>(c);
    g2_issue_w<3>(c);
    g2_issue_w<4>(c);
    g2_issue_w<5>(c);
    LAS char* wl;
    {
      int gq = lane >> 4, chf = gq & 1, h = gq >> 1, q = (lane & 15) >> 2, p = lane & 3;
      wl = lds + (4 * h + q) * 192 + chf * 32 + p * 8;
    }
    bf16x8 wf[3];
    f32x16 acc[3] = {};
    wait_vm_barrier<G2_WIN + 12>();
#pragma unroll
    for (int t = 0; t < 3; ++t) wf[t] = tr_frag(wl + t * 64, wl + t * 64 + 8 * 192);
    g2_run<0>(c, wl, hsrc, hw, wf, acc);
    float* pp = (float*)(ws + WS_PART) + ((size_t)((ks * 64 + 8 * mt + w) * 18 + 3 * nt) * 256 + lane) * 4;
#pragma unroll
    for (int t = 0; t < 3; ++t)
#pragma unroll
      for (int qd = 0; qd < 4; ++qd) {
        f32x4 v = {acc[t][4 * qd], acc[t][4 * qd + 1], acc[t][4 * qd + 2], acc[t][4 * qd + 3]};
        *(f32x4*)(pp + (size_t)(t * 4 + qd) * 256) = v;
      }
  } else {
    const int cw = xcd * 8 + r;
    const int nch = (w == 0) ? 2 : 1;
    const unsigned lds0 = (unsigned)(size_t)lds;
    for (int it = 0; it < nch; ++it) {
      const int slot = it ? 8 : w;
      const int d = 9 * cw + slot;
      const char* kp = (const char*)ssm_k + (size_t)d * S_ * 2;
      const char* up = ws + WS_UR + (size_t)d * 2 * S_ * 2;
      unsigned dst = lds0 + slot * CV_CH;
#pragma unroll
      for (int i = 0; i < 4; ++i) dma16(lane * 16, kp + i * 1024, dst + CV_K + 64 + i * 1024);
#pragma unroll
      for (int i = 0; i < 4; ++i) dma16(lane * 16, up + i * 1024, dst + CV_U0 + i * 1024);
#pragma unroll
      for (int i = 0; i < 4; ++i) dma16(lane * 16, up + 4096 + i * 1024, dst + CV_U1 + i * 1024);
      if (lane < 60) {
        int o = lane < 4 ? CV_K + lane * 16 : (lane < 32 ? CV_U0 + 4096 + (lane - 4) * 16 : CV_U1 + 4096 + (lane - 32) * 16);
        *(LAS u32x4*)(lds + slot * CV_CH + o) = (u32x4){0u, 0u, 0u, 0u};
      }
    }
    wait_vm<0>();
    wait_lgkm0();
    for (int it = 0; it < nch; ++it) {
      const int slot = it ? 8 : w;
      conv_channel(lds + slot * CV_CH, 9 * cw + slot, lane, ssm_D, ws);
    }
  }
}

__global__ __launch_bounds__(192) void k3_kernel(const u16* __restrict__ q, const char* __restrict__ ws, u16* __restrict__ out) {
  __shared__ __attribute__((aligned(16))) char sm[32 * 208 + 96 * 80];
  char* bt = sm;
  char* zt = sm + 32 * 208;
  const int mb = blockIdx.x / 6, grp = blockIdx.x % 6;
  const int tid = threadIdx.x, lane = tid & 63, w = tid >> 6;
  const int dt = 3 * grp + w;
  const f32x4* pp = (const f32x4*)(ws + WS_PART) + (size_t)(mb * 18 + dt) * 256 + lane;
  f32x4 p[4][4];
#pragma unroll
  for (int ks = 0; ks < 4; ++ks)
#pragma unroll
    for (int qd = 0; qd < 4; ++qd) p[ks][qd] = pp[(size_t)ks * (64 * 18 * 256) + qd * 64];
  u32x4 zz[2];
#pragma unroll
  for (int i = 0; i < 2; ++i) {
    int dl = 16 * i + (lane >> 2);
    zz[i] = *(const u32x4*)(ws + WS_Z + ((size_t)(32 * dt + dl) * S_ + 32 * mb) * 2 + (lane & 3) * 16);
  }
  u32x4 qq[2];
  float gg[2];
#pragma unroll
  for (int i = 0; i < 2; ++i) {
    int pc = tid + 192 * i, t = pc / 12, c8 = pc % 12;
    qq[i] = *(const u32x4*)(q + (size_t)(32 * mb + t) * D_ + 96 * grp + 8 * c8);
    gg[i] = *(const float*)(ws + WS_G + (32 * mb + t) * 4);
  }
#pragma unroll
  for (int qd = 0; qd < 4; ++qd) {
    f32x4 s = (p[0][qd] + p[1][qd]) + (p[2][qd] + p[3][qd]);
    u32x2 o = {pk2(s[0], s[1]), pk2(s[2], s[3])};
    *(u32x2*)(bt + (lane & 31) * 208 + (32 * w + 8 * qd + 4 * (lane >> 5)) * 2) = o;
  }
#pragma unroll
  for (int i = 0; i < 2; ++i) *(u32x4*)(zt + (32 * w + 16 * i + (lane >> 2)) * 80 + (lane & 3) * 16) = zz[i];
  __syncthreads();
#pragma unroll
  for (int i = 0; i < 2; ++i) {
    int pc = tid + 192 * i, t = pc / 12, c8 = pc % 12;
    u32x4 bb = *(const u32x4*)(bt + t * 208 + c8 * 16);
    float g = gg[i], og = rbf(1.0f - g);
    float res[8];
#pragma unroll
    for (int j = 0; j < 8; ++j) {
      float z = bf2f(*(const u16*)(zt + (8 * c8 + j) * 80 + t * 2));
      float qv = (j & 1) ? bfhi(qq[i][j >> 1]) : bflo(qq[i][j >> 1]);
      float bv = (j & 1) ? bfhi(bb[j >> 1]) : bflo(bb[j >> 1]);
      float a = rbf(qv * z);
      res[j] = rbf(g * a) + rbf(og * bv);
    }
    u32x4 o = {pk2(res[0], res[1]), pk2(res[2], res[3]), pk2(res[4], res[5]), pk2(res[6], res[7])};
    *(u32x4*)(out + (size_t)(32 * mb + t) * D_ + 96 * grp + 8 * c8) = o;
  }
}

extern "C" void kernel_launch(void* const* d_in, const int* in_sizes, int n_in, void* d_out, int out_size, void* d_ws, size_t ws_size,
                              hipStream_t stream) {
  const u16* q = (const u16*)d_in[0];
  const u16* k = (const u16*)d_in[1];
  const u16* v = (const u16*)d_in[2];
  const u16* shift_k = (const u16*)d_in[3];
  const u16* ssm_k = (const u16*)d_in[4];
  const u16* ssm_D = (const u16*)d_in[5];
  const u16* w1 = (const u16*)d_in[7];
  const u16* w3 = (const u16*)d_in[8];
  const u16* w2 = (const u16*)d_in[9];
  const u16* gate_w = (const u16*)d_in[10];
  char* ws = (char*)d_ws;
  (void)hipFuncSetAttribute((const void*)k1_kernel, hipFuncAttributeMaxDynamicSharedMemorySize, K1_LDS);
  (void)hipFuncSetAttribute((const void*)k2_kernel, hipFuncAttributeMaxDynamicSharedMemorySize, K2_LDS);
  k1_kernel<<<256, 512, K1_LDS, stream>>>(q, k, v, shift_k, w1, w3, gate_w, ws);
  k2_kernel<<<256, 512, K2_LDS, stream>>>(w2, ssm_k, ssm_D, ws);
  k3_kernel<<<384, 192, 0, stream>>>(q, ws, (u16*)d_out);
}
```

```cpp
#include <hip/hip_runtime.h>
#include <hip/hip_bf16.h>

#define LAS __attribute__((address_space(3)))
typedef unsigned short u16;
typedef __bf16 bf16x2 __attribute__((ext_vector_type(2)));
typedef __bf16 bf16x4 __attribute__((ext_vector_type(4)));
typedef __bf16 bf16x8 __attribute__((ext_vector_type(8)));
typedef float f32x2 __attribute__((ext_vector_type(2)));
typedef float f32x4 __attribute__((ext_vector_type(4)));
typedef float f32x16 __attribute__((ext_vector_type(16)));
typedef unsigned u32x2 __attribute__((ext_vector_type(2)));
typedef unsigned u32x4 __attribute__((ext_vector_type(4)));

constexpr int S_ = 2048, D_ = 576, F_ = 4608;
constexpr size_t WS_H = 0;
constexpr size_t WS_PART = WS_H + (size_t)64 * 288 * 1024;
constexpr size_t WS_UR = WS_PART + (size_t)4 * S_ * D_ * 4;
constexpr size_t WS_Z = WS_UR + (size_t)D_ * 2 * S_ * 2;
constexpr size_t WS_G = WS_Z + (size_t)D_ * S_ * 2;
constexpr size_t WS_END = WS_G + S_ * 4;

#define STAMP(arr, i)
#define RSTAMP(arr, i)
#define STDUMP(arr, n, region)

__device__ __forceinline__ float bf2f(u16 v) { return __builtin_bit_cast(float, (unsigned)v << 16); }
__device__ __forceinline__ float bflo(unsigned v) { return __builtin_bit_cast(float, v << 16); }
__device__ __forceinline__ float bfhi(unsigned v) { return __builtin_bit_cast(float, v & 0xffff0000u); }
__device__ __forceinline__ float rbf(float x) { return (float)(__bf16)x; }
__device__ __forceinline__ unsigned pk2(float lo, float hi) {
  f32x2 v = {lo, hi};
  return __builtin_bit_cast(unsigned, __builtin_convertvector(v, bf16x2));
}
__device__ __forceinline__ f32x2 unpk2(unsigned v) { return (f32x2){bflo(v), bfhi(v)}; }
__device__ __forceinline__ f32x2 rbf2(f32x2 v) { return unpk2(pk2(v[0], v[1])); }
#define SCHED_FENCE() __builtin_amdgcn_sched_barrier(0)

__device__ __forceinline__ void dma16(unsigned voff, const void* sbase, unsigned lds_dst) {
  unsigned keep;
  asm volatile("s_mov_b32 %0, m0\n\ts_mov_b32 m0, %3\n\ts_nop 0\n\tglobal_load_lds_dwordx4 %1, %2\n\ts_mov_b32 m0, %0"
               : "=&s"(keep) : "v"(voff), "s"(sbase), "s"(lds_dst) : "memory");
}
__device__ __forceinline__ void dma16_16(unsigned voff, const void* sbase, unsigned lds_dst) {
  unsigned keep;
  asm volatile("s_mov_b32 %0, m0\n\ts_mov_b32 m0, %3\n\ts_mov_b64 exec, 0xffff\n\tglobal_load_lds_dwordx4 %1, %2\n\ts_mov_b64 exec, -1\n\ts_mov_b32 m0, %0"
               : "=&s"(keep) : "v"(voff), "s"(sbase), "s"(lds_dst) : "memory");
}
template <int N> __device__ __forceinline__ void wait_vm_barrier() {
  asm volatile("s_waitcnt vmcnt(%0)\n\ts_barrier" ::"n"(N) : "memory");
}
template <int N> __device__ __forceinline__ void wait_vm() { asm volatile("s_waitcnt vmcnt(%0)" ::"n"(N) : "memory"); }
__device__ __forceinline__ void wait_lgkm0() { asm volatile("s_waitcnt lgkmcnt(0)" ::: "memory"); }
__device__ __forceinline__ LAS char* ldsp(unsigned a) { return (LAS char*)(size_t)a; }

__device__ __forceinline__ bf16x8 tr_frag(LAS char* p0, LAS char* p1) {
  bf16x4 a = __builtin_amdgcn_ds_read_tr16_b64_v4bf16((LAS bf16x4*)p0);
  bf16x4 b = __builtin_amdgcn_ds_read_tr16_b64_v4bf16((LAS bf16x4*)p1);
  return __builtin_shufflevector(a, b, 0, 1, 2, 3, 4, 5, 6, 7);
}
__device__ __forceinline__ float silu_mul(float g, float u) {
  float e = __builtin_amdgcn_exp2f(g * -1.44269504088896f);
  return g * __builtin_amdgcn_rcpf(1.0f + e) * u;
}
__device__ __forceinline__ int mod9(int v) { return v >= 9 ? v - 9 : v; }

constexpr int K1_SLOT = 16384, K1_NSLOT = 4, K1_XOFF = K1_SLOT * K1_NSLOT, K1_XD = 3;
constexpr int K1_LDS = K1_XOFF + 8 * K1_XD * 4096;
constexpr int K1_PF = 4, K1_NS = 324, K1_NSEG = 21;

struct K1Ctx {
  const char* w1; const char* w3; const char* xb;
  char* hb;
  unsigned voff_wg, voff_wu, voff_x0, voff_x1;
  unsigned rd[4];
  unsigned xl0;
  unsigned lane16;
  int w, rot, nl;
};
template <int sg, int half> __device__ __forceinline__ void k1_issue_w(const K1Ctx& c) {
  const unsigned dst = (sg % K1_NSLOT) * K1_SLOT + half * 8192;
  const char* base = half ? c.w3 : c.w1;
  if constexpr (sg < 18) {
    const int ksl = mod9(sg % 9 + c.rot);
    dma16(half ? c.voff_wu : c.voff_wg, base + ((size_t)(64 * ksl + 8 * c.w) * F_ + 128 * c.nl + 64 * (sg / 9)) * 2, dst + c.w * 1024);
  } else {
    unsigned l = c.lane16 >> 4;
    asm volatile("" : "+v"(l));
    const unsigned rp = l >> 1;
    const unsigned vo = (half ? (rp ^ 4u) : rp) * (F_ * 2) + (l & 1u) * 16;
    if constexpr (sg < 20) {
      const int ksl = mod9(4 * (sg - 18) + (c.w >> 1) + c.rot);
      dma16(vo, base + ((size_t)(64 * ksl + 32 * (c.w & 1)) * F_ + 1024 + 16 * c.nl) * 2, dst + c.w * 1024);
    } else {
      const int ksl = mod9(8 + c.rot);
      dma16_16(vo, base + ((size_t)(64 * ksl + 8 * c.w) * F_ + 1024 + 16 * c.nl) * 2, dst + c.w * 256);
    }
  }
}
template <int p, int i> __device__ __forceinline__ void k1_issue_x(const K1Ctx& c) {
  const int ksl = mod9(p + c.rot);
  dma16((i & 1) ? c.voff_x1 : c.voff_x0, c.xb + (size_t)(8 * i) * (D_ * 2) + ksl * 128, K1_XOFF + c.w * (K1_XD * 4096) + (p % K1_XD) * 4096 + i * 1024);
}
template <int p, int j> __device__ __forceinline__ bf16x8 k1_read_x(const K1Ctx& c) {
  return *(LAS bf16x8*)(ldsp(c.xl0 ^ (32 * j)) + (p % K1_XD) * 4096);
}
template <int n> __device__ __forceinline__ bf16x8 k1_read_w(const K1Ctx& c, unsigned rc0, unsigned rc1) {
  constexpr int sg = n / 16, r = n % 16;
  if constexpr (sg < 18) {
    constexpr int t = r / 4, j = r % 4;
    LAS char* p = ldsp(c.rd[t]) + (sg % K1_NSLOT) * K1_SLOT + (16 * j) * 128;
    return tr_frag(p, p + 4 * 128);
  } else {
    const int off = (sg % K1_NSLOT) * K1_SLOT + (16 * r) * 32;
    return tr_frag(ldsp(rc0) + off, ldsp(rc1) + off);
  }
}
constexpr int k1_epi_slot(int chunk, int u) {
  return chunk == 0 ? 144 - 11 + (u * 23) / 32 : (u < 8 ? 288 - 11 + (u * 10) / 8 : 288 - 1 + (u - 8) + ((u - 8) >> 2));
}
constexpr int k1_store_slot(int chunk, int t) { return k1_epi_slot(chunk, 8 * t + 7); }
constexpr int k1_ops_in_slot(int n) {
  int sg = n / 16, r = n % 16, k = 0;
  if ((r == 1 || r == 3) && sg + 3 < K1_NSEG) ++k;
  if (r >= 9 && r <= 12 && sg + K1_XD + 1 < 9) ++k;
  for (int ch = 0; ch < 2; ++ch) for (int t = 0; t < 4; ++t) if (k1_store_slot(ch, t) == n) ++k;
  return k;
}
constexpr int k1_ops_range(int a, int b) { int k = 0; for (int n = a; n < b; ++n) if (n >= 0) k += k1_ops_in_slot(n); return k; }
constexpr int k1_wait(int sg) {
  if (sg + 1 >= K1_NSEG) return 63;
  return sg >= 2 ? k1_ops_range(16 * (sg - 2) + 4, 16 * sg) : -1;
}

template <int n>
__device__ __forceinline__ void k1_slot(K1Ctx& c, bf16x8 (&xf)[36], bf16x8 (&wf)[K1_PF + 1], f32x16 (&acc)[4], float (&he)[2], u32x4& ho, unsigned& rc0,
                                        unsigned& rc1) {
  constexpr int sg = n / 16, r = n % 16;
  if constexpr (n + K1_PF == 288) {
    unsigned l = c.lane16 >> 4;
    asm volatile("" : "+v"(l));
    unsigned mat = (l >> 4) & 1, h = l >> 5, q = (l & 15) >> 2, p = l & 3;
    unsigned base = mat * 8192 + (8 * h + q) * 32 + p * 8;
    rc0 = base + (mat ? 128 : 0);
    rc1 = base + (mat ? 0 : 128);
  }
  if constexpr (n + K1_PF < K1_NS) wf[(n + K1_PF) % (K1_PF + 1)] = k1_read_w<n + K1_PF>(c, rc0, rc1);
  if constexpr (sg + 1 < 9 && r < 4) xf[4 * (sg + 1) + r] = k1_read_x<sg + 1, r>(c);
  {
    constexpr int t = sg < 18 ? r / 4 : 0;
    constexpr int xi = sg < 18 ? 4 * (sg % 9) + (r % 4) : 16 * (sg - 18) + r;
    constexpr bool first = (sg < 18) ? (sg % 9 == 0 && r % 4 == 0) : (n == 288);
    if constexpr (first) acc[t] = __builtin_amdgcn_mfma_f32_32x32x16_bf16(wf[n % (K1_PF + 1)], xf[xi], (f32x16){}, 0, 0, 0);
    else acc[t] = __builtin_amdgcn_mfma_f32_32x32x16_bf16(wf[n % (K1_PF + 1)], xf[xi], acc[t], 0, 0, 0);
  }
  if constexpr ((r == 1 || r == 3) && sg + 3 < K1_NSEG) k1_issue_w<sg + 3, (r == 3)>(c);
  if constexpr (r >= 9 && r <= 12 && sg + K1_XD + 1 < 9) k1_issue_x<sg + K1_XD + 1, r - 9>(c);
#pragma unroll
  for (int ch = 0; ch < 2; ++ch)
#pragma unroll
    for (int u = 0; u < 32; ++u)
      if (k1_epi_slot(ch, u) == n) {
        const int t = u / 8, i = u % 8;
        he[i & 1] = silu_mul(acc[t][i], acc[t][i + 8]);
        if (i & 1) ho[i >> 1] = pk2(he[0], he[1]);
        if (i == 7) *(u32x4*)(c.hb + (size_t)(8 * c.nl + 4 * ch + t) * 1024 + c.lane16) = ho;
      }
  SCHED_FENCE();
  if constexpr (r == 15 && n + 1 < K1_NS) {
    if constexpr (sg + 1 >= 2) wait_vm_barrier<k1_wait(sg + 1)>();
    else wait_vm_barrier<4 + k1_ops_range(0, 16)>();
    STAMP(c.st, sg + 3);
    SCHED_FENCE();
  }
  if constexpr (n + 1 < K1_NS) k1_slot<n + 1>(c, xf, wf, acc, he, ho, rc0, rc1);
}

__global__ __launch_bounds__(512, 2) void k1_kernel(const u16* __restrict__ x, const u16* __restrict__ kk, const u16* __restrict__ vv,
                                                    const u16* __restrict__ shift_k, const u16* __restrict__ w1, const u16* __restrict__ w3,
                                                    const u16* __restrict__ gate_w, char* __restrict__ ws) {
  extern __shared__ __attribute__((aligned(16))) char smem[];
  const int b = blockIdx.x, xcd = b & 7, r = b >> 3;
  const int ml = r & 3, nl = r >> 2, fq = xcd >> 1;
  const int mt = 4 * (xcd & 1) + ml;
  const int tid = threadIdx.x, lane = tid & 63;
  const int w = __builtin_amdgcn_readfirstlane(tid >> 6);
  K1Ctx c;
  RSTAMP(c.st, 26);
  STAMP(c.st, 0);
  c.w = w;
  c.nl = nl;
  c.rot = (nl + 2 * ml) % 9;
  c.lane16 = lane * 16;
  c.w1 = (const char*)w1 + (size_t)(1152 * fq) * 2;
  c.w3 = (const char*)w3 + (size_t)(1152 * fq) * 2;
  c.xb = (const char*)x + (size_t)(256 * mt + 32 * w) * (D_ * 2);
  c.hb = ws + WS_H + (size_t)((8 * mt + w) * 288 + 72 * fq) * 1024;
  {
    int row = lane >> 3, pos = lane & 7;
    int sw = 2 * ((row >> 1) & 1);
    c.voff_wg = row * (F_ * 2) + ((pos ^ sw) * 16);
    c.voff_wu = row * (F_ * 2) + ((pos ^ sw ^ 4) * 16);
    int sx = lane >> 4;
    c.voff_x0 = row * (D_ * 2) + ((pos ^ sx) * 16);
    c.voff_x1 = row * (D_ * 2) + ((pos ^ (sx + 4)) * 16);
  }
  k1_issue_w<0, 0>(c); k1_issue_w<0, 1>(c);
  k1_issue_x<0, 0>(c); k1_issue_x<0, 1>(c); k1_issue_x<0, 2>(c); k1_issue_x<0, 3>(c);

  const int ptid = tid < 288 ? tid : 287;
  const int pd = 72 * (b & 7) + 2 * (ptid % 36), pt0 = 64 * (b >> 3) + 8 * (ptid / 36);
  unsigned kr[12], vr[9];
  u32x4 sk, ga0, ga1, gw0, gw1;
  {
    const unsigned o0 = (unsigned)(pt0 * D_ + pd) * 2;
    const unsigned oneg = pt0 > 0 ? o0 - 4 * (D_ * 2) : o0;
    const unsigned ostep = pt0 > 0 ? D_ * 2 : 0;
#pragma unroll
    for (int i = 0; i < 4; ++i) asm volatile("global_load_dword %0, %1, %2" : "=v"(kr[i]) : "v"(oneg + i * ostep), "s"(kk) : "memory");
#pragma unroll
    for (int i = 0; i < 8; ++i) asm volatile("global_load_dword %0, %1, %2" : "=v"(kr[4 + i]) : "v"(o0 + i * (D_ * 2)), "s"(kk) : "memory");
    asm volatile("global_load_dword %0, %1, %2" : "=v"(vr[0]) : "v"(oneg + 3 * ostep), "s"(vv) : "memory");
#pragma unroll
    for (int i = 0; i < 8; ++i) asm volatile("global_load_dword %0, %1, %2" : "=v"(vr[1 + i]) : "v"(o0 + i * (D_ * 2)), "s"(vv) : "memory");
    asm volatile("global_load_dwordx4 %0, %1, %2" : "=v"(sk) : "v"((unsigned)pd * 8), "s"(shift_k) : "memory");
    const char* xr = (const char*)x + (size_t)(8 * b + w) * (D_ * 2);
    const unsigned l2 = (64 + (lane & 7)) * 16;
    asm volatile("global_load_dwordx4 %0, %1, %2" : "=v"(ga0) : "v"(c.lane16), "s"(xr) : "memory");
    asm volatile("global_load_dwordx4 %0, %1, %2" : "=v"(ga1) : "v"(l2), "s"(xr) : "memory");
    asm volatile("global_load_dwordx4 %0, %1, %2" : "=v"(gw0) : "v"(c.lane16), "s"(gate_w) : "memory");
    asm volatile("global_load_dwordx4 %0, %1, %2" : "=v"(gw1) : "v"(l2), "s"(gate_w) : "memory");
  }
  k1_issue_w<1, 0>(c); k1_issue_w<1, 1>(c);
  k1_issue_x<1, 0>(c); k1_issue_x<1, 1>(c); k1_issue_x<1, 2>(c); k1_issue_x<1, 3>(c);
  k1_issue_w<2, 0>(c); k1_issue_w<2, 1>(c);
  k1_issue_x<2, 0>(c); k1_issue_x<2, 1>(c); k1_issue_x<2, 2>(c); k1_issue_x<2, 3>(c);

  {
    int gq = lane >> 4, mat = gq & 1, h = gq >> 1, q = (lane & 15) >> 2, p = lane & 3;
    int X = 2 * (q >> 1) + 4 * mat;
#pragma unroll
    for (int t = 0; t < 4; ++t) c.rd[t] = mat * 8192 + (8 * h + q) * 128 + (((2 * t) ^ X) + (p >> 1)) * 16 + (p & 1) * 8;
    int cc = lane & 31, hh = lane >> 5;
    c.xl0 = K1_XOFF + w * (K1_XD * 4096) + cc * 128 + ((hh ^ ((cc >> 1) & 7)) * 16);
  }
  asm volatile("s_waitcnt vmcnt(12)"
               : "+v"(kr[0]), "+v"(kr[1]), "+v"(kr[2]), "+v"(kr[3]), "+v"(kr[4]), "+v"(kr[5]), "+v"(kr[6]), "+v"(kr[7]), "+v"(kr[8]), "+v"(kr[9]),
                 "+v"(kr[10]), "+v"(kr[11]), "+v"(vr[0]), "+v"(vr[1]), "+v"(vr[2]), "+v"(vr[3]), "+v"(vr[4]), "+v"(vr[5]), "+v"(vr[6]), "+v"(vr[7]),
                 "+v"(vr[8]), "+v"(sk), "+v"(ga0), "+v"(ga1), "+v"(gw0), "+v"(gw1)::"memory");
  STAMP(c.st, 1);
  bf16x8 xf[36];
  bf16x8 wf[K1_PF + 1];
  f32x16 acc[4];
  xf[0] = k1_read_x<0, 0>(c); xf[1] = k1_read_x<0, 1>(c); xf[2] = k1_read_x<0, 2>(c); xf[3] = k1_read_x<0, 3>(c);
  asm volatile("s_waitcnt lgkmcnt(0)" : "+v"(xf[0]), "+v"(xf[1]), "+v"(xf[2]), "+v"(xf[3])::"memory");
  k1_issue_x<3, 0>(c); k1_issue_x<3, 1>(c); k1_issue_x<3, 2>(c); k1_issue_x<3, 3>(c);
  {
    float sum = 0.f, sum1 = 0.f;
#pragma unroll
    for (int i = 0; i < 4; ++i) {
      f32x2 p0 = rbf2(unpk2(ga0[i]) * unpk2(gw0[i])), p1 = rbf2(unpk2(ga1[i]) * unpk2(gw1[i]));
      sum += p0[0] + p0[1];
      sum1 += p1[0] + p1[1];
    }
    if (lane < 8) sum += sum1;
#pragma unroll
    for (int o = 32; o > 0; o >>= 1) sum += __shfl_xor(sum, o);
    if (lane == 0) {
      float z = rbf(sum);
      float g = rbf(1.0f / (1.0f + __expf(-z)));
      *(float*)(ws + WS_G + (8 * b + w) * 4) = g;
    }
  }
  if (w < 5) {
    if (pt0 == 0) { kr[0] = kr[1] = kr[2] = kr[3] = 0u; vr[0] = 0u; }
    const f32x2 s0 = {bflo(sk[0]), bflo(sk[2])}, s1 = {bfhi(sk[0]), bfhi(sk[2])}, s2 = {bflo(sk[1]), bflo(sk[3])}, s3 = {bfhi(sk[1]), bfhi(sk[3])};
    f32x2 k2[12], u[9];
#pragma unroll
    for (int i = 0; i < 12; ++i) k2[i] = unpk2(kr[i]);
#pragma unroll
    for (int i = 0; i < 9; ++i) {
      f32x2 a = rbf2(k2[i] * s3);
      a = rbf2(a + rbf2(k2[i + 1] * s2));
      a = rbf2(a + rbf2(k2[i + 2] * s1));
      a = rbf2(a + rbf2(k2[i + 3] * s0));
      u[i] = a * unpk2(vr[i]);
    }
    if (tid < 288) {
#pragma unroll
      for (int ch = 0; ch < 2; ++ch) {
        u32x4 c1 = {pk2(u[8][ch], u[7][ch]), pk2(u[6][ch], u[5][ch]), pk2(u[4][ch], u[3][ch]), pk2(u[2][ch], u[1][ch])};
        u32x4 c0 = {pk2(u[7][ch], u[6][ch]), pk2(u[5][ch], u[4][ch]), pk2(u[3][ch], u[2][ch]), pk2(u[1][ch], u[0][ch])};
        char* up = ws + WS_UR + ((size_t)(pd + ch) * 2 * S_ + (2040 - pt0)) * 2;
        *(u32x4*)up = c0;
        *(u32x4*)(up + S_ * 2) = c1;
      }
    }
  }
  wait_vm_barrier<10>();
  STAMP(c.st, 2);
  unsigned rc0 = 0, rc1 = 0;
  wf[0] = k1_read_w<0>(c, rc0, rc1); wf[1] = k1_read_w<1>(c, rc0, rc1); wf[2] = k1_read_w<2>(c, rc0, rc1); wf[3] = k1_read_w<3>(c, rc0, rc1);
  static_assert(K1_PF == 4, "prologue reads");
  SCHED_FENCE();
  float he[2];
  u32x4 ho;
  k1_slot<0>(c, xf, wf, acc, he, ho, rc0, rc1);
  {
    u32x4 o;
#pragma unroll
    for (int i = 0; i < 4; ++i) o[i] = pk2(silu_mul(acc[0][2 * i], acc[0][2 * i + 8]), silu_mul(acc[0][2 * i + 1], acc[0][2 * i + 9]));
    *(u32x4*)(c.hb + (size_t)(64 + nl) * 1024 + c.lane16) = o;
  }
}

constexpr int G2_SLOT = 24576, G2_NSLOT = 4, G2_WIN = 16, G2_PF = 4, G2_NS = 216;
constexpr int CV_K = 0, CV_U0 = 4160, CV_U1 = CV_U0 + 4640, CV_CH = 13440;
constexpr int K2_LDS = 9 * CV_CH;

struct G2Ctx {
  const char* w2; const char* hs; unsigned voff[3]; unsigned rd; unsigned lane16; int w, rot;
};
template <int s, int i> __device__ __forceinline__ void g2_issue_w(const G2Ctx& c) {
  const int ksl = mod9(s + c.rot);
  dma16(c.voff[i], c.w2 + (size_t)(128 * ksl) * (D_ * 2), (s % G2_NSLOT) * G2_SLOT + c.w * 3072 + i * 1024);
}
template <int i> __device__ __forceinline__ u32x4 g2_load_h(const G2Ctx& c) {
  const int ksl = mod9(i / 8 + c.rot);
  return *(const u32x4*)(c.hs + (size_t)(8 * ksl + (i % 8)) * 1024 + c.lane16);
}
template <int n> __device__ __forceinline__ bf16x8 g2_read_w(const G2Ctx& c) {
  constexpr int i = n / 3, t = n % 3, s = i / 8, j = i % 8;
  LAS char* p = ldsp(c.rd) + (s % G2_NSLOT) * G2_SLOT + (16 * j) * 192 + t * 64;
  return tr_frag(p, p + 8 * 192);
}
constexpr int g2_ops_in_slot(int n) {
  int s = n / 24, r = n % 24, k = 0;
  if ((r == 1 || r == 4 || r == 7) && s + 3 < 9 && s >= 1) ++k;
  if (s == 0 && (r == 1 || r == 4 || r == 7 || r == 13 || r == 16 || r == 19)) ++k;
  if (n % 3 == 2 && n / 3 + G2_WIN < 72) ++k;
  return k;
}
constexpr int g2_ops_range(int a, int b) { int k = 0; for (int n = a; n < b; ++n) k += g2_ops_in_slot(n); return k; }
constexpr int cmin(int a, int b) { return a < b ? a : b; }
template <int n>
__device__ __forceinline__ void g2_slot(G2Ctx& c, u32x4 (&hw)[G2_WIN], bf16x8 (&wf)[G2_PF + 1], f32x16 (&acc)[3]) {
  constexpr int i = n / 3, t = n % 3, s = n / 24, r = n % 24;
  if constexpr (n + G2_PF < G2_NS) wf[(n + G2_PF) % (G2_PF + 1)] = g2_read_w<n + G2_PF>(c);
  if constexpr (i == 0) acc[t] = __builtin_amdgcn_mfma_f32_32x32x16_bf16(wf[n % (G2_PF + 1)], __builtin_bit_cast(bf16x8, hw[i % G2_WIN]), (f32x16){}, 0, 0, 0);
  else acc[t] = __builtin_amdgcn_mfma_f32_32x32x16_bf16(wf[n % (G2_PF + 1)], __builtin_bit_cast(bf16x8, hw[i % G2_WIN]), acc[t], 0, 0, 0);
  if constexpr ((r == 1 || r == 4 || r == 7) && s + 3 < 9 && s >= 1) g2_issue_w<s + 3, r / 3>(c);
  if constexpr (s == 0 && (r == 1 || r == 4 || r == 7)) g2_issue_w<2, r / 3>(c);
  if constexpr (s == 0 && (r == 13 || r == 16 || r == 19)) g2_issue_w<3, (r - 12) / 3>(c);
  if constexpr (t == 2 && i + G2_WIN < 72) hw[i % G2_WIN] = g2_load_h<i + G2_WIN>(c);
  SCHED_FENCE();
  if constexpr (n == 11) {
    wait_vm_barrier<8 + g2_ops_range(0, 12)>();
    SCHED_FENCE();
  }
  if constexpr (r == 23 && n + 1 < G2_NS) {
    constexpr int last = s == 0 ? 7 : (s == 1 ? 19 : 24 * (s - 1) + 7);
    wait_vm_barrier<(s + 2 < 9) ? cmin(63, g2_ops_range(last + 1, n + 1)) : 63>();
    STAMP(c.st, s + 2);
    SCHED_FENCE();
  }
  if constexpr (n + 1 < G2_NS) g2_slot<n + 1>(c, hw, wf, acc);
}

typedef unsigned u32x2a4 __attribute__((ext_vector_type(2), aligned(4)));
__device__ __forceinline__ bf16x8 cv_read_a(LAS char* p) {
  u32x2 lo = *(LAS u32x2a4*)p;
  u32x2 hi = *(LAS u32x2a4*)(p + 8);
  u32x4 v = {lo[0], lo[1], hi[0], hi[1]};
  return __builtin_bit_cast(bf16x8, v);
}
template <bool PREF>
__device__ __forceinline__ void cv_block(f32x4 (&acc)[8], const bf16x8 (&af)[8], const bf16x8 (&bf)[8], bf16x8 (&naf)[8], bf16x8 (&nbf)[8], LAS char* ap,
                                         LAS char* bp) {
  int k = 0;
#pragma unroll
  for (int p = 0; p < 8; ++p)
#pragma unroll
    for (int dl = 0; dl + p < 8; ++dl) {
      if (PREF && (k & 1) == 0 && k < 32) {
        const int f = k >> 2;
        if (k & 2) naf[f] = cv_read_a(ap + 512 * f);
        else nbf[f] = *(LAS bf16x8*)(bp + 64 * (56 - 8 * f));
      }
      acc[p + dl] = __builtin_amdgcn_mfma_f32_16x16x32_bf16(af[p], bf[dl], acc[p + dl], 0, 0, 0);
      if (k & 1) SCHED_FENCE();
      ++k;
    }
}
__device__ __forceinline__ void cv_store(const f32x4& y, LAS char* cl, int g, int d, int lane, float Dv, char* __restrict__ ws) {
  const int q = lane >> 4, pi = (lane >> 3) & 1, ii = lane & 7;
  const int T = 256 * g + 32 * ii + 8 * q;
  u32x4 uu = *(LAS u32x4*)(cl + CV_U1 + 2 * (2040 - T));
  float z[4];
#pragma unroll
  for (int k = 0; k < 4; ++k) {
    float u = pi ? bflo(uu[3 - k]) : bfhi(uu[3 - k]);
    z[k] = rbf(y[k]) + rbf(u * Dv);
  }
  unsigned p01 = pk2(z[0], z[1]), p23 = pk2(z[2], z[3]);
  unsigned snd = pi ? p01 : p23;
  unsigned rcv = __builtin_amdgcn_update_dpp(0u, snd, 0x128, 0xf, 0xf, false);
  unsigned E = pi ? rcv : p01, O = pi ? p23 : rcv;
  u32x2 o = {(E & 0xffffu) | (O << 16), (E >> 16) | (O & 0xffff0000u)};
  *(u32x2*)(ws + WS_Z + ((size_t)d * S_ + T + 4 * pi) * 2) = o;
}

__global__ __launch_bounds__(512, 2) void k2_kernel(const u16* __restrict__ w2, const u16* __restrict__ ssm_k, const u16* __restrict__ ssm_D,
                                                    char* __restrict__ ws) {
  extern __shared__ __attribute__((aligned(16))) char smem[];
  LAS char* lds = (LAS char*)smem;
  const int b = blockIdx.x, xcd = b & 7, r = b >> 3;
  const int tid = threadIdx.x, lane = tid & 63;
  const int w = __builtin_amdgcn_readfirstlane(tid >> 6);
  if (r < 24) {
    const int ml = r & 3, nt = r >> 2;
    const int mt = 4 * (xcd & 1) + ml, ks = xcd >> 1;
    G2Ctx c;
    RSTAMP(c.st, 26);
    STAMP(c.st, 0);
    c.w = w;
    c.rot = (nt + 2 * ml) % 9;
    c.lane16 = lane * 16;
    c.w2 = (const char*)w2 + ((size_t)(1152 * ks + 16 * w) * D_ + 96 * nt) * 2;
#pragma unroll
    for (int i = 0; i < 3; ++i) {
      int pos = 1024 * i + 16 * lane;
      c.voff[i] = (pos / 192) * (D_ * 2) + (pos % 192);
    }
    c.hs = ws + WS_H + (size_t)((8 * mt + w) * 288 + 72 * ks) * 1024;
    u32x4 hw[G2_WIN];
    g2_issue_w<0, 0>(c); g2_issue_w<0, 1>(c); g2_issue_w<0, 2>(c);
    SCHED_FENCE();
    hw[0] = g2_load_h<0>(c); hw[1] = g2_load_h<1>(c); hw[2] = g2_load_h<2>(c); hw[3] = g2_load_h<3>(c);
    SCHED_FENCE();
    hw[4] = g2_load_h<4>(c); hw[5] = g2_load_h<5>(c); hw[6] = g2_load_h<6>(c); hw[7] = g2_load_h<7>(c);
    SCHED_FENCE();
    g2_issue_w<1, 0>(c); g2_issue_w<1, 1>(c); g2_issue_w<1, 2>(c);
    SCHED_FENCE();
    hw[8] = g2_load_h<8>(c); hw[9] = g2_load_h<9>(c); hw[10] = g2_load_h<10>(c); hw[11] = g2_load_h<11>(c);
    SCHED_FENCE();
    hw[12] = g2_load_h<12>(c); hw[13] = g2_load_h<13>(c); hw[14] = g2_load_h<14>(c); hw[15] = g2_load_h<15>(c);
    SCHED_FENCE();
    static_assert(G2_WIN == 16, "prologue");
    {
      int gq = lane >> 4, chf = gq & 1, h = gq >> 1, q = (lane & 15) >> 2, p = lane & 3;
      c.rd = (4 * h + q) * 192 + chf * 32 + p * 8;
    }
    bf16x8 wf[G2_PF + 1];
    f32x16 acc[3];
    wait_vm_barrier<19>();
    STAMP(c.st, 1);
    wf[0] = g2_read_w<0>(c); wf[1] = g2_read_w<1>(c); wf[2] = g2_read_w<2>(c); wf[3] = g2_read_w<3>(c);
    SCHED_FENCE();
    g2_slot<0>(c, hw, wf, acc);
    float* pp = (float*)(ws + WS_PART) + ((size_t)((ks * 64 + 8 * mt + w) * 18 + 3 * nt) * 256 + lane) * 4;
#pragma unroll
    for (int t = 0; t < 3; ++t)
#pragma unroll
      for (int qd = 0; qd < 4; ++qd) {
        f32x4 v = {acc[t][4 * qd], acc[t][4 * qd + 1], acc[t][4 * qd + 2], acc[t][4 * qd + 3]};
        *(f32x4*)(pp + (size_t)(t * 4 + qd) * 256) = v;
      }
  } else {
    const int cw = xcd * 8 + (r - 24);
    RSTAMP(st, 26);
    STAMP(st, 0);
    const int d = 9 * cw + w, d8 = 9 * cw + 8;
    {
      const char* kp = (const char*)ssm_k + (size_t)d * S_ * 2;
      const char* up = ws + WS_UR + (size_t)d * 2 * S_ * 2;
      const unsigned dst = w * CV_CH;
#pragma unroll
      for (int i = 0; i < 4; ++i) {
        dma16(lane * 16, kp + i * 1024, dst + CV_K + 64 + i * 1024);
        dma16(lane * 16, up + i * 1024, dst + CV_U0 + i * 1024);
        dma16(lane * 16, up + 4096 + i * 1024, dst + CV_U1 + i * 1024);
      }
      const char* kp8 = (const char*)ssm_k + (size_t)d8 * S_ * 2;
      const char* up8 = ws + WS_UR + (size_t)d8 * 2 * S_ * 2;
      dma16(lane * 16, up8 + w * 1024, 8 * CV_CH + (w < 4 ? CV_U0 + w * 1024 : CV_U1 + (w - 4) * 1024));
      if (w < 4) dma16(lane * 16, kp8 + w * 1024, 8 * CV_CH + CV_K + 64 + w * 1024);
      if (lane < 60) {
        int o = lane < 4 ? CV_K + lane * 16 : (lane < 32 ? CV_U0 + 4096 + (lane - 4) * 16 : CV_U1 + 4096 + (lane - 32) * 16);
        *(LAS u32x4*)(lds + w * CV_CH + o) = (u32x4){0u, 0u, 0u, 0u};
        if (w == 7) *(LAS u32x4*)(lds + 8 * CV_CH + o) = (u32x4){0u, 0u, 0u, 0u};
      }
    }
    const float Dv = bf2f(ssm_D[d]), Dv8 = bf2f(ssm_D[d8]);
    const int rho = lane & 15, q = lane >> 4, pi = (lane >> 3) & 1, ii = lane & 7;
    const int aoff = CV_K + 4 + 4 * rho + 16 * q;
    const int boff = CV_U0 + pi * 4640 + 2 * (2016 - 32 * ii + 8 * q) - 64 * 56;
    LAS char* cl = lds + w * CV_CH;
    asm volatile("s_waitcnt vmcnt(0) lgkmcnt(0)\n\ts_barrier" ::: "memory");
    STAMP(st, 1);
    f32x4 acc[8] = {};
    bf16x8 a0[8], b0[8], a1[8], b1[8];
#pragma unroll
    for (int f = 0; f < 8; ++f) {
      b0[f] = *(LAS bf16x8*)(cl + boff + 64 * (56 - 8 * f));
      a0[f] = cv_read_a(cl + aoff + 512 * f);
    }
    SCHED_FENCE();
#pragma unroll 1
    for (int rr = 0; rr < 8; rr += 2) {
      cv_block<true>(acc, a0, b0, a1, b1, cl + aoff + 64 * (rr + 1), cl + boff + 64 * (rr + 1));
      LAS char* nb = rr < 6 ? cl + 64 * (rr + 2) : lds + 8 * CV_CH + 64 * w;
      cv_block<true>(acc, a1, b1, a0, b0, nb + aoff, nb + boff);
    }
    STAMP(st, 2);
    f32x4 acc8[8] = {};
    cv_block<false>(acc8, a0, b0, a1, b1, cl, cl);
#pragma unroll
    for (int g = 0; g < 8; ++g) cv_store(acc[g], cl, g, d, lane, Dv, ws);
    wait_lgkm0();
#pragma unroll
    for (int g = 0; g < 8; ++g) *(LAS f32x4*)(cl + g * 1024 + lane * 16) = acc8[g];
    asm volatile("s_waitcnt lgkmcnt(0)\n\ts_barrier" ::: "memory");
    STAMP(st, 3);
    f32x4 y = {};
#pragma unroll
    for (int ww = 0; ww < 8; ++ww) y += *(LAS f32x4*)(lds + ww * CV_CH + w * 1024 + lane * 16);
    cv_store(y, lds + 8 * CV_CH, w, d8, lane, Dv8, ws);
  }
}

__global__ __launch_bounds__(192) void k3_kernel(const u16* __restrict__ q, const char* __restrict__ ws, u16* __restrict__ out) {
  __shared__ __attribute__((aligned(16))) char sm[32 * 208 + 96 * 80];
  char* bt = sm;
  char* zt = sm + 32 * 208;
  const int mb = blockIdx.x / 6, grp = blockIdx.x % 6;
  const int tid = threadIdx.x, lane = tid & 63, w = tid >> 6;
  const int dt = 3 * grp + w;
  const f32x4* pp = (const f32x4*)(ws + WS_PART) + (size_t)(mb * 18 + dt) * 256 + lane;
  f32x4 p[4][4];
#pragma unroll
  for (int ks = 0; ks < 4; ++ks)
#pragma unroll
    for (int qd = 0; qd < 4; ++qd) p[ks][qd] = pp[(size_t)ks * (64 * 18 * 256) + qd * 64];
  u32x4 zz[2];
#pragma unroll
  for (int i = 0; i < 2; ++i) {
    int dl = 16 * i + (lane >> 2);
    zz[i] = *(const u32x4*)(ws + WS_Z + ((size_t)(32 * dt + dl) * S_ + 32 * mb) * 2 + (lane & 3) * 16);
  }
  u32x4 qq[2];
  float gg[2];
#pragma unroll
  for (int i = 0; i < 2; ++i) {
    int pc = tid + 192 * i, t = pc / 12, c8 = pc % 12;
    qq[i] = *(const u32x4*)(q + (size_t)(32 * mb + t) * D_ + 96 * grp + 8 * c8);
    gg[i] = *(const float*)(ws + WS_G + (32 * mb + t) * 4);
  }
#pragma unroll
  for (int qd = 0; qd < 4; ++qd) {
    f32x4 s = (p[0][qd] + p[1][qd]) + (p[2][qd] + p[3][qd]);
    u32x2 o = {pk2(s[0], s[1]), pk2(s[2], s[3])};
    *(u32x2*)(bt + (lane & 31) * 208 + (32 * w + 8 * qd + 4 * (lane >> 5)) * 2) = o;
  }
#pragma unroll
  for (int i = 0; i < 2; ++i) *(u32x4*)(zt + (32 * w + 16 * i + (lane >> 2)) * 80 + (lane & 3) * 16) = zz[i];
  __syncthreads();
#pragma unroll
  for (int i = 0; i < 2; ++i) {
    int pc = tid + 192 * i, t = pc / 12, c8 = pc % 12;
    u32x4 bb = *(const u32x4*)(bt + t * 208 + c8 * 16);
    float g = gg[i], og = rbf(1.0f - g);
    float res[8];
#pragma unroll
    for (int j = 0; j < 8; ++j) {
      float z = bf2f(*(const u16*)(zt + (8 * c8 + j) * 80 + t * 2));
      float qv = (j & 1) ? bfhi(qq[i][j >> 1]) : bflo(qq[i][j >> 1]);
      float bv = (j & 1) ? bfhi(bb[j >> 1]) : bflo(bb[j >> 1]);
      float a = rbf(qv * z);
      res[j] = rbf(g * a) + rbf(og * bv);
    }
    u32x4 o = {pk2(res[0], res[1]), pk2(res[2], res[3]), pk2(res[4], res[5]), pk2(res[6], res[7])};
    *(u32x4*)(out + (size_t)(32 * mb + t) * D_ + 96 * grp + 8 * c8) = o;
  }
}

extern "C" void kernel_launch(void* const* d_in, const int* in_sizes, int n_in, void* d_out, int out_size, void* d_ws, size_t ws_size,
                              hipStream_t stream) {
  const u16* q = (const u16*)d_in[0];
  const u16* k = (const u16*)d_in[1];
  const u16* v = (const u16*)d_in[2];
  const u16* shift_k = (const u16*)d_in[3];
  const u16* ssm_k = (const u16*)d_in[4];
  const u16* ssm_D = (const u16*)d_in[5];
  const u16* w1 = (const u16*)d_in[7];
  const u16* w3 = (const u16*)d_in[8];
  const u16* w2 = (const u16*)d_in[9];
  const u16* gate_w = (const u16*)d_in[10];
  char* ws = (char*)d_ws;
  (void)hipFuncSetAttribute((const void*)k1_kernel, hipFuncAttributeMaxDynamicSharedMemorySize, K1_LDS);
  (void)hipFuncSetAttribute((const void*)k2_kernel, hipFuncAttributeMaxDynamicSharedMemorySize, K2_LDS);
  k1_kernel<<<256, 512, K1_LDS, stream>>>(q, k, v, shift_k, w1, w3, gate_w, ws);
  k2_kernel<<<256, 512, K2_LDS, stream>>>(w2, ssm_k, ssm_D, ws);
  k3_kernel<<<384, 192, 0, stream>>>(q, ws, (u16*)d_out);
}
```

```cpp
#include <hip/hip_runtime.h>
#include <hip/hip_bf16.h>

#define LAS __attribute__((address_space(3)))
typedef unsigned short u16;
typedef __bf16 bf16x2 __attribute__((ext_vector_type(2)));
typedef __bf16 bf16x4 __attribute__((ext_vector_type(4)));
typedef __bf16 bf16x8 __attribute__((ext_vector_type(8)));
typedef float f32x2 __attribute__((ext_vector_type(2)));
typedef float f32x4 __attribute__((ext_vector_type(4)));
typedef float f32x16 __attribute__((ext_vector_type(16)));
typedef unsigned u32x2 __attribute__((ext_vector_type(2)));
typedef unsigned u32x4 __attribute__((ext_vector_type(4)));

constexpr int S_ = 2048, D_ = 576, F_ = 4608;
constexpr size_t WS_H = 0;
constexpr size_t WS_PART = WS_H + (size_t)64 * 288 * 1024;
constexpr size_t WS_UR = WS_PART + (size_t)4 * S_ * D_ * 4;
constexpr size_t WS_Z = WS_UR + (size_t)D_ * 2 * S_ * 2;
constexpr size_t WS_G = WS_Z + (size_t)D_ * S_ * 2;
constexpr size_t WS_END = WS_G + S_ * 4;

#define STAMP(arr, i)
#define RSTAMP(arr, i)
#define STDUMP(arr, n, region)

__device__ __forceinline__ float bf2f(u16 v) { return __builtin_bit_cast(float, (unsigned)v << 16); }
__device__ __forceinline__ float bflo(unsigned v) { return __builtin_bit_cast(float, v << 16); }
__device__ __forceinline__ float bfhi(unsigned v) { return __builtin_bit_cast(float, v & 0xffff0000u); }
__device__ __forceinline__ float rbf(float x) { return (float)(__bf16)x; }
__device__ __forceinline__ unsigned pk2(float lo, float hi) {
  f32x2 v = {lo, hi};
  return __builtin_bit_cast(unsigned, __builtin_convertvector(v, bf16x2));
}
__device__ __forceinline__ f32x2 unpk2(unsigned v) { return (f32x2){bflo(v), bfhi(v)}; }
__device__ __forceinline__ f32x2 rbf2(f32x2 v) { return unpk2(pk2(v[0], v[1])); }
#define SCHED_FENCE() __builtin_amdgcn_sched_barrier(0)

__device__ __forceinline__ void dma16(unsigned voff, const void* sbase, unsigned lds_dst) {
  unsigned keep;
  asm volatile("s_mov_b32 %0, m0\n\ts_mov_b32 m0, %3\n\ts_nop 0\n\tglobal_load_lds_dwordx4 %1, %2\n\ts_mov_b32 m0, %0"
               : "=&s"(keep) : "v"(voff), "s"(sbase), "s"(lds_dst) : "memory");
}
__device__ __forceinline__ void dma16_16(unsigned voff, const void* sbase, unsigned lds_dst) {
  unsigned keep;
  asm volatile("s_mov_b32 %0, m0\n\ts_mov_b32 m0, %3\n\ts_mov_b64 exec, 0xffff\n\tglobal_load_lds_dwordx4 %1, %2\n\ts_mov_b64 exec, -1\n\ts_mov_b32 m0, %0"
               : "=&s"(keep) : "v"(voff), "s"(sbase), "s"(lds_dst) : "memory");
}
template <int N> __device__ __forceinline__ void wait_vm_barrier() {
  asm volatile("s_waitcnt vmcnt(%0)\n\ts_barrier" ::"n"(N) : "memory");
}
template <int N> __device__ __forceinline__ void wait_vm() { asm volatile("s_waitcnt vmcnt(%0)" ::"n"(N) : "memory"); }
__device__ __forceinline__ void wait_lgkm0() { asm volatile("s_waitcnt lgkmcnt(0)" ::: "memory"); }
__device__ __forceinline__ LAS char* ldsp(unsigned a) { return (LAS char*)(size_t)a; }

__device__ __forceinline__ bf16x8 tr_frag(LAS char* p0, LAS char* p1) {
  bf16x4 a = __builtin_amdgcn_ds_read_tr16_b64_v4bf16((LAS bf16x4*)p0);
  bf16x4 b = __builtin_amdgcn_ds_read_tr16_b64_v4bf16((LAS bf16x4*)p1);
  return __builtin_shufflevector(a, b, 0, 1, 2, 3, 4, 5, 6, 7);
}
__device__ __forceinline__ float silu_mul(float g, float u) {
  float e = __builtin_amdgcn_exp2f(g * -1.44269504088896f);
  return g * __builtin_amdgcn_rcpf(1.0f + e) * u;
}
__device__ __forceinline__ int mod9(int v) { return v >= 9 ? v - 9 : v; }

constexpr int K1_SLOT = 16384, K1_NSLOT = 4, K1_XOFF = K1_SLOT * K1_NSLOT, K1_XD = 3;
constexpr int K1_LDS = K1_XOFF + 8 * K1_XD * 4096;
constexpr int K1_PF = 4, K1_NS = 324, K1_NSEG = 21;
constexpr int K1_NPREP = 26, K1_PREP0 = 18;

struct K1Sched {
  int seg[K1_NS], tile[K1_NS], j[K1_NS];
  int first_use[K1_NSEG], last_use[K1_NSEG];
  int wissue[K1_NSEG];
  int xissue[9];
  bool bar[K1_NS];
  int ops[K1_NS];
  int wait[K1_NS];
  int epi0[2];
  int prep_ready;
};
constexpr bool k1_dbl_first(int s) { return s == 7 || s == 9 || s == 16; }
constexpr K1Sched k1_make() {
  K1Sched S{};
  int n = 0;
  for (int s = 0; s < 18;) {
    if (k1_dbl_first(s)) {
      for (int t = 0; t < 4; ++t) for (int h = 0; h < 2; ++h) for (int j = 0; j < 4; ++j) { S.seg[n] = s + h; S.tile[n] = t; S.j[n] = j; ++n; }
      s += 2;
    } else {
      for (int t = 0; t < 4; ++t) for (int j = 0; j < 4; ++j) { S.seg[n] = s; S.tile[n] = t; S.j[n] = j; ++n; }
      s += 1;
    }
  }
  for (int i = 0; i < 36; ++i) { S.seg[n] = 18 + i / 16; S.tile[n] = 0; S.j[n] = i % 16; ++n; }
  for (int s = 0; s < K1_NSEG; ++s) { S.first_use[s] = 100000; S.last_use[s] = -1; }
  for (int i = 0; i < K1_NS; ++i) { int s = S.seg[i]; if (i < S.first_use[s]) S.first_use[s] = i; if (i > S.last_use[s]) S.last_use[s] = i; }
  for (int s = 1; s < K1_NSEG; ++s) S.bar[S.first_use[s] - K1_PF - 1] = true;
  S.bar[S.last_use[10]] = true;

  S.wissue[0] = S.wissue[1] = -1;
  S.wissue[2] = 4; S.wissue[3] = 12;
  int busy = 0;
  for (int s = 4; s < K1_NSEG; ++s) {
    int q = S.last_use[s - K1_NSLOT];
    while (!S.bar[q]) ++q;
    int is = q + 1;
    if (is < busy) is = busy;
    S.wissue[s] = is;
    busy = is + 4;
  }
  S.xissue[0] = S.xissue[1] = -1;
  S.xissue[2] = 0;
  for (int p = 3; p < 9; ++p) S.xissue[p] = S.first_use[p - 3] + (p == 3 ? 8 : 4);
  S.epi0[0] = S.first_use[7] + 9;
  S.epi0[1] = S.first_use[16] + 9;
  for (int s = 0; s < K1_NSEG; ++s) if (S.wissue[s] >= 0) { S.ops[S.wissue[s]]++; S.ops[S.wissue[s] + 2]++; }
  for (int p = 0; p < 9; ++p) if (S.xissue[p] >= 0) for (int i = 0; i < 4; ++i) S.ops[S.xissue[p] + i]++;
  for (int i = 0; i < K1_NPREP; ++i) S.ops[K1_PREP0 + i / 2]++;
  for (int c = 0; c < 2; ++c) for (int t = 0; t < 4; ++t) { int sl = S.epi0[c] + 12 * t + 13; if (sl < K1_NS) S.ops[sl]++; }
  S.prep_ready = -1;
  for (int i = 0; i < K1_NS; ++i) {
    S.wait[i] = 63;
    if (!S.bar[i]) continue;
    int youngest = -2;
    for (int s = 1; s < K1_NSEG; ++s)
      if (S.first_use[s] - K1_PF - 1 == i) {
        int y = S.wissue[s] < 0 ? -1 : S.wissue[s] + 2;
        if (s < 9) { int yx = S.xissue[s] < 0 ? -1 : S.xissue[s] + 3; if (yx > y) y = yx; }
        if (y > youngest) youngest = y;
      }
    if (youngest == -2) continue;
    int k = 0;
    for (int q = youngest + 1; q <= i; ++q) k += S.ops[q];
    S.wait[i] = k > 63 ? 63 : k;
    if (S.prep_ready < 0 && youngest >= K1_PREP0 + K1_NPREP / 2 - 1) S.prep_ready = i + 1;
  }
  return S;
}
constexpr K1Sched K1S = k1_make();
static_assert(K1S.prep_ready > 0 && K1S.prep_ready < 80, "prep");

struct K1Ctx {
  const char* w1; const char* w3; const char* xb;
  char* hb;
  const char* kk; const char* vv; const char* sk; const char* gx; const char* gw;
  char* ws;
  unsigned voff_wg, voff_wu, voff_x0, voff_x1;
  unsigned rd[4];
  unsigned xl0;
  unsigned lane16;
  int w, rot, nl, b;
};
struct K1Prep {
  unsigned kr[12], vr[9];
  u32x4 sk, ga0, ga1, gw0, gw1;
  unsigned o0, oneg, ostep; int pd, pt0, tid;
  f32x2 s[4], a; unsigned u[9]; float gs;
};
struct K1Epi { float m[2], p[2], pq[2], r[2], h[2]; u32x4 ho; };

template <int sg, int half> __device__ __forceinline__ void k1_issue_w(const K1Ctx& c) {
  const unsigned dst = (sg % K1_NSLOT) * K1_SLOT + half * 8192;
  const char* base = half ? c.w3 : c.w1;
  if constexpr (sg < 18) {
    const int ksl = mod9(sg % 9 + c.rot);
    dma16(half ? c.voff_wu : c.voff_wg, base + ((size_t)(64 * ksl + 8 * c.w) * F_ + 128 * c.nl + 64 * (sg / 9)) * 2, dst + c.w * 1024);
  } else {
    unsigned l = c.lane16 >> 4;
    asm volatile("" : "+v"(l));
    const unsigned rp = l >> 1;
    const unsigned vo = (half ? (rp ^ 4u) : rp) * (F_ * 2) + (l & 1u) * 16;
    if constexpr (sg < 20) {
      const int ksl = mod9(4 * (sg - 18) + (c.w >> 1) + c.rot);
      dma16(vo, base + ((size_t)(64 * ksl + 32 * (c.w & 1)) * F_ + 1024 + 16 * c.nl) * 2, dst + c.w * 1024);
    } else {
      const int ksl = mod9(8 + c.rot);
      dma16_16(vo, base + ((size_t)(64 * ksl + 8 * c.w) * F_ + 1024 + 16 * c.nl) * 2, dst + c.w * 256);
    }
  }
}
template <int p, int i> __device__ __forceinline__ void k1_issue_x(const K1Ctx& c) {
  const int ksl = mod9(p + c.rot);
  dma16((i & 1) ? c.voff_x1 : c.voff_x0, c.xb + (size_t)(8 * i) * (D_ * 2) + ksl * 128, K1_XOFF + c.w * (K1_XD * 4096) + (p % K1_XD) * 4096 + i * 1024);
}
template <int p, int j> __device__ __forceinline__ bf16x8 k1_read_x(const K1Ctx& c) {
  return *(LAS bf16x8*)(ldsp(c.xl0 ^ (32 * j)) + (p % K1_XD) * 4096);
}
template <int n> __device__ __forceinline__ bf16x8 k1_read_w(const K1Ctx& c, unsigned rc0, unsigned rc1) {
  constexpr int sg = K1S.seg[n], t = K1S.tile[n], j = K1S.j[n];
  if constexpr (sg < 18) {
    LAS char* p = ldsp(c.rd[t]) + (sg % K1_NSLOT) * K1_SLOT + (16 * j) * 128;
    return tr_frag(p, p + 4 * 128);
  } else {
    const int off = (sg % K1_NSLOT) * K1_SLOT + (16 * j) * 32;
    return tr_frag(ldsp(rc0) + off, ldsp(rc1) + off);
  }
}
#define GLD1(dst, voff, sb) asm volatile("global_load_dword %0, %1, %2" : "=v"(dst) : "v"(voff), "s"(sb) : "memory")
#define GLD4(dst, voff, sb) asm volatile("global_load_dwordx4 %0, %1, %2" : "=v"(dst) : "v"(voff), "s"(sb) : "memory")
template <int i> __device__ __forceinline__ void k1_prep_load(const K1Ctx& c, K1Prep& P) {
  if constexpr (i == 0) {
    int tid = (c.lane16 >> 4) + 64 * c.w;
    asm volatile("" : "+v"(tid));
    P.tid = tid;
    const int ptid = tid < 288 ? tid : 287;
    P.pd = 72 * (c.b & 7) + 2 * (ptid % 36);
    P.pt0 = 64 * (c.b >> 3) + 8 * (ptid / 36);
    P.o0 = (unsigned)(P.pt0 * D_ + P.pd) * 2;
    P.oneg = P.pt0 > 0 ? P.o0 - 4 * (D_ * 2) : P.o0;
    P.ostep = P.pt0 > 0 ? D_ * 2 : 0;
  }
  if constexpr (i == 0) GLD4(P.ga0, c.lane16, c.gx);
  else if constexpr (i == 1) GLD4(P.ga1, 1024 + (c.lane16 & 127), c.gx);
  else if constexpr (i == 2) GLD4(P.gw0, c.lane16, c.gw);
  else if constexpr (i == 3) GLD4(P.gw1, 1024 + (c.lane16 & 127), c.gw);
  else if constexpr (i == 4) GLD4(P.sk, (unsigned)P.pd * 8, c.sk);
  else if constexpr (i < 9) GLD1(P.kr[i - 5], P.oneg + (i - 5) * P.ostep, c.kk);
  else if constexpr (i < 17) GLD1(P.kr[i - 5], P.o0 + (i - 9) * (D_ * 2), c.kk);
  else if constexpr (i == 17) GLD1(P.vr[0], P.oneg + 3 * P.ostep, c.vv);
  else GLD1(P.vr[i - 17], P.o0 + (i - 18) * (D_ * 2), c.vv);
}
template <int ctrl> __device__ __forceinline__ float dpp_add(float v) {
  return v + __builtin_bit_cast(float, __builtin_amdgcn_update_dpp(0, __builtin_bit_cast(int, v), ctrl, 0xf, 0xf, true));
}
constexpr int K1_PREP_UNITS = 8 + 1 + 36 + 4;
template <int k> __device__ __forceinline__ void k1_prep_unit(const K1Ctx& c, K1Prep& P) {
  if constexpr (k == 0) {
    asm volatile("" : "+v"(P.kr[0]), "+v"(P.kr[1]), "+v"(P.kr[2]), "+v"(P.kr[3]), "+v"(P.kr[4]), "+v"(P.kr[5]), "+v"(P.kr[6]), "+v"(P.kr[7]),
                 "+v"(P.kr[8]), "+v"(P.kr[9]), "+v"(P.kr[10]), "+v"(P.kr[11]), "+v"(P.vr[0]), "+v"(P.vr[1]), "+v"(P.vr[2]), "+v"(P.vr[3]),
                 "+v"(P.vr[4]), "+v"(P.vr[5]), "+v"(P.vr[6]), "+v"(P.vr[7]), "+v"(P.vr[8]), "+v"(P.sk), "+v"(P.ga0), "+v"(P.ga1), "+v"(P.gw0),
                 "+v"(P.gw1)::"memory");
    P.gs = 0.f;
    if ((c.lane16 >> 4) >= 8) P.ga1 = (u32x4){0u, 0u, 0u, 0u};
  }
  if constexpr (k < 4) {
    f32x2 p0 = rbf2(unpk2(P.ga0[k]) * unpk2(P.gw0[k])), p1 = rbf2(unpk2(P.ga1[k]) * unpk2(P.gw1[k]));
    P.gs += (p0[0] + p0[1]) + (p1[0] + p1[1]);
  } else if constexpr (k == 4) {
    P.gs = dpp_add<0x111>(P.gs); P.gs = dpp_add<0x112>(P.gs);
  } else if constexpr (k == 5) {
    P.gs = dpp_add<0x114>(P.gs); P.gs = dpp_add<0x118>(P.gs);
  } else if constexpr (k == 6) {
    int v = __builtin_bit_cast(int, P.gs);
    float a = __builtin_bit_cast(float, __builtin_amdgcn_readlane(v, 15)), b = __builtin_bit_cast(float, __builtin_amdgcn_readlane(v, 31));
    float d = __builtin_bit_cast(float, __builtin_amdgcn_readlane(v, 47)), e = __builtin_bit_cast(float, __builtin_amdgcn_readlane(v, 63));
    P.gs = rbf((a + b) + (d + e));
  } else if constexpr (k == 7) {
    float g = rbf(__builtin_amdgcn_rcpf(1.0f + __builtin_amdgcn_exp2f(P.gs * -1.44269504088896f)));
    if (c.lane16 == 0) *(float*)(c.ws + WS_G + (8 * c.b + c.w) * 4) = g;
  } else if constexpr (k == 8) {
    if (P.pt0 == 0) { P.kr[0] = P.kr[1] = P.kr[2] = P.kr[3] = 0u; P.vr[0] = 0u; }
    P.s[0] = (f32x2){bflo(P.sk[0]), bflo(P.sk[2])}; P.s[1] = (f32x2){bfhi(P.sk[0]), bfhi(P.sk[2])};
    P.s[2] = (f32x2){bflo(P.sk[1]), bflo(P.sk[3])}; P.s[3] = (f32x2){bfhi(P.sk[1]), bfhi(P.sk[3])};
  } else if constexpr (k < 45) {
    constexpr int i = (k - 9) / 4, q = (k - 9) % 4;
    f32x2 pr = rbf2(unpk2(P.kr[i + q]) * P.s[3 - q]);
    if constexpr (q == 0) P.a = pr;
    else P.a = rbf2(P.a + pr);
    if constexpr (q == 3) { f32x2 u = P.a * unpk2(P.vr[i]); P.u[i] = pk2(u[0], u[1]); }
  } else {
    constexpr int ch = (k - 45) >> 1, cp = (k - 45) & 1;
    constexpr unsigned sel = ch ? 0x07060302u : 0x05040100u;
    u32x4 o;
#pragma unroll
    for (int e = 0; e < 4; ++e) o[e] = __builtin_amdgcn_perm(P.u[7 + cp - 2 * e - 1], P.u[7 + cp - 2 * e], sel);
    if (P.tid < 288) *(u32x4*)(c.ws + WS_UR + ((size_t)((P.pd + ch) * 2 + cp) * S_ + (2040 - P.pt0)) * 2) = o;
  }
}
template <int ch, int e> __device__ __forceinline__ void k1_epi_step(const K1Ctx& c, const f32x16 (&acc)[4], K1Epi& E) {
  constexpr int pr = e / 3, st = e % 3, t = pr / 4, i = 2 * (pr % 4);
  if constexpr (st == 0) {
    if constexpr (pr > 0) { E.h[0] = E.pq[0] * E.r[0]; E.h[1] = E.pq[1] * E.r[1]; }
    if constexpr (pr < 16) {
      E.m[0] = acc[t][i] * -1.44269504088896f; E.m[1] = acc[t][i + 1] * -1.44269504088896f;
      E.p[0] = acc[t][i] * acc[t][i + 8]; E.p[1] = acc[t][i + 1] * acc[t][i + 9];
    }
  } else if constexpr (st == 1) {
    if constexpr (pr < 16) { E.m[0] = __builtin_amdgcn_exp2f(E.m[0]); E.m[1] = __builtin_amdgcn_exp2f(E.m[1]); }
    if constexpr (pr > 0) {
      E.ho[(pr - 1) % 4] = pk2(E.h[0], E.h[1]);
      if constexpr (pr % 4 == 0) *(u32x4*)(c.hb + (size_t)(8 * c.nl + 4 * ch + (pr - 1) / 4) * 1024 + c.lane16) = E.ho;
    }
  } else {
    if constexpr (pr < 16) {
      float d0 = 1.0f + E.m[0], d1 = 1.0f + E.m[1];
      E.r[0] = __builtin_amdgcn_rcpf(d0); E.r[1] = __builtin_amdgcn_rcpf(d1);
      E.pq[0] = E.p[0]; E.pq[1] = E.p[1];
    }
  }
}

template <int n>
__device__ __forceinline__ void k1_slot(K1Ctx& c, bf16x8 (&xf)[36], bf16x8 (&wf)[K1_PF + 1], f32x16 (&acc)[4], K1Epi& E, K1Prep& P, unsigned& rc0,
                                        unsigned& rc1) {
  constexpr int sg = K1S.seg[n];
  if constexpr (n + K1_PF == 288) {
    unsigned l = c.lane16 >> 4;
    asm volatile("" : "+v"(l));
    unsigned mat = (l >> 4) & 1, h = l >> 5, q = (l & 15) >> 2, p = l & 3;
    unsigned base = mat * 8192 + (8 * h + q) * 32 + p * 8;
    rc0 = base + (mat ? 128 : 0);
    rc1 = base + (mat ? 0 : 128);
  }
  if constexpr (n + K1_PF < K1_NS) wf[(n + K1_PF) % (K1_PF + 1)] = k1_read_w<n + K1_PF>(c, rc0, rc1);
  if constexpr (n + 4 < 144) {
    constexpr int sn = K1S.seg[n + 4];
    if constexpr (K1S.tile[n + 4] == 0 && sn >= 1) xf[4 * sn + K1S.j[n + 4]] = k1_read_x<sn, K1S.j[n + 4]>(c);
  }
  {
    constexpr int t = K1S.tile[n];
    constexpr int xi = sg < 18 ? 4 * (sg % 9) + K1S.j[n] : n - 288;
    constexpr bool first = (sg < 18) ? (sg % 9 == 0 && K1S.j[n] == 0) : (n == 288);
    if constexpr (first) acc[t] = __builtin_amdgcn_mfma_f32_32x32x16_bf16(wf[n % (K1_PF + 1)], xf[xi], (f32x16){}, 0, 0, 0);
    else acc[t] = __builtin_amdgcn_mfma_f32_32x32x16_bf16(wf[n % (K1_PF + 1)], xf[xi], acc[t], 0, 0, 0);
  }
  SCHED_FENCE();
  {
    constexpr int ws0 = [] { for (int s = 0; s < K1_NSEG; ++s) if (K1S.wissue[s] == n) return s; return -1; }();
    constexpr int ws1 = [] { for (int s = 0; s < K1_NSEG; ++s) if (K1S.wissue[s] >= 0 && K1S.wissue[s] + 2 == n) return s; return -1; }();
    constexpr int xs = [] { for (int p = 0; p < 9; ++p) if (K1S.xissue[p] >= 0 && n >= K1S.xissue[p] && n < K1S.xissue[p] + 4) return p; return -1; }();
    if constexpr (ws0 >= 0) k1_issue_w<ws0, 0>(c);
    if constexpr (ws1 >= 0) k1_issue_w<ws1, 1>(c);
    if constexpr (xs >= 0) k1_issue_x<xs, n - K1S.xissue[xs]>(c);
  }
  if constexpr (n >= K1_PREP0 && n < K1_PREP0 + K1_NPREP / 2) {
    k1_prep_load<2 * (n - K1_PREP0)>(c, P);
    k1_prep_load<2 * (n - K1_PREP0) + 1>(c, P);
  }
  if constexpr (n >= K1S.prep_ready && n < K1S.prep_ready + K1_PREP_UNITS) k1_prep_unit<n - K1S.prep_ready>(c, P);
  if constexpr (n >= K1S.epi0[0] && n < K1S.epi0[0] + 50) k1_epi_step<0, n - K1S.epi0[0]>(c, acc, E);
  if constexpr (n >= K1S.epi0[1] && n < K1S.epi0[1] + 50) k1_epi_step<1, n - K1S.epi0[1]>(c, acc, E);
  SCHED_FENCE();
  if constexpr (K1S.bar[n]) {
    wait_vm_barrier<K1S.wait[n]>();
    SCHED_FENCE();
  }
  if constexpr (n + 1 < K1_NS) k1_slot<n + 1>(c, xf, wf, acc, E, P, rc0, rc1);
}
static_assert(K1S.epi0[1] + 50 <= K1_NS, "epilogue stream fits");
static_assert(K1S.prep_ready + K1_PREP_UNITS <= K1S.epi0[0], "prep before epilogue");

__global__ __launch_bounds__(512, 2) void k1_kernel(const u16* __restrict__ x, const u16* __restrict__ kk, const u16* __restrict__ vv,
                                                    const u16* __restrict__ shift_k, const u16* __restrict__ w1, const u16* __restrict__ w3,
                                                    const u16* __restrict__ gate_w, char* __restrict__ ws) {
  extern __shared__ __attribute__((aligned(16))) char smem[];
  const int b = blockIdx.x, xcd = b & 7, r = b >> 3;
  const int ml = r & 3, nl = r >> 2, fq = xcd >> 1;
  const int mt = 4 * (xcd & 1) + ml;
  const int tid = threadIdx.x, lane = tid & 63;
  const int w = __builtin_amdgcn_readfirstlane(tid >> 6);
  K1Ctx c;
  RSTAMP(c.st, 26);
  STAMP(c.st, 0);
  c.w = w; c.nl = nl; c.b = b; c.ws = ws;
  c.rot = (nl + 2 * ml) % 9;
  c.lane16 = lane * 16;
  c.w1 = (const char*)w1 + (size_t)(1152 * fq) * 2;
  c.w3 = (const char*)w3 + (size_t)(1152 * fq) * 2;
  c.xb = (const char*)x + (size_t)(256 * mt + 32 * w) * (D_ * 2);
  c.hb = ws + WS_H + (size_t)((8 * mt + w) * 288 + 72 * fq) * 1024;
  c.kk = (const char*)kk; c.vv = (const char*)vv; c.sk = (const char*)shift_k; c.gw = (const char*)gate_w;
  c.gx = (const char*)x + (size_t)(8 * b + w) * (D_ * 2);
  {
    int row = lane >> 3, pos = lane & 7;
    int sw = 2 * ((row >> 1) & 1);
    c.voff_wg = row * (F_ * 2) + ((pos ^ sw) * 16);
    c.voff_wu = row * (F_ * 2) + ((pos ^ sw ^ 4) * 16);
    int sx = lane >> 4;
    c.voff_x0 = row * (D_ * 2) + ((pos ^ sx) * 16);
    c.voff_x1 = row * (D_ * 2) + ((pos ^ (sx + 4)) * 16);
  }
  k1_issue_w<0, 0>(c); k1_issue_w<0, 1>(c);
  k1_issue_x<0, 0>(c); k1_issue_x<0, 1>(c); k1_issue_x<0, 2>(c); k1_issue_x<0, 3>(c);
  k1_issue_w<1, 0>(c); k1_issue_w<1, 1>(c);
  {
    int gq = lane >> 4, mat = gq & 1, h = gq >> 1, q = (lane & 15) >> 2, p = lane & 3;
    int X = 2 * (q >> 1) + 4 * mat;
#pragma unroll
    for (int t = 0; t < 4; ++t) c.rd[t] = mat * 8192 + (8 * h + q) * 128 + (((2 * t) ^ X) + (p >> 1)) * 16 + (p & 1) * 8;
    int cc = lane & 31, hh = lane >> 5;
    c.xl0 = K1_XOFF + w * (K1_XD * 4096) + cc * 128 + ((hh ^ ((cc >> 1) & 7)) * 16);
  }
  bf16x8 xf[36];
  bf16x8 wf[K1_PF + 1];
  f32x16 acc[4];
  unsigned rc0 = 0, rc1 = 0;
  wait_vm_barrier<2>();
  STAMP(c.st, 1);
  k1_issue_x<1, 0>(c); k1_issue_x<1, 1>(c); k1_issue_x<1, 2>(c); k1_issue_x<1, 3>(c);
  xf[0] = k1_read_x<0, 0>(c); wf[0] = k1_read_w<0>(c, rc0, rc1);
  xf[1] = k1_read_x<0, 1>(c); wf[1] = k1_read_w<1>(c, rc0, rc1);
  xf[2] = k1_read_x<0, 2>(c); wf[2] = k1_read_w<2>(c, rc0, rc1);
  xf[3] = k1_read_x<0, 3>(c); wf[3] = k1_read_w<3>(c, rc0, rc1);
  static_assert(K1_PF == 4, "prologue reads");
  SCHED_FENCE();
  STAMP(c.st, 2);
  K1Epi E;
  K1Prep P;
  k1_slot<0>(c, xf, wf, acc, E, P, rc0, rc1);
  {
    u32x4 o;
#pragma unroll
    for (int i = 0; i < 4; ++i) o[i] = pk2(silu_mul(acc[0][2 * i], acc[0][2 * i + 8]), silu_mul(acc[0][2 * i + 1], acc[0][2 * i + 9]));
    *(u32x4*)(c.hb + (size_t)(64 + nl) * 1024 + c.lane16) = o;
  }
}

constexpr int G2_SLOT = 24576, G2_NSLOT = 4, G2_WIN = 16, G2_PF = 4, G2_NS = 216;
constexpr int CV_K = 0, CV_U0 = 4160, CV_U1 = CV_U0 + 4640, CV_CH = 13440;
constexpr int K2_LDS = 9 * CV_CH;

struct G2Ctx {
  const char* w2; const char* hs; unsigned voff[3]; unsigned rd; unsigned lane16; int w, rot;
};
template <int s, int i> __device__ __forceinline__ void g2_issue_w(const G2Ctx& c) {
  const int ksl = mod9(s + c.rot);
  dma16(c.voff[i], c.w2 + (size_t)(128 * ksl) * (D_ * 2), (s % G2_NSLOT) * G2_SLOT + c.w * 3072 + i * 1024);
}
template <int i> __device__ __forceinline__ u32x4 g2_load_h(const G2Ctx& c) {
  const int ksl = mod9(i / 8 + c.rot);
  return *(const u32x4*)(c.hs + (size_t)(8 * ksl + (i % 8)) * 1024 + c.lane16);
}
template <int n> __device__ __forceinline__ bf16x8 g2_read_w(const G2Ctx& c) {
  constexpr int i = n / 3, t = n % 3, s = i / 8, j = i % 8;
  LAS char* p = ldsp(c.rd) + (s % G2_NSLOT) * G2_SLOT + (16 * j) * 192 + t * 64;
  return tr_frag(p, p + 8 * 192);
}
constexpr int g2_ops_in_slot(int n) {
  int s = n / 24, r = n % 24, k = 0;
  if ((r == 1 || r == 4 || r == 7) && s + 3 < 9 && s >= 1) ++k;
  if (s == 0 && (r == 1 || r == 4 || r == 7 || r == 13 || r == 16 || r == 19)) ++k;
  if (n % 3 == 2 && n / 3 + G2_WIN < 72) ++k;
  return k;
}
constexpr int g2_ops_range(int a, int b) { int k = 0; for (int n = a; n < b; ++n) k += g2_ops_in_slot(n); return k; }
constexpr int cmin(int a, int b) { return a < b ? a : b; }
template <int n>
__device__ __forceinline__ void g2_slot(G2Ctx& c, u32x4 (&hw)[G2_WIN], bf16x8 (&wf)[G2_PF + 1], f32x16 (&acc)[3]) {
  constexpr int i = n / 3, t = n % 3, s = n / 24, r = n % 24;
  if constexpr (n + G2_PF < G2_NS) wf[(n + G2_PF) % (G2_PF + 1)] = g2_read_w<n + G2_PF>(c);
  if constexpr (i == 0) acc[t] = __builtin_amdgcn_mfma_f32_32x32x16_bf16(wf[n % (G2_PF + 1)], __builtin_bit_cast(bf16x8, hw[i % G2_WIN]), (f32x16){}, 0, 0, 0);
  else acc[t] = __builtin_amdgcn_mfma_f32_32x32x16_bf16(wf[n % (G2_PF + 1)], __builtin_bit_cast(bf16x8, hw[i % G2_WIN]), acc[t], 0, 0, 0);
  if constexpr ((r == 1 || r == 4 || r == 7) && s + 3 < 9 && s >= 1) g2_issue_w<s + 3, r / 3>(c);
  if constexpr (s == 0 && (r == 1 || r == 4 || r == 7)) g2_issue_w<2, r / 3>(c);
  if constexpr (s == 0 && (r == 13 || r == 16 || r == 19)) g2_issue_w<3, (r - 12) / 3>(c);
  if constexpr (t == 2 && i + G2_WIN < 72) hw[i % G2_WIN] = g2_load_h<i + G2_WIN>(c);
  SCHED_FENCE();
  if constexpr (n == 11) {
    wait_vm_barrier<8 + g2_ops_range(0, 12)>();
    SCHED_FENCE();
  }
  if constexpr (r == 23 && n + 1 < G2_NS) {
    constexpr int last = s == 0 ? 7 : (s == 1 ? 19 : 24 * (s - 1) + 7);
    wait_vm_barrier<(s + 2 < 9) ? cmin(63, g2_ops_range(last + 1, n + 1)) : 63>();
    STAMP(c.st, s + 2);
    SCHED_FENCE();
  }
  if constexpr (n + 1 < G2_NS) g2_slot<n + 1>(c, hw, wf, acc);
}

typedef unsigned u32x2a4 __attribute__((ext_vector_type(2), aligned(4)));
__device__ __forceinline__ bf16x8 cv_read_a(LAS char* p) {
  u32x2 lo = *(LAS u32x2a4*)p;
  u32x2 hi = *(LAS u32x2a4*)(p + 8);
  u32x4 v = {lo[0], lo[1], hi[0], hi[1]};
  return __builtin_bit_cast(bf16x8, v);
}
template <bool PREF>
__device__ __forceinline__ void cv_block(f32x4 (&acc)[8], const bf16x8 (&af)[8], const bf16x8 (&bf)[8], bf16x8 (&naf)[8], bf16x8 (&nbf)[8], LAS char* ap,
                                         LAS char* bp) {
  int k = 0;
#pragma unroll
  for (int p = 0; p < 8; ++p)
#pragma unroll
    for (int dl = 0; dl + p < 8; ++dl) {
      if (PREF && (k & 1) == 0 && k < 32) {
        const int f = k >> 2;
        if (k & 2) naf[f] = cv_read_a(ap + 512 * f);
        else nbf[f] = *(LAS bf16x8*)(bp + 64 * (56 - 8 * f));
      }
      acc[p + dl] = __builtin_amdgcn_mfma_f32_16x16x32_bf16(af[p], bf[dl], acc[p + dl], 0, 0, 0);
      if (k & 1) SCHED_FENCE();
      ++k;
    }
}
__device__ __forceinline__ void cv_store(const f32x4& y, LAS char* cl, int g, int d, int lane, float Dv, char* __restrict__ ws) {
  const int q = lane >> 4, pi = (lane >> 3) & 1, ii = lane & 7;
  const int T = 256 * g + 32 * ii + 8 * q;
  u32x4 uu = *(LAS u32x4*)(cl + CV_U1 + 2 * (2040 - T));
  float z[4];
#pragma unroll
  for (int k = 0; k < 4; ++k) {
    float u = pi ? bflo(uu[3 - k]) : bfhi(uu[3 - k]);
    z[k] = rbf(y[k]) + rbf(u * Dv);
  }
  unsigned p01 = pk2(z[0], z[1]), p23 = pk2(z[2], z[3]);
  unsigned snd = pi ? p01 : p23;
  unsigned rcv = __builtin_amdgcn_update_dpp(0u, snd, 0x128, 0xf, 0xf, false);
  unsigned E = pi ? rcv : p01, O = pi ? p23 : rcv;
  u32x2 o = {(E & 0xffffu) | (O << 16), (E >> 16) | (O & 0xffff0000u)};
  *(u32x2*)(ws + WS_Z + ((size_t)d * S_ + T + 4 * pi) * 2) = o;
}

__global__ __launch_bounds__(512, 2) void k2_kernel(const u16* __restrict__ w2, const u16* __restrict__ ssm_k, const u16* __restrict__ ssm_D,
                                                    char* __restrict__ ws) {
  extern __shared__ __attribute__((aligned(16))) char smem[];
  LAS char* lds = (LAS char*)smem;
  const int b = blockIdx.x, xcd = b & 7, r = b >> 3;
  const int tid = threadIdx.x, lane = tid & 63;
  const int w = __builtin_amdgcn_readfirstlane(tid >> 6);
  if (r < 24) {
    const int ml = r & 3, nt = r >> 2;
    const int mt = 4 * (xcd & 1) + ml, ks = xcd >> 1;
    G2Ctx c;
    RSTAMP(c.st, 26);
    STAMP(c.st, 0);
    c.w = w;
    c.rot = (nt + 2 * ml) % 9;
    c.lane16 = lane * 16;
    c.w2 = (const char*)w2 + ((size_t)(1152 * ks + 16 * w) * D_ + 96 * nt) * 2;
#pragma unroll
    for (int i = 0; i < 3; ++i) {
      int pos = 1024 * i + 16 * lane;
      c.voff[i] = (pos / 192) * (D_ * 2) + (pos % 192);
    }
    c.hs = ws + WS_H + (size_t)((8 * mt + w) * 288 + 72 * ks) * 1024;
    u32x4 hw[G2_WIN];
    g2_issue_w<0, 0>(c); g2_issue_w<0, 1>(c); g2_issue_w<0, 2>(c);
    SCHED_FENCE();
    hw[0] = g2_load_h<0>(c); hw[1] = g2_load_h<1>(c); hw[2] = g2_load_h<2>(c); hw[3] = g2_load_h<3>(c);
    SCHED_FENCE();
    hw[4] = g2_load_h<4>(c); hw[5] = g2_load_h<5>(c); hw[6] = g2_load_h<6>(c); hw[7] = g2_load_h<7>(c);
    SCHED_FENCE();
    g2_issue_w<1, 0>(c); g2_issue_w<1, 1>(c); g2_issue_w<1, 2>(c);
    SCHED_FENCE();
    hw[8] = g2_load_h<8>(c); hw[9] = g2_load_h<9>(c); hw[10] = g2_load_h<10>(c); hw[11] = g2_load_h<11>(c);
    SCHED_FENCE();
    hw[12] = g2_load_h<12>(c); hw[13] = g2_load_h<13>(c); hw[14] = g2_load_h<14>(c); hw[15] = g2_load_h<15>(c);
    SCHED_FENCE();
    static_assert(G2_WIN == 16, "prologue");
    {
      int gq = lane >> 4, chf = gq & 1, h = gq >> 1, q = (lane & 15) >> 2, p = lane & 3;
      c.rd = (4 * h + q) * 192 + chf * 32 + p * 8;
    }
    bf16x8 wf[G2_PF + 1];
    f32x16 acc[3];
    wait_vm_barrier<19>();
    STAMP(c.st, 1);
    wf[0] = g2_read_w<0>(c); wf[1] = g2_read_w<1>(c); wf[2] = g2_read_w<2>(c); wf[3] = g2_read_w<3>(c);
    SCHED_FENCE();
    g2_slot<0>(c, hw, wf, acc);
    float* pp = (float*)(ws + WS_PART) + ((size_t)((ks * 64 + 8 * mt + w) * 18 + 3 * nt) * 256 + lane) * 4;
#pragma unroll
    for (int t = 0; t < 3; ++t)
#pragma unroll
      for (int qd = 0; qd < 4; ++qd) {
        f32x4 v = {acc[t][4 * qd], acc[t][4 * qd + 1], acc[t][4 * qd + 2], acc[t][4 * qd + 3]};
        *(f32x4*)(pp + (size_t)(t * 4 + qd) * 256) = v;
      }
  } else {
    const int cw = xcd * 8 + (r - 24);
    RSTAMP(st, 26);
    STAMP(st, 0);
    const int d = 9 * cw + w, d8 = 9 * cw + 8;
    {
      const char* kp = (const char*)ssm_k + (size_t)d * S_ * 2;
      const char* up = ws + WS_UR + (size_t)d * 2 * S_ * 2;
      const unsigned dst = w * CV_CH;
#pragma unroll
      for (int i = 0; i < 4; ++i) {
        dma16(lane * 16, kp + i * 1024, dst + CV_K + 64 + i * 1024);
        dma16(lane * 16, up + i * 1024, dst + CV_U0 + i * 1024);
        dma16(lane * 16, up + 4096 + i * 1024, dst + CV_U1 + i * 1024);
      }
      const char* kp8 = (const char*)ssm_k + (size_t)d8 * S_ * 2;
      const char* up8 = ws + WS_UR + (size_t)d8 * 2 * S_ * 2;
      dma16(lane * 16, up8 + w * 1024, 8 * CV_CH + (w < 4 ? CV_U0 + w * 1024 : CV_U1 + (w - 4) * 1024));
      if (w < 4) dma16(lane * 16, kp8 + w * 1024, 8 * CV_CH + CV_K + 64 + w * 1024);
      if (lane < 60) {
        int o = lane < 4 ? CV_K + lane * 16 : (lane < 32 ? CV_U0 + 4096 + (lane - 4) * 16 : CV_U1 + 4096 + (lane - 32) * 16);
        *(LAS u32x4*)(lds + w * CV_CH + o) = (u32x4){0u, 0u, 0u, 0u};
        if (w == 7) *(LAS u32x4*)(lds + 8 * CV_CH + o) = (u32x4){0u, 0u, 0u, 0u};
      }
    }
    const float Dv = bf2f(ssm_D[d]), Dv8 = bf2f(ssm_D[d8]);
    const int rho = lane & 15, q = lane >> 4, pi = (lane >> 3) & 1, ii = lane & 7;
    const int aoff = CV_K + 4 + 4 * rho + 16 * q;
    const int boff = CV_U0 + pi * 4640 + 2 * (2016 - 32 * ii + 8 * q) - 64 * 56;
    LAS char* cl = lds + w * CV_CH;
    asm volatile("s_waitcnt vmcnt(0) lgkmcnt(0)\n\ts_barrier" ::: "memory");
    STAMP(st, 1);
    f32x4 acc[8] = {};
    bf16x8 a0[8], b0[8], a1[8], b1[8];
#pragma unroll
    for (int f = 0; f < 8; ++f) {
      b0[f] = *(LAS bf16x8*)(cl + boff + 64 * (56 - 8 * f));
      a0[f] = cv_read_a(cl + aoff + 512 * f);
    }
    SCHED_FENCE();
#pragma unroll 1
    for (int rr = 0; rr < 8; rr += 2) {
      cv_block<true>(acc, a0, b0, a1, b1, cl + aoff + 64 * (rr + 1), cl + boff + 64 * (rr + 1));
      LAS char* nb = rr < 6 ? cl + 64 * (rr + 2) : lds + 8 * CV_CH + 64 * w;
      cv_block<true>(acc, a1, b1, a0, b0, nb + aoff, nb + boff);
    }
    STAMP(st, 2);
    f32x4 acc8[8] = {};
    cv_block<false>(acc8, a0, b0, a1, b1, cl, cl);
#pragma unroll
    for (int g = 0; g < 8; ++g) cv_store(acc[g], cl, g, d, lane, Dv, ws);
    wait_lgkm0();
#pragma unroll
    for (int g = 0; g < 8; ++g) *(LAS f32x4*)(cl + g * 1024 + lane * 16) = acc8[g];
    asm volatile("s_waitcnt lgkmcnt(0)\n\ts_barrier" ::: "memory");
    STAMP(st, 3);
    f32x4 y = {};
#pragma unroll
    for (int ww = 0; ww < 8; ++ww) y += *(LAS f32x4*)(lds + ww * CV_CH + w * 1024 + lane * 16);
    cv_store(y, lds + 8 * CV_CH, w, d8, lane, Dv8, ws);
  }
}

__global__ __launch_bounds__(192) void k3_kernel(const u16* __restrict__ q, const char* __restrict__ ws, u16* __restrict__ out) {
  __shared__ __attribute__((aligned(16))) char sm[32 * 208 + 96 * 80];
  char* bt = sm;
  char* zt = sm + 32 * 208;
  const int mb = blockIdx.x / 6, grp = blockIdx.x % 6;
  const int tid = threadIdx.x, lane = tid & 63, w = tid >> 6;
  const int dt = 3 * grp + w;
  const f32x4* pp = (const f32x4*)(ws + WS_PART) + (size_t)(mb * 18 + dt) * 256 + lane;
  f32x4 p[4][4];
#pragma unroll
  for (int ks = 0; ks < 4; ++ks)
#pragma unroll
    for (int qd = 0; qd < 4; ++qd) p[ks][qd] = pp[(size_t)ks * (64 * 18 * 256) + qd * 64];
  u32x4 zz[2];
#pragma unroll
  for (int i = 0; i < 2; ++i) {
    int dl = 16 * i + (lane >> 2);
    zz[i] = *(const u32x4*)(ws + WS_Z + ((size_t)(32 * dt + dl) * S_ + 32 * mb) * 2 + (lane & 3) * 16);
  }
  u32x4 qq[2];
  float gg[2];
#pragma unroll
  for (int i = 0; i < 2; ++i) {
    int pc = tid + 192 * i, t = pc / 12, c8 = pc % 12;
    qq[i] = *(const u32x4*)(q + (size_t)(32 * mb + t) * D_ + 96 * grp + 8 * c8);
    gg[i] = *(const float*)(ws + WS_G + (32 * mb + t) * 4);
  }
#pragma unroll
  for (int qd = 0; qd < 4; ++qd) {
    f32x4 s = (p[0][qd] + p[1][qd]) + (p[2][qd] + p[3][qd]);
    u32x2 o = {pk2(s[0], s[1]), pk2(s[2], s[3])};
    *(u32x2*)(bt + (lane & 31) * 208 + (32 * w + 8 * qd + 4 * (lane >> 5)) * 2) = o;
  }
#pragma unroll
  for (int i = 0; i < 2; ++i) *(u32x4*)(zt + (32 * w + 16 * i + (lane >> 2)) * 80 + (lane & 3) * 16) = zz[i];
  __syncthreads();
#pragma unroll
  for (int i = 0; i < 2; ++i) {
    int pc = tid + 192 * i, t = pc / 12, c8 = pc % 12;
    u32x4 bb = *(const u32x4*)(bt + t * 208 + c8 * 16);
    float g = gg[i], og = rbf(1.0f - g);
    float res[8];
#pragma unroll
    for (int j = 0; j < 8; ++j) {
      float z = bf2f(*(const u16*)(zt + (8 * c8 + j) * 80 + t * 2));
      float qv = (j & 1) ? bfhi(qq[i][j >> 1]) : bflo(qq[i][j >> 1]);
      float bv = (j & 1) ? bfhi(bb[j >> 1]) : bflo(bb[j >> 1]);
      float a = rbf(qv * z);
      res[j] = rbf(g * a) + rbf(og * bv);
    }
    u32x4 o = {pk2(res[0], res[1]), pk2(res[2], res[3]), pk2(res[4], res[5]), pk2(res[6], res[7])};
    *(u32x4*)(out + (size_t)(32 * mb + t) * D_ + 96 * grp + 8 * c8) = o;
  }
}

extern "C" void kernel_launch(void* const* d_in, const int* in_sizes, int n_in, void* d_out, int out_size, void* d_ws, size_t ws_size,
                              hipStream_t stream) {
  const u16* q = (const u16*)d_in[0];
  const u16* k = (const u16*)d_in[1];
  const u16* v = (const u16*)d_in[2];
  const u16* shift_k = (const u16*)d_in[3];
  const u16* ssm_k = (const u16*)d_in[4];
  const u16* ssm_D = (const u16*)d_in[5];
  const u16* w1 = (const u16*)d_in[7];
  const u16* w3 = (const u16*)d_in[8];
  const u16* w2 = (const u16*)d_in[9];
  const u16* gate_w = (const u16*)d_in[10];
  char* ws = (char*)d_ws;
  (void)hipFuncSetAttribute((const void*)k1_kernel, hipFuncAttributeMaxDynamicSharedMemorySize, K1_LDS);
  (void)hipFuncSetAttribute((const void*)k2_kernel, hipFuncAttributeMaxDynamicSharedMemorySize, K2_LDS);
  k1_kernel<<<256, 512, K1_LDS, stream>>>(q, k, v, shift_k, w1, w3, gate_w, ws);
  k2_kernel<<<256, 512, K2_LDS, stream>>>(w2, ssm_k, ssm_D, ws);
  k3_kernel<<<384, 192, 0, stream>>>(q, ws, (u16*)d_out);
}
```

```cpp
#include <hip/hip_runtime.h>
#include <hip/hip_bf16.h>

#define LAS __attribute__((address_space(3)))
#define GAS __attribute__((address_space(1)))
typedef unsigned short u16;
typedef __bf16 bf16x2 __attribute__((ext_vector_type(2)));
typedef __bf16 bf16x4 __attribute__((ext_vector_type(4)));
typedef __bf16 bf16x8 __attribute__((ext_vector_type(8)));
typedef float f32x2 __attribute__((ext_vector_type(2)));
typedef float f32x4 __attribute__((ext_vector_type(4)));
typedef float f32x16 __attribute__((ext_vector_type(16)));
typedef unsigned u32x2 __attribute__((ext_vector_type(2)));
typedef unsigned u32x4 __attribute__((ext_vector_type(4)));

constexpr int S_ = 2048, D_ = 576, F_ = 4608;
constexpr unsigned WS_H = 0;
constexpr unsigned WS_PART = WS_H + 64 * 288 * 1024;
constexpr unsigned WS_UR = WS_PART + 256 * 49152;
constexpr unsigned WS_Z = WS_UR + D_ * 2 * S_ * 2;
constexpr unsigned WS_G = WS_Z + D_ * S_ * 2;
constexpr unsigned WS_FLAG = WS_G + S_ * 4;
constexpr unsigned WS_EPOCH = WS_FLAG + 256 * 256;
constexpr unsigned WS_END = WS_EPOCH + 1024;
constexpr unsigned FLAG_MAGIC = 0x7c3a91e5u;
constexpr int AUX_SC1 = 16;
constexpr unsigned SPIN_CAP = 1u << 16;

#define STAMP(i)
#define RSTAMP(i)
#define STDUMP()

__device__ __forceinline__ float bf2f(u16 v) { return __builtin_bit_cast(float, (unsigned)v << 16); }
__device__ __forceinline__ float bflo(unsigned v) { return __builtin_bit_cast(float, v << 16); }
__device__ __forceinline__ float bfhi(unsigned v) { return __builtin_bit_cast(float, v & 0xffff0000u); }
__device__ __forceinline__ float rbf(float x) { return (float)(__bf16)x; }
__device__ __forceinline__ unsigned pk2(float lo, float hi) {
  f32x2 v = {lo, hi};
  return __builtin_bit_cast(unsigned, __builtin_convertvector(v, bf16x2));
}
__device__ __forceinline__ f32x2 unpk2(unsigned v) { return (f32x2){bflo(v), bfhi(v)}; }
#define SCHED_FENCE() __builtin_amdgcn_sched_barrier(0)

__device__ __forceinline__ void dma16(unsigned voff, const void* sbase, unsigned lds_dst) {
  unsigned keep;
  asm volatile("s_mov_b32 %0, m0\n\ts_mov_b32 m0, %3\n\ts_nop 0\n\tglobal_load_lds_dwordx4 %1, %2\n\ts_mov_b32 m0, %0"
               : "=&s"(keep) : "v"(voff), "s"(sbase), "s"(lds_dst) : "memory");
}
__device__ __forceinline__ void dma16_16(unsigned voff, const void* sbase, unsigned lds_dst) {
  unsigned keep;
  asm volatile("s_mov_b32 %0, m0\n\ts_mov_b32 m0, %3\n\ts_mov_b64 exec, 0xffff\n\tglobal_load_lds_dwordx4 %1, %2\n\ts_mov_b64 exec, -1\n\ts_mov_b32 m0, %0"
               : "=&s"(keep) : "v"(voff), "s"(sbase), "s"(lds_dst) : "memory");
}
template <int N> __device__ __forceinline__ void wait_vm_barrier() {
  asm volatile("s_waitcnt vmcnt(%0)\n\ts_barrier" ::"n"(N) : "memory");
}
template <int N> __device__ __forceinline__ void wait_vm() { asm volatile("s_waitcnt vmcnt(%0)" ::"n"(N) : "memory"); }
__device__ __forceinline__ void wait_lgkm0() { asm volatile("s_waitcnt lgkmcnt(0)" ::: "memory"); }
__device__ __forceinline__ void full_barrier() { asm volatile("s_waitcnt vmcnt(0) lgkmcnt(0)\n\ts_barrier" ::: "memory"); }
__device__ __forceinline__ LAS char* ldsp(unsigned a) { return (LAS char*)(size_t)a; }

__device__ __forceinline__ bf16x8 tr_frag(LAS char* p0, LAS char* p1) {
  bf16x4 a = __builtin_amdgcn_ds_read_tr16_b64_v4bf16((LAS bf16x4*)p0);
  bf16x4 b = __builtin_amdgcn_ds_read_tr16_b64_v4bf16((LAS bf16x4*)p1);
  return __builtin_shufflevector(a, b, 0, 1, 2, 3, 4, 5, 6, 7);
}
__device__ __forceinline__ float silu_mul(float g, float u) {
  float e = __builtin_amdgcn_exp2f(g * -1.44269504088896f);
  return g * u * __builtin_amdgcn_rcpf(1.0f + e);
}
__device__ __forceinline__ int mod9(int v) { return v >= 9 ? v - 9 : v; }
constexpr int cmin(int a, int b) { return a < b ? a : b; }

constexpr int K1_SLOT = 16384, K1_NSLOT = 4, K1_XOFF = K1_SLOT * K1_NSLOT, K1_XD = 3;
constexpr int LDS_BYTES = K1_XOFF + 8 * K1_XD * 4096;
constexpr int K1_PF = 4, K1_NS = 324, K1_NSEG = 21;
constexpr int K1_NPREP = 26, K1_PREP0 = 18;
constexpr int K1_SPLIT = 144;
constexpr int G2_SLOT = 24576, G2_NSLOT = 4, G2_WIN = 16, G2_PF = 4, G2_NS = 216;
constexpr int CV_K = 0, CV_U0 = 4160, CV_U1 = CV_U0 + 4640, CV_CH = 13440;
constexpr int EV_PUB_U = 112;
constexpr int EV_PUB_A = 204;
constexpr int EV_PRE0 = 190, EV_PRE_STEP = 5;
constexpr int EV_CV_POLL = 150, EV_CV_CHECK = 170, EV_CV_INVW = 187;
constexpr int EV_CV_PRE7 = 300;
constexpr int EV_G2_POLL = 262, EV_G2_CHECK = 283, EV_G2_H0 = 289;

struct K1Sched {
  int seg[K1_NS], tile[K1_NS], j[K1_NS];
  int first_use[K1_NSEG], last_use[K1_NSEG];
  int wissue[K1_NSEG];
  int xissue[9];
  bool bar[K1_NS];
  int ops[K1_NS];
  int wait[K1_NS];
  int done[K1_NS];
  int epi0[2];
  int prep_ready;
  constexpr int ops_after(int a, int b) const { int k = 0; for (int q = a + 1; q <= b; ++q) k += ops[q]; return k; }
};
constexpr bool k1_dbl_first(int s) { return s == 7 || s == 9 || s == 16; }
constexpr K1Sched k1_make(int role) {
  K1Sched S{};
  int n = 0;
  for (int s = 0; s < 18;) {
    if (k1_dbl_first(s)) {
      for (int t = 0; t < 4; ++t) for (int h = 0; h < 2; ++h) for (int j = 0; j < 4; ++j) { S.seg[n] = s + h; S.tile[n] = t; S.j[n] = j; ++n; }
      s += 2;
    } else {
      for (int t = 0; t < 4; ++t) for (int j = 0; j < 4; ++j) { S.seg[n] = s; S.tile[n] = t; S.j[n] = j; ++n; }
      s += 1;
    }
  }
  for (int i = 0; i < 36; ++i) { S.seg[n] = 18 + i / 16; S.tile[n] = 0; S.j[n] = i % 16; ++n; }
  for (int s = 0; s < K1_NSEG; ++s) { S.first_use[s] = 100000; S.last_use[s] = -1; }
  for (int i = 0; i < K1_NS; ++i) { int s = S.seg[i]; if (i < S.first_use[s]) S.first_use[s] = i; if (i > S.last_use[s]) S.last_use[s] = i; }
  for (int s = 1; s < K1_NSEG; ++s) S.bar[S.first_use[s] - K1_PF - 1] = true;
  S.bar[S.last_use[10]] = true;
  S.wissue[0] = S.wissue[1] = -1;
  S.wissue[2] = 4; S.wissue[3] = 12;
  int busy = 0;
  for (int s = 4; s < K1_NSEG; ++s) {
    int q = S.last_use[s - K1_NSLOT];
    while (!S.bar[q]) ++q;
    int is = q + 1;
    if (is < busy) is = busy;
    S.wissue[s] = is;
    busy = is + 4;
  }
  S.xissue[0] = S.xissue[1] = -1;
  S.xissue[2] = 0;
  for (int p = 3; p < 9; ++p) S.xissue[p] = S.first_use[p - 3] + (p == 3 ? 8 : 4);
  S.epi0[0] = S.first_use[7] + 9;
  S.epi0[1] = S.first_use[16] + 9;
  for (int s = 0; s < K1_NSEG; ++s) if (S.wissue[s] >= 0) { S.ops[S.wissue[s]]++; S.ops[S.wissue[s] + 2]++; }
  for (int p = 0; p < 9; ++p) if (S.xissue[p] >= 0) for (int i = 0; i < 4; ++i) S.ops[S.xissue[p] + i]++;
  for (int i = 0; i < K1_NPREP; ++i) S.ops[K1_PREP0 + i / 2]++;
  for (int c = 0; c < 2; ++c) for (int t = 0; t < 4; ++t) S.ops[S.epi0[c] + 12 * t + 13]++;
  if (role == 0) {
    for (int i = 0; i < 12; ++i) S.ops[EV_PRE0 + EV_PRE_STEP * i]++;
    for (int i = 0; i < G2_WIN; ++i) S.ops[EV_G2_H0 + 2 * i]++;
  }
  S.prep_ready = -1;
  for (int i = 0; i < K1_NS; ++i) {
    S.wait[i] = 63;
    S.done[i] = -2;
    if (!S.bar[i]) continue;
    int youngest = -2;
    for (int s = 1; s < K1_NSEG; ++s)
      if (S.first_use[s] - K1_PF - 1 == i) {
        int y = S.wissue[s] < 0 ? -1 : S.wissue[s] + 2;
        if (s < 9) { int yx = S.xissue[s] < 0 ? -1 : S.xissue[s] + 3; if (yx > y) y = yx; }
        if (y > youngest) youngest = y;
      }
    if (youngest == -2) continue;
    S.wait[i] = cmin(63, S.ops_after(youngest, i));
    S.done[i] = youngest;
    if (S.prep_ready < 0 && youngest >= K1_PREP0 + K1_NPREP / 2 - 1) S.prep_ready = i + 1;
  }
  return S;
}
constexpr K1Sched K1S[2] = {k1_make(0), k1_make(1)};
constexpr int K1_PREP_UNITS = 5 + 1 + 18 + 4;
static_assert(K1S[0].prep_ready + K1_PREP_UNITS - 1 <= K1S[0].done[EV_PUB_U - 1] && K1S[0].bar[EV_PUB_U - 1], "u stores complete before publish");
static_assert(K1S[0].epi0[0] + 49 <= K1S[0].done[EV_PUB_A - 1] && K1S[0].bar[EV_PUB_A - 1], "chunk A stores complete before publish");
static_assert(K1S[0].bar[EV_G2_CHECK] && K1S[0].bar[EV_CV_INVW] && K1S[0].bar[EV_CV_PRE7 - 1] && K1S[0].last_use[17] < EV_CV_PRE7, "events");
static_assert(K1S[0].epi0[1] + 50 <= K1_NS && EV_G2_H0 + 2 * G2_WIN <= K1_NS + 1 && EV_PRE0 > EV_CV_INVW, "events");
static_assert(EV_PRE0 + 11 * EV_PRE_STEP <= K1S[0].done[315] && K1S[0].bar[315], "w2 prefetch landed by the last barrier");

struct Ctx {
  const char *w1, *w3, *xb, *kk, *vv, *sk, *gx, *gw, *w2, *ssm_k, *ssm_D, *q;
  char* ws; char* out;
  __amdgpu_buffer_rsrc_t rs;
  unsigned hb;
  unsigned voff_wg, voff_wu, voff_x0, voff_x1;
  unsigned rd[4];
  unsigned xl0, lane16;
  unsigned ev;
  u32x2 poll;
  unsigned rc0, rc1;
  int w, rot, nl, ml, b, mt, fq;
};
struct K1Prep {
  unsigned kr[12], vr[9];
  u32x4 sk, ga0, ga1, gw0, gw1;
  unsigned o0, oneg, ostep; int pd, pt0, tid;
  f32x2 s[4], k2[4], a; unsigned u[9]; float gs;
};
struct K1Epi { float m[2], p[2], pq[2], r[2], h[2]; u32x4 ho; };

__device__ __forceinline__ const char* karg(int i) {
  typedef const char* cptr;
  const __attribute__((address_space(4))) cptr* p = (const __attribute__((address_space(4))) cptr*)__builtin_amdgcn_kernarg_segment_ptr();
  asm volatile("" : "+s"(p));
  return p[i];
}
__device__ __forceinline__ int lane_of(const Ctx& c) {
  unsigned l = c.lane16;
  asm volatile("" : "+v"(l));
  return (int)(l >> 4);
}
__device__ __forceinline__ int g2_wg(int mt, int nt, int ks) { return ((nt * 4 + (mt & 3)) << 3) | (ks * 2 + (mt >> 2)); }
__device__ __forceinline__ int cv_wg(int cw) { return ((24 + (cw & 7)) << 3) | (cw >> 3); }
__device__ __forceinline__ void flag_store(const Ctx& c, int wg, int slot, unsigned epoch) {
  __builtin_amdgcn_raw_buffer_store_b64((u32x2){epoch + 1u, FLAG_MAGIC}, c.rs, WS_FLAG + wg * 256 + slot * 8, 0, AUX_SC1);
}
__device__ __forceinline__ void poll_issue(Ctx& c) {
  const unsigned vo = ((c.lane16 >> 1) & 255u);
  const char* base = c.ws + WS_FLAG + c.b * 256;
  asm volatile("global_load_dwordx2 %0, %1, %2 sc1" : "=v"(c.poll) : "v"(vo), "s"(base) : "memory");
}
__device__ __forceinline__ void poll_wait(Ctx& c, int lo, int hi, bool have) {
  const int l = lane_of(c) & 31;
  const unsigned want = (unsigned)__builtin_amdgcn_readlane((int)c.ev, 14) + 1u;
  const bool need = l >= lo && l < hi;
  bool ok = have && __all(!need || (c.poll[0] == want && c.poll[1] == FLAG_MAGIC));
  if (!ok) {
    GAS unsigned long long* p = (GAS unsigned long long*)(c.ws + WS_FLAG + c.b * 256) + l;
    for (unsigned spin = 0; spin < SPIN_CAP; ++spin) {
      unsigned long long v = __hip_atomic_load(p, __ATOMIC_RELAXED, __HIP_MEMORY_SCOPE_AGENT);
      if (__all(!need || ((unsigned)v == want && (unsigned)(v >> 32) == FLAG_MAGIC))) break;
      __builtin_amdgcn_s_sleep(1);
    }
  }
  asm volatile("" ::: "memory");
}

template <int sg, int half> __device__ __forceinline__ void k1_issue_w(const Ctx& c) {
  const unsigned dst = (sg % K1_NSLOT) * K1_SLOT + half * 8192;
  const char* base = half ? c.w3 : c.w1;
  if constexpr (sg < 18) {
    const int ksl = mod9(sg % 9 + c.rot);
    dma16(half ? (c.voff_wg ^ 64u) : c.voff_wg, base + ((size_t)(64 * ksl + 8 * c.w) * F_ + 128 * c.nl + 64 * (sg / 9)) * 2, dst + c.w * 1024);
  } else {
    const unsigned l = lane_of(c);
    const unsigned rp = l >> 1;
    const unsigned vo = (half ? (rp ^ 4u) : rp) * (F_ * 2) + (l & 1u) * 16;
    if constexpr (sg < 20) {
      const int ksl = mod9(4 * (sg - 18) + (c.w >> 1) + c.rot);
      dma16(vo, base + ((size_t)(64 * ksl + 32 * (c.w & 1)) * F_ + 1024 + 16 * c.nl) * 2, dst + c.w * 1024);
    } else {
      const int ksl = mod9(8 + c.rot);
      dma16_16(vo, base + ((size_t)(64 * ksl + 8 * c.w) * F_ + 1024 + 16 * c.nl) * 2, dst + c.w * 256);
    }
  }
}
template <int p, int i> __device__ __forceinline__ void k1_issue_x(const Ctx& c) {
  const int ksl = mod9(p + c.rot);
  dma16((i & 1) ? c.voff_x1 : c.voff_x0, c.xb + (size_t)(8 * i) * (D_ * 2) + ksl * 128, K1_XOFF + c.w * (K1_XD * 4096) + (p % K1_XD) * 4096 + i * 1024);
}
template <int p, int j> __device__ __forceinline__ bf16x8 k1_read_x(const Ctx& c) {
  return *(LAS bf16x8*)(ldsp(c.xl0 ^ (32 * j)) + (p % K1_XD) * 4096);
}
template <int n> __device__ __forceinline__ bf16x8 k1_read_w(const Ctx& c) {
  constexpr int sg = K1S[0].seg[n], t = K1S[0].tile[n], j = K1S[0].j[n];
  if constexpr (sg < 18) {
    LAS char* p = ldsp(c.rd[t]) + (sg % K1_NSLOT) * K1_SLOT + (16 * j) * 128;
    return tr_frag(p, p + 4 * 128);
  } else {
    const int off = (sg % K1_NSLOT) * K1_SLOT + (16 * j) * 32;
    return tr_frag(ldsp(c.rc0) + off, ldsp(c.rc1) + off);
  }
}
#define GLD1(dst, voff, sb) asm volatile("global_load_dword %0, %1, %2" : "=v"(dst) : "v"(voff), "s"(sb) : "memory")
#define GLD4(dst, voff, sb) asm volatile("global_load_dwordx4 %0, %1, %2" : "=v"(dst) : "v"(voff), "s"(sb) : "memory")
template <int i> __device__ __forceinline__ void k1_prep_load(const Ctx& c, K1Prep& P) {
  if constexpr (i == 0) {
    P.tid = lane_of(c) + 64 * c.w;
    const int ptid = P.tid < 288 ? P.tid : 287;
    P.pd = 72 * (c.b & 7) + 2 * (ptid % 36);
    P.pt0 = 64 * (c.b >> 3) + 8 * (ptid / 36);
    P.o0 = (unsigned)(P.pt0 * D_ + P.pd) * 2;
    P.oneg = P.pt0 > 0 ? P.o0 - 4 * (D_ * 2) : P.o0;
    P.ostep = P.pt0 > 0 ? D_ * 2 : 0;
  }
  if constexpr (i == 0) GLD4(P.ga0, c.lane16, c.gx);
  else if constexpr (i == 1) GLD4(P.ga1, 1024 + (c.lane16 & 127), c.gx);
  else if constexpr (i == 2) GLD4(P.gw0, c.lane16, c.gw);
  else if constexpr (i == 3) GLD4(P.gw1, 1024 + (c.lane16 & 127), c.gw);
  else if constexpr (i == 4) GLD4(P.sk, (unsigned)P.pd * 8, c.sk);
  else if constexpr (i < 9) GLD1(P.kr[i - 5], P.oneg + (i - 5) * P.ostep, c.kk);
  else if constexpr (i < 17) GLD1(P.kr[i - 5], P.o0 + (i - 9) * (D_ * 2), c.kk);
  else if constexpr (i == 17) GLD1(P.vr[0], P.oneg + 3 * P.ostep, c.vv);
  else GLD1(P.vr[i - 17], P.o0 + (i - 18) * (D_ * 2), c.vv);
}
template <int ctrl> __device__ __forceinline__ float dpp_add(float v) {
  return v + __builtin_bit_cast(float, __builtin_amdgcn_update_dpp(0, __builtin_bit_cast(int, v), ctrl, 0xf, 0xf, true));
}
__device__ __forceinline__ float dot2bf(unsigned a, unsigned b, float acc) {
  return __builtin_amdgcn_fdot2_f32_bf16(__builtin_bit_cast(bf16x2, a), __builtin_bit_cast(bf16x2, b), acc, false);
}
template <int k> __device__ __forceinline__ void k1_prep_unit(const Ctx& c, K1Prep& P) {
  if constexpr (k == 0) {
    asm volatile("" : "+v"(P.kr[0]), "+v"(P.kr[1]), "+v"(P.kr[2]), "+v"(P.kr[3]), "+v"(P.kr[4]), "+v"(P.kr[5]), "+v"(P.kr[6]), "+v"(P.kr[7]),
                 "+v"(P.kr[8]), "+v"(P.kr[9]), "+v"(P.kr[10]), "+v"(P.kr[11]), "+v"(P.vr[0]), "+v"(P.vr[1]), "+v"(P.vr[2]), "+v"(P.vr[3]),
                 "+v"(P.vr[4]), "+v"(P.vr[5]), "+v"(P.vr[6]), "+v"(P.vr[7]), "+v"(P.vr[8]), "+v"(P.sk), "+v"(P.ga0), "+v"(P.ga1), "+v"(P.gw0),
                 "+v"(P.gw1)::"memory");
    float s1 = dot2bf(P.ga1[0], P.gw1[0], 0.f), s2 = dot2bf(P.ga1[1], P.gw1[1], 0.f);
    s1 = dot2bf(P.ga1[2], P.gw1[2], s1); s2 = dot2bf(P.ga1[3], P.gw1[3], s2);
    P.gs = c.lane16 < 128 ? s1 + s2 : 0.f;
  } else if constexpr (k == 1) {
    float s1 = dot2bf(P.ga0[0], P.gw0[0], P.gs), s2 = dot2bf(P.ga0[1], P.gw0[1], 0.f);
    s1 = dot2bf(P.ga0[2], P.gw0[2], s1); s2 = dot2bf(P.ga0[3], P.gw0[3], s2);
    P.gs = s1 + s2;
  } else if constexpr (k == 2) {
    P.gs = dpp_add<0x111>(P.gs); P.gs = dpp_add<0x112>(P.gs);
  } else if constexpr (k == 3) {
    P.gs = dpp_add<0x114>(P.gs); P.gs = dpp_add<0x118>(P.gs);
  } else if constexpr (k == 4) {
    int v = __builtin_bit_cast(int, P.gs);
    float a = __builtin_bit_cast(float, __builtin_amdgcn_readlane(v, 15)), b = __builtin_bit_cast(float, __builtin_amdgcn_readlane(v, 31));
    float d = __builtin_bit_cast(float, __builtin_amdgcn_readlane(v, 47)), e = __builtin_bit_cast(float, __builtin_amdgcn_readlane(v, 63));
    float z = rbf((a + b) + (d + e));
    float g = rbf(__builtin_amdgcn_rcpf(1.0f + __builtin_amdgcn_exp2f(z * -1.44269504088896f)));
    if (c.lane16 == 0)
      __builtin_amdgcn_raw_buffer_store_b32(__builtin_bit_cast(unsigned, g), c.rs, WS_G + (256 * c.mt + 64 * c.fq + 8 * c.nl + c.w) * 4, 0, AUX_SC1);
  } else if constexpr (k == 5) {
    if (P.pt0 == 0) { P.kr[0] = P.kr[1] = P.kr[2] = P.kr[3] = 0u; P.vr[0] = 0u; }
    P.s[0] = (f32x2){bflo(P.sk[0]), bflo(P.sk[2])}; P.s[1] = (f32x2){bfhi(P.sk[0]), bfhi(P.sk[2])};
    P.s[2] = (f32x2){bflo(P.sk[1]), bflo(P.sk[3])}; P.s[3] = (f32x2){bfhi(P.sk[1]), bfhi(P.sk[3])};
    P.k2[0] = unpk2(P.kr[0]); P.k2[1] = unpk2(P.kr[1]); P.k2[2] = unpk2(P.kr[2]);
  } else if constexpr (k < 24) {
    constexpr int i = (k - 6) / 2, hf = (k - 6) % 2;
    if constexpr (hf == 0) {
      P.k2[(i + 3) & 3] = unpk2(P.kr[i + 3]);
      P.a = P.k2[i & 3] * P.s[3];
      P.a = P.k2[(i + 1) & 3] * P.s[2] + P.a;
    } else {
      P.a = P.k2[(i + 2) & 3] * P.s[1] + P.a;
      P.a = P.k2[(i + 3) & 3] * P.s[0] + P.a;
      f32x2 u = P.a * unpk2(P.vr[i]);
      P.u[i] = pk2(u[0], u[1]);
    }
  } else {
    constexpr int ch = (k - 24) >> 1, cp = (k - 24) & 1;
    constexpr unsigned sel = ch ? 0x07060302u : 0x05040100u;
    u32x4 o;
#pragma unroll
    for (int e = 0; e < 4; ++e) o[e] = __builtin_amdgcn_perm(P.u[7 + cp - 2 * e - 1], P.u[7 + cp - 2 * e], sel);
    if (P.tid < 288) __builtin_amdgcn_raw_buffer_store_b128(o, c.rs, WS_UR + (((P.pd + ch) * 2 + cp) * S_ + (2040 - P.pt0)) * 2, 0, AUX_SC1);
  }
}
template <int ch, int e> __device__ __forceinline__ void k1_epi_step(const Ctx& c, const f32x16 (&acc)[4], K1Epi& E) {
  constexpr int pr = e / 3, st = e % 3, t = pr / 4, i = 2 * (pr % 4);
  if constexpr (st == 0) {
    if constexpr (pr > 0) { E.h[0] = E.pq[0] * E.r[0]; E.h[1] = E.pq[1] * E.r[1]; }
    if constexpr (pr < 16) {
      E.m[0] = acc[t][i] * -1.44269504088896f; E.m[1] = acc[t][i + 1] * -1.44269504088896f;
      E.p[0] = acc[t][i] * acc[t][i + 8]; E.p[1] = acc[t][i + 1] * acc[t][i + 9];
      asm volatile("" : "+v"(E.p[0]), "+v"(E.p[1]));
    }
  } else if constexpr (st == 1) {
    if constexpr (pr < 16) { E.m[0] = __builtin_amdgcn_exp2f(E.m[0]); E.m[1] = __builtin_amdgcn_exp2f(E.m[1]); }
    if constexpr (pr > 0) {
      E.ho[(pr - 1) % 4] = pk2(E.h[0], E.h[1]);
      if constexpr (pr % 4 == 0)
        __builtin_amdgcn_raw_buffer_store_b128(E.ho, c.rs, c.lane16, c.hb + (8 * c.nl + 4 * ch + (pr - 1) / 4) * 1024, AUX_SC1);
    }
  } else {
    if constexpr (pr < 16) {
      float d0 = 1.0f + E.m[0], d1 = 1.0f + E.m[1];
      E.r[0] = __builtin_amdgcn_rcpf(d0); E.r[1] = __builtin_amdgcn_rcpf(d1);
      E.pq[0] = E.p[0]; E.pq[1] = E.p[1];
    }
  }
}

__device__ __forceinline__ int g2_ft(const Ctx& c, int s, int i8) {
  if (s >= 8) return 64 + i8;
  const int jj = ((s & 3) + c.nl + c.ml) & 3;
  return 16 * jj + 8 * (i8 >> 2) + (i8 & 3) + (s >= 4 ? 4 : 0);
}
template <int s, int i> __device__ __forceinline__ void g2_issue_w(const Ctx& c) {
  const unsigned n16 = 64u * i + (unsigned)lane_of(c);
  const unsigned vo = ((n16 * 171u) >> 11) * (D_ * 2 - 192) + n16 * 16u;
  dma16(vo, c.w2 + (size_t)(16 * g2_ft(c, s, c.w)) * (D_ * 2), K1_XOFF + (s % G2_NSLOT) * G2_SLOT + c.w * 3072 + i * 1024);
}
template <int i> __device__ __forceinline__ u32x4 g2_load_h(const Ctx& c) {
  return __builtin_amdgcn_raw_buffer_load_b128(c.rs, c.lane16, c.hb + g2_ft(c, i / 8, i % 8) * 1024, AUX_SC1);
}
__device__ __forceinline__ unsigned cv_base(int ch) { return ch < 7 ? K1_XOFF + ch * CV_CH : (ch == 7 ? K1_SLOT : 2 * K1_SLOT); }
template <int i> __device__ __forceinline__ void cv_issue(const Ctx& c, int cw, int ch) {
  const int d = 9 * cw + ch;
  if constexpr (i < 4) dma16(c.lane16, c.ssm_k + (size_t)d * (S_ * 2) + i * 1024, cv_base(ch) + CV_K + 64 + i * 1024);
  else dma16(c.lane16, c.ws + WS_UR + (size_t)d * (4 * S_) + (i - 4) * 1024, cv_base(ch) + (i < 8 ? CV_U0 + (i - 4) * 1024 : CV_U1 + (i - 8) * 1024));
}

template <int n, int NEND, int ROLE>
__device__ __forceinline__ void k1_slot(Ctx& c, bf16x8 (&xf)[36], bf16x8 (&wf)[K1_PF + 1], f32x16 (&acc)[4], K1Epi& E, K1Prep& P, u32x4 (&hw)[G2_WIN]) {
  constexpr const K1Sched& S = K1S[ROLE];
  constexpr int sg = S.seg[n];
  if constexpr (n + K1_PF == 288) {
    const unsigned l = lane_of(c);
    unsigned mat = (l >> 4) & 1, h = l >> 5, q = (l & 15) >> 2, p = l & 3;
    unsigned base = mat * 8192 + (8 * h + q) * 32 + p * 8;
    c.rc0 = base + (mat ? 128 : 0);
    c.rc1 = base + (mat ? 0 : 128);
  }
  if constexpr (n + K1_PF < K1_NS) wf[(n + K1_PF) % (K1_PF + 1)] = k1_read_w<n + K1_PF>(c);
  if constexpr (n + 4 < 144) {
    constexpr int sn = S.seg[n + 4];
    if constexpr (S.tile[n + 4] == 0 && sn >= 1) xf[4 * sn + S.j[n + 4]] = k1_read_x<sn, S.j[n + 4]>(c);
  }
  {
    constexpr int t = S.tile[n];
    constexpr int xi = sg < 18 ? 4 * (sg % 9) + S.j[n] : n - 288;
    constexpr bool first = (sg < 18) ? (sg % 9 == 0 && S.j[n] == 0) : (n == 288);
    if constexpr (first) acc[t] = __builtin_amdgcn_mfma_f32_32x32x16_bf16(wf[n % (K1_PF + 1)], xf[xi], (f32x16){}, 0, 0, 0);
    else acc[t] = __builtin_amdgcn_mfma_f32_32x32x16_bf16(wf[n % (K1_PF + 1)], xf[xi], acc[t], 0, 0, 0);
  }
  SCHED_FENCE();
  {
    constexpr int ws0 = [] { for (int s = 0; s < K1_NSEG; ++s) if (K1S[0].wissue[s] == n) return s; return -1; }();
    constexpr int ws1 = [] { for (int s = 0; s < K1_NSEG; ++s) if (K1S[0].wissue[s] >= 0 && K1S[0].wissue[s] + 2 == n) return s; return -1; }();
    constexpr int xs = [] { for (int p = 0; p < 9; ++p) if (K1S[0].xissue[p] >= 0 && n >= K1S[0].xissue[p] && n < K1S[0].xissue[p] + 4) return p; return -1; }();
    if constexpr (ws0 >= 0) k1_issue_w<ws0, 0>(c);
    if constexpr (ws1 >= 0) k1_issue_w<ws1, 1>(c);
    if constexpr (xs >= 0) k1_issue_x<xs, n - S.xissue[xs]>(c);
  }
  if constexpr (n >= K1_PREP0 && n < K1_PREP0 + K1_NPREP / 2) {
    k1_prep_load<2 * (n - K1_PREP0)>(c, P);
    k1_prep_load<2 * (n - K1_PREP0) + 1>(c, P);
  }
  if constexpr (n >= S.prep_ready && n < S.prep_ready + K1_PREP_UNITS) k1_prep_unit<n - S.prep_ready>(c, P);
  if constexpr (n >= S.epi0[0] && n < S.epi0[0] + 50) k1_epi_step<0, n - S.epi0[0]>(c, acc, E);
  if constexpr (n >= S.epi0[1] && n < S.epi0[1] + 50) k1_epi_step<1, n - S.epi0[1]>(c, acc, E);
  if constexpr (n == EV_PUB_U) {
    if (c.w == 1) { const int l = lane_of(c); if (l < 8) flag_store(c, cv_wg(8 * (c.b & 7) + l), c.b >> 3, c.ev); }
  }
  if constexpr (n == EV_PUB_A) {
    if (c.w == 1) { const int l = lane_of(c); if (l >= 8 && l < 14) flag_store(c, g2_wg(c.mt, l - 8, c.fq), c.nl, c.ev); }
  }
  if constexpr (ROLE == 0) {
    if constexpr (n == EV_PRE0 - 2) {
      c.w2 = karg(8) + ((size_t)(1152 * c.fq) * D_ + 96 * c.nl) * 2;
    }
    if constexpr (n >= EV_PRE0 && n < EV_PRE0 + 12 * EV_PRE_STEP && (n - EV_PRE0) % EV_PRE_STEP == 0)
      g2_issue_w<(n - EV_PRE0) / EV_PRE_STEP / 3, (n - EV_PRE0) / EV_PRE_STEP % 3>(c);
    if constexpr (n == EV_G2_POLL) poll_issue(c);
    if constexpr (n == EV_G2_CHECK) {
      asm volatile("s_waitcnt vmcnt(%1)" : "+v"(c.poll) : "n"(S.ops_after(EV_G2_POLL, EV_G2_CHECK)) : "memory");
      if (c.w == 0) poll_wait(c, 0, 8, true);
    }
    if constexpr (n >= EV_G2_H0 && (n - EV_G2_H0) % 2 == 0 && (n - EV_G2_H0) / 2 < G2_WIN) hw[(n - EV_G2_H0) / 2] = g2_load_h<(n - EV_G2_H0) / 2>(c);
  } else {
    const int cw = (c.b & 7) * 8 + (c.b >> 3) - 24;
    if constexpr (n == EV_PRE0 - 2) c.ssm_k = karg(4);
    if constexpr (n == EV_CV_POLL) poll_issue(c);
    if constexpr (n == EV_CV_CHECK) {
      asm volatile("s_waitcnt vmcnt(%1)" : "+v"(c.poll) : "n"(S.ops_after(EV_CV_POLL, EV_CV_CHECK)) : "memory");
      if (c.w == 0) { poll_wait(c, 0, 32, true); asm volatile("buffer_inv sc1" ::: "memory"); }
    }
    if constexpr (n == EV_CV_INVW) wait_vm<S.ops_after(EV_CV_CHECK, EV_CV_INVW)>();
    if constexpr (n >= EV_PRE0 && n < EV_PRE0 + 12 * EV_PRE_STEP && (n - EV_PRE0) % EV_PRE_STEP == 0) {
      if (c.w < 7) cv_issue<(n - EV_PRE0) / EV_PRE_STEP>(c, cw, c.w);
    }
    if constexpr (n >= EV_CV_PRE7 && n < EV_CV_PRE7 + 12) {
      if (c.w == 7) cv_issue<n - EV_CV_PRE7>(c, cw, 7);
    }
  }
  SCHED_FENCE();
  if constexpr (S.bar[n]) {
    wait_vm_barrier<S.wait[n]>();
    SCHED_FENCE();
  }
  if constexpr (n + 1 < NEND) k1_slot<n + 1, NEND, ROLE>(c, xf, wf, acc, E, P, hw);
}
__device__ __forceinline__ void k1_tail(const Ctx& c, const f32x16& a) {
  u32x4 o;
#pragma unroll
  for (int i = 0; i < 4; ++i) o[i] = pk2(silu_mul(a[2 * i], a[2 * i + 8]), silu_mul(a[2 * i + 1], a[2 * i + 9]));
  __builtin_amdgcn_raw_buffer_store_b128(o, c.rs, c.lane16, c.hb + (64 + c.nl) * 1024, AUX_SC1);
}

template <int n> __device__ __forceinline__ bf16x8 g2_read_w(unsigned rd) {
  constexpr int i = n / 3, t = n % 3, s = i / 8, j = i % 8;
  LAS char* p = ldsp(rd) + (s % G2_NSLOT) * G2_SLOT + (16 * j) * 192 + t * 64;
  return tr_frag(p, p + 8 * 192);
}
constexpr int G2_TAILST = 6;
constexpr int G2_POLL = 52, G2_CHECK = 24 * 3 - G2_PF - 1;
constexpr int g2_hslot(int i) {
  int s = 3 * (i - G2_WIN) + 2;
  if (i >= 32 && s < G2_CHECK + 1 + (i - 32)) s = G2_CHECK + 1 + (i - 32);
  return s;
}
constexpr int g2_hload_at(int n) { for (int i = G2_WIN; i < 72; ++i) if (g2_hslot(i) == n) return i; return -1; }
constexpr bool g2_bar(int n) { return n % 24 == 24 - G2_PF - 1 && n + 24 < G2_NS + 0 + 24 && n / 24 < 8; }
constexpr int g2_ops_in_slot(int n) {
  int k = 0;
  for (int s = 1; s + 3 < 9; ++s) for (int i = 0; i < 3; ++i) if (n == 24 * s + 24 - G2_PF + 1 + 3 * i) ++k;
  if (g2_hload_at(n) >= 0) ++k;
  if (n == G2_TAILST) ++k;
  return k;
}
constexpr int g2_ops_after(int a, int b) { int k = 0; for (int n = a + 1; n <= b; ++n) k += g2_ops_in_slot(n); return k; }
static_assert([] { for (int i = G2_WIN; i + 1 < 72; ++i) if (g2_hslot(i) >= g2_hslot(i + 1) || g2_hslot(i) + 12 > 3 * i) return false; return true; }(), "h load slots");
template <int n>
__device__ __forceinline__ void g2_slot(Ctx& c, unsigned rd, u32x4 (&hw)[G2_WIN], bf16x8 (&wf)[G2_PF + 1], f32x16 (&acc)[3], const f32x16& tailacc, u32x4& to) {
  constexpr int i = n / 3, t = n % 3, s = n / 24;
  if constexpr (n + G2_PF < G2_NS) wf[(n + G2_PF) % (G2_PF + 1)] = g2_read_w<n + G2_PF>(rd);
  if constexpr (i == 0) acc[t] = __builtin_amdgcn_mfma_f32_32x32x16_bf16(wf[n % (G2_PF + 1)], __builtin_bit_cast(bf16x8, hw[i % G2_WIN]), (f32x16){}, 0, 0, 0);
  else acc[t] = __builtin_amdgcn_mfma_f32_32x32x16_bf16(wf[n % (G2_PF + 1)], __builtin_bit_cast(bf16x8, hw[i % G2_WIN]), acc[t], 0, 0, 0);
  SCHED_FENCE();
  if constexpr (n >= 2 && n < 6) to[n - 2] = pk2(silu_mul(tailacc[2 * n - 4], tailacc[2 * n + 4]), silu_mul(tailacc[2 * n - 3], tailacc[2 * n + 5]));
  if constexpr (n == G2_TAILST) __builtin_amdgcn_raw_buffer_store_b128(to, c.rs, c.lane16, c.hb + (64 + c.nl) * 1024, AUX_SC1);
  {
    constexpr int m = n - (24 - G2_PF + 1);
    if constexpr (m >= 24 && m % 24 < 9 && m % 3 == 0 && m / 24 + 3 < 9) g2_issue_w<m / 24 + 3, (m % 24) / 3>(c);
  }
  if constexpr (g2_hload_at(n) >= 0) hw[g2_hload_at(n) % G2_WIN] = g2_load_h<g2_hload_at(n)>(c);
  if constexpr (n == G2_POLL) poll_issue(c);
  if constexpr (n == G2_CHECK) {
    asm volatile("s_waitcnt vmcnt(%1)" : "+v"(c.poll) : "n"(g2_ops_after(G2_POLL, G2_CHECK)) : "memory");
    if (c.w == 0) poll_wait(c, 8, 16, true);
  }
  SCHED_FENCE();
  if constexpr (g2_bar(n)) {
    constexpr int last = s + 1 < 4 ? (s == 0 ? G2_TAILST : -1) : 24 * (s - 2) + 24 - G2_PF + 1 + 6;
    wait_vm_barrier<last < 0 ? 63 : cmin(63, g2_ops_after(last, n))>();
    SCHED_FENCE();
    if constexpr (s == 0) {
      if (c.w == 1) { const int l = lane_of(c); if (l >= 8 && l < 14) flag_store(c, g2_wg(c.mt, l - 8, c.fq), 8 + c.nl, c.ev); }
      SCHED_FENCE();
    }
  }
  if constexpr (n + 1 < G2_NS) g2_slot<n + 1>(c, rd, hw, wf, acc, tailacc, to);
}

__device__ __forceinline__ void g2_phase(Ctx& c, u32x4 (&hw)[G2_WIN], const f32x16& tailacc) {
  const int lane = lane_of(c);
  const int w = c.w, ks = c.fq, nt = c.nl, mt = c.mt;
  unsigned rd;
  {
    int gq = lane >> 4, chf = gq & 1, h = gq >> 1, q = (lane & 15) >> 2, p = lane & 3;
    rd = K1_XOFF + (4 * h + q) * 192 + chf * 32 + p * 8;
  }
  bf16x8 wf[G2_PF + 1];
  f32x16 acc[3];
  u32x4 to;
  wf[0] = g2_read_w<0>(rd); wf[1] = g2_read_w<1>(rd); wf[2] = g2_read_w<2>(rd); wf[3] = g2_read_w<3>(rd);
  SCHED_FENCE();
  g2_slot<0>(c, rd, hw, wf, acc, tailacc, to);
  const int kd = w >> 1, half = w & 1;
  c.q = karg(0);
  c.out = (char*)karg(11);
  if (kd != ks) {
    const unsigned base = WS_PART + g2_wg(mt, nt, kd) * 49152 + (ks * 2 + half) * 6144;
#pragma unroll
    for (int t = 0; t < 3; ++t)
#pragma unroll
      for (int qp = 0; qp < 2; ++qp) {
        u32x4 o = {pk2(acc[t][8 * qp], acc[t][8 * qp + 1]), pk2(acc[t][8 * qp + 2], acc[t][8 * qp + 3]), pk2(acc[t][8 * qp + 4], acc[t][8 * qp + 5]),
                   pk2(acc[t][8 * qp + 6], acc[t][8 * qp + 7])};
        __builtin_amdgcn_raw_buffer_store_b128(o, c.rs, c.lane16, base + (t * 2 + qp) * 1024, AUX_SC1);
      }
  } else {
#pragma unroll
    for (int t = 0; t < 3; ++t)
#pragma unroll
      for (int qd = 0; qd < 4; ++qd)
        *(LAS f32x4*)(ldsp(c.lane16) + (half * 12 + t * 4 + qd) * 1024) = (f32x4){acc[t][4 * qd], acc[t][4 * qd + 1], acc[t][4 * qd + 2], acc[t][4 * qd + 3]};
    if (half == 0) {
      const int c0 = (96 * nt) / 9, c1 = (96 * nt + 95) / 9;
      poll_wait(c, 20, 20 + c1 - c0 + 1, false);
    }
  }
  full_barrier();
  STAMP(33);
  if (w == 7) { if (lane >= 16 && lane < 20 && lane - 16 != ks) flag_store(c, g2_wg(mt, nt, lane - 16), 16 + ks, c.ev); }
  const int uh = w / 3, ut = w % 3;
  const int qp = lane >> 5, cc = lane & 31;
  const int tok = 256 * mt + 64 * ks + 32 * uh + cc, d0 = 96 * nt + 32 * ut;
  float ga[16], og = 0.f;
  if (w < 6) {
    u32x4 zz[2], qq[2];
#pragma unroll
    for (int i = 0; i < 2; ++i)
      zz[i] = __builtin_amdgcn_raw_buffer_load_b128(c.rs, ((16 * i + (lane >> 2)) * S_) * 2 + (lane & 3) * 16,
                                                    WS_Z + (d0 * S_ + 256 * mt + 64 * ks + 32 * uh) * 2, AUX_SC1);
    const float g = __builtin_bit_cast(float, __builtin_amdgcn_raw_buffer_load_b32(c.rs, tok * 4, WS_G, AUX_SC1));
#pragma unroll
    for (int i = 0; i < 2; ++i) qq[i] = *(const u32x4*)(c.q + ((size_t)tok * D_ + d0 + 16 * qp + 8 * i) * 2);
    LAS char* zt = ldsp(24576 + w * 2560);
#pragma unroll
    for (int i = 0; i < 2; ++i) *(LAS u32x4*)(zt + (16 * i + (lane >> 2)) * 80 + (lane & 3) * 16) = zz[i];
    og = rbf(1.0f - g);
#pragma unroll
    for (int j = 0; j < 16; ++j) {
      float z = bf2f(*(LAS u16*)(zt + (16 * qp + j) * 80 + cc * 2));
      unsigned qw = qq[j >> 3][(j >> 1) & 3];
      float a = rbf(((j & 1) ? bfhi(qw) : bflo(qw)) * z);
      ga[j] = rbf(g * a);
    }
  } else if (w == 6) {
    const int l = lane & 31;
    const unsigned want = (unsigned)__builtin_amdgcn_readlane((int)c.ev, 14) + 1u;
    const bool need = l >= 16 && l < 20 && l - 16 != ks;
    GAS unsigned long long* p = (GAS unsigned long long*)(c.ws + WS_FLAG + c.b * 256) + l;
    for (unsigned spin = 0; spin < SPIN_CAP; ++spin) {
      unsigned long long v = __hip_atomic_load(p, __ATOMIC_RELAXED, __HIP_MEMORY_SCOPE_AGENT);
      if (__all(!need || ((unsigned)v == want && (unsigned)(v >> 32) == FLAG_MAGIC))) break;
      __builtin_amdgcn_s_sleep(1);
    }
    if (lane == 14) __builtin_amdgcn_raw_buffer_store_b32(c.ev + 1u, c.rs, WS_EPOCH + c.b * 4, 0, AUX_SC1);
  }
  asm volatile("s_barrier" ::: "memory");
  STAMP(34);
  if (w < 6) {
    u32x4 pp[3][2];
#pragma unroll
    for (int s = 0; s < 3; ++s) {
      const int src = s + (s >= ks ? 1 : 0);
#pragma unroll
      for (int h = 0; h < 2; ++h)
        pp[s][h] = __builtin_amdgcn_raw_buffer_load_b128(c.rs, qp * 1024 + (cc + 32 * h) * 16, WS_PART + c.b * 49152 + (src * 2 + uh) * 6144 + ut * 2048, AUX_SC1);
    }
    f32x4 own[2][2];
#pragma unroll
    for (int o = 0; o < 2; ++o)
#pragma unroll
      for (int h = 0; h < 2; ++h) own[o][h] = *(LAS f32x4*)(ldsp((cc + 32 * h) * 16) + (uh * 12 + ut * 4 + 2 * qp + o) * 1024);
#pragma unroll
    for (int o = 0; o < 2; ++o) {
      float res[8];
#pragma unroll
      for (int h = 0; h < 2; ++h)
#pragma unroll
        for (int k = 0; k < 4; ++k) {
          float bsum = own[o][h][k];
#pragma unroll
          for (int s = 0; s < 3; ++s) { unsigned v = pp[s][h][2 * o + (k >> 1)]; bsum += (k & 1) ? bfhi(v) : bflo(v); }
          res[4 * h + k] = ga[8 * o + 4 * h + k] + rbf(og * rbf(bsum));
        }
      u32x4 ov = {pk2(res[0], res[1]), pk2(res[2], res[3]), pk2(res[4], res[5]), pk2(res[6], res[7])};
      *(u32x4*)(c.out + ((size_t)tok * D_ + d0 + 16 * qp + 8 * o) * 2) = ov;
    }
  }
  STAMP(35);
}

typedef unsigned u32x2a4 __attribute__((ext_vector_type(2), aligned(4)));
__device__ __forceinline__ bf16x8 cv_read_a(LAS char* p) {
  u32x2 lo = *(LAS u32x2a4*)p;
  u32x2 hi = *(LAS u32x2a4*)(p + 8);
  u32x4 v = {lo[0], lo[1], hi[0], hi[1]};
  return __builtin_bit_cast(bf16x8, v);
}
template <bool PREF>
__device__ __forceinline__ void cv_block(f32x4 (&acc)[8], const bf16x8 (&af)[8], const bf16x8 (&bf)[8], bf16x8 (&naf)[8], bf16x8 (&nbf)[8], LAS char* ap,
                                         LAS char* bp) {
  int k = 0;
#pragma unroll
  for (int p = 0; p < 8; ++p)
#pragma unroll
    for (int dl = 0; dl + p < 8; ++dl) {
      if (PREF && (k & 1) == 0 && k < 32) {
        const int f = k >> 2;
        if (k & 2) naf[f] = cv_read_a(ap + 512 * f);
        else nbf[f] = *(LAS bf16x8*)(bp + 64 * (56 - 8 * f));
      }
      acc[p + dl] = __builtin_amdgcn_mfma_f32_16x16x32_bf16(af[p], bf[dl], acc[p + dl], 0, 0, 0);
      if (k & 1) SCHED_FENCE();
      ++k;
    }
}
__device__ __forceinline__ void cv_store(const Ctx& c, const f32x4& y, LAS char* cl, int g, int d, int lane, float Dv) {
  const int q = lane >> 4, pi = (lane >> 3) & 1, ii = lane & 7;
  const int T = 256 * g + 32 * ii + 8 * q;
  u32x4 uu = *(LAS u32x4*)(cl + CV_U1 + 2 * (2040 - T));
  float z[4];
#pragma unroll
  for (int k = 0; k < 4; ++k) {
    float u = pi ? bflo(uu[3 - k]) : bfhi(uu[3 - k]);
    z[k] = rbf(y[k]) + rbf(u * Dv);
  }
  unsigned p01 = pk2(z[0], z[1]), p23 = pk2(z[2], z[3]);
  unsigned snd = pi ? p01 : p23;
  unsigned rcv = __builtin_amdgcn_update_dpp(0u, snd, 0x128, 0xf, 0xf, false);
  unsigned E = pi ? rcv : p01, O = pi ? p23 : rcv;
  u32x2 o = {(E & 0xffffu) | (O << 16), (E >> 16) | (O & 0xffff0000u)};
  __builtin_amdgcn_raw_buffer_store_b64(o, c.rs, (T + 4 * pi) * 2, WS_Z + d * (S_ * 2), AUX_SC1);
}
__device__ __forceinline__ void cv_phase(Ctx& c) {
  const int lane = lane_of(c), w = c.w;
  const int cw = (c.b & 7) * 8 + (c.b >> 3) - 24;
  const int d = 9 * cw + w, d8 = 9 * cw + 8;
  c.ssm_D = karg(5);
  LAS char* cl = ldsp(cv_base(w));
  LAS char* cl8 = ldsp(cv_base(8));
  const int ntz = (lane < 32) ? (9 * cw) / 96 : (9 * cw + 8) / 96;
  const int zwg = g2_wg((lane >> 2) & 7, ntz, lane & 3);
  const unsigned zev = *(const unsigned*)(c.ws + WS_EPOCH + zwg * 4);
  const float Dv = bf2f(*(const u16*)(c.ssm_D + d * 2)), Dv8 = bf2f(*(const u16*)(c.ssm_D + d8 * 2));
  dma16(c.lane16, c.ws + WS_UR + (size_t)d8 * (4 * S_) + w * 1024, cv_base(8) + (w < 4 ? CV_U0 + w * 1024 : CV_U1 + (w - 4) * 1024));
  if (w < 4) dma16(c.lane16, c.ssm_k + (size_t)d8 * (S_ * 2) + w * 1024, cv_base(8) + CV_K + 64 + w * 1024);
  if (lane < 60) {
    int o = lane < 4 ? CV_K + lane * 16 : (lane < 32 ? CV_U0 + 4096 + (lane - 4) * 16 : CV_U1 + 4096 + (lane - 32) * 16);
    *(LAS u32x4*)(cl + o) = (u32x4){0u, 0u, 0u, 0u};
    if (w == 7) *(LAS u32x4*)(cl8 + o) = (u32x4){0u, 0u, 0u, 0u};
  }
  const int rho = lane & 15, q = lane >> 4, pi = (lane >> 3) & 1, ii = lane & 7;
  const int aoff = CV_K + 4 + 4 * rho + 16 * q;
  const int boff = CV_U0 + pi * 4640 + 2 * (2016 - 32 * ii + 8 * q) - 64 * 56;
  full_barrier();
  if (w == 1) {
    if (lane >= 8 && lane < 14) flag_store(c, g2_wg(c.mt, lane - 8, c.fq), 8 + c.nl, c.ev);
    if (lane == 14) __builtin_amdgcn_raw_buffer_store_b32(c.ev + 1u, c.rs, WS_EPOCH + c.b * 4, 0, AUX_SC1);
  }
  STAMP(24);
  f32x4 acc[8] = {};
  bf16x8 a0[8], b0[8], a1[8], b1[8];
#pragma unroll
  for (int f = 0; f < 8; ++f) {
    b0[f] = *(LAS bf16x8*)(cl + boff + 64 * (56 - 8 * f));
    a0[f] = cv_read_a(cl + aoff + 512 * f);
  }
  SCHED_FENCE();
#pragma unroll 1
  for (int rr = 0; rr < 8; rr += 2) {
    cv_block<true>(acc, a0, b0, a1, b1, cl + aoff + 64 * (rr + 1), cl + boff + 64 * (rr + 1));
    LAS char* nb = rr < 6 ? cl + 64 * (rr + 2) : cl8 + 64 * w;
    cv_block<true>(acc, a1, b1, a0, b0, nb + aoff, nb + boff);
  }
  STAMP(25);
  f32x4 acc8[8] = {};
  cv_block<false>(acc8, a0, b0, a1, b1, cl, cl);
#pragma unroll
  for (int g = 0; g < 8; ++g) cv_store(c, acc[g], cl, g, d, lane, Dv);
  wait_lgkm0();
#pragma unroll
  for (int g = 0; g < 8; ++g) *(LAS f32x4*)(cl + g * 1024 + lane * 16) = acc8[g];
  asm volatile("s_waitcnt lgkmcnt(0)\n\ts_barrier" ::: "memory");
  STAMP(26);
  f32x4 y = {};
#pragma unroll
  for (int ww = 0; ww < 8; ++ww) y += *(LAS f32x4*)(ldsp(cv_base(ww)) + w * 1024 + lane * 16);
  cv_store(c, y, cl8, w, d8, lane, Dv8);
  full_barrier();
  if (w == 0 && (lane < 32 || (9 * cw) / 96 != (9 * cw + 8) / 96)) flag_store(c, zwg, 20 + cw - (96 * ntz) / 9, zev);
  STAMP(35);
}

template <int ROLE>
__device__ __forceinline__ void fused_body(const int w) {
  const int b = blockIdx.x, xcd = b & 7, r = b >> 3;
  const int ml = r & 3, nl = r >> 2, fq = xcd >> 1;
  const int mt = 4 * (xcd & 1) + ml;
  const int lane = __builtin_amdgcn_mbcnt_hi(~0u, __builtin_amdgcn_mbcnt_lo(~0u, 0u));
  Ctx c;
  const char* x = karg(0);
  char* ws = (char*)karg(10);
  RSTAMP(37);
  STAMP(0);
  c.w = w; c.nl = nl; c.ml = ml; c.b = b; c.ws = ws; c.mt = mt; c.fq = fq;
  c.rs = __builtin_amdgcn_make_buffer_rsrc(ws, 0, WS_END, 0x00020000);
  c.rot = (nl + 2 * ml) % 9;
  c.lane16 = lane * 16;
  c.w1 = karg(6) + (size_t)(1152 * fq) * 2;
  c.w3 = karg(7) + (size_t)(1152 * fq) * 2;
  c.xb = (const char*)x + (size_t)(256 * mt + 32 * w) * (D_ * 2);
  c.hb = WS_H + ((8 * mt + w) * 288 + 72 * fq) * 1024;
  c.kk = karg(1); c.vv = karg(2); c.sk = karg(3); c.gw = karg(9);
  c.gx = (const char*)x + (size_t)(256 * mt + 64 * fq + 8 * nl + w) * (D_ * 2);
  {
    int row = lane >> 3, pos = lane & 7;
    int sw = 2 * ((row >> 1) & 1);
    c.voff_wg = row * (F_ * 2) + ((pos ^ sw) * 16);
    int sx = lane >> 4;
    c.voff_x0 = row * (D_ * 2) + ((pos ^ sx) * 16);
    c.voff_x1 = row * (D_ * 2) + ((pos ^ (sx + 4)) * 16);
  }
  k1_issue_w<0, 0>(c); k1_issue_w<0, 1>(c);
  k1_issue_x<0, 0>(c); k1_issue_x<0, 1>(c); k1_issue_x<0, 2>(c); k1_issue_x<0, 3>(c);
  {
    int ewg = lane < 8 ? cv_wg(8 * xcd + lane) : (lane < 14 ? g2_wg(mt, lane - 8, fq) : ((lane & 60) == 16 ? g2_wg(mt, nl < 6 ? nl : 0, lane & 3) : b));
    GLD1(c.ev, (unsigned)ewg * 4, ws + WS_EPOCH);
  }
  k1_issue_w<1, 0>(c); k1_issue_w<1, 1>(c);
  {
    int gq = lane >> 4, mat = gq & 1, h = gq >> 1, q = (lane & 15) >> 2, p = lane & 3;
    int X = 2 * (q >> 1) + 4 * mat;
#pragma unroll
    for (int t = 0; t < 4; ++t) c.rd[t] = mat * 8192 + (8 * h + q) * 128 + (((2 * t) ^ X) + (p >> 1)) * 16 + (p & 1) * 8;
    int cc = lane & 31, hh = lane >> 5;
    c.xl0 = K1_XOFF + w * (K1_XD * 4096) + cc * 128 + ((hh ^ ((cc >> 1) & 7)) * 16);
  }
  bf16x8 xf[36];
  bf16x8 wf[K1_PF + 1];
  f32x16 acc[4];
  u32x4 hw[G2_WIN];
  asm volatile("s_waitcnt vmcnt(2)\n\ts_barrier" : "+v"(c.ev)::"memory");
  STAMP(1);
  k1_issue_x<1, 0>(c); k1_issue_x<1, 1>(c); k1_issue_x<1, 2>(c); k1_issue_x<1, 3>(c);
  xf[0] = k1_read_x<0, 0>(c); wf[0] = k1_read_w<0>(c);
  xf[1] = k1_read_x<0, 1>(c); wf[1] = k1_read_w<1>(c);
  xf[2] = k1_read_x<0, 2>(c); wf[2] = k1_read_w<2>(c);
  xf[3] = k1_read_x<0, 3>(c); wf[3] = k1_read_w<3>(c);
  static_assert(K1_PF == 4, "prologue reads");
  SCHED_FENCE();
  STAMP(2);
  K1Epi E;
  K1Prep P;
  k1_slot<0, K1_NS, ROLE>(c, xf, wf, acc, E, P, hw);
  STAMP(23);
  if constexpr (ROLE == 1) {
    k1_tail(c, acc[0]);
    cv_phase(c);
  } else {
    g2_phase(c, hw, acc[0]);
  }
}

__global__ __launch_bounds__(512, 2) void fused_kernel(const void* x, const void* kk, const void* vv, const void* shift_k, const void* ssm_k,
                                                       const void* ssm_D, const void* w1, const void* w3, const void* w2, const void* gate_w, void* ws,
                                                       void* out) {
  const int w = __builtin_amdgcn_readfirstlane(threadIdx.x >> 6);
  if (blockIdx.x >= 192) fused_body<1>(w);
  else fused_body<0>(w);
}

extern "C" void kernel_launch(void* const* d_in, const int* in_sizes, int n_in, void* d_out, int out_size, void* d_ws, size_t ws_size,
                              hipStream_t stream) {
  (void)hipFuncSetAttribute((const void*)fused_kernel, hipFuncAttributeMaxDynamicSharedMemorySize, LDS_BYTES);
  fused_kernel<<<256, 512, LDS_BYTES, stream>>>(d_in[0], d_in[1], d_in[2], d_in[3], d_in[4], d_in[5], d_in[7], d_in[8], d_in[9], d_in[10], d_ws, d_out);
}
```

```cpp
#include <hip/hip_runtime.h>
#include <hip/hip_bf16.h>

#define LAS __attribute__((address_space(3)))
#define GAS __attribute__((address_space(1)))
typedef unsigned short u16;
typedef __bf16 bf16x2 __attribute__((ext_vector_type(2)));
typedef __bf16 bf16x4 __attribute__((ext_vector_type(4)));
typedef __bf16 bf16x8 __attribute__((ext_vector_type(8)));
typedef float f32x2 __attribute__((ext_vector_type(2)));
typedef float f32x4 __attribute__((ext_vector_type(4)));
typedef float f32x16 __attribute__((ext_vector_type(16)));
typedef unsigned u32x2 __attribute__((ext_vector_type(2)));
typedef unsigned u32x4 __attribute__((ext_vector_type(4)));

constexpr int S_ = 2048, D_ = 576, F_ = 4608;
constexpr unsigned WS_H = 0;
constexpr unsigned WS_PART = WS_H + 64 * 288 * 1024;
constexpr unsigned WS_UR = WS_PART + 256 * 49152;
constexpr unsigned WS_Z = WS_UR + D_ * 2 * S_ * 2;
constexpr unsigned WS_G = WS_Z + D_ * S_ * 2;
constexpr unsigned WS_FLAG = WS_G + S_ * 4;
constexpr unsigned WS_EPOCH = WS_FLAG + 256 * 512;
constexpr unsigned WS_END = WS_EPOCH + 1024;
constexpr unsigned FLAG_MAGIC = 0x7c3a91e5u;
constexpr int AUX_SC1 = 16;
constexpr unsigned OOB = 0x7ff00000u;
constexpr unsigned SPIN_CAP = 1u << 16;

#define STAMP(i)
#define RSTAMP(i)
#define STDUMP()

__device__ __forceinline__ float bf2f(u16 v) { return __builtin_bit_cast(float, (unsigned)v << 16); }
__device__ __forceinline__ float bflo(unsigned v) { return __builtin_bit_cast(float, v << 16); }
__device__ __forceinline__ float bfhi(unsigned v) { return __builtin_bit_cast(float, v & 0xffff0000u); }
__device__ __forceinline__ float rbf(float x) { return (float)(__bf16)x; }
__device__ __forceinline__ unsigned pk2(float lo, float hi) {
  f32x2 v = {lo, hi};
  return __builtin_bit_cast(unsigned, __builtin_convertvector(v, bf16x2));
}
__device__ __forceinline__ f32x2 unpk2(unsigned v) { return (f32x2){bflo(v), bfhi(v)}; }
#define SCHED_FENCE() __builtin_amdgcn_sched_barrier(0)

__device__ __forceinline__ void dma16(unsigned voff, const void* sbase, unsigned lds_dst) {
  unsigned keep;
  asm volatile("s_mov_b32 %0, m0\n\ts_mov_b32 m0, %3\n\ts_nop 0\n\tglobal_load_lds_dwordx4 %1, %2\n\ts_mov_b32 m0, %0"
               : "=&s"(keep) : "v"(voff), "s"(sbase), "s"(lds_dst) : "memory");
}
__device__ __forceinline__ void dma16_16(unsigned voff, const void* sbase, unsigned lds_dst) {
  unsigned keep;
  asm volatile("s_mov_b32 %0, m0\n\ts_mov_b32 m0, %3\n\ts_mov_b64 exec, 0xffff\n\tglobal_load_lds_dwordx4 %1, %2\n\ts_mov_b64 exec, -1\n\ts_mov_b32 m0, %0"
               : "=&s"(keep) : "v"(voff), "s"(sbase), "s"(lds_dst) : "memory");
}
template <int N> __device__ __forceinline__ void wait_vm_barrier() {
  asm volatile("s_waitcnt vmcnt(%0)\n\ts_barrier" ::"n"(N) : "memory");
}
template <int N> __device__ __forceinline__ void wait_vm() { asm volatile("s_waitcnt vmcnt(%0)" ::"n"(N) : "memory"); }
__device__ __forceinline__ void wait_lgkm0() { asm volatile("s_waitcnt lgkmcnt(0)" ::: "memory"); }
__device__ __forceinline__ void full_barrier() { asm volatile("s_waitcnt vmcnt(0) lgkmcnt(0)\n\ts_barrier" ::: "memory"); }
__device__ __forceinline__ LAS char* ldsp(unsigned a) { return (LAS char*)(size_t)a; }

__device__ __forceinline__ bf16x8 tr_frag(LAS char* p0, LAS char* p1) {
  bf16x4 a = __builtin_amdgcn_ds_read_tr16_b64_v4bf16((LAS bf16x4*)p0);
  bf16x4 b = __builtin_amdgcn_ds_read_tr16_b64_v4bf16((LAS bf16x4*)p1);
  return __builtin_shufflevector(a, b, 0, 1, 2, 3, 4, 5, 6, 7);
}
__device__ __forceinline__ float silu_mul(float g, float u) {
  float e = __builtin_amdgcn_exp2f(g * -1.44269504088896f);
  return g * u * __builtin_amdgcn_rcpf(1.0f + e);
}
__device__ __forceinline__ int mod9(int v) { return v >= 9 ? v - 9 : v; }
constexpr int cmin(int a, int b) { return a < b ? a : b; }

constexpr int K1_SLOT = 16384, K1_NSLOT = 4, K1_XOFF = K1_SLOT * K1_NSLOT, K1_XD = 3;
constexpr int LDS_BYTES = K1_XOFF + 8 * K1_XD * 4096;
constexpr int K1_PF = 4, K1_NS = 324, K1_NSEG = 21;
constexpr int K1_NPREP = 26, K1_PREP0 = 18;
constexpr int K1_PREP_UNITS = 5 + 2 + 27 + 4, K1_PREP_READY = 60;
constexpr int G2_SLOT = 24576, G2_NSLOT = 4, G2_WIN = 16, G2_PF = 4, G2_NS = 216;
constexpr int CV_K = 0, CV_U0 = 4160, CV_U1 = CV_U0 + 4640, CV_CH = 13440;
constexpr int EV_PUB_U = 140;
constexpr int EV_PUB_A = 204;
constexpr int EV_PRE0 = 190, EV_PRE_STEP = 5;
constexpr int EV_CV_POLL = 176, EV_CV_CHECK = 192, EV_CV_INVW = 203;
constexpr int EV_CV_PRE7 = 300;
constexpr int EV_G2_POLL = 262, EV_G2_CHECK = 283, EV_G2_H0 = 289;

struct K1Sched {
  int seg[K1_NS], tile[K1_NS], j[K1_NS];
  int first_use[K1_NSEG], last_use[K1_NSEG];
  int wissue[K1_NSEG];
  int xissue[9];
  bool bar[K1_NS];
  int ops[K1_NS];
  int wait[K1_NS];
  int done[K1_NS];
  int epi0[2];
  int prep_ready;
  constexpr int ops_after(int a, int b) const { int k = 0; for (int q = a + 1; q <= b; ++q) k += ops[q]; return k; }
};
constexpr bool k1_dbl_first(int s) { return s == 7 || s == 9 || s == 16; }
constexpr K1Sched k1_make(int role) {
  K1Sched S{};
  int n = 0;
  for (int s = 0; s < 18;) {
    if (k1_dbl_first(s)) {
      for (int t = 0; t < 4; ++t) for (int h = 0; h < 2; ++h) for (int j = 0; j < 4; ++j) { S.seg[n] = s + h; S.tile[n] = t; S.j[n] = j; ++n; }
      s += 2;
    } else {
      for (int t = 0; t < 4; ++t) for (int j = 0; j < 4; ++j) { S.seg[n] = s; S.tile[n] = t; S.j[n] = j; ++n; }
      s += 1;
    }
  }
  for (int i = 0; i < 36; ++i) { S.seg[n] = 18 + i / 16; S.tile[n] = 0; S.j[n] = i % 16; ++n; }
  for (int s = 0; s < K1_NSEG; ++s) { S.first_use[s] = 100000; S.last_use[s] = -1; }
  for (int i = 0; i < K1_NS; ++i) { int s = S.seg[i]; if (i < S.first_use[s]) S.first_use[s] = i; if (i > S.last_use[s]) S.last_use[s] = i; }
  for (int s = 1; s < K1_NSEG; ++s) S.bar[S.first_use[s] - K1_PF - 1] = true;
  S.bar[S.last_use[10]] = true;
  S.wissue[0] = S.wissue[1] = -1;
  S.wissue[2] = 4; S.wissue[3] = 12;
  int busy = 0;
  for (int s = 4; s < K1_NSEG; ++s) {
    int q = S.last_use[s - K1_NSLOT];
    while (!S.bar[q]) ++q;
    int is = q + 1;
    if (is < busy) is = busy;
    S.wissue[s] = is;
    busy = is + 4;
  }
  S.xissue[0] = S.xissue[1] = -1;
  S.xissue[2] = 0;
  for (int p = 3; p < 9; ++p) S.xissue[p] = S.first_use[p - 3] + (p == 3 ? 8 : 4);
  S.epi0[0] = S.first_use[7] + 9;
  S.epi0[1] = S.first_use[16] + 9;
  for (int s = 0; s < K1_NSEG; ++s) if (S.wissue[s] >= 0) { S.ops[S.wissue[s]]++; S.ops[S.wissue[s] + 2]++; }
  for (int p = 0; p < 9; ++p) if (S.xissue[p] >= 0) for (int i = 0; i < 4; ++i) S.ops[S.xissue[p] + i]++;
  for (int i = 0; i < K1_NPREP; ++i) S.ops[K1_PREP0 + i / 2]++;
  for (int c = 0; c < 2; ++c) for (int t = 0; t < 4; ++t) S.ops[S.epi0[c] + 12 * t + 13]++;
  S.ops[K1_PREP_READY + 4]++;
  for (int i = 0; i < 4; ++i) S.ops[K1_PREP_READY + K1_PREP_UNITS - 4 + i]++;
  if (role == 0) {
    for (int i = 0; i < 12; ++i) S.ops[EV_PRE0 + EV_PRE_STEP * i]++;
    for (int i = 0; i < G2_WIN; ++i) S.ops[EV_G2_H0 + 2 * i]++;
  }
  S.prep_ready = -1;
  for (int i = 0; i < K1_NS; ++i) {
    S.wait[i] = 63;
    S.done[i] = -2;
    if (!S.bar[i]) continue;
    int youngest = -2;
    for (int s = 1; s < K1_NSEG; ++s)
      if (S.first_use[s] - K1_PF - 1 == i) {
        int y = S.wissue[s] < 0 ? -1 : S.wissue[s] + 2;
        if (s < 9) { int yx = S.xissue[s] < 0 ? -1 : S.xissue[s] + 3; if (yx > y) y = yx; }
        if (y > youngest) youngest = y;
      }
    if (youngest == -2) continue;
    S.wait[i] = cmin(63, S.ops_after(youngest, i));
    S.done[i] = youngest;
    if (S.prep_ready < 0 && youngest >= K1_PREP0 + K1_NPREP / 2 - 1) S.prep_ready = i + 1;
  }
  return S;
}
constexpr K1Sched K1S[2] = {k1_make(0), k1_make(1)};
static_assert(K1S[0].prep_ready == K1_PREP_READY && K1S[0].prep_ready + K1_PREP_UNITS - 1 <= K1S[0].done[EV_PUB_U - 1] && K1S[0].bar[EV_PUB_U - 1], "u stores complete before publish");
static_assert(K1S[0].epi0[0] + 49 <= K1S[0].done[EV_PUB_A - 1] && K1S[0].bar[EV_PUB_A - 1], "chunk A stores complete before publish");
static_assert(K1S[0].bar[EV_G2_CHECK] && K1S[0].bar[EV_CV_INVW] && K1S[0].bar[EV_CV_PRE7 - 1] && K1S[0].last_use[17] < EV_CV_PRE7, "events");
static_assert(K1S[0].epi0[1] + 50 <= K1_NS && EV_G2_H0 + 2 * G2_WIN <= K1_NS + 1 && EV_PRE0 + 4 * EV_PRE_STEP > EV_CV_INVW, "events");
static_assert(EV_PRE0 + 11 * EV_PRE_STEP <= K1S[0].done[315] && K1S[0].bar[315], "w2 prefetch landed by the last barrier");

struct Ctx {
  const char *w1, *w3, *xb, *kk, *vv, *sk, *gx, *gw, *w2, *ssm_k, *ssm_D, *q;
  char* ws; char* out;
  __amdgpu_buffer_rsrc_t rs;
  unsigned hb;
  unsigned voff_wg, voff_wu, voff_x0, voff_x1;
  unsigned rd[4];
  unsigned xl0, lane16;
  unsigned ev;
  u32x2 poll;
  unsigned rc0, rc1;
  int w, rot, nl, ml, b, mt, fq;
};
struct K1Prep {
  unsigned kr[12], vr[9];
  u32x4 sk, ga0, ga1, gw0, gw1;
  unsigned o0, oneg, ostep; int pd, pt0, tid;
  f32x2 s[4], k2[4], a, v2; unsigned u[9]; float gs;
};
struct K1Epi { float m[2], p[2], pq[2], r[2], h[2]; u32x4 ho; };

__device__ __forceinline__ const char* karg(int i) {
  typedef const char* cptr;
  const __attribute__((address_space(4))) cptr* p = (const __attribute__((address_space(4))) cptr*)__builtin_amdgcn_kernarg_segment_ptr();
  asm volatile("" : "+s"(p));
  return p[i];
}
__device__ __forceinline__ int lane_of(const Ctx& c) {
  unsigned l = c.lane16;
  asm volatile("" : "+v"(l));
  return (int)(l >> 4);
}
__device__ __forceinline__ int g2_wg(int mt, int nt, int ks) { return ((nt * 4 + (mt & 3)) << 3) | (ks * 2 + (mt >> 2)); }
__device__ __forceinline__ int cv_wg(int cw) { return ((24 + (cw & 7)) << 3) | (cw >> 3); }
__device__ __forceinline__ void flag_store(const Ctx& c, int wg, int slot, unsigned epoch) {
  __builtin_amdgcn_raw_buffer_store_b64((u32x2){epoch + 1u, FLAG_MAGIC}, c.rs, WS_FLAG + wg * 512 + slot * 8, 0, AUX_SC1);
}
__device__ __forceinline__ void poll_issue(Ctx& c) {
  const unsigned vo = c.lane16 >> 1;
  const char* base = c.ws + WS_FLAG + c.b * 512;
  asm volatile("global_load_dwordx2 %0, %1, %2 sc1" : "=v"(c.poll) : "v"(vo), "s"(base) : "memory");
}
__device__ __forceinline__ void poll_wait(Ctx& c, int l, bool need, bool have) {
  const unsigned want = (unsigned)__builtin_amdgcn_readlane((int)c.ev, 14) + 1u;
  bool ok = have && __all(!need || (c.poll[0] == want && c.poll[1] == FLAG_MAGIC));
  if (!ok) {
    GAS unsigned long long* p = (GAS unsigned long long*)(c.ws + WS_FLAG + c.b * 512) + l;
#pragma unroll 1
    for (unsigned spin = 0; spin < SPIN_CAP; ++spin) {
      unsigned long long v = __hip_atomic_load(p, __ATOMIC_RELAXED, __HIP_MEMORY_SCOPE_AGENT);
      if (__all(!need || ((unsigned)v == want && (unsigned)(v >> 32) == FLAG_MAGIC))) break;
      __builtin_amdgcn_s_sleep(1);
    }
  }
  asm volatile("" ::: "memory");
}

template <int sg, int half> __device__ __forceinline__ void k1_issue_w(const Ctx& c) {
  const unsigned dst = (sg % K1_NSLOT) * K1_SLOT + half * 8192;
  const char* base = half ? c.w3 : c.w1;
  if constexpr (sg < 18) {
    const int ksl = mod9(sg % 9 + c.rot);
    dma16(half ? (c.voff_wg ^ 64u) : c.voff_wg, base + ((size_t)(64 * ksl + 8 * c.w) * F_ + 128 * c.nl + 64 * (sg / 9)) * 2, dst + c.w * 1024);
  } else {
    const unsigned l = lane_of(c);
    const unsigned rp = l >> 1;
    const unsigned vo = (half ? (rp ^ 4u) : rp) * (F_ * 2) + (l & 1u) * 16;
    if constexpr (sg < 20) {
      const int ksl = mod9(4 * (sg - 18) + (c.w >> 1) + c.rot);
      dma16(vo, base + ((size_t)(64 * ksl + 32 * (c.w & 1)) * F_ + 1024 + 16 * c.nl) * 2, dst + c.w * 1024);
    } else {
      const int ksl = mod9(8 + c.rot);
      dma16_16(vo, base + ((size_t)(64 * ksl + 8 * c.w) * F_ + 1024 + 16 * c.nl) * 2, dst + c.w * 256);
    }
  }
}
template <int p, int i> __device__ __forceinline__ void k1_issue_x(const Ctx& c) {
  const int ksl = mod9(p + c.rot);
  dma16((i & 1) ? c.voff_x1 : c.voff_x0, c.xb + (size_t)(8 * i) * (D_ * 2) + ksl * 128, K1_XOFF + c.w * (K1_XD * 4096) + (p % K1_XD) * 4096 + i * 1024);
}
template <int p, int j> __device__ __forceinline__ bf16x8 k1_read_x(const Ctx& c) {
  return *(LAS bf16x8*)(ldsp(c.xl0 ^ (32 * j)) + (p % K1_XD) * 4096);
}
template <int n> __device__ __forceinline__ bf16x8 k1_read_w(const Ctx& c) {
  constexpr int sg = K1S[0].seg[n], t = K1S[0].tile[n], j = K1S[0].j[n];
  if constexpr (sg < 18) {
    LAS char* p = ldsp(c.rd[t]) + (sg % K1_NSLOT) * K1_SLOT + (16 * j) * 128;
    return tr_frag(p, p + 4 * 128);
  } else {
    const int off = (sg % K1_NSLOT) * K1_SLOT + (16 * j) * 32;
    return tr_frag(ldsp(c.rc0) + off, ldsp(c.rc1) + off);
  }
}
#define GLD1(dst, voff, sb) asm volatile("global_load_dword %0, %1, %2" : "=v"(dst) : "v"(voff), "s"(sb) : "memory")
#define GLD4(dst, voff, sb) asm volatile("global_load_dwordx4 %0, %1, %2" : "=v"(dst) : "v"(voff), "s"(sb) : "memory")
template <int i> __device__ __forceinline__ void k1_prep_load(const Ctx& c, K1Prep& P) {
  if constexpr (i == 0) {
    P.tid = lane_of(c) + 64 * c.w;
    const int ptid = P.tid < 288 ? P.tid : 287;
    P.pd = 72 * (c.b & 7) + 2 * (ptid % 36);
    P.pt0 = 64 * (c.b >> 3) + 8 * (ptid / 36);
    P.o0 = (unsigned)(P.pt0 * D_ + P.pd) * 2;
    P.oneg = P.pt0 > 0 ? P.o0 - 4 * (D_ * 2) : P.o0;
    P.ostep = P.pt0 > 0 ? D_ * 2 : 0;
  }
  if constexpr (i == 0) GLD4(P.ga0, c.lane16, c.gx);
  else if constexpr (i == 1) GLD4(P.ga1, 1024 + (c.lane16 & 127), c.gx);
  else if constexpr (i == 2) GLD4(P.gw0, c.lane16, c.gw);
  else if constexpr (i == 3) GLD4(P.gw1, 1024 + (c.lane16 & 127), c.gw);
  else if constexpr (i == 4) GLD4(P.sk, (unsigned)P.pd * 8, c.sk);
  else if constexpr (i < 9) GLD1(P.kr[i - 5], P.oneg + (i - 5) * P.ostep, c.kk);
  else if constexpr (i < 17) GLD1(P.kr[i - 5], P.o0 + (i - 9) * (D_ * 2), c.kk);
  else if constexpr (i == 17) GLD1(P.vr[0], P.oneg + 3 * P.ostep, c.vv);
  else GLD1(P.vr[i - 17], P.o0 + (i - 18) * (D_ * 2), c.vv);
}
template <int ctrl> __device__ __forceinline__ float dpp_add(float v) {
  return v + __builtin_bit_cast(float, __builtin_amdgcn_update_dpp(0, __builtin_bit_cast(int, v), ctrl, 0xf, 0xf, true));
}
__device__ __forceinline__ float dot2bf(unsigned a, unsigned b, float acc) {
  return __builtin_amdgcn_fdot2_f32_bf16(__builtin_bit_cast(bf16x2, a), __builtin_bit_cast(bf16x2, b), acc, false);
}
template <int k> __device__ __forceinline__ void k1_prep_unit(const Ctx& c, K1Prep& P) {
  if constexpr (k == 0) {
    asm volatile("" : "+v"(P.kr[0]), "+v"(P.kr[1]), "+v"(P.kr[2]), "+v"(P.kr[3]), "+v"(P.kr[4]), "+v"(P.kr[5]), "+v"(P.kr[6]), "+v"(P.kr[7]),
                 "+v"(P.kr[8]), "+v"(P.kr[9]), "+v"(P.kr[10]), "+v"(P.kr[11]), "+v"(P.vr[0]), "+v"(P.vr[1]), "+v"(P.vr[2]), "+v"(P.vr[3]),
                 "+v"(P.vr[4]), "+v"(P.vr[5]), "+v"(P.vr[6]), "+v"(P.vr[7]), "+v"(P.vr[8]), "+v"(P.sk), "+v"(P.ga0), "+v"(P.ga1), "+v"(P.gw0),
                 "+v"(P.gw1)::"memory");
    float s1 = dot2bf(P.ga1[0], P.gw1[0], 0.f), s2 = dot2bf(P.ga1[1], P.gw1[1], 0.f);
    s1 = dot2bf(P.ga1[2], P.gw1[2], s1); s2 = dot2bf(P.ga1[3], P.gw1[3], s2);
    P.gs = c.lane16 < 128 ? s1 + s2 : 0.f;
  } else if constexpr (k == 1) {
    float s1 = dot2bf(P.ga0[0], P.gw0[0], P.gs), s2 = dot2bf(P.ga0[1], P.gw0[1], 0.f);
    s1 = dot2bf(P.ga0[2], P.gw0[2], s1); s2 = dot2bf(P.ga0[3], P.gw0[3], s2);
    P.gs = s1 + s2;
  } else if constexpr (k == 2) {
    P.gs = dpp_add<0x111>(P.gs); P.gs = dpp_add<0x112>(P.gs);
  } else if constexpr (k == 3) {
    P.gs = dpp_add<0x114>(P.gs); P.gs = dpp_add<0x118>(P.gs);
  } else if constexpr (k == 4) {
    int v = __builtin_bit_cast(int, P.gs);
    float a = __builtin_bit_cast(float, __builtin_amdgcn_readlane(v, 15)), b = __builtin_bit_cast(float, __builtin_amdgcn_readlane(v, 31));
    float d = __builtin_bit_cast(float, __builtin_amdgcn_readlane(v, 47)), e = __builtin_bit_cast(float, __builtin_amdgcn_readlane(v, 63));
    float z = rbf((a + b) + (d + e));
    float g = rbf(__builtin_amdgcn_rcpf(1.0f + __builtin_amdgcn_exp2f(z * -1.44269504088896f)));
    __builtin_amdgcn_raw_buffer_store_b32(__builtin_bit_cast(unsigned, g), c.rs, c.lane16 == 0 ? 0u : OOB, WS_G + (256 * c.mt + 64 * c.fq + 8 * c.nl + c.w) * 4, AUX_SC1);
  } else if constexpr (k == 5) {
    if (P.pt0 == 0) { P.kr[0] = P.kr[1] = P.kr[2] = P.kr[3] = 0u; P.vr[0] = 0u; }
    P.s[0] = (f32x2){bflo(P.sk[0]), bflo(P.sk[2])};
  } else if constexpr (k == 6) {
    P.s[1] = (f32x2){bfhi(P.sk[0]), bfhi(P.sk[2])};
    P.s[2] = (f32x2){bflo(P.sk[1]), bflo(P.sk[3])}; P.s[3] = (f32x2){bfhi(P.sk[1]), bfhi(P.sk[3])};
    P.k2[0] = unpk2(P.kr[0]); P.k2[1] = unpk2(P.kr[1]); P.k2[2] = unpk2(P.kr[2]);
  } else if constexpr (k < 34) {
    constexpr int i = (k - 7) / 3, hf = (k - 7) % 3;
    if constexpr (hf == 0) {
      P.k2[(i + 3) & 3] = unpk2(P.kr[i + 3]);
      P.a[0] = P.k2[i & 3][0] * P.s[3][0]; P.a[1] = P.k2[i & 3][1] * P.s[3][1];
      asm volatile("" : "+v"(P.a[0])); asm volatile("" : "+v"(P.a[1]));
    } else if constexpr (hf == 1) {
      P.a[0] = __builtin_fmaf(P.k2[(i + 1) & 3][0], P.s[2][0], P.a[0]); asm volatile("" : "+v"(P.a[0]));
      P.a[1] = __builtin_fmaf(P.k2[(i + 1) & 3][1], P.s[2][1], P.a[1]); asm volatile("" : "+v"(P.a[1]));
      P.a[0] = __builtin_fmaf(P.k2[(i + 2) & 3][0], P.s[1][0], P.a[0]); asm volatile("" : "+v"(P.a[0]));
      P.a[1] = __builtin_fmaf(P.k2[(i + 2) & 3][1], P.s[1][1], P.a[1]); asm volatile("" : "+v"(P.a[1]));
      P.v2 = unpk2(P.vr[i]);
    } else {
      P.a[0] = __builtin_fmaf(P.k2[(i + 3) & 3][0], P.s[0][0], P.a[0]); asm volatile("" : "+v"(P.a[0]));
      P.a[1] = __builtin_fmaf(P.k2[(i + 3) & 3][1], P.s[0][1], P.a[1]); asm volatile("" : "+v"(P.a[1]));
      float u0 = P.a[0] * P.v2[0]; asm volatile("" : "+v"(u0));
      float u1 = P.a[1] * P.v2[1]; asm volatile("" : "+v"(u1));
      P.u[i] = pk2(u0, u1);
    }
  } else {
    constexpr int ch = (k - 34) >> 1, cp = (k - 34) & 1;
    constexpr unsigned sel = ch ? 0x07060302u : 0x05040100u;
    if constexpr (k == 34) P.o0 = P.tid < 288 ? (unsigned)(P.pd * 2 * S_ + (2040 - P.pt0)) * 2 : OOB;
    u32x4 o;
#pragma unroll
    for (int e = 0; e < 4; ++e) o[e] = __builtin_amdgcn_perm(P.u[7 + cp - 2 * e - 1], P.u[7 + cp - 2 * e], sel);
    __builtin_amdgcn_raw_buffer_store_b128(o, c.rs, P.o0, WS_UR + (ch * 2 + cp) * S_ * 2, AUX_SC1);
  }
}
template <int ch, int e> __device__ __forceinline__ void k1_epi_step(const Ctx& c, const f32x16 (&acc)[4], K1Epi& E) {
  constexpr int pr = e / 3, st = e % 3, t = pr / 4, i = 2 * (pr % 4);
  if constexpr (st == 0) {
    if constexpr (pr > 0) { E.h[0] = E.pq[0] * E.r[0]; E.h[1] = E.pq[1] * E.r[1]; }
    if constexpr (pr < 16) {
      E.m[0] = acc[t][i] * -1.44269504088896f; E.m[1] = acc[t][i + 1] * -1.44269504088896f;
      E.p[0] = acc[t][i] * acc[t][i + 8]; E.p[1] = acc[t][i + 1] * acc[t][i + 9];
      asm volatile("" : "+v"(E.p[0]), "+v"(E.p[1]));
    }
  } else if constexpr (st == 1) {
    if constexpr (pr < 16) { E.m[0] = __builtin_amdgcn_exp2f(E.m[0]); E.m[1] = __builtin_amdgcn_exp2f(E.m[1]); }
    if constexpr (pr > 0) {
      E.ho[(pr - 1) % 4] = pk2(E.h[0], E.h[1]);
      if constexpr (pr % 4 == 0)
        __builtin_amdgcn_raw_buffer_store_b128(E.ho, c.rs, c.lane16, c.hb + (8 * c.nl + 4 * ch + (pr - 1) / 4) * 1024, AUX_SC1);
    }
  } else {
    if constexpr (pr < 16) {
      float d0 = 1.0f + E.m[0], d1 = 1.0f + E.m[1];
      E.r[0] = __builtin_amdgcn_rcpf(d0); E.r[1] = __builtin_amdgcn_rcpf(d1);
      E.pq[0] = E.p[0]; E.pq[1] = E.p[1];
    }
  }
}

__device__ __forceinline__ int g2_ft(const Ctx& c, int s, int i8) {
  if (s >= 8) return 64 + i8;
  const int jj = ((s & 3) + c.nl + c.ml) & 3;
  return 16 * jj + 8 * (i8 >> 2) + (i8 & 3) + (s >= 4 ? 4 : 0);
}
template <int s, int i> __device__ __forceinline__ void g2_issue_w(const Ctx& c) {
  const unsigned n16 = 64u * i + (unsigned)lane_of(c);
  const unsigned vo = ((n16 * 171u) >> 11) * (D_ * 2 - 192) + n16 * 16u;
  dma16(vo, c.w2 + (size_t)(16 * g2_ft(c, s, c.w)) * (D_ * 2), K1_XOFF + (s % G2_NSLOT) * G2_SLOT + c.w * 3072 + i * 1024);
}
template <int i> __device__ __forceinline__ u32x4 g2_load_h(const Ctx& c) {
  return __builtin_amdgcn_raw_buffer_load_b128(c.rs, c.lane16, c.hb + g2_ft(c, i / 8, i % 8) * 1024, AUX_SC1);
}
__device__ __forceinline__ unsigned cv_base(int ch) { return ch < 7 ? K1_XOFF + ch * CV_CH : (ch == 7 ? K1_SLOT : 2 * K1_SLOT); }
template <int i> __device__ __forceinline__ void cv_issue(const Ctx& c, int cw, int ch) {
  const int d = 9 * cw + ch;
  if constexpr (i < 4) dma16(c.lane16, c.ssm_k + (size_t)d * (S_ * 2) + i * 1024, cv_base(ch) + CV_K + 64 + i * 1024);
  else dma16(c.lane16, c.ws + WS_UR + (size_t)d * (4 * S_) + (i - 4) * 1024, cv_base(ch) + (i < 8 ? CV_U0 + (i - 4) * 1024 : CV_U1 + (i - 8) * 1024));
}

template <int n, int NEND, int ROLE>
__device__ __forceinline__ void k1_slot(Ctx& c, bf16x8 (&xf)[36], bf16x8 (&wf)[K1_PF + 1], f32x16 (&acc)[4], K1Epi& E, K1Prep& P, u32x4 (&hw)[G2_WIN]) {
  constexpr const K1Sched& S = K1S[ROLE];
  constexpr int sg = S.seg[n];
  if constexpr (n + K1_PF == 288) {
    const unsigned l = lane_of(c);
    unsigned mat = (l >> 4) & 1, h = l >> 5, q = (l & 15) >> 2, p = l & 3;
    unsigned base = mat * 8192 + (8 * h + q) * 32 + p * 8;
    c.rc0 = base + (mat ? 128 : 0);
    c.rc1 = base + (mat ? 0 : 128);
  }
  if constexpr (n + K1_PF < K1_NS) wf[(n + K1_PF) % (K1_PF + 1)] = k1_read_w<n + K1_PF>(c);
  if constexpr (n + 4 < 144) {
    constexpr int sn = S.seg[n + 4];
    if constexpr (S.tile[n + 4] == 0 && sn >= 1) xf[4 * sn + S.j[n + 4]] = k1_read_x<sn, S.j[n + 4]>(c);
  }
  {
    constexpr int t = S.tile[n];
    constexpr int xi = sg < 18 ? 4 * (sg % 9) + S.j[n] : n - 288;
    constexpr bool first = (sg < 18) ? (sg % 9 == 0 && S.j[n] == 0) : (n == 288);
    if constexpr (first) acc[t] = __builtin_amdgcn_mfma_f32_32x32x16_bf16(wf[n % (K1_PF + 1)], xf[xi], (f32x16){}, 0, 0, 0);
    else acc[t] = __builtin_amdgcn_mfma_f32_32x32x16_bf16(wf[n % (K1_PF + 1)], xf[xi], acc[t], 0, 0, 0);
  }
  SCHED_FENCE();
  {
    constexpr int ws0 = [] { for (int s = 0; s < K1_NSEG; ++s) if (K1S[0].wissue[s] == n) return s; return -1; }();
    constexpr int ws1 = [] { for (int s = 0; s < K1_NSEG; ++s) if (K1S[0].wissue[s] >= 0 && K1S[0].wissue[s] + 2 == n) return s; return -1; }();
    constexpr int xs = [] { for (int p = 0; p < 9; ++p) if (K1S[0].xissue[p] >= 0 && n >= K1S[0].xissue[p] && n < K1S[0].xissue[p] + 4) return p; return -1; }();
    if constexpr (ws0 >= 0) k1_issue_w<ws0, 0>(c);
    if constexpr (ws1 >= 0) k1_issue_w<ws1, 1>(c);
    if constexpr (xs >= 0) k1_issue_x<xs, n - S.xissue[xs]>(c);
  }
  if constexpr (n >= K1_PREP0 && n < K1_PREP0 + K1_NPREP / 2) {
    k1_prep_load<2 * (n - K1_PREP0)>(c, P);
    k1_prep_load<2 * (n - K1_PREP0) + 1>(c, P);
  }
  if constexpr (n >= S.prep_ready && n < S.prep_ready + K1_PREP_UNITS) k1_prep_unit<n - S.prep_ready>(c, P);
  if constexpr (n >= S.epi0[0] && n < S.epi0[0] + 50) k1_epi_step<0, n - S.epi0[0]>(c, acc, E);
  if constexpr (n >= S.epi0[1] && n < S.epi0[1] + 50) k1_epi_step<1, n - S.epi0[1]>(c, acc, E);
  if constexpr (n == EV_PUB_U) {
    if (c.w == 1) { const int l = lane_of(c); if (l < 8) flag_store(c, cv_wg(8 * (c.b & 7) + l), c.b >> 3, c.ev); }
  }
  if constexpr (n == EV_PUB_A) {
    if (c.w == 1) { const int l = lane_of(c); if (l >= 8 && l < 14) flag_store(c, g2_wg(c.mt, l - 8, c.fq), c.nl, c.ev); }
  }
  if constexpr (ROLE == 0) {
    if constexpr (n == EV_PRE0 - 2) {
      c.w2 = karg(8) + ((size_t)(1152 * c.fq) * D_ + 96 * c.nl) * 2;
    }
    if constexpr (n >= EV_PRE0 && n < EV_PRE0 + 12 * EV_PRE_STEP && (n - EV_PRE0) % EV_PRE_STEP == 0)
      g2_issue_w<(n - EV_PRE0) / EV_PRE_STEP / 3, (n - EV_PRE0) / EV_PRE_STEP % 3>(c);
    if constexpr (n == EV_G2_POLL) poll_issue(c);
    if constexpr (n == EV_G2_CHECK) {
      asm volatile("s_waitcnt vmcnt(%1)" : "+v"(c.poll) : "n"(S.ops_after(EV_G2_POLL, EV_G2_CHECK)) : "memory");
      if (c.w == 0) { const int l = lane_of(c); poll_wait(c, l, l < 8, true); }
    }
    if constexpr (n >= EV_G2_H0 && (n - EV_G2_H0) % 2 == 0 && (n - EV_G2_H0) / 2 < G2_WIN) hw[(n - EV_G2_H0) / 2] = g2_load_h<(n - EV_G2_H0) / 2>(c);
  } else {
    const int cw = (c.b & 7) * 8 + (c.b >> 3) - 24;
    if constexpr (n == EV_PRE0 - 2) c.ssm_k = karg(4);
    if constexpr (n == EV_CV_POLL) poll_issue(c);
    if constexpr (n == EV_CV_CHECK) {
      asm volatile("s_waitcnt vmcnt(%1)" : "+v"(c.poll) : "n"(S.ops_after(EV_CV_POLL, EV_CV_CHECK)) : "memory");
      if (c.w == 0) { const int l = lane_of(c); poll_wait(c, l, l < 32, true); asm volatile("buffer_inv sc1" ::: "memory"); }
    }
    if constexpr (n == EV_CV_INVW) wait_vm<S.ops_after(EV_CV_CHECK, EV_CV_INVW)>();
    if constexpr (n >= EV_PRE0 && n < EV_PRE0 + 12 * EV_PRE_STEP && (n - EV_PRE0) % EV_PRE_STEP == 0) {
      if (c.w < 7) cv_issue<(n - EV_PRE0) / EV_PRE_STEP>(c, cw, c.w);
    }
    if constexpr (n >= EV_CV_PRE7 && n < EV_CV_PRE7 + 12) {
      if (c.w == 7) cv_issue<n - EV_CV_PRE7>(c, cw, 7);
    }
  }
  SCHED_FENCE();
  if constexpr (S.bar[n]) {
    wait_vm_barrier<S.wait[n]>();
    SCHED_FENCE();
  }
  if constexpr (n + 1 < NEND) k1_slot<n + 1, NEND, ROLE>(c, xf, wf, acc, E, P, hw);
}
__device__ __forceinline__ void k1_tail(const Ctx& c, const f32x16& a) {
  u32x4 o;
#pragma unroll
  for (int i = 0; i < 4; ++i) o[i] = pk2(silu_mul(a[2 * i], a[2 * i + 8]), silu_mul(a[2 * i + 1], a[2 * i + 9]));
  __builtin_amdgcn_raw_buffer_store_b128(o, c.rs, c.lane16, c.hb + (64 + c.nl) * 1024, AUX_SC1);
}

template <int n> __device__ __forceinline__ bf16x8 g2_read_w(unsigned rd) {
  constexpr int i = n / 3, t = n % 3, s = i / 8, j = i % 8;
  LAS char* p = ldsp(rd) + (s % G2_NSLOT) * G2_SLOT + (16 * j) * 192 + t * 64;
  return tr_frag(p, p + 8 * 192);
}
constexpr int G2_TAILST = 6;
constexpr int G2_PUBBAR = 31, G2_POLL = 54, G2_CHECK = 24 * 3 - G2_PF - 1;
constexpr int g2_hslot(int i) {
  int s = 3 * (i - G2_WIN) + 2;
  if (i >= 32 && s < G2_CHECK + 1 + (i - 32)) s = G2_CHECK + 1 + (i - 32);
  return s;
}
constexpr int g2_hload_at(int n) { for (int i = G2_WIN; i < 72; ++i) if (g2_hslot(i) == n) return i; return -1; }
constexpr bool g2_bar(int n) { return n % 24 == 24 - G2_PF - 1 && n / 24 < 8 && n / 24 >= 1; }
constexpr int g2_ops_in_slot(int n) {
  int k = 0;
  for (int s = 1; s + 3 < 9; ++s) for (int i = 0; i < 3; ++i) if (n == 24 * s + 24 - G2_PF + 1 + 3 * i) ++k;
  if (g2_hload_at(n) >= 0) ++k;
  if (n == G2_TAILST) ++k;
  return k;
}
constexpr int g2_ops_after(int a, int b) { int k = 0; for (int n = a + 1; n <= b; ++n) k += g2_ops_in_slot(n); return k; }
static_assert([] { for (int i = G2_WIN; i + 1 < 72; ++i) if (g2_hslot(i) >= g2_hslot(i + 1) || g2_hslot(i) + 12 > 3 * i) return false; return true; }(), "h load slots");
template <int n>
__device__ __forceinline__ void g2_slot(Ctx& c, unsigned rd, u32x4 (&hw)[G2_WIN], bf16x8 (&wf)[G2_PF + 1], f32x16 (&acc)[3], const f32x16& tailacc, u32x4& to) {
  constexpr int i = n / 3, t = n % 3, s = n / 24;
  if constexpr (n + G2_PF < G2_NS) wf[(n + G2_PF) % (G2_PF + 1)] = g2_read_w<n + G2_PF>(rd);
  if constexpr (i == 0) acc[t] = __builtin_amdgcn_mfma_f32_32x32x16_bf16(wf[n % (G2_PF + 1)], __builtin_bit_cast(bf16x8, hw[i % G2_WIN]), (f32x16){}, 0, 0, 0);
  else acc[t] = __builtin_amdgcn_mfma_f32_32x32x16_bf16(wf[n % (G2_PF + 1)], __builtin_bit_cast(bf16x8, hw[i % G2_WIN]), acc[t], 0, 0, 0);
  SCHED_FENCE();
  if constexpr (n >= 2 && n < 6) to[n - 2] = pk2(silu_mul(tailacc[2 * n - 4], tailacc[2 * n + 4]), silu_mul(tailacc[2 * n - 3], tailacc[2 * n + 5]));
  if constexpr (n == G2_TAILST) __builtin_amdgcn_raw_buffer_store_b128(to, c.rs, c.lane16, c.hb + (64 + c.nl) * 1024, AUX_SC1);
  {
    constexpr int m = n - (24 - G2_PF + 1);
    if constexpr (m >= 24 && m % 24 < 9 && m % 3 == 0 && m / 24 + 3 < 9) g2_issue_w<m / 24 + 3, (m % 24) / 3>(c);
  }
  if constexpr (g2_hload_at(n) >= 0) hw[g2_hload_at(n) % G2_WIN] = g2_load_h<g2_hload_at(n)>(c);
  if constexpr (n == G2_POLL) poll_issue(c);
  if constexpr (n == G2_CHECK) {
    asm volatile("s_waitcnt vmcnt(%1)" : "+v"(c.poll) : "n"(g2_ops_after(G2_POLL, G2_CHECK)) : "memory");
    if (c.w == 0) { const int l = lane_of(c); poll_wait(c, l, l >= 8 && l < 16, true); }
  }
  SCHED_FENCE();
  if constexpr (g2_bar(n)) {
    constexpr int last = s + 1 < 4 ? -1 : 24 * (s - 2) + 24 - G2_PF + 1 + 6;
    wait_vm_barrier<last < 0 ? 63 : cmin(63, g2_ops_after(last, n))>();
    SCHED_FENCE();
  }
  if constexpr (n == G2_PUBBAR) {
    wait_vm_barrier<g2_ops_after(G2_TAILST, n)>();
    if (c.w == 1) { const int l = lane_of(c); if (l >= 8 && l < 14) flag_store(c, g2_wg(c.mt, l - 8, c.fq), 8 + c.nl, c.ev); }
    SCHED_FENCE();
  }
  if constexpr (n + 1 < G2_NS) g2_slot<n + 1>(c, rd, hw, wf, acc, tailacc, to);
}

__device__ __forceinline__ void g2_phase(Ctx& c, u32x4 (&hw)[G2_WIN], const f32x16& tailacc) {
  const int lane = lane_of(c);
  const int w = c.w, ks = c.fq, nt = c.nl, mt = c.mt;
  unsigned rd;
  {
    int gq = lane >> 4, chf = gq & 1, h = gq >> 1, q = (lane & 15) >> 2, p = lane & 3;
    rd = K1_XOFF + (4 * h + q) * 192 + chf * 32 + p * 8;
  }
  bf16x8 wf[G2_PF + 1];
  f32x16 acc[3];
  u32x4 to;
  wf[0] = g2_read_w<0>(rd); wf[1] = g2_read_w<1>(rd); wf[2] = g2_read_w<2>(rd); wf[3] = g2_read_w<3>(rd);
  SCHED_FENCE();
  g2_slot<0>(c, rd, hw, wf, acc, tailacc, to);
  const int kd = w >> 1, half = w & 1;
  c.q = karg(0);
  c.out = (char*)karg(11);
  if (kd != ks) {
    const unsigned base = WS_PART + g2_wg(mt, nt, kd) * 49152 + (ks * 2 + half) * 6144;
#pragma unroll
    for (int t = 0; t < 3; ++t)
#pragma unroll
      for (int qp = 0; qp < 2; ++qp) {
        u32x4 o = {pk2(acc[t][8 * qp], acc[t][8 * qp + 1]), pk2(acc[t][8 * qp + 2], acc[t][8 * qp + 3]), pk2(acc[t][8 * qp + 4], acc[t][8 * qp + 5]),
                   pk2(acc[t][8 * qp + 6], acc[t][8 * qp + 7])};
        __builtin_amdgcn_raw_buffer_store_b128(o, c.rs, c.lane16, base + (t * 2 + qp) * 1024, AUX_SC1);
      }
    wait_vm<0>();
    if (lane == 16 + kd) flag_store(c, g2_wg(mt, nt, kd), 32 + 2 * ks + half, c.ev);
  } else {
#pragma unroll
    for (int t = 0; t < 3; ++t)
#pragma unroll
      for (int qd = 0; qd < 4; ++qd)
        *(LAS f32x4*)(ldsp(c.lane16) + (half * 12 + t * 4 + qd) * 1024) = (f32x4){acc[t][4 * qd], acc[t][4 * qd + 1], acc[t][4 * qd + 2], acc[t][4 * qd + 3]};
    if (half == 0) {
      const int c0 = (96 * nt) / 9, c1 = (96 * nt + 95) / 9;
      poll_wait(c, lane, lane >= 16 && lane <= 16 + c1 - c0, false);
    }
  }
  asm volatile("s_waitcnt lgkmcnt(0)\n\ts_barrier" ::: "memory");
  STAMP(33);
  const int uh = w / 3, ut = w % 3;
  const int qp = lane >> 5, cc = lane & 31;
  const int tok = 256 * mt + 64 * ks + 32 * uh + cc, d0 = 96 * nt + 32 * ut;
  float ga[16], og = 0.f;
  if (w < 6) {
    u32x4 zz[2], qq[2];
#pragma unroll
    for (int i = 0; i < 2; ++i)
      zz[i] = __builtin_amdgcn_raw_buffer_load_b128(c.rs, ((16 * i + (lane >> 2)) * S_) * 2 + (lane & 3) * 16,
                                                    WS_Z + (d0 * S_ + 256 * mt + 64 * ks + 32 * uh) * 2, AUX_SC1);
    const float g = __builtin_bit_cast(float, __builtin_amdgcn_raw_buffer_load_b32(c.rs, tok * 4, WS_G, AUX_SC1));
#pragma unroll
    for (int i = 0; i < 2; ++i) qq[i] = *(const u32x4*)(c.q + ((size_t)tok * D_ + d0 + 16 * qp + 8 * i) * 2);
    LAS char* zt = ldsp(24576 + w * 2560);
#pragma unroll
    for (int i = 0; i < 2; ++i) *(LAS u32x4*)(zt + (16 * i + (lane >> 2)) * 80 + (lane & 3) * 16) = zz[i];
    og = rbf(1.0f - g);
#pragma unroll
    for (int j = 0; j < 16; ++j) {
      float z = bf2f(*(LAS u16*)(zt + (16 * qp + j) * 80 + cc * 2));
      unsigned qw = qq[j >> 3][(j >> 1) & 3];
      float a = rbf(((j & 1) ? bfhi(qw) : bflo(qw)) * z);
      ga[j] = rbf(g * a);
    }
  } else if (w == 6) {
    poll_wait(c, lane, lane >= 32 && lane < 40 && ((lane - 32) >> 1) != ks, false);
    if (lane == 14) __builtin_amdgcn_raw_buffer_store_b32(c.ev + 1u, c.rs, WS_EPOCH + c.b * 4, 0, AUX_SC1);
  }
  asm volatile("s_barrier" ::: "memory");
  STAMP(34);
  if (w < 6) {
    u32x4 pp[3][2];
#pragma unroll
    for (int s = 0; s < 3; ++s) {
      const int src = s + (s >= ks ? 1 : 0);
#pragma unroll
      for (int h = 0; h < 2; ++h)
        pp[s][h] = __builtin_amdgcn_raw_buffer_load_b128(c.rs, qp * 1024 + (cc + 32 * h) * 16, WS_PART + c.b * 49152 + (src * 2 + uh) * 6144 + ut * 2048, AUX_SC1);
    }
    f32x4 own[2][2];
#pragma unroll
    for (int o = 0; o < 2; ++o)
#pragma unroll
      for (int h = 0; h < 2; ++h) own[o][h] = *(LAS f32x4*)(ldsp((cc + 32 * h) * 16) + (uh * 12 + ut * 4 + 2 * qp + o) * 1024);
#pragma unroll
    for (int o = 0; o < 2; ++o) {
      float res[8];
#pragma unroll
      for (int h = 0; h < 2; ++h)
#pragma unroll
        for (int k = 0; k < 4; ++k) {
          float bsum = own[o][h][k];
#pragma unroll
          for (int s = 0; s < 3; ++s) { unsigned v = pp[s][h][2 * o + (k >> 1)]; bsum += (k & 1) ? bfhi(v) : bflo(v); }
          res[4 * h + k] = ga[8 * o + 4 * h + k] + rbf(og * rbf(bsum));
        }
      u32x4 ov = {pk2(res[0], res[1]), pk2(res[2], res[3]), pk2(res[4], res[5]), pk2(res[6], res[7])};
      *(u32x4*)(c.out + ((size_t)tok * D_ + d0 + 16 * qp + 8 * o) * 2) = ov;
    }
  }
  STAMP(35);
}

typedef unsigned u32x2a4 __attribute__((ext_vector_type(2), aligned(4)));
__device__ __forceinline__ bf16x8 cv_read_a(LAS char* p) {
  u32x2 lo = *(LAS u32x2a4*)p;
  u32x2 hi = *(LAS u32x2a4*)(p + 8);
  u32x4 v = {lo[0], lo[1], hi[0], hi[1]};
  return __builtin_bit_cast(bf16x8, v);
}
template <bool PREF, bool FIRST>
__device__ __forceinline__ void cv_block(f32x4 (&acc)[8], const bf16x8 (&af)[8], const bf16x8 (&bf)[8], bf16x8 (&naf)[8], bf16x8 (&nbf)[8],
                                         const unsigned (&ap)[4], unsigned bp, int roff) {
  int k = 0;
#pragma unroll
  for (int p = 0; p < 8; ++p)
#pragma unroll
    for (int dl = 0; dl + p < 8; ++dl) {
      if (PREF && (k & 1) == 0 && k < 32) {
        const int f = k >> 2;
        if (k & 2) naf[f] = cv_read_a(ldsp(ap[f >> 1]) + 512 * (f & 1) + roff);
        else nbf[f] = *(LAS bf16x8*)(ldsp(bp) + 64 * (56 - 8 * f) + roff);
      }
      if (FIRST && p == 0) acc[dl] = __builtin_amdgcn_mfma_f32_16x16x32_bf16(af[p], bf[dl], (f32x4){}, 0, 0, 0);
      else acc[p + dl] = __builtin_amdgcn_mfma_f32_16x16x32_bf16(af[p], bf[dl], acc[p + dl], 0, 0, 0);
      asm volatile("" : "+v"(acc[p + dl]));
      if (k & 1) SCHED_FENCE();
      ++k;
    }
}
__device__ __forceinline__ u32x2 cv_z(const f32x4& y, LAS char* cl, int g, int lane, float Dv) {
  const int q = lane >> 4, pi = (lane >> 3) & 1, ii = lane & 7;
  const int T = 256 * g + 32 * ii + 8 * q;
  u32x4 uu = *(LAS u32x4*)(cl + CV_U1 + 2 * (2040 - T));
  float z[4];
#pragma unroll
  for (int k = 0; k < 4; ++k) {
    float u = pi ? bflo(uu[3 - k]) : bfhi(uu[3 - k]);
    z[k] = rbf(y[k]) + rbf(u * Dv);
  }
  unsigned p01 = pk2(z[0], z[1]), p23 = pk2(z[2], z[3]);
  unsigned snd = pi ? p01 : p23;
  unsigned rcv = __builtin_amdgcn_update_dpp(0u, snd, 0x128, 0xf, 0xf, false);
  unsigned E = pi ? rcv : p01, O = pi ? p23 : rcv;
  return (u32x2){(E & 0xffffu) | (O << 16), (E >> 16) | (O & 0xffff0000u)};
}
__device__ __forceinline__ void cv_phase(Ctx& c, const f32x16& tailacc) {
  const int lane = lane_of(c), w = c.w;
  const int cw = (c.b & 7) * 8 + (c.b >> 3) - 24;
  const int d = 9 * cw + w, d8 = 9 * cw + 8;
  const unsigned cb = cv_base(w), cb8 = cv_base(8);
  LAS char* cl = ldsp(cb);
  LAS char* cl8 = ldsp(cb8);
  if (w == 7) wait_vm<1>();
  dma16(c.lane16, c.ws + WS_UR + (size_t)d8 * (4 * S_) + w * 1024, cb8 + (w < 4 ? CV_U0 + w * 1024 : CV_U1 + (w - 4) * 1024));
  if (w < 4) dma16(c.lane16, c.ssm_k + (size_t)d8 * (S_ * 2) + w * 1024, cb8 + CV_K + 64 + w * 1024);
  if (lane < 60) {
    int o = lane < 4 ? CV_K + lane * 16 : (lane < 32 ? CV_U0 + 4096 + (lane - 4) * 16 : CV_U1 + 4096 + (lane - 32) * 16);
    *(LAS u32x4*)(cl + o) = (u32x4){0u, 0u, 0u, 0u};
    if (w == 7) *(LAS u32x4*)(cl8 + o) = (u32x4){0u, 0u, 0u, 0u};
  }
  const int rho = lane & 15, q = lane >> 4, pi = (lane >> 3) & 1, ii = lane & 7;
  const unsigned aoff = CV_K + 4 + 4 * rho + 16 * q;
  const unsigned boff = CV_U0 + pi * 4640 + 2 * (2016 - 32 * ii + 8 * q) - 64 * 56;
  unsigned ap[4], bp = cb + boff;
#pragma unroll
  for (int i = 0; i < 4; ++i) { ap[i] = cb + aoff + 1024 * i; asm volatile("" : "+v"(ap[i])); }
  f32x4 acc[8], acc8[8];
  bf16x8 a0[8], b0[8], a1[8], b1[8];
#pragma unroll
  for (int f = 0; f < 8; ++f) {
    b0[f] = *(LAS bf16x8*)(ldsp(bp) + 64 * (56 - 8 * f));
    a0[f] = cv_read_a(ldsp(ap[f >> 1]) + 512 * (f & 1));
  }
  SCHED_FENCE();
  k1_tail(c, tailacc);
  c.ssm_D = karg(5);
  const int ntz = (lane < 32) ? (9 * cw) / 96 : (9 * cw + 8) / 96;
  const int zwg = g2_wg((lane >> 2) & 7, ntz, lane & 3);
  const unsigned zev = *(const unsigned*)(c.ws + WS_EPOCH + zwg * 4);
  const unsigned dd = *(const u16*)(c.ssm_D + d * 2), dd8 = *(const u16*)(c.ssm_D + d8 * 2);
  SCHED_FENCE();
  STAMP(24);
  cv_block<true, true>(acc, a0, b0, a1, b1, ap, bp, 64);
  cv_block<true, false>(acc, a1, b1, a0, b0, ap, bp, 128);
  full_barrier();
  if (w == 1) {
    if (lane >= 8 && lane < 14) flag_store(c, g2_wg(c.mt, lane - 8, c.fq), 8 + c.nl, c.ev);
    if (lane == 14) __builtin_amdgcn_raw_buffer_store_b32(c.ev + 1u, c.rs, WS_EPOCH + c.b * 4, 0, AUX_SC1);
  }
  SCHED_FENCE();
  cv_block<true, false>(acc, a0, b0, a1, b1, ap, bp, 192);
  cv_block<true, false>(acc, a1, b1, a0, b0, ap, bp, 256);
  cv_block<true, false>(acc, a0, b0, a1, b1, ap, bp, 320);
  cv_block<true, false>(acc, a1, b1, a0, b0, ap, bp, 384);
  cv_block<true, false>(acc, a0, b0, a1, b1, ap, bp, 448);
  {
    const unsigned dlt = cb8 - cb + 64 * w;
#pragma unroll
    for (int i = 0; i < 4; ++i) ap[i] += dlt;
    bp += dlt;
  }
  cv_block<true, false>(acc, a1, b1, a0, b0, ap, bp, 0);
  STAMP(25);
  cv_block<false, true>(acc8, a0, b0, a1, b1, ap, bp, 0);
  const float Dv = bf2f((u16)dd), Dv8 = bf2f((u16)dd8);
  const int zo = (32 * ii + 8 * q + 4 * pi) * 2;
#pragma unroll
  for (int g = 0; g < 8; ++g) *(LAS u32x2*)(cl + 512 * g + zo) = cv_z(acc[g], cl, g, lane, Dv);
#pragma unroll
  for (int i = 0; i < 4; ++i)
    __builtin_amdgcn_raw_buffer_store_b128(*(LAS u32x4*)(cl + i * 1024 + c.lane16), c.rs, c.lane16, WS_Z + d * (S_ * 2) + i * 1024, AUX_SC1);
  wait_lgkm0();
#pragma unroll
  for (int g = 0; g < 8; ++g) *(LAS f32x4*)(cl + g * 1024 + c.lane16) = acc8[g];
  asm volatile("s_waitcnt lgkmcnt(0)\n\ts_barrier" ::: "memory");
  STAMP(26);
  f32x4 y = {};
#pragma unroll
  for (int ww = 0; ww < 8; ++ww) y += *(LAS f32x4*)(ldsp(cv_base(ww)) + w * 1024 + c.lane16);
  __builtin_amdgcn_raw_buffer_store_b64(cv_z(y, cl8, w, lane, Dv8), c.rs, zo, WS_Z + d8 * (S_ * 2) + 512 * w, AUX_SC1);
  full_barrier();
  if (w == 0 && (lane < 32 || (9 * cw) / 96 != (9 * cw + 8) / 96)) flag_store(c, zwg, 16 + cw - (96 * ntz) / 9, zev);
  STAMP(35);
}

template <int ROLE>
__device__ __forceinline__ void fused_body(const int w) {
  const int b = blockIdx.x, xcd = b & 7, r = b >> 3;
  const int ml = r & 3, nl = r >> 2, fq = xcd >> 1;
  const int mt = 4 * (xcd & 1) + ml;
  const int lane = __builtin_amdgcn_mbcnt_hi(~0u, __builtin_amdgcn_mbcnt_lo(~0u, 0u));
  Ctx c;
  const char* x = karg(0);
  char* ws = (char*)karg(10);
  RSTAMP(37);
  STAMP(0);
  c.w = w; c.nl = nl; c.ml = ml; c.b = b; c.ws = ws; c.mt = mt; c.fq = fq;
  c.rs = __builtin_amdgcn_make_buffer_rsrc(ws, 0, WS_END, 0x00020000);
  c.rot = (nl + 2 * ml) % 9;
  c.lane16 = lane * 16;
  c.w1 = karg(6) + (size_t)(1152 * fq) * 2;
  c.w3 = karg(7) + (size_t)(1152 * fq) * 2;
  c.xb = (const char*)x + (size_t)(256 * mt + 32 * w) * (D_ * 2);
  c.hb = WS_H + ((8 * mt + w) * 288 + 72 * fq) * 1024;
  c.kk = karg(1); c.vv = karg(2); c.sk = karg(3); c.gw = karg(9);
  c.gx = (const char*)x + (size_t)(256 * mt + 64 * fq + 8 * nl + w) * (D_ * 2);
  {
    int row = lane >> 3, pos = lane & 7;
    int sw = 2 * ((row >> 1) & 1);
    c.voff_wg = row * (F_ * 2) + ((pos ^ sw) * 16);
    int sx = lane >> 4;
    c.voff_x0 = row * (D_ * 2) + ((pos ^ sx) * 16);
    c.voff_x1 = row * (D_ * 2) + ((pos ^ (sx + 4)) * 16);
  }
  k1_issue_w<0, 0>(c); k1_issue_w<0, 1>(c);
  k1_issue_x<0, 0>(c); k1_issue_x<0, 1>(c); k1_issue_x<0, 2>(c); k1_issue_x<0, 3>(c);
  {
    int ewg = lane < 8 ? cv_wg(8 * xcd + lane) : (lane < 14 ? g2_wg(mt, lane - 8, fq) : ((lane & 60) == 16 ? g2_wg(mt, nl < 6 ? nl : 0, lane & 3) : b));
    GLD1(c.ev, (unsigned)ewg * 4, ws + WS_EPOCH);
  }
  k1_issue_w<1, 0>(c); k1_issue_w<1, 1>(c);
  {
    int gq = lane >> 4, mat = gq & 1, h = gq >> 1, q = (lane & 15) >> 2, p = lane & 3;
    int X = 2 * (q >> 1) + 4 * mat;
#pragma unroll
    for (int t = 0; t < 4; ++t) c.rd[t] = mat * 8192 + (8 * h + q) * 128 + (((2 * t) ^ X) + (p >> 1)) * 16 + (p & 1) * 8;
    int cc = lane & 31, hh = lane >> 5;
    c.xl0 = K1_XOFF + w * (K1_XD * 4096) + cc * 128 + ((hh ^ ((cc >> 1) & 7)) * 16);
  }
  bf16x8 xf[36];
  bf16x8 wf[K1_PF + 1];
  f32x16 acc[4];
  u32x4 hw[G2_WIN];
  asm volatile("s_waitcnt vmcnt(2)\n\ts_barrier" : "+v"(c.ev)::"memory");
  STAMP(1);
  k1_issue_x<1, 0>(c); k1_issue_x<1, 1>(c); k1_issue_x<1, 2>(c); k1_issue_x<1, 3>(c);
  xf[0] = k1_read_x<0, 0>(c); wf[0] = k1_read_w<0>(c);
  xf[1] = k1_read_x<0, 1>(c); wf[1] = k1_read_w<1>(c);
  xf[2] = k1_read_x<0, 2>(c); wf[2] = k1_read_w<2>(c);
  xf[3] = k1_read_x<0, 3>(c); wf[3] = k1_read_w<3>(c);
  static_assert(K1_PF == 4, "prologue reads");
  SCHED_FENCE();
  STAMP(2);
  K1Epi E;
  K1Prep P;
  k1_slot<0, K1_NS, ROLE>(c, xf, wf, acc, E, P, hw);
  STAMP(23);
  if constexpr (ROLE == 1) {
    cv_phase(c, acc[0]);
  } else {
    g2_phase(c, hw, acc[0]);
  }
}

__global__ __launch_bounds__(512, 2) void fused_kernel(const void* x, const void* kk, const void* vv, const void* shift_k, const void* ssm_k,
                                                       const void* ssm_D, const void* w1, const void* w3, const void* w2, const void* gate_w, void* ws,
                                                       void* out) {
  const int w = __builtin_amdgcn_readfirstlane(threadIdx.x >> 6);
  if (blockIdx.x >= 192) fused_body<1>(w);
  else fused_body<0>(w);
}

extern "C" void kernel_launch(void* const* d_in, const int* in_sizes, int n_in, void* d_out, int out_size, void* d_ws, size_t ws_size,
                              hipStream_t stream) {
  (void)hipFuncSetAttribute((const void*)fused_kernel, hipFuncAttributeMaxDynamicSharedMemorySize, LDS_BYTES);
  fused_kernel<<<256, 512, LDS_BYTES, stream>>>(d_in[0], d_in[1], d_in[2], d_in[3], d_in[4], d_in[5], d_in[7], d_in[8], d_in[9], d_in[10], d_ws, d_out);
}
```

```cpp
#include <hip/hip_runtime.h>
#include <hip/hip_bf16.h>

#define LAS __attribute__((address_space(3)))
#define GAS __attribute__((address_space(1)))
typedef unsigned short u16;
typedef __bf16 bf16x2 __attribute__((ext_vector_type(2)));
typedef __bf16 bf16x4 __attribute__((ext_vector_type(4)));
typedef __bf16 bf16x8 __attribute__((ext_vector_type(8)));
typedef float f32x2 __attribute__((ext_vector_type(2)));
typedef float f32x4 __attribute__((ext_vector_type(4)));
typedef float f32x16 __attribute__((ext_vector_type(16)));
typedef unsigned u32x2 __attribute__((ext_vector_type(2)));
typedef unsigned u32x4 __attribute__((ext_vector_type(4)));

constexpr int S_ = 2048, D_ = 576, F_ = 4608;
constexpr unsigned WS_H = 0;
constexpr unsigned WS_PART = WS_H + 64 * 288 * 1024;
constexpr unsigned WS_UR = WS_PART + 256 * 49152;
constexpr unsigned WS_Z = WS_UR + D_ * 2 * S_ * 2;
constexpr unsigned WS_G = WS_Z + D_ * S_ * 2;
constexpr unsigned WS_FLAG = WS_G + S_ * 4;
constexpr unsigned WS_EPOCH = WS_FLAG + 256 * 512;
constexpr unsigned WS_END = WS_EPOCH + 1024;
constexpr unsigned FLAG_MAGIC = 0x7c3a91e5u;
constexpr int AUX_SC1 = 16;
constexpr unsigned OOB = 0x7ff00000u;
constexpr unsigned SPIN_CAP = 1u << 16;

#define STAMP(i)
#define RSTAMP(i)
#define STDUMP()

__device__ __forceinline__ float bf2f(u16 v) { return __builtin_bit_cast(float, (unsigned)v << 16); }
__device__ __forceinline__ float bflo(unsigned v) { return __builtin_bit_cast(float, v << 16); }
__device__ __forceinline__ float bfhi(unsigned v) { return __builtin_bit_cast(float, v & 0xffff0000u); }
__device__ __forceinline__ float rbf(float x) { return (float)(__bf16)x; }
__device__ __forceinline__ unsigned pk2(float lo, float hi) {
  f32x2 v = {lo, hi};
  return __builtin_bit_cast(unsigned, __builtin_convertvector(v, bf16x2));
}
__device__ __forceinline__ f32x2 unpk2(unsigned v) { return (f32x2){bflo(v), bfhi(v)}; }
#define SCHED_FENCE() __builtin_amdgcn_sched_barrier(0)

__device__ __forceinline__ void dma16(unsigned voff, const void* sbase, unsigned lds_dst) {
  unsigned keep;
  asm volatile("s_mov_b32 %0, m0\n\ts_mov_b32 m0, %3\n\ts_nop 0\n\tglobal_load_lds_dwordx4 %1, %2\n\ts_mov_b32 m0, %0"
               : "=&s"(keep) : "v"(voff), "s"(sbase), "s"(lds_dst) : "memory");
}
__device__ __forceinline__ void dma16_16(unsigned voff, const void* sbase, unsigned lds_dst) {
  unsigned keep;
  asm volatile("s_mov_b32 %0, m0\n\ts_mov_b32 m0, %3\n\ts_mov_b64 exec, 0xffff\n\tglobal_load_lds_dwordx4 %1, %2\n\ts_mov_b64 exec, -1\n\ts_mov_b32 m0, %0"
               : "=&s"(keep) : "v"(voff), "s"(sbase), "s"(lds_dst) : "memory");
}
template <int N> __device__ __forceinline__ void wait_vm_barrier() {
  asm volatile("s_waitcnt vmcnt(%0)\n\ts_barrier" ::"n"(N) : "memory");
}
template <int N> __device__ __forceinline__ void wait_vm() { asm volatile("s_waitcnt vmcnt(%0)" ::"n"(N) : "memory"); }
__device__ __forceinline__ void wait_lgkm0() { asm volatile("s_waitcnt lgkmcnt(0)" ::: "memory"); }
__device__ __forceinline__ void full_barrier() { asm volatile("s_waitcnt vmcnt(0) lgkmcnt(0)\n\ts_barrier" ::: "memory"); }
__device__ __forceinline__ LAS char* ldsp(unsigned a) { return (LAS char*)(size_t)a; }

__device__ __forceinline__ bf16x8 tr_frag(LAS char* p0, LAS char* p1) {
  bf16x4 a = __builtin_amdgcn_ds_read_tr16_b64_v4bf16((LAS bf16x4*)p0);
  bf16x4 b = __builtin_amdgcn_ds_read_tr16_b64_v4bf16((LAS bf16x4*)p1);
  return __builtin_shufflevector(a, b, 0, 1, 2, 3, 4, 5, 6, 7);
}
__device__ __forceinline__ float silu_mul(float g, float u) {
  float e = __builtin_amdgcn_exp2f(g * -1.44269504088896f);
  return g * u * __builtin_amdgcn_rcpf(1.0f + e);
}
__device__ __forceinline__ int mod9(int v) { return v >= 9 ? v - 9 : v; }
constexpr int cmin(int a, int b) { return a < b ? a : b; }

constexpr int K1_SLOT = 16384, K1_NSLOT = 4, K1_XOFF = K1_SLOT * K1_NSLOT, K1_XD = 3;
constexpr int LDS_BYTES = K1_XOFF + 8 * K1_XD * 4096;
constexpr int K1_PF = 4, K1_NS = 324, K1_NSEG = 21;
constexpr int K1_NPREP = 26, K1_PREP0 = 18;
constexpr int K1_PREP_UNITS = 5 + 2 + 27 + 3 + 4, K1_PREP_READY = 60;
constexpr int G2_SLOT = 24576, G2_NSLOT = 4, G2_WIN = 16, G2_PF = 4, G2_NS = 216;
constexpr int CV_K = 0, CV_U0 = 4160, CV_U1 = CV_U0 + 4640, CV_CH = 13440;
constexpr int EV_PUB_U = 140;
constexpr int EV_PUB_A = 204;
constexpr int EV_PRE0 = 190, EV_PRE_STEP = 5;
constexpr int EV_CV_POLL = 172, EV_CV_CHECK = 188, EV_CV_INVW = 219;
constexpr int EV_CVPRE0 = 200;
constexpr int EV_CV_PRE7 = 300;
constexpr int EV_G2_POLL = 262, EV_G2_CHECK = 283, EV_G2_H0 = 289;

struct K1Sched {
  int seg[K1_NS], tile[K1_NS], j[K1_NS];
  int first_use[K1_NSEG], last_use[K1_NSEG];
  int wissue[K1_NSEG];
  int xissue[9];
  bool bar[K1_NS];
  int ops[K1_NS];
  int wait[K1_NS];
  int done[K1_NS];
  int epi0[2];
  int prep_ready;
  constexpr int ops_after(int a, int b) const { int k = 0; for (int q = a + 1; q <= b; ++q) k += ops[q]; return k; }
};
constexpr bool k1_dbl_first(int s) { return s == 7 || s == 9 || s == 16; }
constexpr K1Sched k1_make(int role) {
  K1Sched S{};
  int n = 0;
  for (int s = 0; s < 18;) {
    if (k1_dbl_first(s)) {
      for (int t = 0; t < 4; ++t) for (int h = 0; h < 2; ++h) for (int j = 0; j < 4; ++j) { S.seg[n] = s + h; S.tile[n] = t; S.j[n] = j; ++n; }
      s += 2;
    } else {
      for (int t = 0; t < 4; ++t) for (int j = 0; j < 4; ++j) { S.seg[n] = s; S.tile[n] = t; S.j[n] = j; ++n; }
      s += 1;
    }
  }
  for (int i = 0; i < 36; ++i) { S.seg[n] = 18 + i / 16; S.tile[n] = 0; S.j[n] = i % 16; ++n; }
  for (int s = 0; s < K1_NSEG; ++s) { S.first_use[s] = 100000; S.last_use[s] = -1; }
  for (int i = 0; i < K1_NS; ++i) { int s = S.seg[i]; if (i < S.first_use[s]) S.first_use[s] = i; if (i > S.last_use[s]) S.last_use[s] = i; }
  for (int s = 1; s < K1_NSEG; ++s) S.bar[S.first_use[s] - K1_PF - 1] = true;
  S.bar[S.last_use[10]] = true;
  S.wissue[0] = S.wissue[1] = -1;
  S.wissue[2] = 4; S.wissue[3] = 12;
  int busy = 0;
  for (int s = 4; s < K1_NSEG; ++s) {
    int q = S.last_use[s - K1_NSLOT];
    while (!S.bar[q]) ++q;
    int is = q + 1;
    if (is < busy) is = busy;
    S.wissue[s] = is;
    busy = is + 4;
  }
  S.xissue[0] = S.xissue[1] = -1;
  S.xissue[2] = 0;
  for (int p = 3; p < 9; ++p) S.xissue[p] = S.first_use[p - 3] + (p == 3 ? 8 : 4);
  S.epi0[0] = S.first_use[7] + 9;
  S.epi0[1] = S.first_use[16] + 9;
  for (int s = 0; s < K1_NSEG; ++s) if (S.wissue[s] >= 0) { S.ops[S.wissue[s]]++; S.ops[S.wissue[s] + 2]++; }
  for (int p = 0; p < 9; ++p) if (S.xissue[p] >= 0) for (int i = 0; i < 4; ++i) S.ops[S.xissue[p] + i]++;
  for (int i = 0; i < K1_NPREP; ++i) S.ops[K1_PREP0 + i / 2]++;
  for (int c = 0; c < 2; ++c) for (int t = 0; t < 4; ++t) S.ops[S.epi0[c] + 12 * t + 13]++;
  S.ops[K1_PREP_READY + 4]++;
  for (int i = 0; i < 4; ++i) S.ops[K1_PREP_READY + K1_PREP_UNITS - 4 + i]++;
  if (role == 0) {
    for (int i = 0; i < 12; ++i) S.ops[EV_PRE0 + EV_PRE_STEP * i]++;
    for (int i = 0; i < G2_WIN; ++i) S.ops[EV_G2_H0 + 2 * i]++;
  }
  if (role == 1) for (int i = 0; i < 12; ++i) S.ops[EV_CVPRE0 + EV_PRE_STEP * i]++;
  if (role == 2) for (int i = 0; i < 12; ++i) S.ops[EV_CV_PRE7 + i]++;
  S.prep_ready = -1;
  for (int i = 0; i < K1_NS; ++i) {
    S.wait[i] = 63;
    S.done[i] = -2;
    if (!S.bar[i]) continue;
    int youngest = -2;
    for (int s = 1; s < K1_NSEG; ++s)
      if (S.first_use[s] - K1_PF - 1 == i) {
        int y = S.wissue[s] < 0 ? -1 : S.wissue[s] + 2;
        if (s < 9) { int yx = S.xissue[s] < 0 ? -1 : S.xissue[s] + 3; if (yx > y) y = yx; }
        if (y > youngest) youngest = y;
      }
    if (youngest == -2) continue;
    S.wait[i] = cmin(63, S.ops_after(youngest, i));
    S.done[i] = youngest;
    if (S.prep_ready < 0 && youngest >= K1_PREP0 + K1_NPREP / 2 - 1) S.prep_ready = i + 1;
  }
  return S;
}
constexpr K1Sched K1S[3] = {k1_make(0), k1_make(1), k1_make(2)};
static_assert(K1S[0].prep_ready == K1_PREP_READY && K1S[0].prep_ready + K1_PREP_UNITS - 1 <= K1S[0].done[EV_PUB_U - 1] && K1S[0].bar[EV_PUB_U - 1], "u stores complete before publish");
static_assert(K1S[0].epi0[0] + 49 <= K1S[0].done[EV_PUB_A - 1] && K1S[0].bar[EV_PUB_A - 1], "chunk A stores complete before publish");
static_assert(K1S[0].bar[EV_G2_CHECK] && K1S[0].bar[EV_CV_INVW] && K1S[0].bar[EV_CV_PRE7 - 1] && K1S[0].last_use[17] < EV_CV_PRE7, "events");
static_assert(K1S[0].epi0[1] + 50 <= K1_NS && EV_G2_H0 + 2 * G2_WIN <= K1_NS + 1 && EV_CVPRE0 + 4 * EV_PRE_STEP > EV_CV_INVW && EV_CVPRE0 + 11 * EV_PRE_STEP <= K1S[1].done[315], "events");
static_assert(EV_PRE0 + 11 * EV_PRE_STEP <= K1S[0].done[315] && K1S[0].bar[315], "w2 prefetch landed by the last barrier");

struct Ctx {
  const char *w1, *w3, *xb, *kk, *vv, *sk, *gx, *gw, *w2, *ssm_k, *ssm_D, *q;
  char* ws; char* out;
  __amdgpu_buffer_rsrc_t rs;
  unsigned hb;
  unsigned voff_wg, voff_wu, voff_x0, voff_x1;
  unsigned rd[4];
  unsigned xl0, lane16;
  unsigned ev;
  u32x2 poll;
  unsigned rc0, rc1;
  int w, rot, nl, ml, b, mt, fq;
};
struct K1Prep {
  unsigned kr[12], vr[9];
  u32x4 sk, ga0, ga1, gw0, gw1;
  unsigned o0, oneg, ostep; int pd, pt0, tid;
  f32x2 s[4], k2[4], a, v2; unsigned u[9]; float gs;
};
struct K1Epi { float m[2], p[2], pq[2], r[2], h[2]; u32x4 ho; };

__device__ __forceinline__ const char* karg(int i) {
  typedef const char* cptr;
  const __attribute__((address_space(4))) cptr* p = (const __attribute__((address_space(4))) cptr*)__builtin_amdgcn_kernarg_segment_ptr();
  asm volatile("" : "+s"(p));
  return p[i];
}
__device__ __forceinline__ int lane_of(const Ctx& c) {
  unsigned l = c.lane16;
  asm volatile("" : "+v"(l));
  return (int)(l >> 4);
}
__device__ __forceinline__ int g2_wg(int mt, int nt, int ks) { return ((nt * 4 + (mt & 3)) << 3) | (ks * 2 + (mt >> 2)); }
__device__ __forceinline__ int cv_wg(int cw) { return ((24 + (cw & 7)) << 3) | (cw >> 3); }
__device__ __forceinline__ void flag_store(const Ctx& c, int wg, int slot, unsigned epoch) {
  __builtin_amdgcn_raw_buffer_store_b64((u32x2){epoch + 1u, FLAG_MAGIC}, c.rs, WS_FLAG + wg * 512 + slot * 8, 0, AUX_SC1);
}
__device__ __forceinline__ void poll_issue(Ctx& c) {
  const unsigned vo = c.lane16 >> 1;
  const char* base = c.ws + WS_FLAG + c.b * 512;
  asm volatile("global_load_dwordx2 %0, %1, %2 sc1" : "=v"(c.poll) : "v"(vo), "s"(base) : "memory");
}
__device__ __forceinline__ void poll_wait(Ctx& c, int l, bool need, bool have) {
  const unsigned want = (unsigned)__builtin_amdgcn_readlane((int)c.ev, 14) + 1u;
  bool ok = have && __all(!need || (c.poll[0] == want && c.poll[1] == FLAG_MAGIC));
  if (!ok) {
    GAS unsigned long long* p = (GAS unsigned long long*)(c.ws + WS_FLAG + c.b * 512) + l;
#pragma unroll 1
    for (unsigned spin = 0; spin < SPIN_CAP; ++spin) {
      unsigned long long v = __hip_atomic_load(p, __ATOMIC_RELAXED, __HIP_MEMORY_SCOPE_AGENT);
      if (__all(!need || ((unsigned)v == want && (unsigned)(v >> 32) == FLAG_MAGIC))) break;
      __builtin_amdgcn_s_sleep(1);
    }
  }
  asm volatile("" ::: "memory");
}

template <int sg, int half> __device__ __forceinline__ void k1_issue_w(const Ctx& c) {
  const unsigned dst = (sg % K1_NSLOT) * K1_SLOT + half * 8192;
  const char* base = half ? c.w3 : c.w1;
  if constexpr (sg < 18) {
    const int ksl = mod9(sg % 9 + c.rot);
    dma16(half ? (c.voff_wg ^ 64u) : c.voff_wg, base + ((size_t)(64 * ksl + 8 * c.w) * F_ + 128 * c.nl + 64 * (sg / 9)) * 2, dst + c.w * 1024);
  } else {
    const unsigned l = lane_of(c);
    const unsigned rp = l >> 1;
    const unsigned vo = (half ? (rp ^ 4u) : rp) * (F_ * 2) + (l & 1u) * 16;
    if constexpr (sg < 20) {
      const int ksl = mod9(4 * (sg - 18) + (c.w >> 1) + c.rot);
      dma16(vo, base + ((size_t)(64 * ksl + 32 * (c.w & 1)) * F_ + 1024 + 16 * c.nl) * 2, dst + c.w * 1024);
    } else {
      const int ksl = mod9(8 + c.rot);
      dma16_16(vo, base + ((size_t)(64 * ksl + 8 * c.w) * F_ + 1024 + 16 * c.nl) * 2, dst + c.w * 256);
    }
  }
}
template <int p, int i> __device__ __forceinline__ void k1_issue_x(const Ctx& c) {
  const int ksl = mod9(p + c.rot);
  dma16((i & 1) ? c.voff_x1 : c.voff_x0, c.xb + (size_t)(8 * i) * (D_ * 2) + ksl * 128, K1_XOFF + c.w * (K1_XD * 4096) + (p % K1_XD) * 4096 + i * 1024);
}
template <int p, int j> __device__ __forceinline__ bf16x8 k1_read_x(const Ctx& c) {
  return *(LAS bf16x8*)(ldsp(c.xl0 ^ (32 * j)) + (p % K1_XD) * 4096);
}
template <int n> __device__ __forceinline__ bf16x8 k1_read_w(const Ctx& c) {
  constexpr int sg = K1S[0].seg[n], t = K1S[0].tile[n], j = K1S[0].j[n];
  if constexpr (sg < 18) {
    LAS char* p = ldsp(c.rd[t]) + (sg % K1_NSLOT) * K1_SLOT + (16 * j) * 128;
    return tr_frag(p, p + 4 * 128);
  } else {
    const int off = (sg % K1_NSLOT) * K1_SLOT + (16 * j) * 32;
    return tr_frag(ldsp(c.rc0) + off, ldsp(c.rc1) + off);
  }
}
#define GLD1(dst, voff, sb) asm volatile("global_load_dword %0, %1, %2" : "=v"(dst) : "v"(voff), "s"(sb) : "memory")
#define GLD4(dst, voff, sb) asm volatile("global_load_dwordx4 %0, %1, %2" : "=v"(dst) : "v"(voff), "s"(sb) : "memory")
template <int i> __device__ __forceinline__ void k1_prep_load(const Ctx& c, K1Prep& P) {
  if constexpr (i == 0) {
    const int l = lane_of(c);
    const int dp = 8 * c.w + (l & 7);
    P.pd = 72 * (c.b & 7) + 2 * (dp < 36 ? dp : 35);
    P.pt0 = 64 * (c.b >> 3) + (c.w < 5 ? 8 * (l >> 3) : 0);
    P.o0 = (unsigned)(P.pt0 * D_ + P.pd) * 2;
    P.oneg = P.pt0 > 0 ? P.o0 - 4 * (D_ * 2) : P.o0;
    P.ostep = P.pt0 > 0 ? D_ * 2 : 0;
  }
  if constexpr (i == 0) GLD4(P.ga0, c.lane16, c.gx);
  else if constexpr (i == 1) GLD4(P.ga1, 1024 + (c.lane16 & 127), c.gx);
  else if constexpr (i == 2) GLD4(P.gw0, c.lane16, c.gw);
  else if constexpr (i == 3) GLD4(P.gw1, 1024 + (c.lane16 & 127), c.gw);
  else if constexpr (i == 4) GLD4(P.sk, (unsigned)P.pd * 8, c.sk);
  else if constexpr (i < 9) GLD1(P.kr[i - 5], P.oneg + (i - 5) * P.ostep, c.kk);
  else if constexpr (i < 17) GLD1(P.kr[i - 5], P.o0 + (i - 9) * (D_ * 2), c.kk);
  else if constexpr (i == 17) GLD1(P.vr[0], P.oneg + 3 * P.ostep, c.vv);
  else GLD1(P.vr[i - 17], P.o0 + (i - 18) * (D_ * 2), c.vv);
}
template <int ctrl> __device__ __forceinline__ float dpp_add(float v) {
  return v + __builtin_bit_cast(float, __builtin_amdgcn_update_dpp(0, __builtin_bit_cast(int, v), ctrl, 0xf, 0xf, true));
}
__device__ __forceinline__ float dot2bf(unsigned a, unsigned b, float acc) {
  return __builtin_amdgcn_fdot2_f32_bf16(__builtin_bit_cast(bf16x2, a), __builtin_bit_cast(bf16x2, b), acc, false);
}
template <int k> __device__ __forceinline__ void k1_prep_unit(const Ctx& c, K1Prep& P) {
  if constexpr (k == 0) {
    asm volatile("" : "+v"(P.kr[0]), "+v"(P.kr[1]), "+v"(P.kr[2]), "+v"(P.kr[3]), "+v"(P.kr[4]), "+v"(P.kr[5]), "+v"(P.kr[6]), "+v"(P.kr[7]),
                 "+v"(P.kr[8]), "+v"(P.kr[9]), "+v"(P.kr[10]), "+v"(P.kr[11]), "+v"(P.vr[0]), "+v"(P.vr[1]), "+v"(P.vr[2]), "+v"(P.vr[3]),
                 "+v"(P.vr[4]), "+v"(P.vr[5]), "+v"(P.vr[6]), "+v"(P.vr[7]), "+v"(P.vr[8]), "+v"(P.sk), "+v"(P.ga0), "+v"(P.ga1), "+v"(P.gw0),
                 "+v"(P.gw1)::"memory");
    float s1 = dot2bf(P.ga1[0], P.gw1[0], 0.f), s2 = dot2bf(P.ga1[1], P.gw1[1], 0.f);
    s1 = dot2bf(P.ga1[2], P.gw1[2], s1); s2 = dot2bf(P.ga1[3], P.gw1[3], s2);
    P.gs = c.lane16 < 128 ? s1 + s2 : 0.f;
  } else if constexpr (k == 1) {
    float s1 = dot2bf(P.ga0[0], P.gw0[0], P.gs), s2 = dot2bf(P.ga0[1], P.gw0[1], 0.f);
    s1 = dot2bf(P.ga0[2], P.gw0[2], s1); s2 = dot2bf(P.ga0[3], P.gw0[3], s2);
    P.gs = s1 + s2;
  } else if constexpr (k == 2) {
    P.gs = dpp_add<0x111>(P.gs); P.gs = dpp_add<0x112>(P.gs);
  } else if constexpr (k == 3) {
    P.gs = dpp_add<0x114>(P.gs); P.gs = dpp_add<0x118>(P.gs);
  } else if constexpr (k == 4) {
    int v = __builtin_bit_cast(int, P.gs);
    float a = __builtin_bit_cast(float, __builtin_amdgcn_readlane(v, 15)), b = __builtin_bit_cast(float, __builtin_amdgcn_readlane(v, 31));
    float d = __builtin_bit_cast(float, __builtin_amdgcn_readlane(v, 47)), e = __builtin_bit_cast(float, __builtin_amdgcn_readlane(v, 63));
    float z = rbf((a + b) + (d + e));
    float g = rbf(__builtin_amdgcn_rcpf(1.0f + __builtin_amdgcn_exp2f(z * -1.44269504088896f)));
    __builtin_amdgcn_raw_buffer_store_b32(__builtin_bit_cast(unsigned, g), c.rs, c.lane16 == 0 ? 0u : OOB, WS_G + (256 * c.mt + 64 * c.fq + 8 * c.nl + c.w) * 4, AUX_SC1);
  } else if constexpr (k == 5) {
    if (P.pt0 == 0) { P.kr[0] = P.kr[1] = P.kr[2] = P.kr[3] = 0u; P.vr[0] = 0u; }
    P.s[0] = (f32x2){bflo(P.sk[0]), bflo(P.sk[2])};
    const int l = lane_of(c);
    P.tid = (((7 - (l & 7)) << 3) | (l >> 3)) * 4;
  } else if constexpr (k == 6) {
    P.s[1] = (f32x2){bfhi(P.sk[0]), bfhi(P.sk[2])};
    P.s[2] = (f32x2){bflo(P.sk[1]), bflo(P.sk[3])}; P.s[3] = (f32x2){bfhi(P.sk[1]), bfhi(P.sk[3])};
    P.k2[0] = unpk2(P.kr[0]); P.k2[1] = unpk2(P.kr[1]); P.k2[2] = unpk2(P.kr[2]);
  } else if constexpr (k < 34) {
    constexpr int i = (k - 7) / 3, hf = (k - 7) % 3;
    if constexpr (hf == 0) {
      P.k2[(i + 3) & 3] = unpk2(P.kr[i + 3]);
      P.a[0] = P.k2[i & 3][0] * P.s[3][0]; P.a[1] = P.k2[i & 3][1] * P.s[3][1];
      asm volatile("" : "+v"(P.a[0])); asm volatile("" : "+v"(P.a[1]));
    } else if constexpr (hf == 1) {
      P.a[0] = __builtin_fmaf(P.k2[(i + 1) & 3][0], P.s[2][0], P.a[0]); asm volatile("" : "+v"(P.a[0]));
      P.a[1] = __builtin_fmaf(P.k2[(i + 1) & 3][1], P.s[2][1], P.a[1]); asm volatile("" : "+v"(P.a[1]));
      P.a[0] = __builtin_fmaf(P.k2[(i + 2) & 3][0], P.s[1][0], P.a[0]); asm volatile("" : "+v"(P.a[0]));
      P.a[1] = __builtin_fmaf(P.k2[(i + 2) & 3][1], P.s[1][1], P.a[1]); asm volatile("" : "+v"(P.a[1]));
      P.v2 = unpk2(P.vr[i]);
    } else {
      P.a[0] = __builtin_fmaf(P.k2[(i + 3) & 3][0], P.s[0][0], P.a[0]); asm volatile("" : "+v"(P.a[0]));
      P.a[1] = __builtin_fmaf(P.k2[(i + 3) & 3][1], P.s[0][1], P.a[1]); asm volatile("" : "+v"(P.a[1]));
      float u0 = P.a[0] * P.v2[0]; asm volatile("" : "+v"(u0));
      float u1 = P.a[1] * P.v2[1]; asm volatile("" : "+v"(u1));
      P.u[i] = (unsigned)__builtin_amdgcn_ds_bpermute(P.tid, (int)pk2(u0, u1));
    }
  } else if constexpr (k < 37) {
    if constexpr (k == 36) {
      const int l = lane_of(c);
      const int dp = 8 * c.w + (l >> 3);
      P.o0 = dp < 36 ? (unsigned)((72 * (c.b & 7) + 2 * dp) * 2 * S_ + (1984 - 64 * (c.b >> 3)) + 8 * (l & 7)) * 2 : OOB;
    }
  } else {
    constexpr int ch = (k - 37) >> 1, cp = (k - 37) & 1;
    constexpr unsigned sel = ch ? 0x07060302u : 0x05040100u;
    u32x4 o;
#pragma unroll
    for (int e = 0; e < 4; ++e) o[e] = __builtin_amdgcn_perm(P.u[7 + cp - 2 * e - 1], P.u[7 + cp - 2 * e], sel);
    __builtin_amdgcn_raw_buffer_store_b128(o, c.rs, P.o0, WS_UR + (ch * 2 + cp) * S_ * 2, AUX_SC1);
  }
}
template <int ch, int e> __device__ __forceinline__ void k1_epi_step(const Ctx& c, const f32x16 (&acc)[4], K1Epi& E) {
  constexpr int pr = e / 3, st = e % 3, t = pr / 4, i = 2 * (pr % 4);
  if constexpr (st == 0) {
    if constexpr (pr > 0) { E.h[0] = E.pq[0] * E.r[0]; E.h[1] = E.pq[1] * E.r[1]; }
    if constexpr (pr < 16) {
      E.m[0] = acc[t][i] * -1.44269504088896f; E.m[1] = acc[t][i + 1] * -1.44269504088896f;
      E.p[0] = acc[t][i] * acc[t][i + 8]; E.p[1] = acc[t][i + 1] * acc[t][i + 9];
      asm volatile("" : "+v"(E.p[0]), "+v"(E.p[1]));
    }
  } else if constexpr (st == 1) {
    if constexpr (pr < 16) { E.m[0] = __builtin_amdgcn_exp2f(E.m[0]); E.m[1] = __builtin_amdgcn_exp2f(E.m[1]); }
    if constexpr (pr > 0) {
      E.ho[(pr - 1) % 4] = pk2(E.h[0], E.h[1]);
      if constexpr (pr % 4 == 0)
        __builtin_amdgcn_raw_buffer_store_b128(E.ho, c.rs, c.lane16, c.hb + (8 * c.nl + 4 * ch + (pr - 1) / 4) * 1024, AUX_SC1);
    }
  } else {
    if constexpr (pr < 16) {
      float d0 = 1.0f + E.m[0], d1 = 1.0f + E.m[1];
      E.r[0] = __builtin_amdgcn_rcpf(d0); E.r[1] = __builtin_amdgcn_rcpf(d1);
      E.pq[0] = E.p[0]; E.pq[1] = E.p[1];
    }
  }
}

__device__ __forceinline__ int g2_ft(const Ctx& c, int s, int i8) {
  if (s >= 8) return 64 + i8;
  const int jj = ((s & 3) + c.nl + c.ml) & 3;
  return 16 * jj + 8 * (i8 >> 2) + (i8 & 3) + (s >= 4 ? 4 : 0);
}
template <int s, int i> __device__ __forceinline__ void g2_issue_w(const Ctx& c) {
  const unsigned n16 = 64u * i + (unsigned)lane_of(c);
  const unsigned vo = ((n16 * 171u) >> 11) * (D_ * 2 - 192) + n16 * 16u;
  dma16(vo, c.w2 + (size_t)(16 * g2_ft(c, s, c.w)) * (D_ * 2), K1_XOFF + (s % G2_NSLOT) * G2_SLOT + c.w * 3072 + i * 1024);
}
template <int i> __device__ __forceinline__ u32x4 g2_load_h(const Ctx& c) {
  return __builtin_amdgcn_raw_buffer_load_b128(c.rs, c.lane16, c.hb + g2_ft(c, i / 8, i % 8) * 1024, AUX_SC1);
}
__device__ __forceinline__ unsigned cv_base(int ch) { return ch < 7 ? K1_XOFF + ch * CV_CH : (ch == 7 ? K1_SLOT : 2 * K1_SLOT); }
template <int i> __device__ __forceinline__ void cv_issue(const Ctx& c, int cw, int ch) {
  const int d = 9 * cw + ch;
  if constexpr (i < 4) dma16(c.lane16, c.ssm_k + (size_t)d * (S_ * 2) + i * 1024, cv_base(ch) + CV_K + 64 + i * 1024);
  else dma16(c.lane16, c.ws + WS_UR + (size_t)d * (4 * S_) + (i - 4) * 1024, cv_base(ch) + (i < 8 ? CV_U0 + (i - 4) * 1024 : CV_U1 + (i - 8) * 1024));
}

template <int n, int NEND, int ROLE>
__device__ __forceinline__ void k1_slot(Ctx& c, bf16x8 (&xf)[36], bf16x8 (&wf)[K1_PF + 1], f32x16 (&acc)[4], K1Epi& E, K1Prep& P, u32x4 (&hw)[G2_WIN]) {
  constexpr const K1Sched& S = K1S[ROLE];
  constexpr int sg = S.seg[n];
  if constexpr (n + K1_PF == 288) {
    const unsigned l = lane_of(c);
    unsigned mat = (l >> 4) & 1, h = l >> 5, q = (l & 15) >> 2, p = l & 3;
    unsigned base = mat * 8192 + (8 * h + q) * 32 + p * 8;
    c.rc0 = base + (mat ? 128 : 0);
    c.rc1 = base + (mat ? 0 : 128);
  }
  if constexpr (n + K1_PF < K1_NS) wf[(n + K1_PF) % (K1_PF + 1)] = k1_read_w<n + K1_PF>(c);
  if constexpr (n + 4 < 144) {
    constexpr int sn = S.seg[n + 4];
    if constexpr (S.tile[n + 4] == 0 && sn >= 1) xf[4 * sn + S.j[n + 4]] = k1_read_x<sn, S.j[n + 4]>(c);
  }
  {
    constexpr int t = S.tile[n];
    constexpr int xi = sg < 18 ? 4 * (sg % 9) + S.j[n] : n - 288;
    constexpr bool first = (sg < 18) ? (sg % 9 == 0 && S.j[n] == 0) : (n == 288);
    if constexpr (first) acc[t] = __builtin_amdgcn_mfma_f32_32x32x16_bf16(wf[n % (K1_PF + 1)], xf[xi], (f32x16){}, 0, 0, 0);
    else acc[t] = __builtin_amdgcn_mfma_f32_32x32x16_bf16(wf[n % (K1_PF + 1)], xf[xi], acc[t], 0, 0, 0);
  }
  SCHED_FENCE();
  {
    constexpr int ws0 = [] { for (int s = 0; s < K1_NSEG; ++s) if (K1S[0].wissue[s] == n) return s; return -1; }();
    constexpr int ws1 = [] { for (int s = 0; s < K1_NSEG; ++s) if (K1S[0].wissue[s] >= 0 && K1S[0].wissue[s] + 2 == n) return s; return -1; }();
    constexpr int xs = [] { for (int p = 0; p < 9; ++p) if (K1S[0].xissue[p] >= 0 && n >= K1S[0].xissue[p] && n < K1S[0].xissue[p] + 4) return p; return -1; }();
    if constexpr (ws0 >= 0) k1_issue_w<ws0, 0>(c);
    if constexpr (ws1 >= 0) k1_issue_w<ws1, 1>(c);
    if constexpr (xs >= 0) k1_issue_x<xs, n - S.xissue[xs]>(c);
  }
  if constexpr (n >= K1_PREP0 && n < K1_PREP0 + K1_NPREP / 2) {
    k1_prep_load<2 * (n - K1_PREP0)>(c, P);
    k1_prep_load<2 * (n - K1_PREP0) + 1>(c, P);
  }
  if constexpr (n >= S.prep_ready && n < S.prep_ready + K1_PREP_UNITS) k1_prep_unit<n - S.prep_ready>(c, P);
  if constexpr (n >= S.epi0[0] && n < S.epi0[0] + 50) k1_epi_step<0, n - S.epi0[0]>(c, acc, E);
  if constexpr (n >= S.epi0[1] && n < S.epi0[1] + 50) k1_epi_step<1, n - S.epi0[1]>(c, acc, E);
  if constexpr (n == EV_PUB_U) {
    if (c.w == 1) { const int l = lane_of(c); if (l < 8) flag_store(c, cv_wg(8 * (c.b & 7) + l), c.b >> 3, c.ev); }
  }
  if constexpr (n == EV_PUB_A) {
    if (c.w == 1) { const int l = lane_of(c); if (l >= 8 && l < 14) flag_store(c, g2_wg(c.mt, l - 8, c.fq), c.nl, c.ev); }
  }
  if constexpr (ROLE == 0) {
    if constexpr (n == EV_PRE0 - 2) {
      c.w2 = karg(8) + ((size_t)(1152 * c.fq) * D_ + 96 * c.nl) * 2;
    }
    if constexpr (n >= EV_PRE0 && n < EV_PRE0 + 12 * EV_PRE_STEP && (n - EV_PRE0) % EV_PRE_STEP == 0)
      g2_issue_w<(n - EV_PRE0) / EV_PRE_STEP / 3, (n - EV_PRE0) / EV_PRE_STEP % 3>(c);
    if constexpr (n == EV_G2_POLL) poll_issue(c);
    if constexpr (n == EV_G2_CHECK) {
      asm volatile("s_waitcnt vmcnt(%1)" : "+v"(c.poll) : "n"(S.ops_after(EV_G2_POLL, EV_G2_CHECK)) : "memory");
      if (c.w == 0) { const int l = lane_of(c); poll_wait(c, l, l < 8, true); }
    }
    if constexpr (n >= EV_G2_H0 && (n - EV_G2_H0) % 2 == 0 && (n - EV_G2_H0) / 2 < G2_WIN) hw[(n - EV_G2_H0) / 2] = g2_load_h<(n - EV_G2_H0) / 2>(c);
  } else {
    const int cw = (c.b & 7) * 8 + (c.b >> 3) - 24;
    if constexpr (n == EV_PRE0 - 2) { c.ssm_k = karg(4); c.ssm_D = karg(5); }
    if constexpr (n == EV_CV_POLL) poll_issue(c);
    if constexpr (n == EV_CV_CHECK) {
      asm volatile("s_waitcnt vmcnt(%1)" : "+v"(c.poll) : "n"(K1S[2].ops_after(EV_CV_POLL, EV_CV_CHECK)) : "memory");
      if (c.w == 0) { const int l = lane_of(c); poll_wait(c, l, l < 32, true); asm volatile("buffer_inv sc1" ::: "memory"); }
    }
    if constexpr (n == EV_CV_INVW) { if (c.w == 0) wait_vm<S.ops_after(EV_CV_CHECK, EV_CV_INVW)>(); }
    if constexpr (n >= EV_CVPRE0 && n < EV_CVPRE0 + 12 * EV_PRE_STEP && (n - EV_CVPRE0) % EV_PRE_STEP == 0) {
      if (c.w < 7) cv_issue<(n - EV_CVPRE0) / EV_PRE_STEP>(c, cw, c.w);
    }
    if constexpr (n >= EV_CV_PRE7 && n < EV_CV_PRE7 + 12) {
      if (c.w == 7) cv_issue<n - EV_CV_PRE7>(c, cw, 7);
    }
  }
  SCHED_FENCE();
  if constexpr (S.bar[n]) {
    if constexpr (ROLE == 1 && K1S[1].wait[n] != K1S[2].wait[n]) {
      if (c.w == 7) wait_vm<K1S[2].wait[n]>();
      else wait_vm<K1S[1].wait[n]>();
      wait_vm_barrier<63>();
    } else {
      wait_vm_barrier<S.wait[n]>();
    }
    SCHED_FENCE();
  }
  if constexpr (n + 1 < NEND) k1_slot<n + 1, NEND, ROLE>(c, xf, wf, acc, E, P, hw);
}
__device__ __forceinline__ void k1_tail(const Ctx& c, const f32x16& a) {
  u32x4 o;
#pragma unroll
  for (int i = 0; i < 4; ++i) o[i] = pk2(silu_mul(a[2 * i], a[2 * i + 8]), silu_mul(a[2 * i + 1], a[2 * i + 9]));
  __builtin_amdgcn_raw_buffer_store_b128(o, c.rs, c.lane16, c.hb + (64 + c.nl) * 1024, AUX_SC1);
}

template <int n> __device__ __forceinline__ bf16x8 g2_read_w(unsigned rd) {
  constexpr int i = n / 3, t = n % 3, s = i / 8, j = i % 8;
  LAS char* p = ldsp(rd) + (s % G2_NSLOT) * G2_SLOT + (16 * j) * 192 + t * 64;
  return tr_frag(p, p + 8 * 192);
}
constexpr int G2_TAILST = 6;
constexpr int G2_ZPOLL = 198, G2_ZCHECK = 212;
constexpr int G2_PUBBAR = 31, G2_POLL = 54, G2_CHECK = 24 * 3 - G2_PF - 1;
constexpr int g2_hslot(int i) {
  int s = 3 * (i - G2_WIN) + 2;
  if (i >= 32 && s < G2_CHECK + 1 + (i - 32)) s = G2_CHECK + 1 + (i - 32);
  return s;
}
constexpr int g2_hload_at(int n) { for (int i = G2_WIN; i < 72; ++i) if (g2_hslot(i) == n) return i; return -1; }
constexpr bool g2_bar(int n) { return n % 24 == 24 - G2_PF - 1 && n / 24 < 8 && n / 24 >= 1; }
constexpr int g2_ops_in_slot(int n) {
  int k = 0;
  for (int s = 1; s + 3 < 9; ++s) for (int i = 0; i < 3; ++i) if (n == 24 * s + 24 - G2_PF + 1 + 3 * i) ++k;
  if (g2_hload_at(n) >= 0) ++k;
  if (n == G2_TAILST) ++k;
  return k;
}
constexpr int g2_ops_after(int a, int b) { int k = 0; for (int n = a + 1; n <= b; ++n) k += g2_ops_in_slot(n); return k; }
static_assert([] { for (int i = G2_WIN; i + 1 < 72; ++i) if (g2_hslot(i) >= g2_hslot(i + 1) || g2_hslot(i) + 12 > 3 * i) return false; return true; }(), "h load slots");
template <int n>
__device__ __forceinline__ void g2_slot(Ctx& c, unsigned rd, u32x4 (&hw)[G2_WIN], bf16x8 (&wf)[G2_PF + 1], f32x16 (&acc)[3], const f32x16& tailacc, u32x4& to) {
  constexpr int i = n / 3, t = n % 3, s = n / 24;
  if constexpr (n + G2_PF < G2_NS) wf[(n + G2_PF) % (G2_PF + 1)] = g2_read_w<n + G2_PF>(rd);
  if constexpr (i == 0) acc[t] = __builtin_amdgcn_mfma_f32_32x32x16_bf16(wf[n % (G2_PF + 1)], __builtin_bit_cast(bf16x8, hw[i % G2_WIN]), (f32x16){}, 0, 0, 0);
  else acc[t] = __builtin_amdgcn_mfma_f32_32x32x16_bf16(wf[n % (G2_PF + 1)], __builtin_bit_cast(bf16x8, hw[i % G2_WIN]), acc[t], 0, 0, 0);
  SCHED_FENCE();
  if constexpr (n >= 2 && n < 6) to[n - 2] = pk2(silu_mul(tailacc[2 * n - 4], tailacc[2 * n + 4]), silu_mul(tailacc[2 * n - 3], tailacc[2 * n + 5]));
  if constexpr (n == G2_TAILST) __builtin_amdgcn_raw_buffer_store_b128(to, c.rs, c.lane16, c.hb + (64 + c.nl) * 1024, AUX_SC1);
  {
    constexpr int m = n - (24 - G2_PF + 1);
    if constexpr (m >= 24 && m % 24 < 9 && m % 3 == 0 && m / 24 + 3 < 9) g2_issue_w<m / 24 + 3, (m % 24) / 3>(c);
  }
  if constexpr (g2_hload_at(n) >= 0) hw[g2_hload_at(n) % G2_WIN] = g2_load_h<g2_hload_at(n)>(c);
  if constexpr (n == G2_POLL) poll_issue(c);
  if constexpr (n == G2_CHECK) {
    asm volatile("s_waitcnt vmcnt(%1)" : "+v"(c.poll) : "n"(g2_ops_after(G2_POLL, G2_CHECK)) : "memory");
    if (c.w == 0) { const int l = lane_of(c); poll_wait(c, l, l >= 8 && l < 16, true); }
  }
  if constexpr (n == G2_ZPOLL) poll_issue(c);
  if constexpr (n == G2_ZCHECK) {
    asm volatile("s_waitcnt vmcnt(%1)" : "+v"(c.poll) : "n"(g2_ops_after(G2_ZPOLL, G2_ZCHECK)) : "memory");
    if (c.w == 0) {
      const int l = lane_of(c);
      poll_wait(c, l, l >= 16 && l <= 16 + (96 * c.nl + 95) / 9 - (96 * c.nl) / 9, true);
    }
    wait_vm_barrier<63>();
  }
  SCHED_FENCE();
  if constexpr (g2_bar(n)) {
    constexpr int last = s + 1 < 4 ? -1 : 24 * (s - 2) + 24 - G2_PF + 1 + 6;
    wait_vm_barrier<last < 0 ? 63 : cmin(63, g2_ops_after(last, n))>();
    SCHED_FENCE();
  }
  if constexpr (n == G2_PUBBAR) {
    wait_vm_barrier<g2_ops_after(G2_TAILST, n)>();
    if (c.w == 1) { const int l = lane_of(c); if (l >= 8 && l < 14) flag_store(c, g2_wg(c.mt, l - 8, c.fq), 8 + c.nl, c.ev); }
    SCHED_FENCE();
  }
  if constexpr (n + 1 < G2_NS) g2_slot<n + 1>(c, rd, hw, wf, acc, tailacc, to);
}

__device__ __forceinline__ void g2_phase(Ctx& c, u32x4 (&hw)[G2_WIN], const f32x16& tailacc) {
  const int lane = lane_of(c);
  const int w = c.w, ks = c.fq, nt = c.nl, mt = c.mt;
  unsigned rd;
  {
    int gq = lane >> 4, chf = gq & 1, h = gq >> 1, q = (lane & 15) >> 2, p = lane & 3;
    rd = K1_XOFF + (4 * h + q) * 192 + chf * 32 + p * 8;
  }
  bf16x8 wf[G2_PF + 1];
  f32x16 acc[3];
  u32x4 to;
  wf[0] = g2_read_w<0>(rd); wf[1] = g2_read_w<1>(rd); wf[2] = g2_read_w<2>(rd); wf[3] = g2_read_w<3>(rd);
  SCHED_FENCE();
  g2_slot<0>(c, rd, hw, wf, acc, tailacc, to);
  const int kd = w >> 1, half = w & 1;
  const int uh = w / 3, ut = w % 3;
  const int qp = lane >> 5, cc = lane & 31;
  const int tok = 256 * mt + 64 * ks + 32 * (uh & 1) + cc, d0 = 96 * nt + 32 * ut;
  if (kd != ks) {
    const unsigned base = WS_PART + g2_wg(mt, nt, kd) * 49152 + (ks * 2 + half) * 6144;
#pragma unroll
    for (int t = 0; t < 3; ++t)
#pragma unroll
      for (int p2 = 0; p2 < 2; ++p2) {
        u32x4 o = {pk2(acc[t][8 * p2], acc[t][8 * p2 + 1]), pk2(acc[t][8 * p2 + 2], acc[t][8 * p2 + 3]), pk2(acc[t][8 * p2 + 4], acc[t][8 * p2 + 5]),
                   pk2(acc[t][8 * p2 + 6], acc[t][8 * p2 + 7])};
        __builtin_amdgcn_raw_buffer_store_b128(o, c.rs, c.lane16, base + (t * 2 + p2) * 1024, AUX_SC1);
      }
  } else {
#pragma unroll
    for (int t = 0; t < 3; ++t)
#pragma unroll
      for (int qd = 0; qd < 4; ++qd)
        *(LAS f32x4*)(ldsp(c.lane16) + (half * 12 + t * 4 + qd) * 1024) = (f32x4){acc[t][4 * qd], acc[t][4 * qd + 1], acc[t][4 * qd + 2], acc[t][4 * qd + 3]};
  }
  u32x4 zz[2], qq[2];
  c.q = karg(0);
  c.out = (char*)karg(11);
#pragma unroll
  for (int i = 0; i < 2; ++i)
    zz[i] = __builtin_amdgcn_raw_buffer_load_b128(c.rs, ((16 * i + (lane >> 2)) * S_) * 2 + (lane & 3) * 16,
                                                  WS_Z + (d0 * S_ + 256 * mt + 64 * ks + 32 * (uh & 1)) * 2, AUX_SC1);
  const float g = __builtin_bit_cast(float, __builtin_amdgcn_raw_buffer_load_b32(c.rs, tok * 4, WS_G, AUX_SC1));
#pragma unroll
  for (int i = 0; i < 2; ++i) qq[i] = *(const u32x4*)(c.q + ((size_t)tok * D_ + d0 + 16 * qp + 8 * i) * 2);
  SCHED_FENCE();
  wait_vm<5>();
  if (kd != ks && lane == 16 + kd) flag_store(c, g2_wg(mt, nt, kd), 32 + 2 * ks + half, c.ev);
  SCHED_FENCE();
  STAMP(33);
  float ga[16], og = 0.f;
  if (w < 6) {
    LAS char* zt = ldsp(24576 + w * 2560);
#pragma unroll
    for (int i = 0; i < 2; ++i) *(LAS u32x4*)(zt + (16 * i + (lane >> 2)) * 80 + (lane & 3) * 16) = zz[i];
    og = rbf(1.0f - g);
#pragma unroll
    for (int j = 0; j < 16; ++j) {
      float z = bf2f(*(LAS u16*)(zt + (16 * qp + j) * 80 + cc * 2));
      unsigned qw = qq[j >> 3][(j >> 1) & 3];
      float a = rbf(((j & 1) ? bfhi(qw) : bflo(qw)) * z);
      ga[j] = rbf(g * a);
    }
  } else if (w == 6) {
    poll_wait(c, lane, lane >= 32 && lane < 40 && ((lane - 32) >> 1) != ks, false);
    if (lane == 14) __builtin_amdgcn_raw_buffer_store_b32(c.ev + 1u, c.rs, WS_EPOCH + c.b * 4, 0, AUX_SC1);
  }
  wait_lgkm0();
  asm volatile("s_barrier" ::: "memory");
  STAMP(34);
  if (w < 6) {
    u32x4 pp[3][2];
#pragma unroll
    for (int s = 0; s < 3; ++s) {
      const int src = s + (s >= ks ? 1 : 0);
#pragma unroll
      for (int h = 0; h < 2; ++h)
        pp[s][h] = __builtin_amdgcn_raw_buffer_load_b128(c.rs, qp * 1024 + (cc + 32 * h) * 16, WS_PART + c.b * 49152 + (src * 2 + uh) * 6144 + ut * 2048, AUX_SC1);
    }
    f32x4 own[2][2];
#pragma unroll
    for (int o = 0; o < 2; ++o)
#pragma unroll
      for (int h = 0; h < 2; ++h) own[o][h] = *(LAS f32x4*)(ldsp((cc + 32 * h) * 16) + (uh * 12 + ut * 4 + 2 * qp + o) * 1024);
#pragma unroll
    for (int o = 0; o < 2; ++o) {
      float res[8];
#pragma unroll
      for (int h = 0; h < 2; ++h)
#pragma unroll
        for (int k = 0; k < 4; ++k) {
          float bsum = own[o][h][k];
#pragma unroll
          for (int s = 0; s < 3; ++s) { unsigned v = pp[s][h][2 * o + (k >> 1)]; bsum += (k & 1) ? bfhi(v) : bflo(v); }
          res[4 * h + k] = ga[8 * o + 4 * h + k] + rbf(og * rbf(bsum));
        }
      u32x4 ov = {pk2(res[0], res[1]), pk2(res[2], res[3]), pk2(res[4], res[5]), pk2(res[6], res[7])};
      *(u32x4*)(c.out + ((size_t)tok * D_ + d0 + 16 * qp + 8 * o) * 2) = ov;
    }
  }
  STAMP(35);
}

typedef unsigned u32x2a4 __attribute__((ext_vector_type(2), aligned(4)));
__device__ __forceinline__ bf16x8 cv_read_a(LAS char* p) {
  u32x2 lo = *(LAS u32x2a4*)p;
  u32x2 hi = *(LAS u32x2a4*)(p + 8);
  u32x4 v = {lo[0], lo[1], hi[0], hi[1]};
  return __builtin_bit_cast(bf16x8, v);
}
template <bool PREF, bool FIRST>
__device__ __forceinline__ void cv_block(f32x4 (&acc)[8], const bf16x8 (&af)[8], const bf16x8 (&bf)[8], bf16x8 (&naf)[8], bf16x8 (&nbf)[8],
                                         const unsigned (&ap)[4], unsigned bp, int roff) {
  int k = 0;
#pragma unroll
  for (int p = 0; p < 8; ++p)
#pragma unroll
    for (int dl = 0; dl + p < 8; ++dl) {
      if (PREF && (k & 1) == 0 && k < 32) {
        const int f = k >> 2;
        if (k & 2) naf[f] = cv_read_a(ldsp(ap[f >> 1]) + 512 * (f & 1) + roff);
        else nbf[f] = *(LAS bf16x8*)(ldsp(bp) + 64 * (56 - 8 * f) + roff);
      }
      if (FIRST && p == 0) acc[dl] = __builtin_amdgcn_mfma_f32_16x16x32_bf16(af[p], bf[dl], (f32x4){}, 0, 0, 0);
      else acc[p + dl] = __builtin_amdgcn_mfma_f32_16x16x32_bf16(af[p], bf[dl], acc[p + dl], 0, 0, 0);
      asm volatile("" : "+v"(acc[p + dl]));
      if (k & 1) SCHED_FENCE();
      ++k;
    }
}
__device__ __forceinline__ u32x2 cv_z(const f32x4& y, LAS char* cl, int g, int lane, float Dv) {
  const int q = lane >> 4, pi = (lane >> 3) & 1, ii = lane & 7;
  const int T = 256 * g + 32 * ii + 8 * q;
  u32x4 uu = *(LAS u32x4*)(cl + CV_U1 + 2 * (2040 - T));
  float z[4];
#pragma unroll
  for (int k = 0; k < 4; ++k) {
    float u = pi ? bflo(uu[3 - k]) : bfhi(uu[3 - k]);
    z[k] = rbf(y[k]) + rbf(u * Dv);
  }
  unsigned p01 = pk2(z[0], z[1]), p23 = pk2(z[2], z[3]);
  unsigned snd = pi ? p01 : p23;
  unsigned rcv = __builtin_amdgcn_update_dpp(0u, snd, 0x128, 0xf, 0xf, false);
  unsigned E = pi ? rcv : p01, O = pi ? p23 : rcv;
  return (u32x2){(E & 0xffffu) | (O << 16), (E >> 16) | (O & 0xffff0000u)};
}
__device__ __forceinline__ void cv_phase(Ctx& c, const f32x16& tailacc) {
  const int lane = lane_of(c), w = c.w;
  const int cw = (c.b & 7) * 8 + (c.b >> 3) - 24;
  const int d = 9 * cw + w, d8 = 9 * cw + 8;
  const unsigned cb = cv_base(w), cb8 = cv_base(8);
  LAS char* cl = ldsp(cb);
  LAS char* cl8 = ldsp(cb8);
  if (w == 7) wait_vm<1>();
  dma16(c.lane16, c.ws + WS_UR + (size_t)d8 * (4 * S_) + w * 1024, cb8 + (w < 4 ? CV_U0 + w * 1024 : CV_U1 + (w - 4) * 1024));
  if (w < 4) dma16(c.lane16, c.ssm_k + (size_t)d8 * (S_ * 2) + w * 1024, cb8 + CV_K + 64 + w * 1024);
  if (lane < 60) {
    int o = lane < 4 ? CV_K + lane * 16 : (lane < 32 ? CV_U0 + 4096 + (lane - 4) * 16 : CV_U1 + 4096 + (lane - 32) * 16);
    *(LAS u32x4*)(cl + o) = (u32x4){0u, 0u, 0u, 0u};
    if (w == 7) *(LAS u32x4*)(cl8 + o) = (u32x4){0u, 0u, 0u, 0u};
  }
  const int rho = lane & 15, q = lane >> 4, pi = (lane >> 3) & 1, ii = lane & 7;
  const unsigned aoff = CV_K + 4 + 4 * rho + 16 * q;
  const unsigned boff = CV_U0 + pi * 4640 + 2 * (2016 - 32 * ii + 8 * q) - 64 * 56;
  unsigned ap[4], bp = cb + boff, ap8[4], bp8 = cb8 + boff + 64 * w;
#pragma unroll
  for (int i = 0; i < 4; ++i) {
    ap[i] = cb + aoff + 1024 * i; asm volatile("" : "+v"(ap[i]));
    ap8[i] = cb8 + aoff + 1024 * i + 64 * w; asm volatile("" : "+v"(ap8[i]));
  }
  f32x4 acc[8], acc8[8];
  bf16x8 a0[8], b0[8], a1[8], b1[8];
#pragma unroll
  for (int f = 0; f < 8; ++f) {
    b0[f] = *(LAS bf16x8*)(ldsp(bp) + 64 * (56 - 8 * f));
    a0[f] = cv_read_a(ldsp(ap[f >> 1]) + 512 * (f & 1));
  }
  SCHED_FENCE();
  k1_tail(c, tailacc);
  const int ntz = (lane < 32) ? (9 * cw) / 96 : (9 * cw + 8) / 96;
  const int zwg = g2_wg((lane >> 2) & 7, ntz, lane & 3);
  unsigned zev, dd;
  GLD1(zev, (unsigned)zwg * 4, c.ws + WS_EPOCH);
  asm volatile("global_load_ushort %0, %1, %2" : "=v"(dd) : "v"((unsigned)(lane & 1 ? d8 : d) * 2), "s"(c.ssm_D) : "memory");
  SCHED_FENCE();
  STAMP(24);
  cv_block<true, true>(acc, a0, b0, a1, b1, ap, bp, 64);
  cv_block<true, false>(acc, a1, b1, a0, b0, ap, bp, 128);
  asm volatile("s_waitcnt vmcnt(0) lgkmcnt(0)\n\ts_barrier" : "+v"(zev), "+v"(dd)::"memory");
  if (w == 1) {
    if (lane >= 8 && lane < 14) flag_store(c, g2_wg(c.mt, lane - 8, c.fq), 8 + c.nl, c.ev);
    if (lane == 14) __builtin_amdgcn_raw_buffer_store_b32(c.ev + 1u, c.rs, WS_EPOCH + c.b * 4, 0, AUX_SC1);
  }
  SCHED_FENCE();
  cv_block<true, false>(acc, a0, b0, a1, b1, ap8, bp8, 0);
  cv_block<true, true>(acc8, a1, b1, a0, b0, ap, bp, 192);
  LAS char* sc = ldsp(((w & 2) ? 3 * K1_SLOT : 0) + (w & 1) * 8192 + c.lane16);
  if (w >= 4) {
#pragma unroll
    for (int g = 0; g < 8; ++g) *(LAS f32x4*)(sc + g * 1024) = acc8[g];
  }
  SCHED_FENCE();
  cv_block<true, false>(acc, a0, b0, a1, b1, ap, bp, 256);
  asm volatile("s_waitcnt lgkmcnt(0)\n\ts_barrier" ::: "memory");
  if (w < 4) {
#pragma unroll
    for (int g = 0; g < 8; ++g) *(LAS f32x4*)(sc + g * 1024) = acc8[g] + *(LAS f32x4*)(sc + g * 1024);
  }
  SCHED_FENCE();
  cv_block<true, false>(acc, a1, b1, a0, b0, ap, bp, 320);
  asm volatile("s_waitcnt lgkmcnt(0)\n\ts_barrier" ::: "memory");
  const float Dv = bflo((unsigned)__builtin_amdgcn_readlane((int)dd, 0)), Dv8 = bflo((unsigned)__builtin_amdgcn_readlane((int)dd, 1));
  const int zo = (32 * ii + 8 * q + 4 * pi) * 2;
  {
    LAS char* s0 = ldsp(w * 1024 + c.lane16);
    f32x4 y = (*(LAS f32x4*)s0 + *(LAS f32x4*)(s0 + 8192)) + (*(LAS f32x4*)(s0 + 3 * K1_SLOT) + *(LAS f32x4*)(s0 + 3 * K1_SLOT + 8192));
    __builtin_amdgcn_raw_buffer_store_b64(cv_z(y, cl8, w, lane, Dv8), c.rs, zo, WS_Z + d8 * (S_ * 2) + 512 * w, AUX_SC1);
  }
  SCHED_FENCE();
  cv_block<true, false>(acc, a0, b0, a1, b1, ap, bp, 384);
  cv_block<true, false>(acc, a1, b1, a0, b0, ap, bp, 448);
  cv_block<false, false>(acc, a0, b0, a1, b1, ap, bp, 0);
  STAMP(25);
#pragma unroll
  for (int g = 0; g < 8; ++g) *(LAS u32x2*)(cl + 512 * g + zo) = cv_z(acc[g], cl, g, lane, Dv);
#pragma unroll
  for (int i = 0; i < 4; ++i)
    __builtin_amdgcn_raw_buffer_store_b128(*(LAS u32x4*)(cl + i * 1024 + c.lane16), c.rs, c.lane16, WS_Z + d * (S_ * 2) + i * 1024, AUX_SC1);
  STAMP(26);
  full_barrier();
  if (w == 0 && (lane < 32 || (9 * cw) / 96 != (9 * cw + 8) / 96)) flag_store(c, zwg, 16 + cw - (96 * ntz) / 9, zev);
  STAMP(35);
}

template <int ROLE>
__device__ __forceinline__ void fused_body(const int w) {
  const int b = blockIdx.x, xcd = b & 7, r = b >> 3;
  const int ml = r & 3, nl = r >> 2, fq = xcd >> 1;
  const int mt = 4 * (xcd & 1) + ml;
  const int lane = __builtin_amdgcn_mbcnt_hi(~0u, __builtin_amdgcn_mbcnt_lo(~0u, 0u));
  Ctx c;
  const char* x = karg(0);
  char* ws = (char*)karg(10);
  RSTAMP(37);
  STAMP(0);
  c.w = w; c.nl = nl; c.ml = ml; c.b = b; c.ws = ws; c.mt = mt; c.fq = fq;
  c.rs = __builtin_amdgcn_make_buffer_rsrc(ws, 0, WS_END, 0x00020000);
  c.rot = (nl + 2 * ml) % 9;
  c.lane16 = lane * 16;
  c.w1 = karg(6) + (size_t)(1152 * fq) * 2;
  c.w3 = karg(7) + (size_t)(1152 * fq) * 2;
  c.xb = (const char*)x + (size_t)(256 * mt + 32 * w) * (D_ * 2);
  c.hb = WS_H + ((8 * mt + w) * 288 + 72 * fq) * 1024;
  c.kk = karg(1); c.vv = karg(2); c.sk = karg(3); c.gw = karg(9);
  c.gx = (const char*)x + (size_t)(256 * mt + 64 * fq + 8 * nl + w) * (D_ * 2);
  {
    int row = lane >> 3, pos = lane & 7;
    int sw = 2 * ((row >> 1) & 1);
    c.voff_wg = row * (F_ * 2) + ((pos ^ sw) * 16);
    int sx = lane >> 4;
    c.voff_x0 = row * (D_ * 2) + ((pos ^ sx) * 16);
    c.voff_x1 = row * (D_ * 2) + ((pos ^ (sx + 4)) * 16);
  }
  k1_issue_w<0, 0>(c); k1_issue_w<0, 1>(c);
  k1_issue_x<0, 0>(c); k1_issue_x<0, 1>(c); k1_issue_x<0, 2>(c); k1_issue_x<0, 3>(c);
  {
    int ewg = lane < 8 ? cv_wg(8 * xcd + lane) : (lane < 14 ? g2_wg(mt, lane - 8, fq) : ((lane & 60) == 16 ? g2_wg(mt, nl < 6 ? nl : 0, lane & 3) : b));
    GLD1(c.ev, (unsigned)ewg * 4, ws + WS_EPOCH);
  }
  k1_issue_w<1, 0>(c); k1_issue_w<1, 1>(c);
  {
    int gq = lane >> 4, mat = gq & 1, h = gq >> 1, q = (lane & 15) >> 2, p = lane & 3;
    int X = 2 * (q >> 1) + 4 * mat;
#pragma unroll
    for (int t = 0; t < 4; ++t) c.rd[t] = mat * 8192 + (8 * h + q) * 128 + (((2 * t) ^ X) + (p >> 1)) * 16 + (p & 1) * 8;
    int cc = lane & 31, hh = lane >> 5;
    c.xl0 = K1_XOFF + w * (K1_XD * 4096) + cc * 128 + ((hh ^ ((cc >> 1) & 7)) * 16);
  }
  bf16x8 xf[36];
  bf16x8 wf[K1_PF + 1];
  f32x16 acc[4];
  u32x4 hw[G2_WIN];
  asm volatile("s_waitcnt vmcnt(2)\n\ts_barrier" : "+v"(c.ev)::"memory");
  STAMP(1);
  k1_issue_x<1, 0>(c); k1_issue_x<1, 1>(c); k1_issue_x<1, 2>(c); k1_issue_x<1, 3>(c);
  xf[0] = k1_read_x<0, 0>(c); wf[0] = k1_read_w<0>(c);
  xf[1] = k1_read_x<0, 1>(c); wf[1] = k1_read_w<1>(c);
  xf[2] = k1_read_x<0, 2>(c); wf[2] = k1_read_w<2>(c);
  xf[3] = k1_read_x<0, 3>(c); wf[3] = k1_read_w<3>(c);
  static_assert(K1_PF == 4, "prologue reads");
  SCHED_FENCE();
  STAMP(2);
  K1Epi E;
  K1Prep P;
  k1_slot<0, K1_NS, ROLE>(c, xf, wf, acc, E, P, hw);
  STAMP(23);
  if constexpr (ROLE == 1) {
    cv_phase(c, acc[0]);
  } else {
    g2_phase(c, hw, acc[0]);
  }
}

__global__ __launch_bounds__(512, 2) void fused_kernel(const void* x, const void* kk, const void* vv, const void* shift_k, const void* ssm_k,
                                                       const void* ssm_D, const void* w1, const void* w3, const void* w2, const void* gate_w, void* ws,
                                                       void* out) {
  const int w = __builtin_amdgcn_readfirstlane(threadIdx.x >> 6);
  if (blockIdx.x >= 192) fused_body<1>(w);
  else fused_body<0>(w);
}

extern "C" void kernel_launch(void* const* d_in, const int* in_sizes, int n_in, void* d_out, int out_size, void* d_ws, size_t ws_size,
                              hipStream_t stream) {
  (void)hipFuncSetAttribute((const void*)fused_kernel, hipFuncAttributeMaxDynamicSharedMemorySize, LDS_BYTES);
  fused_kernel<<<256, 512, LDS_BYTES, stream>>>(d_in[0], d_in[1], d_in[2], d_in[3], d_in[4], d_in[5], d_in[7], d_in[8], d_in[9], d_in[10], d_ws, d_out);
}
```

```cpp
#include <hip/hip_runtime.h>
#include <hip/hip_bf16.h>

#define LAS __attribute__((address_space(3)))
#define GAS __attribute__((address_space(1)))
typedef unsigned short u16;
typedef __bf16 bf16x2 __attribute__((ext_vector_type(2)));
typedef __bf16 bf16x4 __attribute__((ext_vector_type(4)));
typedef __bf16 bf16x8 __attribute__((ext_vector_type(8)));
typedef float f32x2 __attribute__((ext_vector_type(2)));
typedef float f32x4 __attribute__((ext_vector_type(4)));
typedef float f32x16 __attribute__((ext_vector_type(16)));
typedef unsigned u32x2 __attribute__((ext_vector_type(2)));
typedef unsigned u32x4 __attribute__((ext_vector_type(4)));

constexpr int S_ = 2048, D_ = 576, F_ = 4608;
constexpr unsigned WS_H = 0;
constexpr unsigned WS_PART = WS_H + 64 * 288 * 1024;
constexpr unsigned WS_UR = WS_PART + 256 * 49152;
constexpr unsigned WS_Z = WS_UR + D_ * 2 * S_ * 2;
constexpr unsigned WS_G = WS_Z + D_ * S_ * 2;
constexpr unsigned WS_FLAG = WS_G + S_ * 4;
constexpr unsigned WS_EPOCH = WS_FLAG + 256 * 512;
constexpr unsigned WS_END = WS_EPOCH + 1024;
constexpr unsigned FLAG_MAGIC = 0x7c3a91e5u, SENT = 0xffffffffu;
constexpr int AUX_SC1 = 16;
constexpr unsigned OOB = 0x7ff00000u;
constexpr unsigned SPIN_CAP = 1u << 16;

#define STAMP(i)
#define RSTAMP(i)
#define STDUMP()

__device__ __forceinline__ float bflo(unsigned v) { return __builtin_bit_cast(float, v << 16); }
__device__ __forceinline__ float bfhi(unsigned v) { return __builtin_bit_cast(float, v & 0xffff0000u); }
__device__ __forceinline__ float rbf(float x) { return (float)(__bf16)x; }
__device__ __forceinline__ unsigned pk2(float lo, float hi) {
  f32x2 v = {lo, hi};
  return __builtin_bit_cast(unsigned, __builtin_convertvector(v, bf16x2));
}
__device__ __forceinline__ f32x2 unpk2(unsigned v) { return (f32x2){bflo(v), bfhi(v)}; }
__device__ __forceinline__ float radd(float a, float b) { const unsigned t = pk2(a, b); return bflo(t) + bfhi(t); }
#define SCHED_FENCE() __builtin_amdgcn_sched_barrier(0)

template <int LC, int SC> __device__ __forceinline__ void dma16(unsigned voff, const u32x4& srd, unsigned so, unsigned lb) {
  unsigned t;
  asm volatile("s_add_u32 m0, %4, %5\n\ts_add_u32 %0, %3, %6\n\tbuffer_load_dwordx4 %1, %2, %0 offen lds"
               : "=&s"(t) : "v"(voff), "s"(srd), "s"(so), "s"(lb), "n"(LC), "n"(SC) : "memory", "scc");
}
template <int N> __device__ __forceinline__ void wait_vm_barrier() {
  asm volatile("s_waitcnt vmcnt(%0)\n\ts_barrier" ::"n"(N) : "memory");
}
template <int N> __device__ __forceinline__ void wait_vm() { asm volatile("s_waitcnt vmcnt(%0)" ::"n"(N) : "memory"); }
__device__ __forceinline__ void lds_barrier() { asm volatile("s_waitcnt lgkmcnt(0)\n\ts_barrier" ::: "memory"); }
__device__ __forceinline__ LAS char* ldsp(unsigned a) { return (LAS char*)(size_t)a; }
__device__ __forceinline__ bf16x8 tr_frag(LAS char* p0, LAS char* p1) {
  bf16x4 a = __builtin_amdgcn_ds_read_tr16_b64_v4bf16((LAS bf16x4*)p0);
  bf16x4 b = __builtin_amdgcn_ds_read_tr16_b64_v4bf16((LAS bf16x4*)p1);
  return __builtin_shufflevector(a, b, 0, 1, 2, 3, 4, 5, 6, 7);
}
constexpr int cmin(int a, int b) { return a < b ? a : b; }
__device__ __forceinline__ const char* karg(int i) {
  typedef const char* cptr;
  const __attribute__((address_space(4))) cptr* p = (const __attribute__((address_space(4))) cptr*)__builtin_amdgcn_kernarg_segment_ptr();
  asm volatile("" : "+s"(p));
  return p[i];
}
__device__ __forceinline__ u32x4 make_srd(const char* base, unsigned off, unsigned total) {
  const unsigned long long a = (unsigned long long)base + off;
  return (u32x4){(unsigned)a, (unsigned)(a >> 32) & 0xffffu, total - off, 0x00020000u};
}

constexpr int K1_SLOT = 16384, K1_XOFF = 65536, K1_XD = 2, K1_POFF = 131072, K1_PV = 10240;
constexpr int LDS_BYTES = 163840;
constexpr int K1_PF = 4, K1_RD = 3, K1_NS = 324, K1_NSEG = 21;
constexpr int K1_PREP0 = 28, K1_PREP_UNITS = 8 + 2 + 63 + 1 + 4;
constexpr int G2_SLOT = 24576, G2_NSLOT = 4, G2_WIN = 16, G2_PF = 4, G2_NS = 216;
constexpr int CV_K = 0, CV_U0 = 4160, CV_U1 = CV_U0 + 4640, CV_CH = 13440;
constexpr int k1_wbase(int s) { return s == 11 ? K1_XOFF : (s == 12 ? K1_XOFF + K1_SLOT : ((s < 11 ? s : s - 2) % 4) * K1_SLOT); }
constexpr int k1_wprev(int s) { return s < 4 || s == 11 || s == 12 ? -1 : (s < 11 ? s - 4 : (s < 17 ? s - 6 : s - 4)); }
constexpr int EV_PUB_U = 140;
constexpr int EV_PUB_A = 204;
constexpr int EV_PRE0 = 221, EV_PRE_STEP = 4;
constexpr int EV_CV_POLL = 176, EV_CV_CHECK = 190, EV_CV_INVW = 219;
constexpr int EV_CV_PRE7 = 300;
constexpr int EV_CV_PRE8 = 316;
constexpr int EV_SENT0 = 177, EV_SENT_STEP = 7, EV_PUB_S = 236;
constexpr int EV_G2_POLL = 266, EV_G2_CHECK = 283, EV_G2_TOUCH = 285, EV_G2_H0 = 289;
constexpr int WARM_N = 10;
constexpr int WARM_SLOT[WARM_N] = {18, 34, 50, 66, 82, 98, 118, 142, 158, 178};
constexpr int WARM_DONE = 219;
constexpr int K1_EPI0[2] = {125, 277};
constexpr int k1_epi_slot(int ch, int e) { return K1_EPI0[ch] + (ch == 0 ? e - e / 12 : e); }

struct K1Sched {
  int seg[K1_NS], tile[K1_NS], j[K1_NS];
  int first_use[K1_NSEG], last_use[K1_NSEG];
  int wissue[K1_NSEG];
  int xissue[9];
  bool bar[K1_NS];
  int ops[K1_NS];
  int wait[K1_NS];
  int done[K1_NS];
  constexpr int ops_after(int a, int b) const { int k = 0; for (int q = a + 1; q <= b; ++q) k += ops[q]; return k; }
};
constexpr K1Sched k1_make(int role) {
  K1Sched S{};
  int n = 0;
  for (int s = 0; s < 18;) {
    if (s == 7 || s == 9) {
      const int tt[8] = {0, 1, 0, 1, 2, 2, 3, 3}, hh[8] = {0, 0, 1, 1, 0, 1, 0, 1};
      for (int q = 0; q < 8; ++q) for (int j = 0; j < 4; ++j) { S.seg[n] = s + hh[q]; S.tile[n] = tt[q]; S.j[n] = j; ++n; }
      s += 2;
    } else {
      for (int t = 0; t < 4; ++t) for (int j = 0; j < 4; ++j) { S.seg[n] = s; S.tile[n] = t; S.j[n] = j; ++n; }
      s += 1;
    }
  }
  for (int i = 0; i < 36; ++i) { S.seg[n] = 18 + i / 16; S.tile[n] = 0; S.j[n] = i % 16; ++n; }
  for (int s = 0; s < K1_NSEG; ++s) { S.first_use[s] = 100000; S.last_use[s] = -1; }
  for (int i = 0; i < K1_NS; ++i) { int s = S.seg[i]; if (i < S.first_use[s]) S.first_use[s] = i; if (i > S.last_use[s]) S.last_use[s] = i; }
  for (int s = 1; s < K1_NSEG; ++s) S.bar[S.first_use[s] - K1_PF - 1] = true;
  S.wissue[0] = S.wissue[1] = -1;
  S.wissue[2] = 0; S.wissue[3] = 12;
  int busy = 0;
  for (int s = 4; s < K1_NSEG; ++s) {
    int q = k1_wprev(s) >= 0 ? S.last_use[k1_wprev(s)] : S.first_use[8] + 4;
    while (!S.bar[q]) ++q;
    int is = q + 1;
    if (is < busy) is = busy;
    S.wissue[s] = is;
    busy = is + (s >= 11 ? 8 : 4);
  }
  S.xissue[0] = S.xissue[1] = -1;
  for (int p = 2; p < 9; ++p) S.xissue[p] = S.first_use[p - 2] + 4;
  for (int s = 0; s < K1_NSEG; ++s) if (S.wissue[s] >= 0) { S.ops[S.wissue[s]]++; S.ops[S.wissue[s] + 2]++; }
  for (int p = 0; p < 9; ++p) if (S.xissue[p] >= 0) for (int i = 0; i < 4; ++i) S.ops[S.xissue[p] + i]++;
  S.ops[K1_PREP0 + 7]++;
  for (int i = 0; i < WARM_N; ++i) S.ops[WARM_SLOT[i]]++;
  for (int i = 0; i < 4; ++i) S.ops[K1_PREP0 + K1_PREP_UNITS - 4 + i]++;
  for (int c = 0; c < 2; ++c) for (int t = 0; t < 4; ++t) if (k1_epi_slot(c, 12 * t + 13) < K1_NS) S.ops[k1_epi_slot(c, 12 * t + 13)]++;
  if (role == 0) {
    for (int i = 0; i < G2_WIN; ++i) S.ops[EV_G2_H0 + 2 * i]++;
    S.ops[EV_G2_POLL]++;
    S.ops[EV_G2_TOUCH]++;
    for (int i = 0; i < 6; ++i) S.ops[EV_SENT0 + EV_SENT_STEP * i]++;
  } else {
    S.ops[EV_CV_POLL]++;
    S.ops[EV_CV_PRE8]++; S.ops[EV_CV_PRE8 + 1]++;
  }
  if (role != 2) for (int i = 0; i < 12; ++i) S.ops[EV_PRE0 + EV_PRE_STEP * i]++;
  if (role == 2) for (int i = 0; i < 12; ++i) S.ops[EV_CV_PRE7 + i]++;
  for (int i = 0; i < K1_NS; ++i) {
    S.wait[i] = 63;
    S.done[i] = -2;
    if (!S.bar[i]) continue;
    int youngest = -2;
    for (int s = 1; s < K1_NSEG; ++s)
      if (S.first_use[s] - K1_PF - 1 == i) {
        int y = S.wissue[s] < 0 ? -1 : S.wissue[s] + 2;
        if (s < 9) { int yx = S.xissue[s] < 0 ? -1 : S.xissue[s] + 3; if (yx > y) y = yx; }
        if (y > youngest) youngest = y;
      }
    if (i == EV_PUB_A - 1 && youngest < k1_epi_slot(0, 49)) youngest = k1_epi_slot(0, 49);
    if (i == EV_PUB_S - 1 && youngest < EV_SENT0 + 5 * EV_SENT_STEP) youngest = EV_SENT0 + 5 * EV_SENT_STEP;
    if (i == WARM_DONE && youngest < WARM_SLOT[WARM_N - 1]) youngest = WARM_SLOT[WARM_N - 1];
    if (i == 299 && youngest < EV_PRE0 + 11 * EV_PRE_STEP) youngest = EV_PRE0 + 11 * EV_PRE_STEP;
    S.wait[i] = cmin(63, S.ops_after(youngest, i));
    S.done[i] = youngest;
  }
  return S;
}
constexpr K1Sched K1S[3] = {k1_make(0), k1_make(1), k1_make(2)};
static_assert(K1_PREP0 + K1_PREP_UNITS - 1 <= K1S[0].done[EV_PUB_U - 1] && K1S[0].bar[EV_PUB_U - 1], "u stores complete before publish");
static_assert(K1S[0].bar[EV_PUB_A - 1] && K1S[0].bar[EV_PUB_S - 1] && K1S[0].bar[299] && K1S[0].bar[315], "events");
static_assert(K1S[0].bar[EV_G2_CHECK] && K1S[0].bar[EV_CV_INVW] && K1S[0].bar[EV_CV_PRE7 - 1] && K1S[0].last_use[17] < EV_CV_PRE7, "events");
static_assert(K1S[0].bar[EV_CV_PRE8 - 1] && K1S[0].last_use[18] < EV_CV_PRE8 && EV_G2_H0 + 2 * G2_WIN <= K1_NS + 1, "events");
static_assert(EV_PRE0 > EV_CV_INVW && K1S[0].last_use[12] <= EV_CV_INVW && K1S[0].done[315] >= EV_G2_TOUCH, "events");
static_assert([] {
  for (int t = 0; t < 4; ++t) {
    int last = 0, next = 1000;
    for (int i = 0; i < 144; ++i) if (K1S[0].tile[i] == t) last = i;
    for (int i = 287; i >= 144; --i) if (K1S[0].tile[i] == t) next = i;
    if (k1_epi_slot(0, 12 * t) < last + 2 || k1_epi_slot(0, 12 * t + 9) >= next) return false;
  }
  return true;
}(), "epilogue windows");

struct Ctx {
  u32x4 sw1, sw3, sx, s2, s3, sws;
  __amdgpu_buffer_rsrc_t rs;
  unsigned wo[3], xo[3];
  unsigned hb;
  unsigned voff_w, voff_x;
  unsigned rd[4];
  unsigned xl0, lane16;
  unsigned ev;
  u32x2 poll;
  unsigned touch, warm;
  unsigned rc0, rc1;
  int w, rot, nl, ml, b, mt, fq;
};
struct K1Prep {
  u32x4 sk, ga0, ga1, gw0, gw1;
  unsigned lp, kn, vn, o0;
  f32x2 s[4], k2[4], a, p, q; unsigned u[9]; float gs;
};
struct K1Epi { float m[2], p[2], pq[2], r[2], h[2]; u32x4 ho; };
struct CvRegs { unsigned ap[4], bp; bf16x8 a0[8], b0[8]; };

__device__ __forceinline__ int lane_of(const Ctx& c) {
  unsigned l = c.lane16;
  asm volatile("" : "+v"(l));
  return (int)(l >> 4);
}
__device__ __forceinline__ u32x4 sent4() {
  u32x4 v = {SENT, SENT, SENT, SENT};
  asm volatile("" : "+v"(v));
  return v;
}
__device__ __forceinline__ u32x2 sent2() {
  u32x2 v = {SENT, SENT};
  asm volatile("" : "+v"(v));
  return v;
}
__device__ __forceinline__ u32x4 zero4() {
  u32x4 v = {0u, 0u, 0u, 0u};
  asm volatile("" : "+v"(v));
  return v;
}
__device__ __forceinline__ int g2_wg(int mt, int nt, int ks) { return ((nt * 4 + (mt & 3)) << 3) | (ks * 2 + (mt >> 2)); }
__device__ __forceinline__ int cv_wg(int cw) { return ((24 + (cw & 7)) << 3) | (cw >> 3); }
__device__ __forceinline__ int cv_id(const Ctx& c) { return (c.b & 7) * 8 + (c.b >> 3) - 24; }
__device__ __forceinline__ void flag_store(const Ctx& c, bool act, int wg, int slot, unsigned epoch) {
  asm volatile("" : "+v"(epoch));
  __builtin_amdgcn_raw_buffer_store_b64((u32x2){epoch + 1u, FLAG_MAGIC}, c.rs, act ? wg * 512 + slot * 8 : OOB, WS_FLAG, AUX_SC1);
}
__device__ __forceinline__ void publish_h(const Ctx& c, int slot0) {
  const int l = lane_of(c);
  flag_store(c, l >= 8 && l < 14, g2_wg(c.mt, l - 8, c.fq), slot0 + c.nl, c.ev);
}
__device__ __forceinline__ void poll_issue(Ctx& c) {
  const unsigned vo = lane_of(c) * 8;
  const unsigned so = WS_FLAG + c.b * 512;
  asm volatile("buffer_load_dwordx2 %0, %1, %2, %3 offen sc1" : "=v"(c.poll) : "v"(vo), "s"(c.sws), "s"(so) : "memory");
}
__device__ __forceinline__ void poll_wait(Ctx& c, bool need) {
  const unsigned want = (unsigned)__builtin_amdgcn_readlane((int)c.ev, 14) + 1u;
  u32x2 v = c.poll;
#pragma unroll 1
  for (unsigned spin = 0; spin < SPIN_CAP; ++spin) {
    if (__all(!need || (v[0] == want && v[1] == FLAG_MAGIC))) break;
    __builtin_amdgcn_s_sleep(1);
    asm volatile("" ::: "memory");
    v = __builtin_amdgcn_raw_buffer_load_b64(c.rs, lane_of(c) * 8, WS_FLAG + c.b * 512, AUX_SC1);
  }
  asm volatile("" ::: "memory");
}
#define POLL_LANDED(N) asm volatile("s_waitcnt vmcnt(%1)" : "+v"(c.poll) : "n"(N) : "memory")

__device__ __forceinline__ int kslice(int p, int r) {
  const int g = (p >= 6) ? 6 : (p >= 3 ? 3 : 0);
  const int v = p - g + r;
  return g + (v >= 3 ? v - 3 : v);
}
template <int sg, int half> __device__ __forceinline__ void k1_issue_w(const Ctx& c) {
  constexpr int dst = k1_wbase(sg) + half * 8192;
  if constexpr (sg < 18) {
    constexpr int p = sg % 9;
    dma16<dst, (p / 3 * 3) * 64 * F_ * 2 + 128 * (sg / 9)>(half ? (c.voff_w ^ 64u) : c.voff_w, half ? c.sw3 : c.sw1, c.wo[p % 3], c.w * 1024);
  } else {
    const unsigned l = lane_of(c);
    const unsigned rp = l >> 1;
    unsigned vo = (half ? (rp ^ 4u) : rp) * (F_ * 2) + (l & 1u) * 16;
    const int col = (1024 + 16 * c.nl - 128 * c.nl) * 2;
    if constexpr (sg < 20) {
      const int ksl = kslice(4 * (sg - 18) + (c.w >> 1), c.rot);
      dma16<dst, 0>(vo, half ? c.sw3 : c.sw1, (64 * ksl + 32 * (c.w & 1)) * (F_ * 2) + col, c.w * 1024);
    } else {
      if (l >= 16) vo = OOB;
      dma16<dst, 6 * 64 * F_ * 2>(vo, half ? c.sw3 : c.sw1, c.wo[2] + col + 8 * c.w * (F_ * 2), c.w * 1024);
    }
  }
}
template <int p, int i> __device__ __forceinline__ void k1_issue_x(const Ctx& c) {
  dma16<K1_XOFF + (p % K1_XD) * 4096 + i * 1024, 8 * i * D_ * 2 + (p / 3 * 3) * 128>((i & 1) ? (c.voff_x ^ 64u) : c.voff_x, c.sx, c.xo[p % 3],
                                                                                     c.w * (K1_XD * 4096));
}
template <int p, int j> __device__ __forceinline__ bf16x8 k1_read_x(const Ctx& c) {
  return *(LAS bf16x8*)(ldsp(c.xl0 ^ (32 * j)) + (p % K1_XD) * 4096);
}
template <int n> __device__ __forceinline__ bf16x8 k1_read_w(Ctx& c) {
  constexpr int sg = K1S[0].seg[n], t = K1S[0].tile[n], j = K1S[0].j[n];
  if constexpr (sg < 18) {
    if constexpr ((sg == 11 || sg == 13) && j == 0) {
      c.rd[t] += sg == 11 ? K1_XOFF : -K1_XOFF;
      asm volatile("" : "+v"(c.rd[t]));
    }
    LAS char* p = ldsp(c.rd[t]) + k1_wbase(sg) % K1_XOFF + (16 * j) * 128;
    return tr_frag(p, p + 4 * 128);
  } else if constexpr (sg < 20) {
    const int off = k1_wbase(sg) + (16 * j) * 32;
    return tr_frag(ldsp(c.rc0) + off, ldsp(c.rc1) + off);
  } else {
    const unsigned ex = (lane_of(c) >> 5) * 768u;
    const int off = k1_wbase(sg) + j * 2048;
    return tr_frag(ldsp(c.rc0 + ex) + off, ldsp(c.rc1 + ex) + off);
  }
}
#define GLD1(dst, voff, sb) asm volatile("global_load_dword %0, %1, %2" : "=v"(dst) : "v"(voff), "s"(sb) : "memory")
#define GLD4(dst, voff, sb) asm volatile("global_load_dwordx4 %0, %1, %2" : "=v"(dst) : "v"(voff), "s"(sb) : "memory")
template <int ctrl> __device__ __forceinline__ float dpp_add(float v) {
  return v + __builtin_bit_cast(float, __builtin_amdgcn_update_dpp(0, __builtin_bit_cast(int, v), ctrl, 0xf, 0xf, true));
}
__device__ __forceinline__ float rb1(float x) { return __builtin_bit_cast(float, pk2(0.f, x)); }
__device__ __forceinline__ f32x2 rb2(f32x2 v) { return (f32x2){rb1(v[0]), rb1(v[1])}; }
template <int k> __device__ __forceinline__ void k1_prep_unit(const Ctx& c, K1Prep& P) {
  if constexpr (k == 0) {
    asm volatile("" : "+v"(P.sk), "+v"(P.ga0), "+v"(P.ga1), "+v"(P.gw0), "+v"(P.gw1)::"memory");
    P.gs = 0.f;
  }
  if constexpr (k < 4) {
    const u32x4 &a = k < 2 ? P.ga1 : P.ga0, &g = k < 2 ? P.gw1 : P.gw0;
#pragma unroll
    for (int i = 2 * (k & 1); i < 2 * (k & 1) + 2; ++i) P.gs = __builtin_fmaf(bfhi(a[i]), bfhi(g[i]), __builtin_fmaf(bflo(a[i]), bflo(g[i]), P.gs));
    if constexpr (k == 1) P.gs = lane_of(c) < 8 ? P.gs : 0.f;
    asm volatile("" : "+v"(P.gs));
  } else if constexpr (k == 4) {
    P.gs = dpp_add<0x111>(P.gs); P.gs = dpp_add<0x112>(P.gs);
    P.u[0] = *(LAS unsigned*)(ldsp(P.lp)); P.u[1] = *(LAS unsigned*)(ldsp(P.lp) + 144);
  } else if constexpr (k == 5) {
    P.gs = dpp_add<0x114>(P.gs); P.gs = dpp_add<0x118>(P.gs);
    P.u[2] = *(LAS unsigned*)(ldsp(P.lp) + 288); P.kn = *(LAS unsigned*)(ldsp(P.lp) + 432);
  } else if constexpr (k == 6) {
    int v = __builtin_bit_cast(int, P.gs);
    float a = __builtin_bit_cast(float, __builtin_amdgcn_readlane(v, 15)), b = __builtin_bit_cast(float, __builtin_amdgcn_readlane(v, 31));
    float d = __builtin_bit_cast(float, __builtin_amdgcn_readlane(v, 47)), e = __builtin_bit_cast(float, __builtin_amdgcn_readlane(v, 63));
    P.gs = __builtin_amdgcn_exp2f(rb1((a + b) + (d + e)) * -1.44269504088896f);
  } else if constexpr (k == 7) {
    float g = rb1(__builtin_amdgcn_rcpf(1.0f + P.gs));
    __builtin_amdgcn_raw_buffer_store_b32(__builtin_bit_cast(unsigned, g), c.rs, lane_of(c) == 0 ? 0u : OOB,
                                          WS_G + (256 * c.mt + 64 * c.fq + 8 * c.nl + c.w) * 4, AUX_SC1);
    P.s[0] = (f32x2){bflo(P.sk[0]), bflo(P.sk[2])};
  } else if constexpr (k == 8) {
    P.s[1] = (f32x2){bfhi(P.sk[0]), bfhi(P.sk[2])};
    P.s[2] = (f32x2){bflo(P.sk[1]), bflo(P.sk[3])}; P.s[3] = (f32x2){bfhi(P.sk[1]), bfhi(P.sk[3])};
    asm volatile("" : "+v"(P.s[0]), "+v"(P.s[1]), "+v"(P.s[2]), "+v"(P.s[3]));
  } else if constexpr (k == 9) {
    P.k2[0] = unpk2(P.u[0]); P.k2[1] = unpk2(P.u[1]); P.k2[2] = unpk2(P.u[2]);
    asm volatile("" : "+v"(P.k2[0]), "+v"(P.k2[1]), "+v"(P.k2[2]));
  } else if constexpr (k < 73) {
    constexpr int i = (k - 10) / 7, st = (k - 10) % 7;
    auto mul = [](f32x2 x, f32x2 y) { f32x2 r = {x[0] * y[0], x[1] * y[1]}; return r; };
    if constexpr (st == 0) { P.a = mul(P.k2[i & 3], P.s[3]); P.p = mul(P.k2[(i + 1) & 3], P.s[2]); P.vn = *(LAS unsigned*)(ldsp(P.lp) + K1_PV + 144 * i); }
    else if constexpr (st == 1) { P.a = rb2(P.a); P.p = rb2(P.p); }
    else if constexpr (st == 2) { P.a += P.p; P.p = mul(P.k2[(i + 2) & 3], P.s[1]); P.k2[(i + 3) & 3] = unpk2(P.kn); }
    else if constexpr (st == 3) {
      P.a = rb2(P.a); P.p = rb2(P.p);
      if constexpr (i < 8) P.kn = *(LAS unsigned*)(ldsp(P.lp) + 144 * (i + 4));
    }
    else if constexpr (st == 4) { P.a += P.p; P.p = mul(P.k2[(i + 3) & 3], P.s[0]); P.q = unpk2(P.vn); }
    else if constexpr (st == 5) { P.a = rb2(P.a); P.p = rb2(P.p); }
    else { P.a = mul(rb2(P.a + P.p), P.q); P.u[i] = pk2(P.a[0], P.a[1]); }
    if constexpr (st == 6) asm volatile("" : "+v"(P.u[i]));
    else if constexpr (st == 2) asm volatile("" : "+v"(P.a), "+v"(P.p), "+v"(P.k2[(i + 3) & 3]));
    else if constexpr (st == 4) asm volatile("" : "+v"(P.a), "+v"(P.p), "+v"(P.q));
    else asm volatile("" : "+v"(P.a), "+v"(P.p));
  } else if constexpr (k == 73) {
    const int l = lane_of(c);
    const int dp = 8 * c.w + (l >> 3);
    P.o0 = dp < 36 ? (unsigned)((72 * (c.b & 7) + 2 * dp) * 2 * S_ + (1984 - 64 * (c.b >> 3)) + 8 * (l & 7)) * 2 : OOB;
  } else {
    constexpr int ch = (k - 74) >> 1, cp = (k - 74) & 1;
    constexpr unsigned sel = ch ? 0x07060302u : 0x05040100u;
    u32x4 o;
#pragma unroll
    for (int e = 0; e < 4; ++e) o[e] = __builtin_amdgcn_perm(P.u[7 + cp - 2 * e - 1], P.u[7 + cp - 2 * e], sel);
    __builtin_amdgcn_raw_buffer_store_b128(o, c.rs, P.o0, WS_UR + (ch * 2 + cp) * S_ * 2, AUX_SC1);
  }
}
template <int NT, int e> __device__ __forceinline__ void k1_epi_step(const Ctx& c, const f32x16* acc, K1Epi& E, int ft0) {
  constexpr int pr = e / 3, st = e % 3, t = pr / 4, i = 2 * (pr % 4);
  if constexpr (st == 0) {
    if constexpr (pr > 0) { E.h[0] = E.pq[0] * E.r[0]; E.h[1] = E.pq[1] * E.r[1]; }
    if constexpr (pr < 4 * NT) {
      E.m[0] = acc[t][i] * -1.44269504088896f; E.m[1] = acc[t][i + 1] * -1.44269504088896f;
      E.p[0] = acc[t][i] * acc[t][i + 8]; E.p[1] = acc[t][i + 1] * acc[t][i + 9];
      asm volatile("" : "+v"(E.p[0]), "+v"(E.p[1]));
    }
  } else if constexpr (st == 1) {
    if constexpr (pr < 4 * NT) { E.m[0] = __builtin_amdgcn_exp2f(E.m[0]); E.m[1] = __builtin_amdgcn_exp2f(E.m[1]); }
    if constexpr (pr > 0) {
      E.ho[(pr - 1) % 4] = pk2(E.h[0], E.h[1]);
      if constexpr (pr % 4 == 0) __builtin_amdgcn_raw_buffer_store_b128(E.ho, c.rs, c.lane16, c.hb + (ft0 + (pr - 1) / 4) * 1024, AUX_SC1);
    }
  } else {
    if constexpr (pr < 4 * NT) {
      float d0 = 1.0f + E.m[0], d1 = 1.0f + E.m[1];
      E.r[0] = __builtin_amdgcn_rcpf(d0); E.r[1] = __builtin_amdgcn_rcpf(d1);
      E.pq[0] = E.p[0]; E.pq[1] = E.p[1];
    }
  }
}

template <int ch, int n, int e = 0> __device__ __forceinline__ void k1_epi_at(const Ctx& c, const f32x16* acc, K1Epi& E) {
  if constexpr (n >= K1_EPI0[ch] && n <= k1_epi_slot(ch, 49)) {
    if constexpr (k1_epi_slot(ch, e) == n) k1_epi_step<4, e>(c, acc, E, 8 * c.nl + 4 * ch);
    if constexpr (e < 49 && k1_epi_slot(ch, e < 49 ? e + 1 : e) <= n) k1_epi_at<ch, n, e + 1>(c, acc, E);
  }
}
template <int i> __device__ __forceinline__ void warm_touch(Ctx& c, const u32x4& sw2, const u32x4& ssk) {
  constexpr unsigned long long m = i < 4 ? (0x3full << (6 * (i >> 1))) : (i < 8 ? (0x3full << (16 + 6 * (i - 4))) : (0x1full << (48 + 5 * (i - 8))));
  const u32x4& srd = i < 4 ? ((i & 1) ? c.sw3 : c.sw1) : (i < 8 ? sw2 : ssk);
  asm volatile("s_mov_b32 exec_lo, %3\n\ts_mov_b32 exec_hi, %4\n\tbuffer_load_dword %0, %1, %2, 0 offen\n\ts_mov_b64 exec, -1"
               : "+v"(c.touch) : "v"(c.warm), "s"(srd), "n"((int)(unsigned)m), "n"((int)(unsigned)(m >> 32)) : "memory");
}
template <int s0, int ns> __device__ __forceinline__ void g2_touch(Ctx& c) {
  constexpr int per = (64 * ns + 5) / 6;
  const int l = lane_of(c);
  const int i = 64 * (s0 & 3) + per * c.nl + l;
  const unsigned vo = (l < per && i < 64 * ((s0 & 3) + ns)) ? (unsigned)(((i >> 5) * 8 + ((i >> 3) & 3)) * 1024 + (i & 7) * 128) : OOB;
  const unsigned so = c.hb + (s0 & 4) * 1024;
  asm volatile("buffer_load_dword %0, %1, %2, %3 offen sc1" : "=v"(c.touch) : "v"(vo), "s"(c.sws), "s"(so) : "memory");
}

__device__ __forceinline__ int g2_ft(const Ctx& c, int s, int i8) {
  return s >= 8 ? 64 + i8 : 16 * (s & 3) + 8 * (i8 >> 2) + (i8 & 3) + (s & 4);
}
template <int s, int i> __device__ __forceinline__ void g2_issue_w(const Ctx& c) {
  const unsigned n16 = 64u * i + (unsigned)lane_of(c);
  const unsigned vo = ((n16 * 171u) >> 11) * (D_ * 2 - 192) + n16 * 16u;
  dma16<K1_XOFF + (s % G2_NSLOT) * G2_SLOT + i * 1024, 0>(vo, c.s2, 16 * g2_ft(c, s, c.w) * (D_ * 2) + 192 * c.nl, c.w * 3072);
}
template <int i> __device__ __forceinline__ u32x4 g2_load_h(const Ctx& c) {
  return __builtin_amdgcn_raw_buffer_load_b128(c.rs, c.lane16, c.hb + g2_ft(c, i / 8, i % 8) * 1024, AUX_SC1);
}
__device__ __forceinline__ unsigned g2_out_base(const Ctx& c) {
  return WS_PART + g2_wg(c.mt, c.nl, c.w >> 1) * 49152 + (c.fq * 2 + (c.w & 1)) * 6144;
}
__device__ __forceinline__ unsigned cv_base(int ch) { return ch < 7 ? K1_XOFF + ch * CV_CH : (ch == 7 ? k1_wbase(17) : k1_wbase(18)); }
template <int i> __device__ __forceinline__ void cv_issue(const Ctx& c, int ch) {
  const int d = 9 * cv_id(c) + ch;
  if constexpr (i < 4) dma16<CV_K + 64 + i * 1024, i * 1024>(c.lane16, c.s2, d * (S_ * 2), cv_base(ch));
  else dma16<(i < 8 ? CV_U0 + (i - 4) * 1024 : CV_U1 + (i - 8) * 1024), WS_UR + (i - 4) * 1024>(c.lane16, c.sws, d * (4 * S_), cv_base(ch));
}
typedef unsigned u32x2a4 __attribute__((ext_vector_type(2), aligned(4)));
__device__ __forceinline__ bf16x8 cv_read_a(LAS char* p) {
  u32x2 lo = *(LAS u32x2a4*)p;
  u32x2 hi = *(LAS u32x2a4*)(p + 8);
  u32x4 v = {lo[0], lo[1], hi[0], hi[1]};
  return __builtin_bit_cast(bf16x8, v);
}
__device__ __forceinline__ int cv_zwg(const Ctx& c, int lane, int& slot) {
  const int cw = cv_id(c);
  const int n0 = (9 * cw) / 96, n1 = (9 * cw + 8) / 96;
  const int s0 = 16 + cw - (96 * n0) / 9, s1 = n1 != n0 ? 16 + cw - (96 * n1) / 9 : -1;
  slot = lane < 32 ? s0 : s1;
  return g2_wg((lane >> 2) & 7, lane < 32 ? n0 : n1, lane & 3);
}

template <int n, int ROLE>
__device__ __forceinline__ void k1_slot(Ctx& c, bf16x8 (&xf)[36], bf16x8 (&wf)[K1_RD + 1], f32x16 (&acc)[4], K1Epi& E, K1Prep& P, u32x4 (&hw)[G2_WIN],
                                        CvRegs& V) {
  constexpr const K1Sched& S = K1S[ROLE];
  constexpr int sg = S.seg[n];
  if constexpr (n + K1_RD == 288) {
    const unsigned l = lane_of(c);
    unsigned mat = (l >> 4) & 1, h = l >> 5, q = (l & 15) >> 2, p = l & 3;
    unsigned base = mat * 8192 + (8 * h + q) * 32 + p * 8;
    c.rc0 = base + (mat ? 128 : 0);
    c.rc1 = base + (mat ? 0 : 128);
  }
  if constexpr (n + K1_RD < K1_NS) wf[(n + K1_RD) % (K1_RD + 1)] = k1_read_w<n + K1_RD>(c);
  SCHED_FENCE();
  if constexpr (n + 4 < 144) {
    constexpr int sn = S.seg[n + 4];
    if constexpr (S.tile[n + 4] == 0 && sn >= 1) xf[4 * sn + S.j[n + 4]] = k1_read_x<sn, S.j[n + 4]>(c);
  }
  {
    constexpr int t = S.tile[n];
    constexpr int xi = sg < 18 ? 4 * (sg % 9) + S.j[n] : n - 288;
    constexpr bool first = (sg < 18) ? (sg % 9 == 0 && S.j[n] == 0) : (n == 288);
    if constexpr (first) acc[t] = __builtin_amdgcn_mfma_f32_32x32x16_bf16(wf[n % (K1_RD + 1)], xf[xi], (f32x16){}, 0, 0, 0);
    else acc[t] = __builtin_amdgcn_mfma_f32_32x32x16_bf16(wf[n % (K1_RD + 1)], xf[xi], acc[t], 0, 0, 0);
  }
  SCHED_FENCE();
  {
    constexpr int ws0 = [] { for (int s = 0; s < K1_NSEG; ++s) if (K1S[0].wissue[s] == n) return s; return -1; }();
    constexpr int ws1 = [] { for (int s = 0; s < K1_NSEG; ++s) if (K1S[0].wissue[s] >= 0 && K1S[0].wissue[s] + 2 == n) return s; return -1; }();
    constexpr int xs = [] { for (int p = 0; p < 9; ++p) if (K1S[0].xissue[p] >= 0 && n >= K1S[0].xissue[p] && n < K1S[0].xissue[p] + 4) return p; return -1; }();
    if constexpr (ws0 >= 0) k1_issue_w<ws0, 0>(c);
    if constexpr (ws1 >= 0) k1_issue_w<ws1, 1>(c);
    if constexpr (xs >= 0) k1_issue_x<xs, n - S.xissue[xs]>(c);
  }
  if constexpr (n >= K1_PREP0 && n < K1_PREP0 + K1_PREP_UNITS) k1_prep_unit<n - K1_PREP0>(c, P);
  k1_epi_at<0, n>(c, acc, E);
  k1_epi_at<1, n>(c, acc, E);
  {
    constexpr int wi = [] { for (int i = 0; i < WARM_N; ++i) if (WARM_SLOT[i] == n) return i; return -1; }();
    if constexpr (wi >= 0) warm_touch<wi>(c, ROLE == 0 ? c.s2 : c.s3, ROLE == 0 ? c.s3 : c.s2);
    if constexpr (n == WARM_DONE + 1) asm volatile("" : "+v"(c.touch)::"memory");
  }
  if constexpr (n == EV_PUB_U) {
    if (c.w == 1) { const int l = lane_of(c); flag_store(c, l < 8, cv_wg(8 * (c.b & 7) + l), c.b >> 3, c.ev); }
  }
  if constexpr (n == EV_PUB_A) {
    if (c.w == 1) publish_h(c, 0);
  }
  if constexpr (ROLE == 0) {
    if constexpr (n >= EV_SENT0 && n < EV_SENT0 + 6 * EV_SENT_STEP && (n - EV_SENT0) % EV_SENT_STEP == 0)
      __builtin_amdgcn_raw_buffer_store_b128(sent4(), c.rs, c.lane16, (c.w >> 1) == c.fq ? OOB : g2_out_base(c) + (n - EV_SENT0) / EV_SENT_STEP * 1024, AUX_SC1);
    if constexpr (n == EV_PUB_S) {
      if (c.w == 2) { const int l = lane_of(c); flag_store(c, (l & 60) == 16, g2_wg(c.mt, c.nl, l & 3), 32 + c.fq, c.ev); }
    }
    if constexpr (n >= EV_PRE0 && n < EV_PRE0 + 12 * EV_PRE_STEP && (n - EV_PRE0) % EV_PRE_STEP == 0)
      g2_issue_w<(n - EV_PRE0) / EV_PRE_STEP / 3, (n - EV_PRE0) / EV_PRE_STEP % 3>(c);
    if constexpr (n == EV_G2_POLL) poll_issue(c);
    if constexpr (n == EV_G2_CHECK) {
      POLL_LANDED(S.ops_after(EV_G2_POLL, EV_G2_CHECK));
      if (c.w == 0) poll_wait(c, lane_of(c) < 8);
    }
    if constexpr (n == EV_G2_TOUCH) g2_touch<1, 3>(c);
    if constexpr (n == 316) asm volatile("" : "+v"(c.touch)::"memory");
    if constexpr (n >= EV_G2_H0 && (n - EV_G2_H0) % 2 == 0 && (n - EV_G2_H0) / 2 < G2_WIN) hw[(n - EV_G2_H0) / 2] = g2_load_h<(n - EV_G2_H0) / 2>(c);
  } else {
    if constexpr (n == EV_CV_POLL) poll_issue(c);
    if constexpr (n == EV_CV_CHECK) {
      POLL_LANDED(K1S[2].ops_after(EV_CV_POLL, EV_CV_CHECK));
      if (c.w == 0) {
        poll_wait(c, lane_of(c) < 32);
        asm volatile("buffer_inv sc1" ::: "memory");
        __builtin_amdgcn_raw_buffer_store_b32(c.ev + 1u, c.rs, lane_of(c) == 14 ? 0u : OOB, WS_EPOCH + c.b * 4, AUX_SC1);
      }
    }
    if constexpr (n == EV_CV_INVW) { if (c.w == 0) wait_vm<S.ops_after(EV_CV_CHECK, EV_CV_INVW)>(); }
    if constexpr (n >= EV_PRE0 && n < EV_PRE0 + 12 * EV_PRE_STEP && (n - EV_PRE0) % EV_PRE_STEP == 0) {
      if (c.w < 7) cv_issue<(n - EV_PRE0) / EV_PRE_STEP>(c, c.w);
    }
    if constexpr (n >= EV_CV_PRE7 && n < EV_CV_PRE7 + 12) {
      if (c.w == 7) cv_issue<n - EV_CV_PRE7>(c, 7);
    }
    if constexpr (n == EV_CV_PRE7 + 1) {
      const int l = lane_of(c);
      const unsigned cb = cv_base(c.w);
      if (l < 60) {
        int o = l < 4 ? CV_K + l * 16 : (l < 32 ? CV_U0 + 4096 + (l - 4) * 16 : CV_U1 + 4096 + (l - 32) * 16);
        *(LAS u32x4*)(ldsp(cb) + o) = zero4();
      }
    }
    if constexpr (n == EV_CV_PRE7 + 4) {
      const int l = lane_of(c);
      const unsigned cb = cv_base(c.w);
      const int rho = l & 15, q = l >> 4, pi = (l >> 3) & 1, ii = l & 7;
      V.bp = cb + CV_U0 + pi * 4640 + 2 * (2016 - 32 * ii + 8 * q) - 64 * 56;
#pragma unroll
      for (int i = 0; i < 4; ++i) { V.ap[i] = cb + CV_K + 4 + 4 * rho + 16 * q + 1024 * i; asm volatile("" : "+v"(V.ap[i])); }
    }
    if constexpr (n == EV_CV_PRE8) {
      dma16<k1_wbase(18), WS_UR>(c.lane16, c.sws, (9 * cv_id(c) + 8) * (4 * S_) + c.w * 1024, c.w < 4 ? CV_U0 + c.w * 1024 : CV_U1 + (c.w - 4) * 1024);
    }
    if constexpr (n == EV_CV_PRE8 + 1) {
      dma16<k1_wbase(18) + CV_K + 64, 0>(c.lane16, c.s2, (9 * cv_id(c) + 8) * (S_ * 2) + (c.w >> 1) * 1024, (c.w >> 1) * 1024);
      if (c.w == 6) {
        const int l = lane_of(c);
        if (l < 60) {
          int o = l < 4 ? CV_K + l * 16 : (l < 32 ? CV_U0 + 4096 + (l - 4) * 16 : CV_U1 + 4096 + (l - 32) * 16);
          *(LAS u32x4*)(ldsp(k1_wbase(18)) + o) = zero4();
        }
      }
    }
    if constexpr (n == 319) { if (c.w == 7) wait_vm<K1S[2].ops_after(EV_CV_PRE7 + 11, 319)>(); }
    if constexpr (n >= 320) {
#pragma unroll
      for (int f = 2 * (n - 320); f < 2 * (n - 320) + 2; ++f) {
        V.b0[f] = *(LAS bf16x8*)(ldsp(V.bp) + 64 * (56 - 8 * f));
        V.a0[f] = cv_read_a(ldsp(V.ap[f >> 1]) + 512 * (f & 1));
      }
    }
  }
  SCHED_FENCE();
  if constexpr (S.bar[n]) {
    if constexpr (ROLE == 1 && K1S[1].wait[n] != K1S[2].wait[n]) {
      if (c.w == 7) wait_vm<K1S[2].wait[n]>();
      else wait_vm<K1S[1].wait[n]>();
      wait_vm_barrier<63>();
    } else {
      wait_vm_barrier<S.wait[n]>();
    }
    SCHED_FENCE();
  }
  if constexpr (n + 1 < K1_NS) k1_slot<n + 1, ROLE>(c, xf, wf, acc, E, P, hw, V);
}

template <int n> __device__ __forceinline__ bf16x8 g2_read_w(unsigned rd) {
  constexpr int i = n / 3, t = n % 3, s = i / 8, j = i % 8;
  LAS char* p = ldsp(rd) + (s % G2_NSLOT) * G2_SLOT + (16 * j) * 192 + t * 64;
  return tr_frag(p, p + 8 * 192);
}
constexpr int G2_BST = k1_epi_slot(1, 49) - K1_NS;
constexpr int G2_TAIL0 = G2_BST + 1, G2_TAILST = G2_TAIL0 + 13;
constexpr int G2_PUBB = 19, G2_PUBC = 67;
constexpr int G2_CHECKB = 67, G2_LEAD = 20;
constexpr bool g2_check(int n) { return n >= G2_CHECKB && n <= G2_CHECKB + 96 && (n - G2_CHECKB) % 24 == 0; }
constexpr bool g2_touch_at(int n) { return n == G2_CHECKB + 1 || n == G2_CHECKB + 25 || n == G2_CHECKB + 49; }
constexpr int G2_PRE3 = 190;
constexpr int g2_hslot(int i) {
  int s = 3 * (i - G2_WIN) + 2;
  if (i >= 32 && s < G2_CHECKB + 2 + (i - 32)) s = G2_CHECKB + 2 + (i - 32);
  return s;
}
constexpr int g2_hload_at(int n) { for (int i = G2_WIN; i < 72; ++i) if (g2_hslot(i) == n) return i; return -1; }
constexpr bool g2_bar(int n) { return n % 24 == 24 - G2_PF - 1 && n / 24 < 8; }
constexpr int g2_ops_in_slot(int n) {
  int k = 0;
  for (int s = 1; s + 3 < 9; ++s) for (int i = 0; i < 3; ++i) if (n == 24 * s + 24 - G2_PF + 1 + 3 * i) ++k;
  if (g2_hload_at(n) >= 0) ++k;
  if (n == G2_TAILST || n == G2_BST) ++k;
  if (g2_check(n + G2_LEAD)) ++k;
  if (g2_touch_at(n)) ++k;
  return k;
}
constexpr int g2_ops_after(int a, int b) { int k = 0; for (int n = a + 1; n <= b; ++n) k += g2_ops_in_slot(n); return k; }
static_assert([] { for (int i = G2_WIN; i + 1 < 72; ++i) if (g2_hslot(i) >= g2_hslot(i + 1) || g2_hslot(i) + 12 > 3 * i) return false; return true; }(), "h load slots");
static_assert(g2_bar(G2_PUBB) && g2_bar(G2_PUBC) && g2_bar(G2_CHECKB) && g2_hslot(48) > G2_CHECKB + 24 && g2_hslot(56) > G2_CHECKB + 48 &&
              g2_hslot(64) > G2_CHECKB + 72 && G2_PRE3 > G2_CHECKB + 96 && G2_LEAD < 23, "events");
struct G2Fin { u32x4 zz[2], qq[2]; float g; };
__device__ __forceinline__ void g2_fin_load(const Ctx& c, G2Fin& Fn, bool all) {
  const int lane = lane_of(c);
  const int uh = (c.w / 3) & 1, ut = c.w % 3;
  const int t0 = 256 * c.mt + 64 * c.fq + 32 * uh, d0 = 96 * c.nl + 32 * ut;
#pragma unroll
  for (int i = 0; i < 2; ++i)
    Fn.zz[i] = __builtin_amdgcn_raw_buffer_load_b128(c.rs, ((16 * i + (lane >> 2)) * S_) * 2 + (lane & 3) * 16, WS_Z + (d0 * S_ + t0) * 2, AUX_SC1);
  if (all) {
    const char* qptr = karg(0);
#pragma unroll
    for (int i = 0; i < 2; ++i) Fn.qq[i] = *(const u32x4*)(qptr + ((size_t)(t0 + (lane & 31)) * D_ + d0 + 16 * (lane >> 5) + 8 * i) * 2);
    Fn.g = __builtin_bit_cast(float, __builtin_amdgcn_raw_buffer_load_b32(c.rs, (lane & 31) * 4, WS_G + t0 * 4, AUX_SC1));
  }
}
template <int n>
__device__ __forceinline__ void g2_slot(Ctx& c, unsigned rd, u32x4 (&hw)[G2_WIN], bf16x8 (&wf)[G2_PF + 1], f32x16 (&acc)[3], const f32x16* tailacc, K1Epi& E,
                                        G2Fin& Fn) {
  constexpr int i = n / 3, t = n % 3, s = n / 24;
  if constexpr (n + G2_PF < G2_NS) wf[(n + G2_PF) % (G2_PF + 1)] = g2_read_w<n + G2_PF>(rd);
  if constexpr (i == 0) acc[t] = __builtin_amdgcn_mfma_f32_32x32x16_bf16(wf[n % (G2_PF + 1)], __builtin_bit_cast(bf16x8, hw[i % G2_WIN]), (f32x16){}, 0, 0, 0);
  else acc[t] = __builtin_amdgcn_mfma_f32_32x32x16_bf16(wf[n % (G2_PF + 1)], __builtin_bit_cast(bf16x8, hw[i % G2_WIN]), acc[t], 0, 0, 0);
  SCHED_FENCE();
  k1_epi_at<1, K1_NS + n>(c, tailacc, E);
  if constexpr (n >= G2_TAIL0 && n <= G2_TAILST) k1_epi_step<1, n - G2_TAIL0>(c, tailacc, E, 64 + c.nl);
  if constexpr (n == G2_CHECKB + 1) g2_touch<4, 2>(c);
  if constexpr (n == G2_CHECKB + 25) g2_touch<6, 1>(c);
  if constexpr (n == G2_CHECKB + 49) g2_touch<7, 1>(c);
  {
    constexpr int m = n - (24 - G2_PF + 1);
    if constexpr (m >= 24 && m % 24 < 9 && m % 3 == 0 && m / 24 + 3 < 9) g2_issue_w<m / 24 + 3, (m % 24) / 3>(c);
  }
  if constexpr (g2_hload_at(n) >= 0) hw[g2_hload_at(n) % G2_WIN] = g2_load_h<g2_hload_at(n)>(c);
  if constexpr (n == G2_PRE3) g2_fin_load(c, Fn, true);
  if constexpr (g2_check(n + G2_LEAD)) poll_issue(c);
  if constexpr (g2_check(n)) {
    constexpr int k = (n - G2_CHECKB) / 24;
    POLL_LANDED(g2_ops_after(n - G2_LEAD, n));
    if constexpr (k >= 1 && k <= 3) asm volatile("" : "+v"(c.touch)::"memory");
    if (c.w == 0) {
      const int l = lane_of(c);
      if constexpr (k == 0) poll_wait(c, (l & 60) == 8);
      if constexpr (k == 1) poll_wait(c, (l & 62) == 12);
      if constexpr (k == 2) poll_wait(c, (l & 62) == 14);
      if constexpr (k == 3) poll_wait(c, (l & 56) == 40);
      if constexpr (k == 4) {
        poll_wait(c, (l >= 16 && l <= 16 + (96 * c.nl + 95) / 9 - (96 * c.nl) / 9) || (l & 60) == 32);
        __builtin_amdgcn_raw_buffer_store_b32(c.ev + 1u, c.rs, l == 14 ? 0u : OOB, WS_EPOCH + c.b * 4, AUX_SC1);
      }
    }
  }
  SCHED_FENCE();
  if constexpr (g2_bar(n)) {
    constexpr int wt = n == G2_PUBB ? g2_ops_after(G2_BST, n)
                                    : (n == G2_PUBC ? g2_ops_after(G2_TAILST, n) : (s + 1 < 4 ? 63 : g2_ops_after(24 * (s - 2) + 24 - G2_PF + 1 + 6, n)));
    wait_vm_barrier<cmin(63, wt)>();
    if constexpr (n == G2_PUBB) { if (c.w == 1) publish_h(c, 8); }
    if constexpr (n == G2_PUBC) { if (c.w == 1) publish_h(c, 40); }
    SCHED_FENCE();
  }
  if constexpr (n + 1 < G2_NS) g2_slot<n + 1>(c, rd, hw, wf, acc, tailacc, E, Fn);
}
__device__ __forceinline__ unsigned umax4(const u32x4& v) {
  unsigned a = v[0] > v[1] ? v[0] : v[1], b = v[2] > v[3] ? v[2] : v[3];
  return a > b ? a : b;
}

__device__ __forceinline__ void g2_phase(Ctx& c, u32x4 (&hw)[G2_WIN], const f32x16* tailacc, K1Epi& E) {
  const int lane = lane_of(c);
  const int w = c.w, ks = c.fq, nt = c.nl, mt = c.mt;
  unsigned rd;
  {
    int gq = lane >> 4, chf = gq & 1, h = gq >> 1, q = (lane & 15) >> 2, p = lane & 3;
    rd = K1_XOFF + (4 * h + q) * 192 + chf * 32 + p * 8;
  }
  bf16x8 wf[G2_PF + 1];
  f32x16 acc[3];
  wf[0] = g2_read_w<0>(rd); wf[1] = g2_read_w<1>(rd); wf[2] = g2_read_w<2>(rd); wf[3] = g2_read_w<3>(rd);
  SCHED_FENCE();
  G2Fin Fn;
  g2_slot<0>(c, rd, hw, wf, acc, tailacc, E, Fn);
  const int kd = w >> 1, half = w & 1;
  if (kd != ks) {
    const unsigned base = g2_out_base(c);
#pragma unroll
    for (int t = 0; t < 3; ++t)
#pragma unroll
      for (int p2 = 0; p2 < 2; ++p2) {
        u32x4 o = {pk2(acc[t][8 * p2], acc[t][8 * p2 + 1]), pk2(acc[t][8 * p2 + 2], acc[t][8 * p2 + 3]), pk2(acc[t][8 * p2 + 4], acc[t][8 * p2 + 5]),
                   pk2(acc[t][8 * p2 + 6], acc[t][8 * p2 + 7])};
        __builtin_amdgcn_raw_buffer_store_b128(o, c.rs, c.lane16, base + (t * 2 + p2) * 1024, AUX_SC1);
      }
  } else {
#pragma unroll
    for (int t = 0; t < 3; ++t)
#pragma unroll
      for (int qd = 0; qd < 4; ++qd)
        *(LAS f32x4*)(ldsp(c.lane16) + (half * 12 + t * 4 + qd) * 1024) = (f32x4){acc[t][4 * qd], acc[t][4 * qd + 1], acc[t][4 * qd + 2], acc[t][4 * qd + 3]};
  }
  const int uh = w / 3, ut = w % 3;
  const int qp = lane >> 5, cc = lane & 31;
  float ga[16], og = 0.f;
  if (w < 6) {
#pragma unroll 1
    for (unsigned spin = 0; spin < SPIN_CAP; ++spin) {
      if (__all(umax4(Fn.zz[0]) != SENT && umax4(Fn.zz[1]) != SENT)) break;
      asm volatile("" ::: "memory");
      g2_fin_load(c, Fn, false);
    }
    LAS char* zt = ldsp(24576 + w * 2560);
#pragma unroll
    for (int i = 0; i < 2; ++i) *(LAS u32x4*)(zt + (16 * i + (lane >> 2)) * 80 + (lane & 3) * 16) = Fn.zz[i];
    unsigned zv[16];
#pragma unroll
    for (int j = 0; j < 16; ++j) zv[j] = *(LAS u16*)(zt + (16 * qp + j) * 80 + cc * 2);
    og = rbf(1.0f - Fn.g);
#pragma unroll
    for (int j = 0; j < 16; j += 2) {
      const unsigned qw = Fn.qq[j >> 3][(j >> 1) & 3];
      const unsigned a2 = pk2(bflo(qw) * bflo(zv[j]), bfhi(qw) * bflo(zv[j + 1]));
      ga[j] = Fn.g * bflo(a2); ga[j + 1] = Fn.g * bfhi(a2);
    }
  }
  lds_barrier();
  STAMP(33);
  if (w < 6) {
    char* optr = (char*)karg(11);
    f32x4 own[2][2];
#pragma unroll
    for (int o = 0; o < 2; ++o)
#pragma unroll
      for (int h = 0; h < 2; ++h) own[o][h] = *(LAS f32x4*)(ldsp((cc + 32 * h) * 16) + (uh * 12 + ut * 4 + 2 * qp + o) * 1024);
    u32x4 pp[3][2];
#pragma unroll 1
    for (unsigned spin = 0; spin < SPIN_CAP; ++spin) {
      asm volatile("" ::: "memory");
#pragma unroll
      for (int s = 0; s < 3; ++s) {
        const int src = s + (s >= ks ? 1 : 0);
#pragma unroll
        for (int h = 0; h < 2; ++h)
          pp[s][h] = __builtin_amdgcn_raw_buffer_load_b128(c.rs, qp * 1024 + (cc + 32 * h) * 16, WS_PART + c.b * 49152 + (src * 2 + uh) * 6144 + ut * 2048, AUX_SC1);
      }
      u32x4 mx = {umax4(pp[0][0]), umax4(pp[0][1]), umax4(pp[1][0]), umax4(pp[1][1])};
      if (__all(umax4(mx) != SENT && umax4(pp[2][0]) != SENT && umax4(pp[2][1]) != SENT)) break;
    }
    STAMP(34);
#pragma unroll
    for (int o = 0; o < 2; ++o) {
      float res[8];
#pragma unroll
      for (int h = 0; h < 2; ++h)
#pragma unroll
        for (int k = 0; k < 4; k += 2) {
          f32x2 bs = {own[o][h][k], own[o][h][k + 1]};
#pragma unroll
          for (int s = 0; s < 3; ++s) bs += unpk2(pp[s][h][2 * o + (k >> 1)]);
          const f32x2 b2 = unpk2(pk2(bs[0], bs[1]));
          res[4 * h + k] = radd(ga[8 * o + 4 * h + k], og * b2[0]);
          res[4 * h + k + 1] = radd(ga[8 * o + 4 * h + k + 1], og * b2[1]);
        }
      u32x4 ov = {pk2(res[0], res[1]), pk2(res[2], res[3]), pk2(res[4], res[5]), pk2(res[6], res[7])};
      *(u32x4*)(optr + ((size_t)(256 * mt + 64 * ks + 32 * uh + cc) * D_ + 96 * nt + 32 * ut + 16 * qp + 8 * o) * 2) = ov;
    }
  }
  STAMP(35);
}

template <bool PREF, bool FIRST, bool TAIL>
__device__ __forceinline__ void cv_block(const Ctx& c, f32x4 (&acc)[8], const bf16x8 (&af)[8], const bf16x8 (&bf)[8], bf16x8 (&naf)[8], bf16x8 (&nbf)[8],
                                         const unsigned (&ap)[4], unsigned bp, int roff, const f32x16* tailacc, K1Epi& E) {
  auto step = [&](auto K) {
    constexpr int k = decltype(K)::value;
    constexpr int p = [] { int r = k, q = 0; while (r >= 8 - q) { r -= 8 - q; ++q; } return q; }();
    constexpr int dl = [] { int r = k, q = 0; while (r >= 8 - q) { r -= 8 - q; ++q; } return r; }();
    if constexpr (PREF && (k & 1) == 0 && k < 32) {
      constexpr int f = k >> 2;
      if constexpr ((k & 2) != 0) naf[f] = cv_read_a(ldsp(ap[f >> 1]) + 512 * (f & 1) + roff);
      else nbf[f] = *(LAS bf16x8*)(ldsp(bp) + 64 * (56 - 8 * f) + roff);
    }
    if constexpr (FIRST && p == 0) acc[dl] = __builtin_amdgcn_mfma_f32_16x16x32_bf16(af[p], bf[dl], (f32x4){}, 0, 0, 0);
    else acc[p + dl] = __builtin_amdgcn_mfma_f32_16x16x32_bf16(af[p], bf[dl], acc[p + dl], 0, 0, 0);
    asm volatile("" : "+v"(acc[p + dl]));
    if constexpr (TAIL && (k & 1)) {
      k1_epi_at<1, K1_NS + k / 2>(c, tailacc, E);
      if constexpr (k / 2 >= G2_TAIL0 && k / 2 <= G2_TAILST) k1_epi_step<1, k / 2 - G2_TAIL0>(c, tailacc, E, 64 + c.nl);
    }
    if constexpr (k & 1) SCHED_FENCE();
  };
  [&]<int... Ks>(std::integer_sequence<int, Ks...>) { (step(std::integral_constant<int, Ks>{}), ...); }(std::make_integer_sequence<int, 36>{});
}
__device__ __forceinline__ u32x4 cv_u(LAS char* cl, int g, int lane) {
  const int q = lane >> 4, pi = (lane >> 3) & 1, ii = lane & 7;
  return *(LAS u32x4*)(cl + (pi ? CV_U1 : CV_U0) + 2 * (2040 - (256 * g + 32 * ii + 8 * q)));
}
__device__ __forceinline__ u32x2 cv_z(const f32x4& y, const u32x4& uu, int lane, float Dv) {
  const int pi = (lane >> 3) & 1;
  float z[4];
#pragma unroll
  for (int k = 0; k < 4; ++k) z[k] = radd(y[k], bflo(uu[3 - k]) * Dv);
  unsigned p01 = pk2(z[0], z[1]), p23 = pk2(z[2], z[3]);
  unsigned snd = pi ? p01 : p23;
  unsigned rcv = __builtin_amdgcn_update_dpp(0u, snd, 0x128, 0xf, 0xf, false);
  unsigned Ev = pi ? rcv : p01, Od = pi ? p23 : rcv;
  return (u32x2){__builtin_amdgcn_perm(Od, Ev, 0x05040100u), __builtin_amdgcn_perm(Od, Ev, 0x07060302u)};
}
__device__ __forceinline__ void cv_phase(Ctx& c, const f32x16* tailacc, K1Epi& E, CvRegs& V) {
  const int w = c.w;
  f32x4 acc[8], acc8[8];
  bf16x8 a1[8], b1[8];
  cv_block<true, true, true>(c, acc, V.a0, V.b0, a1, b1, V.ap, V.bp, 64, tailacc, E);
  wait_vm_barrier<1>();
  if (w == 1) publish_h(c, 8);
  const int lane = lane_of(c);
  const unsigned zb = WS_Z + 9 * cv_id(c) * (S_ * 2);
#pragma unroll
  for (int i = 0; i < 4; ++i) __builtin_amdgcn_raw_buffer_store_b128(sent4(), c.rs, c.lane16, zb + w * (S_ * 2) + i * 1024, AUX_SC1);
  __builtin_amdgcn_raw_buffer_store_b64(sent2(), c.rs, lane * 8, zb + 8 * (S_ * 2) + 512 * w, AUX_SC1);
  int zsl;
  const int zwg = cv_zwg(c, lane, zsl);
  const unsigned zev = __builtin_amdgcn_raw_buffer_load_b32(c.rs, zwg * 4, WS_EPOCH, 0);
  const unsigned dd = *(const u16*)(karg(5) + (9 * cv_id(c) + ((lane & 1) ? 8 : w)) * 2);
  SCHED_FENCE();
  cv_block<true, false, false>(c, acc, a1, b1, V.a0, V.b0, V.ap, V.bp, 128, tailacc, E);
  const unsigned cb = cv_base(w), cb8 = cv_base(8);
  unsigned ap8[4];
#pragma unroll
  for (int i = 0; i < 4; ++i) ap8[i] = V.ap[i] + (cb8 - cb + 64 * w);
  SCHED_FENCE();
  cv_block<true, false, false>(c, acc, V.a0, V.b0, a1, b1, ap8, V.bp + (cb8 - cb + 64 * w), 0, tailacc, E);
  cv_block<true, true, false>(c, acc8, a1, b1, V.a0, V.b0, V.ap, V.bp, 192, tailacc, E);
  LAS char* sc = ldsp(((w & 2) ? k1_wbase(20) : k1_wbase(19)) + (w & 1) * 8192 + lane * 16);
  if (w >= 4) {
#pragma unroll
    for (int g = 0; g < 8; ++g) *(LAS f32x4*)(sc + g * 1024) = acc8[g];
  }
  SCHED_FENCE();
  cv_block<true, false, false>(c, acc, V.a0, V.b0, a1, b1, V.ap, V.bp, 256, tailacc, E);
  asm volatile("s_waitcnt vmcnt(0) lgkmcnt(0)\n\ts_barrier" ::: "memory");
  if (w == 5) publish_h(c, 40);
  if (w == 6) flag_store(c, zsl >= 0, zwg, zsl, zev);
  if (w < 4) {
    f32x4 o[8];
#pragma unroll
    for (int g = 0; g < 8; ++g) o[g] = *(LAS f32x4*)(sc + g * 1024);
#pragma unroll
    for (int g = 0; g < 8; ++g) *(LAS f32x4*)(sc + g * 1024) = acc8[g] + o[g];
  }
  SCHED_FENCE();
  cv_block<true, false, false>(c, acc, a1, b1, V.a0, V.b0, V.ap, V.bp, 320, tailacc, E);
  lds_barrier();
  const float Dv = bflo((unsigned)__builtin_amdgcn_readlane((int)dd, 0)), Dv8 = bflo((unsigned)__builtin_amdgcn_readlane((int)dd, 1));
  const int zo = (32 * (lane & 7) + 8 * (lane >> 4) + 4 * ((lane >> 3) & 1)) * 2;
  {
    LAS char* s0 = ldsp(w * 1024 + lane * 16);
    f32x4 y = (*(LAS f32x4*)(s0 + k1_wbase(19)) + *(LAS f32x4*)(s0 + k1_wbase(19) + 8192)) +
              (*(LAS f32x4*)(s0 + k1_wbase(20)) + *(LAS f32x4*)(s0 + k1_wbase(20) + 8192));
    __builtin_amdgcn_raw_buffer_store_b64(cv_z(y, cv_u(ldsp(cb8), w, lane), lane, Dv8), c.rs, zo, zb + 8 * (S_ * 2) + 512 * w, AUX_SC1);
  }
  SCHED_FENCE();
  cv_block<true, false, false>(c, acc, V.a0, V.b0, a1, b1, V.ap, V.bp, 384, tailacc, E);
  cv_block<true, false, false>(c, acc, a1, b1, V.a0, V.b0, V.ap, V.bp, 448, tailacc, E);
  LAS char* cl = ldsp(cb);
  u32x4 uu[8];
#pragma unroll
  for (int g = 0; g < 8; ++g) uu[g] = cv_u(cl, g, lane);
  SCHED_FENCE();
  cv_block<false, false, false>(c, acc, V.a0, V.b0, a1, b1, V.ap, V.bp, 0, tailacc, E);
  STAMP(25);
#pragma unroll
  for (int g = 0; g < 8; ++g) *(LAS u32x2*)(cl + 512 * g + zo) = cv_z(acc[g], uu[g], lane, Dv);
#pragma unroll
  for (int i = 0; i < 4; ++i)
    __builtin_amdgcn_raw_buffer_store_b128(*(LAS u32x4*)(cl + i * 1024 + c.lane16), c.rs, c.lane16, zb + w * (S_ * 2) + i * 1024, AUX_SC1);
  STAMP(35);
}

template <int ROLE>
__device__ __forceinline__ void fused_body(const int w) {
  const int b = blockIdx.x, xcd = b & 7, r = b >> 3;
  const int ml = r & 3, nl = r >> 2, fq = xcd >> 1;
  const int mt = 4 * (xcd & 1) + ml;
  const int lane = __builtin_amdgcn_mbcnt_hi(~0u, __builtin_amdgcn_mbcnt_lo(~0u, 0u));
  Ctx c;
  RSTAMP(37);
  STAMP(0);
  c.w = w; c.nl = nl; c.ml = ml; c.b = b; c.mt = mt; c.fq = fq;
  c.rot = (nl + ml) % 3;
  c.lane16 = lane * 16;
  asm volatile("" : "+v"(c.lane16));
  c.sw1 = make_srd(karg(6), (1152 * fq + 128 * nl) * 2, D_ * F_ * 2);
  c.sw3 = make_srd(karg(7), (1152 * fq + 128 * nl) * 2, D_ * F_ * 2);
  c.sx = make_srd(karg(0), (256 * mt + 32 * w) * (D_ * 2), S_ * D_ * 2);
  {
    const char* ws = karg(10);
    c.sws = make_srd(ws, 0, WS_END);
    c.rs = __builtin_amdgcn_make_buffer_rsrc((void*)ws, 0, WS_END, 0x00020000);
  }
#pragma unroll
  for (int i = 0; i < 3; ++i) { const int k = kslice(i, c.rot); c.wo[i] = k * (64 * F_ * 2); c.xo[i] = k * 128; }
  c.hb = WS_H + ((8 * mt + w) * 288 + 72 * fq) * 1024;
  {
    int row = lane >> 3, pos = lane & 7;
    c.voff_w = (8 * w + row) * (F_ * 2) + ((pos ^ (2 * ((row >> 1) & 1))) * 16);
    c.voff_x = row * (D_ * 2) + ((pos ^ (lane >> 4)) * 16);
  }
  k1_issue_w<0, 0>(c); k1_issue_w<0, 1>(c);
  k1_issue_x<0, 0>(c); k1_issue_x<0, 1>(c); k1_issue_x<0, 2>(c); k1_issue_x<0, 3>(c);
  {
    const int ewg = lane < 8 ? cv_wg(8 * xcd + lane) : (lane < 14 ? g2_wg(mt, lane - 8, fq) : ((lane & 60) == 16 ? g2_wg(mt, nl < 6 ? nl : 0, lane & 3) : b));
    const unsigned so = WS_EPOCH;
    asm volatile("buffer_load_dword %0, %1, %2, %3 offen" : "=v"(c.ev) : "v"((unsigned)ewg * 4), "s"(c.sws), "s"(so) : "memory");
  }
  k1_issue_w<1, 0>(c); k1_issue_w<1, 1>(c);
  {
    int gq = lane >> 4, mat = gq & 1, h = gq >> 1, q = (lane & 15) >> 2, p = lane & 3;
    int X = 2 * (q >> 1) + 4 * mat;
#pragma unroll
    for (int t = 0; t < 4; ++t) c.rd[t] = mat * 8192 + (8 * h + q) * 128 + (((2 * t) ^ X) + (p >> 1)) * 16 + (p & 1) * 8;
    int cc = lane & 31, hh = lane >> 5;
    c.xl0 = K1_XOFF + w * (K1_XD * 4096) + cc * 128 + ((hh ^ ((cc >> 1) & 7)) * 16);
  }
  {
    const u32x4 sw2 = make_srd(karg(8), 1152 * fq * D_ * 2, F_ * D_ * 2), ssk = make_srd(karg(4), 0, D_ * S_ * 2);
    c.s2 = ROLE == 0 ? sw2 : ssk;
    c.s3 = ROLE == 0 ? ssk : sw2;
    const unsigned wv = 8 * r + w, mh = xcd & 1;
    unsigned o = OOB;
    if (lane < 12) {
      const unsigned i = wv + 256 * lane, row = (i * 6554u) >> 16, cl = i - 10 * row;
      if (i < 2880) o = (2 * row + mh + 1) * (F_ * 2) + (cl < 8 ? 2 * cl + 1 : cl + 8) * 128 - 256 * nl - F_ * 2;
      if (i < 2880 && 2 * row + mh == 0 && (cl < 8 ? 2 * cl + 1 : cl + 8) * 128 < 256u * nl) o = OOB;
    } else if (lane >= 16 && lane < 37) {
      const unsigned i = wv + 256 * (lane - 16), row = (i * 7282u) >> 16, cl = i - 9 * row;
      if (i < 5184) o = (2 * row + mh) * (D_ * 2) + cl * 128;
    } else if (lane >= 48 && lane < 57) {
      o = 72 * xcd * (S_ * 2) + (wv + 256 * (lane - 48)) * 128;
    }
    c.warm = o;
    c.touch = 0;
    asm volatile("" : "+v"(c.warm), "+v"(c.touch));
  }
  K1Prep P;
  {
    const u32x4 sk_ = make_srd(karg(1), 0, S_ * D_ * 2), sv_ = make_srd(karg(2), 0, S_ * D_ * 2);
#pragma unroll
    for (int j = 0; j < 3; ++j) {
      const int q = 3 * w + j, isv = q >= 10;
      const unsigned pc = (unsigned)(64 * (q - 10 * isv) + lane), row = (pc * 7282u) >> 16;
      const int t = 64 * r - (isv ? 1 : 4) + (int)row;
      const bool ok = q < 20 && t >= 0 && row < (isv ? 65u : 68u);
      const unsigned vo = ok ? (unsigned)t * (D_ * 2) + (pc - 9 * row) * 16 + 144 * xcd : OOB;
      const u32x4 sd = {isv ? sv_[0] : sk_[0], isv ? sv_[1] : sk_[1], sk_[2], sk_[3]};
      dma16<K1_POFF, 0>(vo, sd, 0u, q < 20 ? (unsigned)((q - 10 * isv) * 1024 + isv * K1_PV) : 24576u + (q - 20) * 1024);
    }
    const char *skp = karg(3), *gw = karg(9);
    const char* gx = karg(0) + (size_t)(256 * mt + 64 * fq + 8 * nl + w) * (D_ * 2);
    const int dp = 8 * w + (lane >> 3), dc = dp < 36 ? dp : 35;
    P.lp = K1_POFF + 8 * (7 - (lane & 7)) * 144 + dc * 4;
    asm volatile("" : "+v"(P.lp));
    GLD4(P.sk, (unsigned)(72 * xcd + 2 * dc) * 8, skp);
    GLD4(P.ga0, c.lane16, gx);
    GLD4(P.ga1, 1024 + (c.lane16 & 127), gx);
    GLD4(P.gw0, c.lane16, gw);
    GLD4(P.gw1, 1024 + (c.lane16 & 127), gw);
  }
  bf16x8 xf[36];
  bf16x8 wf[K1_RD + 1];
  f32x16 acc[4];
  u32x4 hw[G2_WIN];
  asm volatile("s_waitcnt vmcnt(10)\n\ts_barrier" : "+v"(c.ev)::"memory");
  STAMP(1);
  k1_issue_x<1, 0>(c); k1_issue_x<1, 1>(c); k1_issue_x<1, 2>(c); k1_issue_x<1, 3>(c);
  xf[0] = k1_read_x<0, 0>(c); wf[0] = k1_read_w<0>(c);
  xf[1] = k1_read_x<0, 1>(c); wf[1] = k1_read_w<1>(c);
  xf[2] = k1_read_x<0, 2>(c); wf[2] = k1_read_w<2>(c);
  xf[3] = k1_read_x<0, 3>(c);
  static_assert(K1_RD == 3, "prologue reads");
  SCHED_FENCE();
  STAMP(2);
  K1Epi E;
  CvRegs V;
  k1_slot<0, ROLE>(c, xf, wf, acc, E, P, hw, V);
  STAMP(23);
  if constexpr (ROLE == 1) cv_phase(c, acc, E, V);
  else g2_phase(c, hw, acc, E);
}

__global__ __launch_bounds__(512, 2) void fused_kernel(const void* x, const void* kk, const void* vv, const void* shift_k, const void* ssm_k,
                                                       const void* ssm_D, const void* w1, const void* w3, const void* w2, const void* gate_w, void* ws,
                                                       void* out) {
  const int w = __builtin_amdgcn_readfirstlane(threadIdx.x >> 6);
  if (blockIdx.x >= 192) fused_body<1>(w);
  else fused_body<0>(w);
}

extern "C" void kernel_launch(void* const* d_in, const int* in_sizes, int n_in, void* d_out, int out_size, void* d_ws, size_t ws_size,
                              hipStream_t stream) {
  (void)hipFuncSetAttribute((const void*)fused_kernel, hipFuncAttributeMaxDynamicSharedMemorySize, LDS_BYTES);
  fused_kernel<<<256, 512, LDS_BYTES, stream>>>(d_in[0], d_in[1], d_in[2], d_in[3], d_in[4], d_in[5], d_in[7], d_in[8], d_in[9], d_in[10], d_ws, d_out);
}
```

```cpp
#include <hip/hip_runtime.h>
#include <hip/hip_bf16.h>

#define LAS __attribute__((address_space(3)))
#define GAS __attribute__((address_space(1)))
typedef unsigned short u16;
typedef __bf16 bf16x2 __attribute__((ext_vector_type(2)));
typedef __bf16 bf16x4 __attribute__((ext_vector_type(4)));
typedef __bf16 bf16x8 __attribute__((ext_vector_type(8)));
typedef float f32x2 __attribute__((ext_vector_type(2)));
typedef float f32x4 __attribute__((ext_vector_type(4)));
typedef float f32x16 __attribute__((ext_vector_type(16)));
typedef unsigned u32x2 __attribute__((ext_vector_type(2)));
typedef unsigned u32x4 __attribute__((ext_vector_type(4)));

constexpr int S_ = 2048, D_ = 576, F_ = 4608;
constexpr unsigned WS_H = 0;
constexpr unsigned WS_PART = WS_H + 64 * 288 * 1024;
constexpr unsigned WS_UR = WS_PART + 256 * 49152;
constexpr unsigned WS_Z = WS_UR + D_ * 2 * S_ * 2;
constexpr unsigned WS_G = WS_Z + D_ * S_ * 2;
constexpr unsigned WS_FLAG = WS_G + S_ * 4;
constexpr unsigned WS_EPOCH = WS_FLAG + 256 * 512;
constexpr unsigned WS_END = WS_EPOCH + 1024;
constexpr unsigned FLAG_MAGIC = 0x7c3a91e5u, SENT = 0xffffffffu;
constexpr int AUX_SC1 = 16;
constexpr unsigned OOB = 0x7ff00000u;
constexpr unsigned SPIN_CAP = 1u << 16;

#define STAMP(i)
#define RSTAMP(i)
#define STDUMP()
#define DBGCNT(i, x)

__device__ __forceinline__ float bflo(unsigned v) { return __builtin_bit_cast(float, v << 16); }
__device__ __forceinline__ float bfhi(unsigned v) { return __builtin_bit_cast(float, v & 0xffff0000u); }
__device__ __forceinline__ float rbf(float x) { return (float)(__bf16)x; }
__device__ __forceinline__ unsigned pk2(float lo, float hi) {
  f32x2 v = {lo, hi};
  return __builtin_bit_cast(unsigned, __builtin_convertvector(v, bf16x2));
}
__device__ __forceinline__ f32x2 unpk2(unsigned v) { return (f32x2){bflo(v), bfhi(v)}; }
__device__ __forceinline__ float radd(float a, float b) { const unsigned t = pk2(a, b); return bflo(t) + bfhi(t); }
#define SCHED_FENCE() __builtin_amdgcn_sched_barrier(0)
__device__ __forceinline__ void st128(u32x4 v, __amdgpu_buffer_rsrc_t rs, unsigned voff, unsigned soff, int) {
  __builtin_amdgcn_raw_buffer_store_b128(v, rs, voff, soff, AUX_SC1);
  asm volatile("s_nop 1" ::"v"(v));
}

template <int LC, int SC> __device__ __forceinline__ void dma16(unsigned voff, const u32x4& srd, unsigned so, unsigned lb) {
  unsigned t;
  asm volatile("s_add_u32 m0, %4, %5\n\ts_add_u32 %0, %3, %6\n\tbuffer_load_dwordx4 %1, %2, %0 offen lds"
               : "=&s"(t) : "v"(voff), "s"(srd), "s"(so), "s"(lb), "n"(LC), "n"(SC) : "memory", "scc");
}
template <int N> __device__ __forceinline__ void wait_vm_barrier() {
  asm volatile("s_waitcnt vmcnt(%0)\n\ts_barrier" ::"n"(N) : "memory");
}
template <int N> __device__ __forceinline__ void wait_vm() { asm volatile("s_waitcnt vmcnt(%0)" ::"n"(N) : "memory"); }
__device__ __forceinline__ void lds_barrier() { asm volatile("s_waitcnt lgkmcnt(0)\n\ts_barrier" ::: "memory"); }
__device__ __forceinline__ LAS char* ldsp(unsigned a) { return (LAS char*)(size_t)a; }
__device__ __forceinline__ bf16x8 tr_frag(LAS char* p0, LAS char* p1) {
  bf16x4 a = __builtin_amdgcn_ds_read_tr16_b64_v4bf16((LAS bf16x4*)p0);
  bf16x4 b = __builtin_amdgcn_ds_read_tr16_b64_v4bf16((LAS bf16x4*)p1);
  return __builtin_shufflevector(a, b, 0, 1, 2, 3, 4, 5, 6, 7);
}
constexpr int cmin(int a, int b) { return a < b ? a : b; }
__device__ __forceinline__ const char* karg(int i) {
  typedef const char* cptr;
  const __attribute__((address_space(4))) cptr* p = (const __attribute__((address_space(4))) cptr*)__builtin_amdgcn_kernarg_segment_ptr();
  asm volatile("" : "+s"(p));
  return p[i];
}
__device__ __forceinline__ u32x4 make_srd(const char* base, unsigned off, unsigned total) {
  const unsigned long long a = (unsigned long long)base + off;
  return (u32x4){(unsigned)a, (unsigned)(a >> 32) & 0xffffu, total - off, 0x00020000u};
}

constexpr int K1_SLOT = 16384, K1_XOFF = 65536, K1_XD = 2, K1_POFF = 131072, K1_PV = 10240;
constexpr int LDS_BYTES = 163840;
constexpr int K1_PF = 4, K1_RD = 3, K1_NS = 324, K1_NSEG = 21;
constexpr int K1_PREP0 = 28, K1_PREP_UNITS = 8 + 2 + 63 + 1 + 4;
constexpr int G2_SLOT = 24576, G2_NSLOT = 4, G2_WIN = 16, G2_PF = 4, G2_NS = 216;
constexpr int CV_K = 0, CV_U0 = 4160, CV_U1 = CV_U0 + 4640, CV_CH = 13440;
constexpr int k1_wbase(int s) { return s == 11 ? K1_XOFF : (s == 12 ? K1_XOFF + K1_SLOT : ((s < 11 ? s : s - 2) % 4) * K1_SLOT); }
constexpr int k1_wprev(int s) { return s < 4 || s == 11 || s == 12 ? -1 : (s < 11 ? s - 4 : (s < 17 ? s - 6 : s - 4)); }
constexpr int EV_PUB_U = 140;
constexpr int EV_PUB_A = 204;
constexpr int EV_PRE0 = 221, EV_PRE_STEP = 4;
constexpr int EV_CV_POLL = 176, EV_CV_CHECK = 190, EV_CV_INVW = 219;
constexpr int EV_CV_PRE7 = 300;
constexpr int EV_CV_PRE8 = 316;
constexpr int EV_SENT0 = 177, EV_SENT_STEP = 7, EV_PUB_S = 236;
constexpr int EV_G2_POLL = 266, EV_G2_CHECK = 283, EV_G2_TOUCH = 285, EV_G2_H0 = 289;
constexpr int WARM_N = 10;
constexpr int WARM_SLOT[WARM_N] = {18, 34, 50, 66, 82, 98, 118, 142, 158, 178};
constexpr int WARM_DONE = 219;
constexpr int EV_PUB_B1 = 316;
constexpr int K1_EPI0[2] = {125, 269};
constexpr int k1_epi_slot(int ch, int e) { return K1_EPI0[ch] + (ch == 0 ? e - e / 12 : e); }

struct K1Sched {
  int seg[K1_NS], tile[K1_NS], j[K1_NS];
  int first_use[K1_NSEG], last_use[K1_NSEG];
  int wissue[K1_NSEG];
  int xissue[9];
  bool bar[K1_NS];
  int ops[K1_NS];
  int wait[K1_NS];
  int done[K1_NS];
  constexpr int ops_after(int a, int b) const { int k = 0; for (int q = a + 1; q <= b; ++q) k += ops[q]; return k; }
};
constexpr K1Sched k1_make(int role) {
  K1Sched S{};
  int n = 0;
  for (int s = 0; s < 18;) {
    if (s == 7 || s == 9 || s == 16) {
      const int tt[8] = {0, 1, 0, 1, 2, 2, 3, 3}, hh[8] = {0, 0, 1, 1, 0, 1, 0, 1};
      for (int q = 0; q < 8; ++q) for (int j = 0; j < 4; ++j) { S.seg[n] = s + hh[q]; S.tile[n] = tt[q]; S.j[n] = j; ++n; }
      s += 2;
    } else {
      for (int t = 0; t < 4; ++t) for (int j = 0; j < 4; ++j) { S.seg[n] = s; S.tile[n] = t; S.j[n] = j; ++n; }
      s += 1;
    }
  }
  for (int i = 0; i < 36; ++i) { S.seg[n] = 18 + i / 16; S.tile[n] = 0; S.j[n] = i % 16; ++n; }
  for (int s = 0; s < K1_NSEG; ++s) { S.first_use[s] = 100000; S.last_use[s] = -1; }
  for (int i = 0; i < K1_NS; ++i) { int s = S.seg[i]; if (i < S.first_use[s]) S.first_use[s] = i; if (i > S.last_use[s]) S.last_use[s] = i; }
  for (int s = 1; s < K1_NSEG; ++s) S.bar[S.first_use[s] - K1_PF - 1] = true;
  S.wissue[0] = S.wissue[1] = -1;
  S.wissue[2] = 0; S.wissue[3] = 12;
  int busy = 0;
  for (int s = 4; s < K1_NSEG; ++s) {
    int q = k1_wprev(s) >= 0 ? S.last_use[k1_wprev(s)] : S.first_use[8] + 4;
    while (!S.bar[q]) ++q;
    int is = q + 1;
    if (is < busy) is = busy;
    S.wissue[s] = is;
    busy = is + (s >= 11 ? 8 : 4);
  }
  S.xissue[0] = S.xissue[1] = -1;
  for (int p = 2; p < 9; ++p) S.xissue[p] = S.first_use[p - 2] + 4;
  for (int s = 0; s < K1_NSEG; ++s) if (S.wissue[s] >= 0) { S.ops[S.wissue[s]]++; S.ops[S.wissue[s] + 2]++; }
  for (int p = 0; p < 9; ++p) if (S.xissue[p] >= 0) for (int i = 0; i < 4; ++i) S.ops[S.xissue[p] + i]++;
  S.ops[K1_PREP0 + 7]++;
  for (int i = 0; i < WARM_N; ++i) S.ops[WARM_SLOT[i]]++;
  for (int i = 0; i < 4; ++i) S.ops[K1_PREP0 + K1_PREP_UNITS - 4 + i]++;
  for (int c = 0; c < 2; ++c) for (int t = 0; t < 4; ++t) if (k1_epi_slot(c, 12 * t + 13) < K1_NS) S.ops[k1_epi_slot(c, 12 * t + 13)]++;
  if (role == 0) {
    for (int i = 0; i < G2_WIN; ++i) S.ops[EV_G2_H0 + 2 * i]++;
    S.ops[EV_G2_TOUCH]++;
    for (int i = 0; i < 6; ++i) S.ops[EV_SENT0 + EV_SENT_STEP * i]++;
  } else {
    S.ops[EV_CV_PRE8]++; S.ops[EV_CV_PRE8 + 1]++;
  }
  if (role != 2) for (int i = 0; i < 12; ++i) S.ops[EV_PRE0 + EV_PRE_STEP * i]++;
  if (role == 2) for (int i = 0; i < 12; ++i) S.ops[EV_CV_PRE7 + i]++;
  for (int i = 0; i < K1_NS; ++i) {
    S.wait[i] = 63;
    S.done[i] = -2;
    if (!S.bar[i]) continue;
    int youngest = -2;
    for (int s = 1; s < K1_NSEG; ++s)
      if (S.first_use[s] - K1_PF - 1 == i) {
        int y = S.wissue[s] < 0 ? -1 : S.wissue[s] + 2;
        if (s < 9) { int yx = S.xissue[s] < 0 ? -1 : S.xissue[s] + 3; if (yx > y) y = yx; }
        if (y > youngest) youngest = y;
      }
    if (i == EV_PUB_A - 1 && youngest < k1_epi_slot(0, 49)) youngest = k1_epi_slot(0, 49);
    if (i == EV_PUB_S - 1 && youngest < EV_SENT0 + 5 * EV_SENT_STEP) youngest = EV_SENT0 + 5 * EV_SENT_STEP;
    if (i == WARM_DONE && youngest < WARM_SLOT[WARM_N - 1]) youngest = WARM_SLOT[WARM_N - 1];
    if (i == EV_PUB_B1 - 1 && youngest < k1_epi_slot(1, 25)) youngest = k1_epi_slot(1, 25);
    if (i == 299 && youngest < EV_PRE0 + 11 * EV_PRE_STEP) youngest = EV_PRE0 + 11 * EV_PRE_STEP;
    S.wait[i] = cmin(63, S.ops_after(youngest, i));
    S.done[i] = youngest;
  }
  return S;
}
constexpr K1Sched K1S[3] = {k1_make(0), k1_make(1), k1_make(2)};
static_assert(K1_PREP0 + K1_PREP_UNITS - 1 <= K1S[0].done[EV_PUB_U - 1] && K1S[0].bar[EV_PUB_U - 1], "u stores complete before publish");
static_assert(K1S[0].bar[EV_PUB_A - 1] && K1S[0].bar[EV_PUB_S - 1] && K1S[0].bar[299] && K1S[0].bar[315], "events");
static_assert(K1S[0].bar[EV_G2_CHECK] && K1S[0].bar[EV_CV_INVW] && K1S[0].bar[EV_CV_PRE7 - 1] && K1S[0].last_use[17] < EV_CV_PRE7, "events");
static_assert(K1S[0].bar[EV_CV_PRE8 - 1] && K1S[0].last_use[18] < EV_CV_PRE8 && EV_G2_H0 + 2 * G2_WIN <= K1_NS + 1, "events");
static_assert(EV_PRE0 > EV_CV_INVW && K1S[0].last_use[12] <= EV_CV_INVW && K1S[0].done[315] >= EV_G2_TOUCH, "events");
static_assert([] {
  for (int t = 0; t < 4; ++t) {
    int last = 0, next = 1000;
    for (int i = 0; i < 144; ++i) if (K1S[0].tile[i] == t) last = i;
    for (int i = 287; i >= 144; --i) if (K1S[0].tile[i] == t) next = i;
    if (k1_epi_slot(0, 12 * t) < last + 2 || k1_epi_slot(0, 12 * t + 9) >= next) return false;
  }
  return true;
}(), "epilogue windows");

struct Ctx {
  u32x4 sw1, sw3, sx, s2, s3, sws;
  __amdgpu_buffer_rsrc_t rs;
  unsigned wo[3], xo[3];
  unsigned hb;
  unsigned voff_w, voff_x;
  unsigned rd[4];
  unsigned xl0, lane16;
  unsigned ev;
  u32x2 poll;
  unsigned touch, warm;
  unsigned rc0, rc1;
  int w, rot, nl, ml, b, mt, fq;
};
struct K1Prep {
  u32x4 sk, ga0, ga1, gw0, gw1;
  unsigned lp, kn, vn, o0;
  f32x2 s[4], k2[4], a, p, q; unsigned u[9]; float gs;
};
struct K1Epi { float m[2], p[2], pq[2], r[2], h[2]; u32x4 ho; };
struct CvRegs { unsigned ap[4], bp; bf16x8 a0[8], b0[8]; };

__device__ __forceinline__ int lane_of(const Ctx& c) {
  unsigned l = c.lane16;
  asm volatile("" : "+v"(l));
  return (int)(l >> 4);
}
__device__ __forceinline__ u32x4 sent4() {
  u32x4 v = {SENT, SENT, SENT, SENT};
  asm volatile("" : "+v"(v));
  return v;
}
__device__ __forceinline__ u32x2 sent2() {
  u32x2 v = {SENT, SENT};
  asm volatile("" : "+v"(v));
  return v;
}
__device__ __forceinline__ u32x4 zero4() {
  u32x4 v = {0u, 0u, 0u, 0u};
  asm volatile("" : "+v"(v));
  return v;
}
__device__ __forceinline__ int g2_wg(int mt, int nt, int ks) { return ((nt * 4 + (mt & 3)) << 3) | (ks * 2 + (mt >> 2)); }
__device__ __forceinline__ int cv_wg(int cw) { return ((24 + (cw & 7)) << 3) | (cw >> 3); }
__device__ __forceinline__ int cv_id(const Ctx& c) { return (c.b & 7) * 8 + (c.b >> 3) - 24; }
__device__ __forceinline__ void flag_store(const Ctx& c, bool act, int wg, int slot, unsigned epoch) {
  asm volatile("" : "+v"(epoch));
  __builtin_amdgcn_raw_buffer_store_b64((u32x2){epoch + 1u, FLAG_MAGIC}, c.rs, act ? wg * 512 + slot * 8 : OOB, WS_FLAG, AUX_SC1);
}
__device__ __forceinline__ void publish_h(const Ctx& c, int slot0) {
  const int l = lane_of(c);
  flag_store(c, l >= 8 && l < 14, g2_wg(c.mt, l - 8, c.fq), slot0 + c.nl, c.ev);
}
__device__ __forceinline__ void poll_issue(Ctx& c) {
  const unsigned vo = lane_of(c) * 8;
  const unsigned so = WS_FLAG + c.b * 512;
  asm volatile("s_cmp_lg_u32 %4, 0\n\ts_cbranch_scc1 1f\n\tbuffer_load_dwordx2 %0, %1, %2, %3 offen sc1\n1:"
               : "=v"(c.poll) : "v"(vo), "s"(c.sws), "s"(so), "s"(c.w) : "memory", "scc");
}
template <int DBG = 0> __device__ __forceinline__ void poll_wait(Ctx& c, bool need) {
  const unsigned want = (unsigned)__builtin_amdgcn_readlane((int)c.ev, 14) + 1u;
  u32x2 v = c.poll;
  unsigned spin = 0;
#pragma unroll 1
  for (; spin < SPIN_CAP; ++spin) {
    if (__all(!need || (v[0] == want && v[1] == FLAG_MAGIC))) break;
    const unsigned vo = lane_of(c) * 8, so = WS_FLAG + c.b * 512;
    asm volatile("s_sleep 1\n\tbuffer_load_dwordx2 %0, %1, %2, %3 offen sc1\n\ts_waitcnt vmcnt(0)" : "=&v"(v) : "v"(vo), "s"(c.sws), "s"(so) : "memory");
  }
  DBGCNT(40 + DBG, spin);
  asm volatile("" ::: "memory");
}
#define POLL_LANDED(N) asm volatile("s_cmp_lg_u32 %2, 0\n\ts_cbranch_scc1 1f\n\ts_waitcnt vmcnt(%1)\n1:" : "+v"(c.poll) : "n"(N), "s"(c.w) : "memory", "scc")

__device__ __forceinline__ int kslice(int p, int r) {
  const int g = (p >= 6) ? 6 : (p >= 3 ? 3 : 0);
  const int v = p - g + r;
  return g + (v >= 3 ? v - 3 : v);
}
template <int sg, int half> __device__ __forceinline__ void k1_issue_w(const Ctx& c) {
  constexpr int dst = k1_wbase(sg) + half * 8192;
  if constexpr (sg < 18) {
    constexpr int p = sg % 9;
    dma16<dst, (p / 3 * 3) * 64 * F_ * 2 + 128 * (sg / 9)>(half ? (c.voff_w ^ 64u) : c.voff_w, half ? c.sw3 : c.sw1, c.wo[p % 3], c.w * 1024);
  } else {
    const unsigned l = lane_of(c);
    const unsigned rp = l >> 1;
    unsigned vo = (half ? (rp ^ 4u) : rp) * (F_ * 2) + (l & 1u) * 16;
    const int col = (1024 + 16 * c.nl - 128 * c.nl) * 2;
    if constexpr (sg < 20) {
      const int ksl = kslice(4 * (sg - 18) + (c.w >> 1), c.rot);
      dma16<dst, 0>(vo, half ? c.sw3 : c.sw1, (64 * ksl + 32 * (c.w & 1)) * (F_ * 2) + col, c.w * 1024);
    } else {
      if (l >= 16) vo = OOB;
      dma16<dst, 6 * 64 * F_ * 2>(vo, half ? c.sw3 : c.sw1, c.wo[2] + col + 8 * c.w * (F_ * 2), c.w * 1024);
    }
  }
}
template <int p, int i> __device__ __forceinline__ void k1_issue_x(const Ctx& c) {
  dma16<K1_XOFF + (p % K1_XD) * 4096 + i * 1024, 8 * i * D_ * 2 + (p / 3 * 3) * 128>((i & 1) ? (c.voff_x ^ 64u) : c.voff_x, c.sx, c.xo[p % 3],
                                                                                     c.w * (K1_XD * 4096));
}
template <int p, int j> __device__ __forceinline__ bf16x8 k1_read_x(const Ctx& c) {
  return *(LAS bf16x8*)(ldsp(c.xl0 ^ (32 * j)) + (p % K1_XD) * 4096);
}
template <int n> __device__ __forceinline__ bf16x8 k1_read_w(Ctx& c) {
  constexpr int sg = K1S[0].seg[n], t = K1S[0].tile[n], j = K1S[0].j[n];
  if constexpr (sg < 18) {
    if constexpr ((sg == 11 || sg == 13) && j == 0) {
      c.rd[t] += sg == 11 ? K1_XOFF : -K1_XOFF;
      asm volatile("" : "+v"(c.rd[t]));
    }
    LAS char* p = ldsp(c.rd[t]) + k1_wbase(sg) % K1_XOFF + (16 * j) * 128;
    return tr_frag(p, p + 4 * 128);
  } else if constexpr (sg < 20) {
    const int off = k1_wbase(sg) + (16 * j) * 32;
    return tr_frag(ldsp(c.rc0) + off, ldsp(c.rc1) + off);
  } else {
    const unsigned ex = (lane_of(c) >> 5) * 768u;
    const int off = k1_wbase(sg) + j * 2048;
    return tr_frag(ldsp(c.rc0 + ex) + off, ldsp(c.rc1 + ex) + off);
  }
}
#define GLD1(dst, voff, sb) asm volatile("global_load_dword %0, %1, %2" : "=v"(dst) : "v"(voff), "s"(sb) : "memory")
#define GLD4(dst, voff, sb) asm volatile("global_load_dwordx4 %0, %1, %2" : "=v"(dst) : "v"(voff), "s"(sb) : "memory")
template <int ctrl> __device__ __forceinline__ float dpp_add(float v) {
  return v + __builtin_bit_cast(float, __builtin_amdgcn_update_dpp(0, __builtin_bit_cast(int, v), ctrl, 0xf, 0xf, true));
}
__device__ __forceinline__ float rb1(float x) { return __builtin_bit_cast(float, pk2(0.f, x)); }
__device__ __forceinline__ f32x2 rb2(f32x2 v) { return (f32x2){rb1(v[0]), rb1(v[1])}; }
template <int k> __device__ __forceinline__ void k1_prep_unit(const Ctx& c, K1Prep& P) {
  if constexpr (k == 0) {
    asm volatile("" : "+v"(P.sk), "+v"(P.ga0), "+v"(P.ga1), "+v"(P.gw0), "+v"(P.gw1)::"memory");
    P.gs = 0.f;
  }
  if constexpr (k < 4) {
    const u32x4 &a = k < 2 ? P.ga1 : P.ga0, &g = k < 2 ? P.gw1 : P.gw0;
#pragma unroll
    for (int i = 2 * (k & 1); i < 2 * (k & 1) + 2; ++i) P.gs = __builtin_fmaf(bfhi(a[i]), bfhi(g[i]), __builtin_fmaf(bflo(a[i]), bflo(g[i]), P.gs));
    if constexpr (k == 1) P.gs = lane_of(c) < 8 ? P.gs : 0.f;
    asm volatile("" : "+v"(P.gs));
  } else if constexpr (k == 4) {
    P.gs = dpp_add<0x111>(P.gs); P.gs = dpp_add<0x112>(P.gs);
    P.u[0] = *(LAS unsigned*)(ldsp(P.lp)); P.u[1] = *(LAS unsigned*)(ldsp(P.lp) + 144);
  } else if constexpr (k == 5) {
    P.gs = dpp_add<0x114>(P.gs); P.gs = dpp_add<0x118>(P.gs);
    P.u[2] = *(LAS unsigned*)(ldsp(P.lp) + 288); P.kn = *(LAS unsigned*)(ldsp(P.lp) + 432);
  } else if constexpr (k == 6) {
    int v = __builtin_bit_cast(int, P.gs);
    float a = __builtin_bit_cast(float, __builtin_amdgcn_readlane(v, 15)), b = __builtin_bit_cast(float, __builtin_amdgcn_readlane(v, 31));
    float d = __builtin_bit_cast(float, __builtin_amdgcn_readlane(v, 47)), e = __builtin_bit_cast(float, __builtin_amdgcn_readlane(v, 63));
    P.gs = __builtin_amdgcn_exp2f(rb1((a + b) + (d + e)) * -1.44269504088896f);
  } else if constexpr (k == 7) {
    float g = rb1(__builtin_amdgcn_rcpf(1.0f + P.gs));
    __builtin_amdgcn_raw_buffer_store_b32(__builtin_bit_cast(unsigned, g), c.rs, lane_of(c) == 0 ? 0u : OOB,
                                          WS_G + (256 * c.mt + 64 * c.fq + 8 * c.nl + c.w) * 4, AUX_SC1);
    P.s[0] = (f32x2){bflo(P.sk[0]), bflo(P.sk[2])};
  } else if constexpr (k == 8) {
    P.s[1] = (f32x2){bfhi(P.sk[0]), bfhi(P.sk[2])};
    P.s[2] = (f32x2){bflo(P.sk[1]), bflo(P.sk[3])}; P.s[3] = (f32x2){bfhi(P.sk[1]), bfhi(P.sk[3])};
    asm volatile("" : "+v"(P.s[0]), "+v"(P.s[1]), "+v"(P.s[2]), "+v"(P.s[3]));
  } else if constexpr (k == 9) {
    P.k2[0] = unpk2(P.u[0]); P.k2[1] = unpk2(P.u[1]); P.k2[2] = unpk2(P.u[2]);
    asm volatile("" : "+v"(P.k2[0]), "+v"(P.k2[1]), "+v"(P.k2[2]));
  } else if constexpr (k < 73) {
    constexpr int i = (k - 10) / 7, st = (k - 10) % 7;
    auto mul = [](f32x2 x, f32x2 y) { f32x2 r = {x[0] * y[0], x[1] * y[1]}; return r; };
    if constexpr (st == 0) { P.a = mul(P.k2[i & 3], P.s[3]); P.p = mul(P.k2[(i + 1) & 3], P.s[2]); P.vn = *(LAS unsigned*)(ldsp(P.lp) + K1_PV + 144 * i); }
    else if constexpr (st == 1) { P.a = rb2(P.a); P.p = rb2(P.p); }
    else if constexpr (st == 2) { P.a += P.p; P.p = mul(P.k2[(i + 2) & 3], P.s[1]); P.k2[(i + 3) & 3] = unpk2(P.kn); }
    else if constexpr (st == 3) {
      P.a = rb2(P.a); P.p = rb2(P.p);
      if constexpr (i < 8) P.kn = *(LAS unsigned*)(ldsp(P.lp) + 144 * (i + 4));
    }
    else if constexpr (st == 4) { P.a += P.p; P.p = mul(P.k2[(i + 3) & 3], P.s[0]); P.q = unpk2(P.vn); }
    else if constexpr (st == 5) { P.a = rb2(P.a); P.p = rb2(P.p); }
    else { P.a = mul(rb2(P.a + P.p), P.q); P.u[i] = pk2(P.a[0], P.a[1]); }
    if constexpr (st == 6) asm volatile("" : "+v"(P.u[i]));
    else if constexpr (st == 2) asm volatile("" : "+v"(P.a), "+v"(P.p), "+v"(P.k2[(i + 3) & 3]));
    else if constexpr (st == 4) asm volatile("" : "+v"(P.a), "+v"(P.p), "+v"(P.q));
    else asm volatile("" : "+v"(P.a), "+v"(P.p));
  } else if constexpr (k == 73) {
    const int l = lane_of(c);
    const int dp = 8 * c.w + (l >> 3);
    P.o0 = dp < 36 ? (unsigned)((72 * (c.b & 7) + 2 * dp) * 2 * S_ + (1984 - 64 * (c.b >> 3)) + 8 * (l & 7)) * 2 : OOB;
  } else {
    constexpr int ch = (k - 74) >> 1, cp = (k - 74) & 1;
    constexpr unsigned sel = ch ? 0x07060302u : 0x05040100u;
    u32x4 o;
#pragma unroll
    for (int e = 0; e < 4; ++e) o[e] = __builtin_amdgcn_perm(P.u[7 + cp - 2 * e - 1], P.u[7 + cp - 2 * e], sel);
    st128(o, c.rs, P.o0, WS_UR + (ch * 2 + cp) * S_ * 2, AUX_SC1);
  }
}
template <int NT, int e> __device__ __forceinline__ void k1_epi_step(const Ctx& c, const f32x16* acc, K1Epi& E, int ft0) {
  constexpr int pr = e / 3, st = e % 3, t = pr / 4, i = 2 * (pr % 4);
  if constexpr (st == 0) {
    if constexpr (pr > 0) { E.h[0] = E.pq[0] * E.r[0]; E.h[1] = E.pq[1] * E.r[1]; }
    if constexpr (pr < 4 * NT) {
      E.m[0] = acc[t][i] * -1.44269504088896f; E.m[1] = acc[t][i + 1] * -1.44269504088896f;
      E.p[0] = acc[t][i] * acc[t][i + 8]; E.p[1] = acc[t][i + 1] * acc[t][i + 9];
      asm volatile("" : "+v"(E.p[0]), "+v"(E.p[1]));
    }
  } else if constexpr (st == 1) {
    if constexpr (pr < 4 * NT) { E.m[0] = __builtin_amdgcn_exp2f(E.m[0]); E.m[1] = __builtin_amdgcn_exp2f(E.m[1]); }
    if constexpr (pr > 0) {
      E.ho[(pr - 1) % 4] = pk2(E.h[0], E.h[1]);
      if constexpr (pr % 4 == 0) st128(E.ho, c.rs, c.lane16, c.hb + (ft0 + (pr - 1) / 4) * 1024, AUX_SC1);
    }
  } else {
    if constexpr (pr < 4 * NT) {
      float d0 = 1.0f + E.m[0], d1 = 1.0f + E.m[1];
      E.r[0] = __builtin_amdgcn_rcpf(d0); E.r[1] = __builtin_amdgcn_rcpf(d1);
      E.pq[0] = E.p[0]; E.pq[1] = E.p[1];
    }
  }
}

template <int ch, int n, int e = 0> __device__ __forceinline__ void k1_epi_at(const Ctx& c, const f32x16* acc, K1Epi& E) {
  if constexpr (n >= K1_EPI0[ch] && n <= k1_epi_slot(ch, 49)) {
    if constexpr (k1_epi_slot(ch, e) == n) k1_epi_step<4, e>(c, acc, E, 8 * c.nl + 4 * ch);
    if constexpr (e < 49 && k1_epi_slot(ch, e < 49 ? e + 1 : e) <= n) k1_epi_at<ch, n, e + 1>(c, acc, E);
  }
}
template <int i> __device__ __forceinline__ void warm_touch(Ctx& c, const u32x4& sw2, const u32x4& ssk) {
  constexpr unsigned long long m = i < 4 ? (0x3full << (6 * (i >> 1))) : (i < 8 ? (0x3full << (16 + 6 * (i - 4))) : (0x1full << (48 + 5 * (i - 8))));
  const u32x4& srd = i < 4 ? ((i & 1) ? c.sw3 : c.sw1) : (i < 8 ? sw2 : ssk);
  asm volatile("s_mov_b32 exec_lo, %3\n\ts_mov_b32 exec_hi, %4\n\tbuffer_load_dword %0, %1, %2, 0 offen\n\ts_mov_b64 exec, -1"
               : "+v"(c.touch) : "v"(c.warm), "s"(srd), "n"((int)(unsigned)m), "n"((int)(unsigned)(m >> 32)) : "memory");
}
__device__ __forceinline__ int g2_ft(int s, int i8) {
  return s >= 8 ? 64 + i8 : (s < 4 ? 16 * s + 8 * (i8 >> 2) + (i8 & 3) : 32 * (s & 1) + 8 * (i8 >> 1) + (i8 & 1) + 4 + (s & 2));
}
template <int s0, int ns> __device__ __forceinline__ void g2_touch(Ctx& c) {
  constexpr int per = (64 * ns + 5) / 6;
  const int l = lane_of(c);
  const int i = per * c.nl + l;
  const unsigned vo = (l < per && i < 64 * ns) ? (unsigned)(g2_ft(ns > 1 ? s0 + (i >> 6) : s0, (i >> 3) & 7) * 1024 + (i & 7) * 128) : OOB;
  asm volatile("buffer_load_dword %0, %1, %2, %3 offen sc1" : "+v"(c.touch) : "v"(vo), "s"(c.sws), "s"(c.hb) : "memory");
}
template <int s, int i> __device__ __forceinline__ void g2_issue_w(const Ctx& c) {
  const unsigned n16 = 64u * i + (unsigned)lane_of(c);
  const unsigned vo = ((n16 * 171u) >> 11) * (D_ * 2 - 192) + n16 * 16u;
  dma16<K1_XOFF + (s % G2_NSLOT) * G2_SLOT + i * 1024, 0>(vo, c.s2, 16 * g2_ft(s, c.w) * (D_ * 2) + 192 * c.nl, c.w * 3072);
}
template <int i> __device__ __forceinline__ u32x4 g2_load_h(const Ctx& c) {
  return __builtin_amdgcn_raw_buffer_load_b128(c.rs, c.lane16, c.hb + g2_ft(i / 8, i % 8) * 1024, AUX_SC1);
}
__device__ __forceinline__ unsigned g2_out_base(const Ctx& c) {
  return WS_PART + g2_wg(c.mt, c.nl, c.w >> 1) * 49152 + (c.fq * 2 + (c.w & 1)) * 6144;
}
__device__ __forceinline__ unsigned cv_base(int ch) { return ch < 7 ? K1_XOFF + ch * CV_CH : (ch == 7 ? k1_wbase(17) : k1_wbase(18)); }
template <int i> __device__ __forceinline__ void cv_issue(const Ctx& c, int ch) {
  const int d = 9 * cv_id(c) + ch;
  if constexpr (i < 4) dma16<CV_K + 64 + i * 1024, i * 1024>(c.lane16, c.s2, d * (S_ * 2), cv_base(ch));
  else dma16<(i < 8 ? CV_U0 + (i - 4) * 1024 : CV_U1 + (i - 8) * 1024), WS_UR + (i - 4) * 1024>(c.lane16, c.sws, d * (4 * S_), cv_base(ch));
}
typedef unsigned u32x2a4 __attribute__((ext_vector_type(2), aligned(4)));
__device__ __forceinline__ bf16x8 cv_read_a(LAS char* p) {
  u32x2 lo = *(LAS u32x2a4*)p;
  u32x2 hi = *(LAS u32x2a4*)(p + 8);
  u32x4 v = {lo[0], lo[1], hi[0], hi[1]};
  return __builtin_bit_cast(bf16x8, v);
}
__device__ __forceinline__ int cv_zwg(const Ctx& c, int lane, int& slot) {
  const int cw = cv_id(c);
  const int n0 = (9 * cw) / 96, n1 = (9 * cw + 8) / 96;
  const int s0 = 16 + cw - (96 * n0) / 9, s1 = n1 != n0 ? 16 + cw - (96 * n1) / 9 : -1;
  slot = lane < 32 ? s0 : s1;
  return g2_wg((lane >> 2) & 7, lane < 32 ? n0 : n1, lane & 3);
}

template <int n, int ROLE>
__device__ __forceinline__ void k1_slot(Ctx& c, bf16x8 (&xf)[36], bf16x8 (&wf)[K1_RD + 1], f32x16 (&acc)[4], K1Epi& E, K1Prep& P, u32x4 (&hw)[G2_WIN],
                                        CvRegs& V) {
  constexpr const K1Sched& S = K1S[ROLE];
  constexpr int sg = S.seg[n];
  if constexpr (n + K1_RD == 288) {
    const unsigned l = lane_of(c);
    unsigned mat = (l >> 4) & 1, h = l >> 5, q = (l & 15) >> 2, p = l & 3;
    unsigned base = mat * 8192 + (8 * h + q) * 32 + p * 8;
    c.rc0 = base + (mat ? 128 : 0);
    c.rc1 = base + (mat ? 0 : 128);
  }
  if constexpr (n + K1_RD < K1_NS) wf[(n + K1_RD) % (K1_RD + 1)] = k1_read_w<n + K1_RD>(c);
  SCHED_FENCE();
  if constexpr (n + 4 < 144) {
    constexpr int sn = S.seg[n + 4];
    if constexpr (S.tile[n + 4] == 0 && sn >= 1) xf[4 * sn + S.j[n + 4]] = k1_read_x<sn, S.j[n + 4]>(c);
  }
  {
    constexpr int t = S.tile[n];
    constexpr int xi = sg < 18 ? 4 * (sg % 9) + S.j[n] : n - 288;
    constexpr bool first = (sg < 18) ? (sg % 9 == 0 && S.j[n] == 0) : (n == 288);
    if constexpr (first) acc[t] = __builtin_amdgcn_mfma_f32_32x32x16_bf16(wf[n % (K1_RD + 1)], xf[xi], (f32x16){}, 0, 0, 0);
    else acc[t] = __builtin_amdgcn_mfma_f32_32x32x16_bf16(wf[n % (K1_RD + 1)], xf[xi], acc[t], 0, 0, 0);
  }
  SCHED_FENCE();
  {
    constexpr int ws0 = [] { for (int s = 0; s < K1_NSEG; ++s) if (K1S[0].wissue[s] == n) return s; return -1; }();
    constexpr int ws1 = [] { for (int s = 0; s < K1_NSEG; ++s) if (K1S[0].wissue[s] >= 0 && K1S[0].wissue[s] + 2 == n) return s; return -1; }();
    constexpr int xs = [] { for (int p = 0; p < 9; ++p) if (K1S[0].xissue[p] >= 0 && n >= K1S[0].xissue[p] && n < K1S[0].xissue[p] + 4) return p; return -1; }();
    if constexpr (ws0 >= 0) k1_issue_w<ws0, 0>(c);
    if constexpr (ws1 >= 0) k1_issue_w<ws1, 1>(c);
    if constexpr (xs >= 0) k1_issue_x<xs, n - S.xissue[xs]>(c);
  }
  if constexpr (n >= K1_PREP0 && n < K1_PREP0 + K1_PREP_UNITS) k1_prep_unit<n - K1_PREP0>(c, P);
  k1_epi_at<0, n>(c, acc, E);
  k1_epi_at<1, n>(c, acc, E);
  {
    constexpr int wi = [] { for (int i = 0; i < WARM_N; ++i) if (WARM_SLOT[i] == n) return i; return -1; }();
    if constexpr (wi >= 0) warm_touch<wi>(c, ROLE == 0 ? c.s2 : c.s3, ROLE == 0 ? c.s3 : c.s2);
    if constexpr (n == WARM_DONE + 1) asm volatile("" : "+v"(c.touch)::"memory");
  }
  if constexpr (n == EV_PUB_U) {
    if (c.w == 1) { const int l = lane_of(c); flag_store(c, l < 8, cv_wg(8 * (c.b & 7) + l), c.b >> 3, c.ev); }
  }
  if constexpr (n == EV_PUB_A) {
    if (c.w == 1) publish_h(c, 0);
  }
  if constexpr (n == EV_PUB_B1) {
    if (c.w == 1) publish_h(c, 8);
  }
  if constexpr (ROLE == 0) {
    if constexpr (n >= EV_SENT0 && n < EV_SENT0 + 6 * EV_SENT_STEP && (n - EV_SENT0) % EV_SENT_STEP == 0)
      st128(sent4(), c.rs, c.lane16, (c.w >> 1) == c.fq ? OOB : g2_out_base(c) + (n - EV_SENT0) / EV_SENT_STEP * 1024, AUX_SC1);
    if constexpr (n == EV_PUB_S) {
      if (c.w == 2) { const int l = lane_of(c); flag_store(c, (l & 60) == 16, g2_wg(c.mt, c.nl, l & 3), 32 + c.fq, c.ev); }
    }
    if constexpr (n >= EV_PRE0 && n < EV_PRE0 + 12 * EV_PRE_STEP && (n - EV_PRE0) % EV_PRE_STEP == 0)
      g2_issue_w<(n - EV_PRE0) / EV_PRE_STEP / 3, (n - EV_PRE0) / EV_PRE_STEP % 3>(c);
    if constexpr (n == EV_G2_POLL) poll_issue(c);
    if constexpr (n == EV_G2_CHECK) {
      POLL_LANDED(S.ops_after(EV_G2_POLL, EV_G2_CHECK));
      if (c.w == 0) poll_wait(c, lane_of(c) < 8);
    }
    if constexpr (n == EV_G2_TOUCH) g2_touch<1, 3>(c);
    if constexpr (n >= EV_G2_H0 && (n - EV_G2_H0) % 2 == 0 && (n - EV_G2_H0) / 2 < G2_WIN) hw[(n - EV_G2_H0) / 2] = g2_load_h<(n - EV_G2_H0) / 2>(c);
  } else {
    if constexpr (n == EV_CV_POLL) poll_issue(c);
    if constexpr (n == EV_CV_CHECK) {
      POLL_LANDED(K1S[1].ops_after(EV_CV_POLL, EV_CV_CHECK));
      if (c.w == 0) {
        poll_wait(c, lane_of(c) < 32);
        asm volatile("buffer_inv sc1" ::: "memory");
        __builtin_amdgcn_raw_buffer_store_b32(c.ev + 1u, c.rs, lane_of(c) == 14 ? 0u : OOB, WS_EPOCH + c.b * 4, AUX_SC1);
      }
    }
    if constexpr (n == EV_CV_INVW) { if (c.w == 0) wait_vm<S.ops_after(EV_CV_CHECK, EV_CV_INVW)>(); }
    if constexpr (n >= EV_PRE0 && n < EV_PRE0 + 12 * EV_PRE_STEP && (n - EV_PRE0) % EV_PRE_STEP == 0) {
      if (c.w < 7) cv_issue<(n - EV_PRE0) / EV_PRE_STEP>(c, c.w);
    }
    if constexpr (n >= EV_CV_PRE7 && n < EV_CV_PRE7 + 12) {
      if (c.w == 7) cv_issue<n - EV_CV_PRE7>(c, 7);
    }
    if constexpr (n == EV_CV_PRE7 + 1) {
      const int l = lane_of(c);
      const unsigned cb = cv_base(c.w);
      if (l < 60) {
        int o = l < 4 ? CV_K + l * 16 : (l < 32 ? CV_U0 + 4096 + (l - 4) * 16 : CV_U1 + 4096 + (l - 32) * 16);
        *(LAS u32x4*)(ldsp(cb) + o) = zero4();
      }
    }
    if constexpr (n == EV_CV_PRE7 + 4) {
      const int l = lane_of(c);
      const unsigned cb = cv_base(c.w);
      const int rho = l & 15, q = l >> 4, pi = (l >> 3) & 1, ii = l & 7;
      V.bp = cb + CV_U0 + pi * 4640 + 2 * (2016 - 32 * ii + 8 * q) - 64 * 56;
#pragma unroll
      for (int i = 0; i < 4; ++i) { V.ap[i] = cb + CV_K + 4 + 4 * rho + 16 * q + 1024 * i; asm volatile("" : "+v"(V.ap[i])); }
    }
    if constexpr (n == EV_CV_PRE8) {
      dma16<k1_wbase(18), WS_UR>(c.lane16, c.sws, (9 * cv_id(c) + 8) * (4 * S_) + c.w * 1024, c.w < 4 ? CV_U0 + c.w * 1024 : CV_U1 + (c.w - 4) * 1024);
    }
    if constexpr (n == EV_CV_PRE8 + 1) {
      dma16<k1_wbase(18) + CV_K + 64, 0>(c.lane16, c.s2, (9 * cv_id(c) + 8) * (S_ * 2) + (c.w >> 1) * 1024, (c.w >> 1) * 1024);
      if (c.w == 6) {
        const int l = lane_of(c);
        if (l < 60) {
          int o = l < 4 ? CV_K + l * 16 : (l < 32 ? CV_U0 + 4096 + (l - 4) * 16 : CV_U1 + 4096 + (l - 32) * 16);
          *(LAS u32x4*)(ldsp(k1_wbase(18)) + o) = zero4();
        }
      }
    }
    if constexpr (n == 319) { if (c.w == 7) wait_vm<K1S[2].ops_after(EV_CV_PRE7 + 11, 319)>(); }
    if constexpr (n >= 320) {
#pragma unroll
      for (int f = 2 * (n - 320); f < 2 * (n - 320) + 2; ++f) {
        V.b0[f] = *(LAS bf16x8*)(ldsp(V.bp) + 64 * (56 - 8 * f));
        V.a0[f] = cv_read_a(ldsp(V.ap[f >> 1]) + 512 * (f & 1));
      }
    }
  }
  SCHED_FENCE();
  if constexpr (S.bar[n]) {
    if constexpr (ROLE == 1 && K1S[1].wait[n] != K1S[2].wait[n]) {
      if (c.w == 7) wait_vm<K1S[2].wait[n]>();
      else wait_vm<K1S[1].wait[n]>();
      wait_vm_barrier<63>();
    } else {
      wait_vm_barrier<S.wait[n]>();
    }
    SCHED_FENCE();
  }
  if constexpr (n + 1 < K1_NS) k1_slot<n + 1, ROLE>(c, xf, wf, acc, E, P, hw, V);
}

template <int n> __device__ __forceinline__ bf16x8 g2_read_w(unsigned rd) {
  constexpr int i = n / 3, t = n % 3, s = i / 8, j = i % 8;
  LAS char* p = ldsp(rd) + (s % G2_NSLOT) * G2_SLOT + (16 * j) * 192 + t * 64;
  return tr_frag(p, p + 8 * 192);
}
static_assert(k1_epi_slot(1, 49) < K1_NS, "chunk B epilogue ends in phase 1");
constexpr int G2_TAIL0 = 2, G2_TAILST = G2_TAIL0 + 13;
constexpr int G2_PUBB = 19, G2_PUBC = 43;
constexpr int G2_CHECK0 = 43, G2_LEAD = 20;
constexpr bool g2_check(int n) { return n >= G2_CHECK0 && n <= G2_CHECK0 + 120 && (n - G2_CHECK0) % 24 == 0; }
constexpr bool g2_touch_at(int n) { return n <= G2_CHECK0 + 97 && g2_check(n - 1); }
constexpr int G2_PRE3 = 200;
constexpr int g2_hslot(int i) { return 3 * (i - G2_WIN) + 2; }
constexpr int g2_hload_at(int n) { for (int i = G2_WIN; i < 72; ++i) if (g2_hslot(i) == n) return i; return -1; }
constexpr bool g2_bar(int n) { return n % 24 == 24 - G2_PF - 1 && n / 24 < 8; }
constexpr int g2_ops_in_slot(int n) {
  int k = 0;
  for (int s = 1; s + 3 < 9; ++s) for (int i = 0; i < 3; ++i) if (n == 24 * s + 24 - G2_PF + 1 + 3 * i) ++k;
  if (g2_hload_at(n) >= 0) ++k;
  if (n == G2_TAILST) ++k;
  if (g2_touch_at(n)) ++k;
  return k;
}
constexpr int g2_ops_after(int a, int b) { int k = 0; for (int n = a + 1; n <= b; ++n) k += g2_ops_in_slot(n); return k; }
static_assert([] { for (int i = G2_WIN; i + 1 < 72; ++i) if (g2_hslot(i) >= g2_hslot(i + 1) || g2_hslot(i) + 12 > 3 * i) return false; return true; }(), "h load slots");
static_assert(g2_bar(G2_PUBB) && g2_bar(G2_PUBC) && g2_bar(G2_CHECK0) && g2_hslot(32) > G2_CHECK0 && G2_PRE3 > G2_CHECK0 + 120 && G2_LEAD < 23, "events");
struct G2Fin { u32x4 zz[2], qq[2]; float g; };
__device__ __forceinline__ void g2_fin_load(const Ctx& c, G2Fin& Fn, bool all) {
  const int lane = lane_of(c);
  const int uh = (c.w / 3) & 1, ut = c.w % 3;
  const int t0 = 256 * c.mt + 64 * c.fq + 32 * uh, d0 = 96 * c.nl + 32 * ut;
#pragma unroll
  for (int i = 0; i < 2; ++i)
    Fn.zz[i] = __builtin_amdgcn_raw_buffer_load_b128(c.rs, ((16 * i + (lane >> 2)) * S_) * 2 + (lane & 3) * 16, WS_Z + (d0 * S_ + t0) * 2, AUX_SC1);
  if (all) {
    const char* qptr = karg(0);
#pragma unroll
    for (int i = 0; i < 2; ++i) Fn.qq[i] = *(const u32x4*)(qptr + ((size_t)(t0 + (lane & 31)) * D_ + d0 + 16 * (lane >> 5) + 8 * i) * 2);
    Fn.g = __builtin_bit_cast(float, __builtin_amdgcn_raw_buffer_load_b32(c.rs, (lane & 31) * 4, WS_G + t0 * 4, AUX_SC1));
  }
}
template <int n>
__device__ __forceinline__ void g2_slot(Ctx& c, unsigned rd, u32x4 (&hw)[G2_WIN], bf16x8 (&wf)[G2_PF + 1], f32x16 (&acc)[3], const f32x16* tailacc, K1Epi& E,
                                        G2Fin& Fn) {
  constexpr int i = n / 3, t = n % 3, s = n / 24;
  if constexpr (n + G2_PF < G2_NS) wf[(n + G2_PF) % (G2_PF + 1)] = g2_read_w<n + G2_PF>(rd);
  if constexpr (i == 0) acc[t] = __builtin_amdgcn_mfma_f32_32x32x16_bf16(wf[n % (G2_PF + 1)], __builtin_bit_cast(bf16x8, hw[i % G2_WIN]), (f32x16){}, 0, 0, 0);
  else acc[t] = __builtin_amdgcn_mfma_f32_32x32x16_bf16(wf[n % (G2_PF + 1)], __builtin_bit_cast(bf16x8, hw[i % G2_WIN]), acc[t], 0, 0, 0);
  SCHED_FENCE();
  if constexpr (n >= G2_TAIL0 && n <= G2_TAILST) k1_epi_step<1, n - G2_TAIL0>(c, tailacc, E, 64 + c.nl);
  if constexpr (g2_touch_at(n)) g2_touch<4 + (n - G2_CHECK0) / 24, 1>(c);
  {
    constexpr int m = n - (24 - G2_PF + 1);
    if constexpr (m >= 24 && m % 24 < 9 && m % 3 == 0 && m / 24 + 3 < 9) g2_issue_w<m / 24 + 3, (m % 24) / 3>(c);
  }
  if constexpr (g2_hload_at(n) >= 0) hw[g2_hload_at(n) % G2_WIN] = g2_load_h<g2_hload_at(n)>(c);
  if constexpr (n == G2_PRE3) {
    asm volatile("s_waitcnt vmcnt(%1)" : "+v"(c.touch) : "n"(g2_ops_after(G2_CHECK0 + 97, n)) : "memory");
    g2_fin_load(c, Fn, true);
  }
  if constexpr (g2_check(n + G2_LEAD)) poll_issue(c);
  if constexpr (g2_check(n)) {
    constexpr int k = (n - G2_CHECK0) / 24;
    POLL_LANDED(g2_ops_after(n - G2_LEAD, n));
    if (c.w == 0) {
      const int l = lane_of(c);
      if constexpr (k == 0) poll_wait<1>(c, (l & 60) == 8);
      if constexpr (k == 1) poll_wait<2>(c, (l & 60) == 12);
      if constexpr (k == 2) poll_wait<3>(c, (l & 60) == 48);
      if constexpr (k == 3) poll_wait<4>(c, (l & 60) == 52);
      if constexpr (k == 4) poll_wait<7>(c, (l & 56) == 40);
      if constexpr (k == 5) {
        poll_wait<5>(c, (l >= 16 && l <= 16 + (96 * c.nl + 95) / 9 - (96 * c.nl) / 9) || (l & 60) == 32);
        __builtin_amdgcn_raw_buffer_store_b32(c.ev + 1u, c.rs, l == 14 ? 0u : OOB, WS_EPOCH + c.b * 4, AUX_SC1);
      }
    }
  }
  SCHED_FENCE();
  if constexpr (g2_bar(n)) {
    constexpr int wt = n == G2_PUBB ? K1S[0].ops_after(k1_epi_slot(1, 49), K1_NS - 1) + g2_ops_after(-1, n)
                                    : (n == G2_PUBC ? g2_ops_after(G2_TAILST, n) : (s + 1 < 4 ? 63 : g2_ops_after(24 * (s - 2) + 24 - G2_PF + 1 + 6, n)));
    wait_vm_barrier<cmin(63, wt)>();
    if constexpr (n == G2_PUBB) { if (c.w == 1) publish_h(c, 48); }
    if constexpr (n == G2_PUBC) { if (c.w == 1) publish_h(c, 40); }
    SCHED_FENCE();
  }
  if constexpr (n + 1 < G2_NS) g2_slot<n + 1>(c, rd, hw, wf, acc, tailacc, E, Fn);
}
__device__ __forceinline__ unsigned umax4(const u32x4& v) {
  unsigned a = v[0] > v[1] ? v[0] : v[1], b = v[2] > v[3] ? v[2] : v[3];
  return a > b ? a : b;
}

__device__ __forceinline__ void g2_phase(Ctx& c, u32x4 (&hw)[G2_WIN], const f32x16* tailacc, K1Epi& E) {
  const int lane = lane_of(c);
  const int w = c.w, ks = c.fq, nt = c.nl, mt = c.mt;
  unsigned rd;
  {
    int gq = lane >> 4, chf = gq & 1, h = gq >> 1, q = (lane & 15) >> 2, p = lane & 3;
    rd = K1_XOFF + (4 * h + q) * 192 + chf * 32 + p * 8;
  }
  bf16x8 wf[G2_PF + 1];
  f32x16 acc[3];
  wf[0] = g2_read_w<0>(rd); wf[1] = g2_read_w<1>(rd); wf[2] = g2_read_w<2>(rd); wf[3] = g2_read_w<3>(rd);
  SCHED_FENCE();
  G2Fin Fn;
  g2_slot<0>(c, rd, hw, wf, acc, tailacc, E, Fn);
  const int kd = w >> 1, half = w & 1;
  if (kd != ks) {
    const unsigned base = g2_out_base(c);
#pragma unroll
    for (int t = 0; t < 3; ++t)
#pragma unroll
      for (int p2 = 0; p2 < 2; ++p2) {
        u32x4 o = {pk2(acc[t][8 * p2], acc[t][8 * p2 + 1]), pk2(acc[t][8 * p2 + 2], acc[t][8 * p2 + 3]), pk2(acc[t][8 * p2 + 4], acc[t][8 * p2 + 5]),
                   pk2(acc[t][8 * p2 + 6], acc[t][8 * p2 + 7])};
        st128(o, c.rs, c.lane16, base + (t * 2 + p2) * 1024, AUX_SC1);
      }
  } else {
#pragma unroll
    for (int t = 0; t < 3; ++t)
#pragma unroll
      for (int qd = 0; qd < 4; ++qd)
        *(LAS f32x4*)(ldsp(c.lane16) + (half * 12 + t * 4 + qd) * 1024) = (f32x4){acc[t][4 * qd], acc[t][4 * qd + 1], acc[t][4 * qd + 2], acc[t][4 * qd + 3]};
  }
  const int uh = w / 3, ut = w % 3;
  const int qp = lane >> 5, cc = lane & 31;
  lds_barrier();
  STAMP(33);
  if (w < 6) {
    char* optr = (char*)karg(11);
    u32x4 pp[3][2];
    float ga[16], og = 0.f;
    f32x4 own[2][2];
    bool zdone = false;
    unsigned spin = 0;
#pragma unroll 1
    for (; spin < SPIN_CAP; ++spin) {
      asm volatile("" ::: "memory");
      const bool zok = __all(umax4(Fn.zz[0]) != SENT && umax4(Fn.zz[1]) != SENT) || spin == SPIN_CAP - 1;
      if (!zok) g2_fin_load(c, Fn, false);
#pragma unroll
      for (int s = 0; s < 3; ++s) {
        const int src = s + (s >= ks ? 1 : 0);
#pragma unroll
        for (int h = 0; h < 2; ++h)
          pp[s][h] = __builtin_amdgcn_raw_buffer_load_b128(c.rs, qp * 1024 + (cc + 32 * h) * 16, WS_PART + c.b * 49152 + (src * 2 + uh) * 6144 + ut * 2048, AUX_SC1);
      }
      SCHED_FENCE();
      if (zok && !zdone) {
        zdone = true;
        LAS char* zt = ldsp(24576 + w * 2560);
#pragma unroll
        for (int i = 0; i < 2; ++i) *(LAS u32x4*)(zt + (16 * i + (lane >> 2)) * 80 + (lane & 3) * 16) = Fn.zz[i];
        unsigned zv[16];
#pragma unroll
        for (int j = 0; j < 16; ++j) zv[j] = *(LAS u16*)(zt + (16 * qp + j) * 80 + cc * 2);
#pragma unroll
        for (int o = 0; o < 2; ++o)
#pragma unroll
          for (int h = 0; h < 2; ++h) own[o][h] = *(LAS f32x4*)(ldsp((cc + 32 * h) * 16) + (uh * 12 + ut * 4 + 2 * qp + o) * 1024);
        og = rbf(1.0f - Fn.g);
#pragma unroll
        for (int j = 0; j < 16; j += 2) {
          const unsigned qw = Fn.qq[j >> 3][(j >> 1) & 3];
          const unsigned a2 = pk2(bflo(qw) * bflo(zv[j]), bfhi(qw) * bflo(zv[j + 1]));
          ga[j] = Fn.g * bflo(a2); ga[j + 1] = Fn.g * bfhi(a2);
        }
      }
      SCHED_FENCE();
      u32x4 mx = {umax4(pp[0][0]), umax4(pp[0][1]), umax4(pp[1][0]), umax4(pp[1][1])};
      if (zdone && __all(umax4(mx) != SENT && umax4(pp[2][0]) != SENT && umax4(pp[2][1]) != SENT)) break;
    }
    DBGCNT(46, spin);
    STAMP(34);
#pragma unroll
    for (int o = 0; o < 2; ++o) {
      float res[8];
#pragma unroll
      for (int h = 0; h < 2; ++h)
#pragma unroll
        for (int k = 0; k < 4; k += 2) {
          f32x2 bs = {own[o][h][k], own[o][h][k + 1]};
#pragma unroll
          for (int s = 0; s < 3; ++s) bs += unpk2(pp[s][h][2 * o + (k >> 1)]);
          const f32x2 b2 = unpk2(pk2(bs[0], bs[1]));
          res[4 * h + k] = radd(ga[8 * o + 4 * h + k], og * b2[0]);
          res[4 * h + k + 1] = radd(ga[8 * o + 4 * h + k + 1], og * b2[1]);
        }
      u32x4 ov = {pk2(res[0], res[1]), pk2(res[2], res[3]), pk2(res[4], res[5]), pk2(res[6], res[7])};
      *(u32x4*)(optr + ((size_t)(256 * mt + 64 * ks + 32 * uh + cc) * D_ + 96 * nt + 32 * ut + 16 * qp + 8 * o) * 2) = ov;
    }
  }
  STAMP(35);
}

template <bool PREF, bool FIRST, bool TAIL>
__device__ __forceinline__ void cv_block(const Ctx& c, f32x4 (&acc)[8], const bf16x8 (&af)[8], const bf16x8 (&bf)[8], bf16x8 (&naf)[8], bf16x8 (&nbf)[8],
                                         const unsigned (&ap)[4], unsigned bp, int roff, const f32x16* tailacc, K1Epi& E) {
  auto step = [&](auto K) {
    constexpr int k = decltype(K)::value;
    constexpr int p = [] { int r = k, q = 0; while (r >= 8 - q) { r -= 8 - q; ++q; } return q; }();
    constexpr int dl = [] { int r = k, q = 0; while (r >= 8 - q) { r -= 8 - q; ++q; } return r; }();
    if constexpr (PREF && (k & 1) == 0 && k < 32) {
      constexpr int f = k >> 2;
      if constexpr ((k & 2) != 0) naf[f] = cv_read_a(ldsp(ap[f >> 1]) + 512 * (f & 1) + roff);
      else nbf[f] = *(LAS bf16x8*)(ldsp(bp) + 64 * (56 - 8 * f) + roff);
    }
    if constexpr (FIRST && p == 0) acc[dl] = __builtin_amdgcn_mfma_f32_16x16x32_bf16(af[p], bf[dl], (f32x4){}, 0, 0, 0);
    else acc[p + dl] = __builtin_amdgcn_mfma_f32_16x16x32_bf16(af[p], bf[dl], acc[p + dl], 0, 0, 0);
    asm volatile("" : "+v"(acc[p + dl]));
    if constexpr (TAIL && (k & 1)) {
      if constexpr (k / 2 >= G2_TAIL0 && k / 2 <= G2_TAILST) k1_epi_step<1, k / 2 - G2_TAIL0>(c, tailacc, E, 64 + c.nl);
    }
    if constexpr (k & 1) SCHED_FENCE();
  };
  [&]<int... Ks>(std::integer_sequence<int, Ks...>) { (step(std::integral_constant<int, Ks>{}), ...); }(std::make_integer_sequence<int, 36>{});
}
__device__ __forceinline__ u32x4 cv_u(LAS char* cl, int g, int lane) {
  const int q = lane >> 4, pi = (lane >> 3) & 1, ii = lane & 7;
  return *(LAS u32x4*)(cl + (pi ? CV_U1 : CV_U0) + 2 * (2040 - (256 * g + 32 * ii + 8 * q)));
}
__device__ __forceinline__ u32x2 cv_z(const f32x4& y, const u32x4& uu, int lane, float Dv) {
  const int pi = (lane >> 3) & 1;
  float z[4];
#pragma unroll
  for (int k = 0; k < 4; ++k) z[k] = radd(y[k], bflo(uu[3 - k]) * Dv);
  unsigned p01 = pk2(z[0], z[1]), p23 = pk2(z[2], z[3]);
  unsigned snd = pi ? p01 : p23;
  unsigned rcv = __builtin_amdgcn_update_dpp(0u, snd, 0x128, 0xf, 0xf, false);
  unsigned Ev = pi ? rcv : p01, Od = pi ? p23 : rcv;
  return (u32x2){__builtin_amdgcn_perm(Od, Ev, 0x05040100u), __builtin_amdgcn_perm(Od, Ev, 0x07060302u)};
}
__device__ __forceinline__ void cv_phase(Ctx& c, const f32x16* tailacc, K1Epi& E, CvRegs& V) {
  const int w = c.w;
  f32x4 acc[8], acc8[8];
  bf16x8 a1[8], b1[8];
  cv_block<true, true, true>(c, acc, V.a0, V.b0, a1, b1, V.ap, V.bp, 64, tailacc, E);
  STAMP(24); SCHED_FENCE();
  wait_vm_barrier<1>();
  if (w == 1) publish_h(c, 48);
  const int lane = lane_of(c);
  const unsigned zb = WS_Z + 9 * cv_id(c) * (S_ * 2);
#pragma unroll
  for (int i = 0; i < 4; ++i) st128(sent4(), c.rs, c.lane16, zb + w * (S_ * 2) + i * 1024, AUX_SC1);
  __builtin_amdgcn_raw_buffer_store_b64(sent2(), c.rs, lane * 8, zb + 8 * (S_ * 2) + 512 * w, AUX_SC1);
  int zsl;
  const int zwg = cv_zwg(c, lane, zsl);
  const unsigned zev = __builtin_amdgcn_raw_buffer_load_b32(c.rs, zwg * 4, WS_EPOCH, 0);
  const unsigned dd = *(const u16*)(karg(5) + (9 * cv_id(c) + ((lane & 1) ? 8 : w)) * 2);
  SCHED_FENCE();
  cv_block<true, false, false>(c, acc, a1, b1, V.a0, V.b0, V.ap, V.bp, 128, tailacc, E);
  STAMP(25); SCHED_FENCE();
  const unsigned cb = cv_base(w), cb8 = cv_base(8);
  unsigned ap8[4];
#pragma unroll
  for (int i = 0; i < 4; ++i) ap8[i] = V.ap[i] + (cb8 - cb + 64 * w);
  SCHED_FENCE();
  cv_block<true, false, false>(c, acc, V.a0, V.b0, a1, b1, ap8, V.bp + (cb8 - cb + 64 * w), 0, tailacc, E);
  STAMP(26); SCHED_FENCE();
  cv_block<true, true, false>(c, acc8, a1, b1, V.a0, V.b0, V.ap, V.bp, 192, tailacc, E);
  STAMP(27); SCHED_FENCE();
  LAS char* sc = ldsp(((w & 2) ? k1_wbase(20) : k1_wbase(19)) + (w & 1) * 8192 + lane * 16);
  if (w >= 4) {
#pragma unroll
    for (int g = 0; g < 8; ++g) *(LAS f32x4*)(sc + g * 1024) = acc8[g];
  }
  SCHED_FENCE();
  cv_block<true, false, false>(c, acc, V.a0, V.b0, a1, b1, V.ap, V.bp, 256, tailacc, E);
  STAMP(28); SCHED_FENCE();
  asm volatile("s_waitcnt vmcnt(0) lgkmcnt(0)\n\ts_barrier" ::: "memory");
  if (w == 5) publish_h(c, 40);
  if (w == 6) flag_store(c, zsl >= 0, zwg, zsl, zev);
  if (w < 4) {
    f32x4 o[8];
#pragma unroll
    for (int g = 0; g < 8; ++g) o[g] = *(LAS f32x4*)(sc + g * 1024);
#pragma unroll
    for (int g = 0; g < 8; ++g) *(LAS f32x4*)(sc + g * 1024) = acc8[g] + o[g];
  }
  SCHED_FENCE();
  cv_block<true, false, false>(c, acc, a1, b1, V.a0, V.b0, V.ap, V.bp, 320, tailacc, E);
  STAMP(29); SCHED_FENCE();
  lds_barrier();
  const float Dv = bflo((unsigned)__builtin_amdgcn_readlane((int)dd, 0)), Dv8 = bflo((unsigned)__builtin_amdgcn_readlane((int)dd, 1));
  const int zo = (32 * (lane & 7) + 8 * (lane >> 4) + 4 * ((lane >> 3) & 1)) * 2;
  {
    LAS char* s0 = ldsp(w * 1024 + lane * 16);
    f32x4 y = (*(LAS f32x4*)(s0 + k1_wbase(19)) + *(LAS f32x4*)(s0 + k1_wbase(19) + 8192)) +
              (*(LAS f32x4*)(s0 + k1_wbase(20)) + *(LAS f32x4*)(s0 + k1_wbase(20) + 8192));
    __builtin_amdgcn_raw_buffer_store_b64(cv_z(y, cv_u(ldsp(cb8), w, lane), lane, Dv8), c.rs, zo, zb + 8 * (S_ * 2) + 512 * w, AUX_SC1);
  }
  SCHED_FENCE();
  cv_block<true, false, false>(c, acc, V.a0, V.b0, a1, b1, V.ap, V.bp, 384, tailacc, E);
  STAMP(30); SCHED_FENCE();
  cv_block<true, false, false>(c, acc, a1, b1, V.a0, V.b0, V.ap, V.bp, 448, tailacc, E);
  STAMP(31); SCHED_FENCE();
  LAS char* cl = ldsp(cb);
  u32x4 uu[8];
#pragma unroll
  for (int g = 0; g < 8; ++g) uu[g] = cv_u(cl, g, lane);
  SCHED_FENCE();
  cv_block<false, false, false>(c, acc, V.a0, V.b0, a1, b1, V.ap, V.bp, 0, tailacc, E);
  STAMP(32); SCHED_FENCE();
#pragma unroll
  for (int g = 0; g < 8; ++g) *(LAS u32x2*)(cl + 512 * g + zo) = cv_z(acc[g], uu[g], lane, Dv);
#pragma unroll
  for (int i = 0; i < 4; ++i)
    st128(*(LAS u32x4*)(cl + i * 1024 + c.lane16), c.rs, c.lane16, zb + w * (S_ * 2) + i * 1024, AUX_SC1);
  STAMP(35);
}

template <int ROLE>
__device__ __forceinline__ void fused_body(const int w) {
  const int b = (blockIdx.x + 192) & 255, xcd = b & 7, r = b >> 3;
  const int ml = r & 3, nl = r >> 2, fq = xcd >> 1;
  const int mt = 4 * (xcd & 1) + ml;
  const int lane = __builtin_amdgcn_mbcnt_hi(~0u, __builtin_amdgcn_mbcnt_lo(~0u, 0u));
  Ctx c;
  RSTAMP(37);
  STAMP(0);
  c.w = w; c.nl = nl; c.ml = ml; c.b = b; c.mt = mt; c.fq = fq;
  c.rot = (nl + ml) % 3;
  c.lane16 = lane * 16;
  asm volatile("" : "+v"(c.lane16));
  c.sw1 = make_srd(karg(6), (1152 * fq + 128 * nl) * 2, D_ * F_ * 2);
  c.sw3 = make_srd(karg(7), (1152 * fq + 128 * nl) * 2, D_ * F_ * 2);
  c.sx = make_srd(karg(0), (256 * mt + 32 * w) * (D_ * 2), S_ * D_ * 2);
  {
    const char* ws = karg(10);
    c.sws = make_srd(ws, 0, WS_END);
    c.rs = __builtin_amdgcn_make_buffer_rsrc((void*)ws, 0, WS_END, 0x00020000);
  }
#pragma unroll
  for (int i = 0; i < 3; ++i) { const int k = kslice(i, c.rot); c.wo[i] = k * (64 * F_ * 2); c.xo[i] = k * 128; }
  c.hb = WS_H + ((8 * mt + w) * 288 + 72 * fq) * 1024;
  {
    int row = lane >> 3, pos = lane & 7;
    c.voff_w = (8 * w + row) * (F_ * 2) + ((pos ^ (2 * ((row >> 1) & 1))) * 16);
    c.voff_x = row * (D_ * 2) + ((pos ^ (lane >> 4)) * 16);
  }
  k1_issue_w<0, 0>(c); k1_issue_w<0, 1>(c);
  k1_issue_x<0, 0>(c); k1_issue_x<0, 1>(c); k1_issue_x<0, 2>(c); k1_issue_x<0, 3>(c);
  {
    const int ewg = lane < 8 ? cv_wg(8 * xcd + lane) : (lane < 14 ? g2_wg(mt, lane - 8, fq) : ((lane & 60) == 16 ? g2_wg(mt, nl < 6 ? nl : 0, lane & 3) : b));
    const unsigned so = WS_EPOCH;
    asm volatile("buffer_load_dword %0, %1, %2, %3 offen" : "=v"(c.ev) : "v"((unsigned)ewg * 4), "s"(c.sws), "s"(so) : "memory");
  }
  k1_issue_w<1, 0>(c); k1_issue_w<1, 1>(c);
  {
    int gq = lane >> 4, mat = gq & 1, h = gq >> 1, q = (lane & 15) >> 2, p = lane & 3;
    int X = 2 * (q >> 1) + 4 * mat;
#pragma unroll
    for (int t = 0; t < 4; ++t) c.rd[t] = mat * 8192 + (8 * h + q) * 128 + (((2 * t) ^ X) + (p >> 1)) * 16 + (p & 1) * 8;
    int cc = lane & 31, hh = lane >> 5;
    c.xl0 = K1_XOFF + w * (K1_XD * 4096) + cc * 128 + ((hh ^ ((cc >> 1) & 7)) * 16);
  }
  {
    const u32x4 sw2 = make_srd(karg(8), 1152 * fq * D_ * 2, F_ * D_ * 2), ssk = make_srd(karg(4), 0, D_ * S_ * 2);
    c.s2 = ROLE == 0 ? sw2 : ssk;
    c.s3 = ROLE == 0 ? ssk : sw2;
    const unsigned wv = 8 * r + w, mh = xcd & 1;
    unsigned o = OOB;
    if (lane < 12) {
      const unsigned i = wv + 256 * lane, row = (i * 6554u) >> 16, cl = i - 10 * row;
      if (i < 2880) o = (2 * row + mh + 1) * (F_ * 2) + (cl < 8 ? 2 * cl + 1 : cl + 8) * 128 - 256 * nl - F_ * 2;
      if (i < 2880 && 2 * row + mh == 0 && (cl < 8 ? 2 * cl + 1 : cl + 8) * 128 < 256u * nl) o = OOB;
    } else if (lane >= 16 && lane < 37) {
      const unsigned i = wv + 256 * (lane - 16), row = (i * 7282u) >> 16, cl = i - 9 * row;
      if (i < 5184) o = (2 * row + mh) * (D_ * 2) + cl * 128;
    } else if (lane >= 48 && lane < 57) {
      o = 72 * xcd * (S_ * 2) + (wv + 256 * (lane - 48)) * 128;
    }
    c.warm = o;
    c.touch = 0;
    asm volatile("" : "+v"(c.warm), "+v"(c.touch));
  }
  K1Prep P;
  {
    const u32x4 sk_ = make_srd(karg(1), 0, S_ * D_ * 2), sv_ = make_srd(karg(2), 0, S_ * D_ * 2);
#pragma unroll
    for (int j = 0; j < 3; ++j) {
      const int q = 3 * w + j, isv = q >= 10;
      const unsigned pc = (unsigned)(64 * (q - 10 * isv) + lane), row = (pc * 7282u) >> 16;
      const int t = 64 * r - (isv ? 1 : 4) + (int)row;
      const bool ok = q < 20 && t >= 0 && row < (isv ? 65u : 68u);
      const unsigned vo = ok ? (unsigned)t * (D_ * 2) + (pc - 9 * row) * 16 + 144 * xcd : OOB;
      const u32x4 sd = {isv ? sv_[0] : sk_[0], isv ? sv_[1] : sk_[1], sk_[2], sk_[3]};
      dma16<K1_POFF, 0>(vo, sd, 0u, q < 20 ? (unsigned)((q - 10 * isv) * 1024 + isv * K1_PV) : 24576u + (q - 20) * 1024);
    }
    const char *skp = karg(3), *gw = karg(9);
    const char* gx = karg(0) + (size_t)(256 * mt + 64 * fq + 8 * nl + w) * (D_ * 2);
    const int dp = 8 * w + (lane >> 3), dc = dp < 36 ? dp : 35;
    P.lp = K1_POFF + 8 * (7 - (lane & 7)) * 144 + dc * 4;
    asm volatile("" : "+v"(P.lp));
    GLD4(P.sk, (unsigned)(72 * xcd + 2 * dc) * 8, skp);
    GLD4(P.ga0, c.lane16, gx);
    GLD4(P.ga1, 1024 + (c.lane16 & 127), gx);
    GLD4(P.gw0, c.lane16, gw);
    GLD4(P.gw1, 1024 + (c.lane16 & 127), gw);
  }
  bf16x8 xf[36];
  bf16x8 wf[K1_RD + 1];
  f32x16 acc[4];
  u32x4 hw[G2_WIN];
  asm volatile("s_waitcnt vmcnt(10)\n\ts_barrier" : "+v"(c.ev)::"memory");
  STAMP(1);
  k1_issue_x<1, 0>(c); k1_issue_x<1, 1>(c); k1_issue_x<1, 2>(c); k1_issue_x<1, 3>(c);
  xf[0] = k1_read_x<0, 0>(c); wf[0] = k1_read_w<0>(c);
  xf[1] = k1_read_x<0, 1>(c); wf[1] = k1_read_w<1>(c);
  xf[2] = k1_read_x<0, 2>(c); wf[2] = k1_read_w<2>(c);
  xf[3] = k1_read_x<0, 3>(c);
  static_assert(K1_RD == 3, "prologue reads");
  SCHED_FENCE();
  STAMP(2);
  K1Epi E;
  CvRegs V;
  k1_slot<0, ROLE>(c, xf, wf, acc, E, P, hw, V);
  STAMP(23);
  if constexpr (ROLE == 1) cv_phase(c, acc, E, V);
  else {
    g2_phase(c, hw, acc, E);
    __builtin_amdgcn_s_waitcnt(0);
  }
}

__global__ __launch_bounds__(512, 2) void fused_kernel(const void* x, const void* kk, const void* vv, const void* shift_k, const void* ssm_k,
                                                       const void* ssm_D, const void* w1, const void* w3, const void* w2, const void* gate_w, void* ws,
                                                       void* out) {
  const int w = __builtin_amdgcn_readfirstlane(threadIdx.x >> 6);
  if (blockIdx.x < 64) fused_body<1>(w);
  else fused_body<0>(w);
}

extern "C" void kernel_launch(void* const* d_in, const int* in_sizes, int n_in, void* d_out, int out_size, void* d_ws, size_t ws_size,
                              hipStream_t stream) {
  (void)hipFuncSetAttribute((const void*)fused_kernel, hipFuncAttributeMaxDynamicSharedMemorySize, LDS_BYTES);
  fused_kernel<<<256, 512, LDS_BYTES, stream>>>(d_in[0], d_in[1], d_in[2], d_in[3], d_in[4], d_in[5], d_in[7], d_in[8], d_in[9], d_in[10], d_ws, d_out);
}
```

```cpp
#include <hip/hip_runtime.h>
#include <hip/hip_bf16.h>

#define LAS __attribute__((address_space(3)))
#define GAS __attribute__((address_space(1)))
typedef unsigned short u16;
typedef __bf16 bf16x2 __attribute__((ext_vector_type(2)));
typedef __bf16 bf16x4 __attribute__((ext_vector_type(4)));
typedef __bf16 bf16x8 __attribute__((ext_vector_type(8)));
typedef float f32x2 __attribute__((ext_vector_type(2)));
typedef float f32x4 __attribute__((ext_vector_type(4)));
typedef float f32x16 __attribute__((ext_vector_type(16)));
typedef unsigned u32x2 __attribute__((ext_vector_type(2)));
typedef unsigned u32x4 __attribute__((ext_vector_type(4)));

constexpr int S_ = 2048, D_ = 576, F_ = 4608;
constexpr unsigned WS_H = 0;
constexpr unsigned WS_PART = WS_H + 64 * 288 * 1024;
constexpr unsigned WS_UR = WS_PART + 256 * 49152;
constexpr unsigned WS_Z = WS_UR + D_ * 2 * S_ * 2;
constexpr unsigned WS_G = WS_Z + D_ * S_ * 2;
constexpr unsigned WS_FLAG = WS_G + S_ * 4;
constexpr unsigned WS_EPOCH = WS_FLAG + 256 * 512;
constexpr unsigned WS_END = WS_EPOCH + 1024;
constexpr unsigned FLAG_MAGIC = 0x7c3a91e5u, SENT = 0xffffffffu;
constexpr int AUX_SC1 = 16;
constexpr unsigned OOB = 0x7ff00000u;
constexpr unsigned SPIN_CAP = 1u << 16;

#define STAMP(i)
#define RSTAMP(i)
#define STDUMP()
#define DBGCNT(i, x)

__device__ __forceinline__ float bflo(unsigned v) { return __builtin_bit_cast(float, v << 16); }
__device__ __forceinline__ float bfhi(unsigned v) { return __builtin_bit_cast(float, v & 0xffff0000u); }
__device__ __forceinline__ float rbf(float x) { return (float)(__bf16)x; }
__device__ __forceinline__ unsigned pk2(float lo, float hi) {
  f32x2 v = {lo, hi};
  return __builtin_bit_cast(unsigned, __builtin_convertvector(v, bf16x2));
}
__device__ __forceinline__ f32x2 unpk2(unsigned v) { return (f32x2){bflo(v), bfhi(v)}; }
__device__ __forceinline__ float radd(float a, float b) { const unsigned t = pk2(a, b); return bflo(t) + bfhi(t); }
#define SCHED_FENCE() __builtin_amdgcn_sched_barrier(0)
__device__ __forceinline__ void st128(u32x4 v, __amdgpu_buffer_rsrc_t rs, unsigned voff, unsigned soff, int) {
  __builtin_amdgcn_raw_buffer_store_b128(v, rs, voff, soff, AUX_SC1);
  asm volatile("s_nop 1" ::"v"(v));
}

template <int LC, int SC> __device__ __forceinline__ void dma16(unsigned voff, const u32x4& srd, unsigned so, unsigned lb) {
  unsigned t;
  asm volatile("s_add_u32 m0, %4, %5\n\ts_add_u32 %0, %3, %6\n\tbuffer_load_dwordx4 %1, %2, %0 offen lds"
               : "=&s"(t) : "v"(voff), "s"(srd), "s"(so), "s"(lb), "n"(LC), "n"(SC) : "memory", "scc");
}
template <int N> __device__ __forceinline__ void wait_vm_barrier() {
  asm volatile("s_waitcnt vmcnt(%0)\n\ts_barrier" ::"n"(N) : "memory");
}
template <int N> __device__ __forceinline__ void wait_vm() { asm volatile("s_waitcnt vmcnt(%0)" ::"n"(N) : "memory"); }
__device__ __forceinline__ void lds_barrier() { asm volatile("s_waitcnt lgkmcnt(0)\n\ts_barrier" ::: "memory"); }
__device__ __forceinline__ LAS char* ldsp(unsigned a) { return (LAS char*)(size_t)a; }
__device__ __forceinline__ bf16x8 tr_frag(LAS char* p0, LAS char* p1) {
  bf16x4 a = __builtin_amdgcn_ds_read_tr16_b64_v4bf16((LAS bf16x4*)p0);
  bf16x4 b = __builtin_amdgcn_ds_read_tr16_b64_v4bf16((LAS bf16x4*)p1);
  return __builtin_shufflevector(a, b, 0, 1, 2, 3, 4, 5, 6, 7);
}
constexpr int cmin(int a, int b) { return a < b ? a : b; }
__device__ __forceinline__ const char* karg(int i) {
  typedef const char* cptr;
  const __attribute__((address_space(4))) cptr* p = (const __attribute__((address_space(4))) cptr*)__builtin_amdgcn_kernarg_segment_ptr();
  asm volatile("" : "+s"(p));
  return p[i];
}
__device__ __forceinline__ u32x4 make_srd(const char* base, unsigned off, unsigned total) {
  const unsigned long long a = (unsigned long long)base + off;
  return (u32x4){(unsigned)a, (unsigned)(a >> 32) & 0xffffu, total - off, 0x00020000u};
}

constexpr int K1_SLOT = 16384, K1_XOFF = 65536, K1_XD = 2, K1_POFF = 131072, K1_PV = 10240;
constexpr int LDS_BYTES = 163840;
constexpr int K1_PF = 4, K1_RD = 3, K1_NS = 324, K1_NSEG = 21;
constexpr int K1_PREP0 = 28, K1_PREP_UNITS = 8 + 2 + 63 + 1 + 4;
constexpr int G2_SLOT = 24576, G2_NSLOT = 4, G2_WIN = 16, G2_PF = 4, G2_NS = 216;
constexpr int CV_K = 0, CV_U0 = 4160, CV_U1 = CV_U0 + 4640, CV_CH = 13440;
constexpr int k1_wbase(int s) { return s == 11 ? K1_XOFF : (s == 12 ? K1_XOFF + K1_SLOT : ((s < 11 ? s : s - 2) % 4) * K1_SLOT); }
constexpr int k1_wprev(int s) { return s < 4 || s == 11 || s == 12 ? -1 : (s < 11 ? s - 4 : (s < 17 ? s - 6 : s - 4)); }
constexpr int EV_PUB_U = 140;
constexpr int EV_PUB_A = 204;
constexpr int EV_PRE0 = 221, EV_PRE_STEP = 4;
constexpr int EV_CV_POLL = 176, EV_CV_CHECK = 190, EV_CV_INVW = 219;
constexpr int EV_CV_PRE7 = 300;
constexpr int EV_CV_PRE8 = 316;
constexpr int EV_SENT0 = 177, EV_SENT_STEP = 7, EV_PUB_S = 236;
constexpr int EV_G2_POLL = 264, EV_G2_CHECK = 280, EV_G2_H0 = 289;
constexpr int WARM_N = 10;
constexpr int WARM_SLOT[WARM_N] = {18, 34, 50, 66, 82, 98, 118, 142, 158, 178};
constexpr int WARM_DONE = 219;
constexpr int EV_PUB_B1 = 316;
constexpr int K1_EPI0[2] = {125, 269};
constexpr int k1_epi_slot(int ch, int e) { return K1_EPI0[ch] + (ch == 0 ? e - e / 12 : e); }

struct K1Sched {
  int seg[K1_NS], tile[K1_NS], j[K1_NS];
  int first_use[K1_NSEG], last_use[K1_NSEG];
  int wissue[K1_NSEG];
  int xissue[9];
  bool bar[K1_NS];
  int ops[K1_NS];
  int wait[K1_NS];
  int done[K1_NS];
  constexpr int ops_after(int a, int b) const { int k = 0; for (int q = a + 1; q <= b; ++q) k += ops[q]; return k; }
};
constexpr K1Sched k1_make(int role) {
  K1Sched S{};
  int n = 0;
  for (int s = 0; s < 18;) {
    if (s == 7 || s == 9 || s == 16) {
      const int tt[8] = {0, 1, 0, 1, 2, 2, 3, 3}, hh[8] = {0, 0, 1, 1, 0, 1, 0, 1};
      for (int q = 0; q < 8; ++q) for (int j = 0; j < 4; ++j) { S.seg[n] = s + hh[q]; S.tile[n] = tt[q]; S.j[n] = j; ++n; }
      s += 2;
    } else {
      for (int t = 0; t < 4; ++t) for (int j = 0; j < 4; ++j) { S.seg[n] = s; S.tile[n] = t; S.j[n] = j; ++n; }
      s += 1;
    }
  }
  for (int i = 0; i < 36; ++i) { S.seg[n] = 18 + i / 16; S.tile[n] = 0; S.j[n] = i % 16; ++n; }
  for (int s = 0; s < K1_NSEG; ++s) { S.first_use[s] = 100000; S.last_use[s] = -1; }
  for (int i = 0; i < K1_NS; ++i) { int s = S.seg[i]; if (i < S.first_use[s]) S.first_use[s] = i; if (i > S.last_use[s]) S.last_use[s] = i; }
  for (int s = 1; s < K1_NSEG; ++s) S.bar[S.first_use[s] - K1_PF - 1] = true;
  S.wissue[0] = S.wissue[1] = -1;
  S.wissue[2] = 0; S.wissue[3] = 12;
  int busy = 0;
  for (int s = 4; s < K1_NSEG; ++s) {
    int q = k1_wprev(s) >= 0 ? S.last_use[k1_wprev(s)] : S.first_use[8] + 4;
    while (!S.bar[q]) ++q;
    int is = q + 1;
    if (is < busy) is = busy;
    S.wissue[s] = is;
    busy = is + (s >= 11 ? 8 : 4);
  }
  S.xissue[0] = S.xissue[1] = -1;
  for (int p = 2; p < 9; ++p) S.xissue[p] = S.first_use[p - 2] + 4;
  for (int s = 0; s < K1_NSEG; ++s) if (S.wissue[s] >= 0) { S.ops[S.wissue[s]]++; S.ops[S.wissue[s] + 2]++; }
  for (int p = 0; p < 9; ++p) if (S.xissue[p] >= 0) for (int i = 0; i < 4; ++i) S.ops[S.xissue[p] + i]++;
  S.ops[K1_PREP0 + 7]++;
  for (int i = 0; i < WARM_N; ++i) S.ops[WARM_SLOT[i]]++;
  for (int i = 0; i < 4; ++i) S.ops[K1_PREP0 + K1_PREP_UNITS - 4 + i]++;
  for (int c = 0; c < 2; ++c) for (int t = 0; t < 4; ++t) if (k1_epi_slot(c, 12 * t + 13) < K1_NS) S.ops[k1_epi_slot(c, 12 * t + 13)]++;
  if (role == 0) {
    for (int i = 0; i < G2_WIN; ++i) S.ops[EV_G2_H0 + 2 * i]++;
    for (int i = 0; i < 6; ++i) S.ops[EV_SENT0 + EV_SENT_STEP * i]++;
  } else {
    S.ops[EV_CV_PRE8]++; S.ops[EV_CV_PRE8 + 1]++;
  }
  if (role != 2) for (int i = 0; i < 12; ++i) S.ops[EV_PRE0 + EV_PRE_STEP * i]++;
  if (role == 2) for (int i = 0; i < 12; ++i) S.ops[EV_CV_PRE7 + i]++;
  for (int i = 0; i < K1_NS; ++i) {
    S.wait[i] = 63;
    S.done[i] = -2;
    if (!S.bar[i]) continue;
    int youngest = -2;
    for (int s = 1; s < K1_NSEG; ++s)
      if (S.first_use[s] - K1_PF - 1 == i) {
        int y = S.wissue[s] < 0 ? -1 : S.wissue[s] + 2;
        if (s < 9) { int yx = S.xissue[s] < 0 ? -1 : S.xissue[s] + 3; if (yx > y) y = yx; }
        if (y > youngest) youngest = y;
      }
    if (i == EV_PUB_A - 1 && youngest < k1_epi_slot(0, 49)) youngest = k1_epi_slot(0, 49);
    if (i == EV_PUB_S - 1 && youngest < EV_SENT0 + 5 * EV_SENT_STEP) youngest = EV_SENT0 + 5 * EV_SENT_STEP;
    if (i == WARM_DONE && youngest < WARM_SLOT[WARM_N - 1]) youngest = WARM_SLOT[WARM_N - 1];
    if (i == EV_PUB_B1 - 1 && youngest < k1_epi_slot(1, 25)) youngest = k1_epi_slot(1, 25);
    if (i == 299 && youngest < EV_PRE0 + 11 * EV_PRE_STEP) youngest = EV_PRE0 + 11 * EV_PRE_STEP;
    S.wait[i] = cmin(63, S.ops_after(youngest, i));
    S.done[i] = youngest;
  }
  return S;
}
constexpr K1Sched K1S[3] = {k1_make(0), k1_make(1), k1_make(2)};
static_assert(K1_PREP0 + K1_PREP_UNITS - 1 <= K1S[0].done[EV_PUB_U - 1] && K1S[0].bar[EV_PUB_U - 1], "u stores complete before publish");
static_assert(K1S[0].bar[EV_PUB_A - 1] && K1S[0].bar[EV_PUB_S - 1] && K1S[0].bar[299] && K1S[0].bar[315], "events");
static_assert(K1S[0].bar[EV_G2_CHECK + 3] && K1S[0].bar[EV_CV_INVW] && K1S[0].bar[EV_CV_PRE7 - 1] && K1S[0].last_use[17] < EV_CV_PRE7, "events");
static_assert(K1S[0].bar[EV_CV_PRE8 - 1] && K1S[0].last_use[18] < EV_CV_PRE8 && EV_G2_H0 + 2 * G2_WIN <= K1_NS + 1, "events");
static_assert(EV_PRE0 > EV_CV_INVW && K1S[0].last_use[12] <= EV_CV_INVW, "events");
static_assert([] {
  for (int t = 0; t < 4; ++t) {
    int last = 0, next = 1000;
    for (int i = 0; i < 144; ++i) if (K1S[0].tile[i] == t) last = i;
    for (int i = 287; i >= 144; --i) if (K1S[0].tile[i] == t) next = i;
    if (k1_epi_slot(0, 12 * t) < last + 2 || k1_epi_slot(0, 12 * t + 9) >= next) return false;
  }
  return true;
}(), "epilogue windows");

struct Ctx {
  u32x4 sw1, sw3, sx, s2, s3, sws;
  __amdgpu_buffer_rsrc_t rs;
  unsigned wo[3], xo[3];
  unsigned hb;
  unsigned voff_w, voff_x;
  unsigned rd[4];
  unsigned xl0, lane16;
  unsigned ev;
  u32x2 poll;
  unsigned touch, warm;
  const char* dptr;
  unsigned rc0, rc1;
  int w, rot, nl, ml, b, mt, fq;
};
struct K1Prep {
  u32x4 sk, ga0, ga1, gw0, gw1;
  unsigned lp, kn, vn, o0;
  f32x2 s[4], k2[4], a, p, q; unsigned u[9]; float gs;
};
struct K1Epi { float m[2], p[2], pq[2], r[2], h[2]; u32x4 ho; };
struct CvRegs { unsigned ap[4], bp; bf16x8 a0[8], b0[8]; };

__device__ __forceinline__ int lane_of(const Ctx& c) {
  unsigned l = c.lane16;
  asm volatile("" : "+v"(l));
  return (int)(l >> 4);
}
__device__ __forceinline__ u32x4 sent4() {
  u32x4 v = {SENT, SENT, SENT, SENT};
  asm volatile("" : "+v"(v));
  return v;
}
__device__ __forceinline__ u32x2 sent2() {
  u32x2 v = {SENT, SENT};
  asm volatile("" : "+v"(v));
  return v;
}
__device__ __forceinline__ u32x4 zero4() {
  u32x4 v = {0u, 0u, 0u, 0u};
  asm volatile("" : "+v"(v));
  return v;
}
__device__ __forceinline__ int g2_wg(int mt, int nt, int ks) { return ((nt * 4 + (mt & 3)) << 3) | (ks * 2 + (mt >> 2)); }
__device__ __forceinline__ int cv_wg(int cw) { return ((24 + (cw & 7)) << 3) | (cw >> 3); }
__device__ __forceinline__ int cv_id(const Ctx& c) { return (c.b & 7) * 8 + (c.b >> 3) - 24; }
__device__ __forceinline__ void flag_store(const Ctx& c, bool act, int wg, int slot, unsigned epoch) {
  asm volatile("" : "+v"(epoch));
  __builtin_amdgcn_raw_buffer_store_b64((u32x2){epoch + 1u, FLAG_MAGIC}, c.rs, act ? wg * 512 + slot * 8 : OOB, WS_FLAG, AUX_SC1);
}
__device__ __forceinline__ void publish_h(const Ctx& c, int slot0) {
  const int l = lane_of(c);
  flag_store(c, l >= 8 && l < 14, g2_wg(c.mt, l - 8, c.fq), slot0 + c.nl, c.ev);
}
__device__ __forceinline__ void poll_issue(Ctx& c) {
  const unsigned vo = lane_of(c) * 8;
  const unsigned so = WS_FLAG + c.b * 512;
  asm volatile("s_cmp_lg_u32 %4, 0\n\ts_cbranch_scc1 1f\n\tbuffer_load_dwordx2 %0, %1, %2, %3 offen sc1\n1:"
               : "=v"(c.poll) : "v"(vo), "s"(c.sws), "s"(so), "s"(c.w) : "memory", "scc");
}
template <int DBG = 0> __device__ __forceinline__ void poll_wait(Ctx& c, bool need) {
  const unsigned want = (unsigned)__builtin_amdgcn_readlane((int)c.ev, 14) + 1u;
  u32x2 v = c.poll;
  unsigned spin = 0;
#pragma unroll 1
  for (; spin < SPIN_CAP; ++spin) {
    if (__all(!need || (v[0] == want && v[1] == FLAG_MAGIC))) break;
    const unsigned vo = lane_of(c) * 8, so = WS_FLAG + c.b * 512;
    asm volatile("s_sleep 1\n\tbuffer_load_dwordx2 %0, %1, %2, %3 offen sc1\n\ts_waitcnt vmcnt(0)" : "=&v"(v) : "v"(vo), "s"(c.sws), "s"(so) : "memory");
  }
  DBGCNT(40 + DBG, spin);
  asm volatile("" ::: "memory");
}
#define POLL_LANDED(N) asm volatile("s_cmp_lg_u32 %2, 0\n\ts_cbranch_scc1 1f\n\ts_waitcnt vmcnt(%1)\n1:" : "+v"(c.poll) : "n"(N), "s"(c.w) : "memory", "scc")

__device__ __forceinline__ int kslice(int p, int r) {
  const int g = (p >= 6) ? 6 : (p >= 3 ? 3 : 0);
  const int v = p - g + r;
  return g + (v >= 3 ? v - 3 : v);
}
template <int sg, int half> __device__ __forceinline__ void k1_issue_w(const Ctx& c) {
  constexpr int dst = k1_wbase(sg) + half * 8192;
  if constexpr (sg < 18) {
    constexpr int p = sg % 9;
    dma16<dst, (p / 3 * 3) * 64 * F_ * 2 + 128 * (sg / 9)>(half ? (c.voff_w ^ 64u) : c.voff_w, half ? c.sw3 : c.sw1, c.wo[p % 3], c.w * 1024);
  } else {
    const unsigned l = lane_of(c);
    const unsigned rp = l >> 1;
    unsigned vo = (half ? (rp ^ 4u) : rp) * (F_ * 2) + (l & 1u) * 16;
    const int col = (1024 + 16 * c.nl - 128 * c.nl) * 2;
    if constexpr (sg < 20) {
      const int ksl = kslice(4 * (sg - 18) + (c.w >> 1), c.rot);
      dma16<dst, 0>(vo, half ? c.sw3 : c.sw1, (64 * ksl + 32 * (c.w & 1)) * (F_ * 2) + col, c.w * 1024);
    } else {
      if (l >= 16) vo = OOB;
      dma16<dst, 6 * 64 * F_ * 2>(vo, half ? c.sw3 : c.sw1, c.wo[2] + col + 8 * c.w * (F_ * 2), c.w * 1024);
    }
  }
}
template <int p, int i> __device__ __forceinline__ void k1_issue_x(const Ctx& c) {
  dma16<K1_XOFF + (p % K1_XD) * 4096 + i * 1024, 8 * i * D_ * 2 + (p / 3 * 3) * 128>((i & 1) ? (c.voff_x ^ 64u) : c.voff_x, c.sx, c.xo[p % 3],
                                                                                     c.w * (K1_XD * 4096));
}
template <int p, int j> __device__ __forceinline__ bf16x8 k1_read_x(const Ctx& c) {
  return *(LAS bf16x8*)(ldsp(c.xl0 ^ (32 * j)) + (p % K1_XD) * 4096);
}
template <int n> __device__ __forceinline__ bf16x8 k1_read_w(Ctx& c) {
  constexpr int sg = K1S[0].seg[n], t = K1S[0].tile[n], j = K1S[0].j[n];
  if constexpr (sg < 18) {
    if constexpr ((sg == 11 || sg == 13) && j == 0) {
      c.rd[t] += sg == 11 ? K1_XOFF : -K1_XOFF;
      asm volatile("" : "+v"(c.rd[t]));
    }
    LAS char* p = ldsp(c.rd[t]) + k1_wbase(sg) % K1_XOFF + (16 * j) * 128;
    return tr_frag(p, p + 4 * 128);
  } else if constexpr (sg < 20) {
    const int off = k1_wbase(sg) + (16 * j) * 32;
    return tr_frag(ldsp(c.rc0) + off, ldsp(c.rc1) + off);
  } else {
    const unsigned ex = (lane_of(c) >> 5) * 768u;
    const int off = k1_wbase(sg) + j * 2048;
    return tr_frag(ldsp(c.rc0 + ex) + off, ldsp(c.rc1 + ex) + off);
  }
}
#define GLD1(dst, voff, sb) asm volatile("global_load_dword %0, %1, %2" : "=v"(dst) : "v"(voff), "s"(sb) : "memory")
#define GLD4(dst, voff, sb) asm volatile("global_load_dwordx4 %0, %1, %2" : "=v"(dst) : "v"(voff), "s"(sb) : "memory")
template <int ctrl> __device__ __forceinline__ float dpp_add(float v) {
  return v + __builtin_bit_cast(float, __builtin_amdgcn_update_dpp(0, __builtin_bit_cast(int, v), ctrl, 0xf, 0xf, true));
}
__device__ __forceinline__ float rb1(float x) { return __builtin_bit_cast(float, pk2(0.f, x)); }
__device__ __forceinline__ f32x2 rb2(f32x2 v) { return (f32x2){rb1(v[0]), rb1(v[1])}; }
template <int k> __device__ __forceinline__ void k1_prep_unit(Ctx& c, K1Prep& P) {
  if constexpr (k == 0) {
    asm volatile("" : "+v"(P.sk), "+v"(P.ga0), "+v"(P.ga1), "+v"(P.gw0), "+v"(P.gw1), "+v"(c.ev)::"memory");
    P.gs = 0.f;
  }
  if constexpr (k < 4) {
    const u32x4 &a = k < 2 ? P.ga1 : P.ga0, &g = k < 2 ? P.gw1 : P.gw0;
#pragma unroll
    for (int i = 2 * (k & 1); i < 2 * (k & 1) + 2; ++i) P.gs = __builtin_fmaf(bfhi(a[i]), bfhi(g[i]), __builtin_fmaf(bflo(a[i]), bflo(g[i]), P.gs));
    if constexpr (k == 1) P.gs = lane_of(c) < 8 ? P.gs : 0.f;
    asm volatile("" : "+v"(P.gs));
  } else if constexpr (k == 4) {
    P.gs = dpp_add<0x111>(P.gs); P.gs = dpp_add<0x112>(P.gs);
    P.u[0] = *(LAS unsigned*)(ldsp(P.lp)); P.u[1] = *(LAS unsigned*)(ldsp(P.lp) + 144);
  } else if constexpr (k == 5) {
    P.gs = dpp_add<0x114>(P.gs); P.gs = dpp_add<0x118>(P.gs);
    P.u[2] = *(LAS unsigned*)(ldsp(P.lp) + 288); P.kn = *(LAS unsigned*)(ldsp(P.lp) + 432);
  } else if constexpr (k == 6) {
    int v = __builtin_bit_cast(int, P.gs);
    float a = __builtin_bit_cast(float, __builtin_amdgcn_readlane(v, 15)), b = __builtin_bit_cast(float, __builtin_amdgcn_readlane(v, 31));
    float d = __builtin_bit_cast(float, __builtin_amdgcn_readlane(v, 47)), e = __builtin_bit_cast(float, __builtin_amdgcn_readlane(v, 63));
    P.gs = __builtin_amdgcn_exp2f(rb1((a + b) + (d + e)) * -1.44269504088896f);
  } else if constexpr (k == 7) {
    float g = rb1(__builtin_amdgcn_rcpf(1.0f + P.gs));
    __builtin_amdgcn_raw_buffer_store_b32(__builtin_bit_cast(unsigned, g), c.rs, lane_of(c) == 0 ? 0u : OOB,
                                          WS_G + (256 * c.mt + 64 * c.fq + 8 * c.nl + c.w) * 4, AUX_SC1);
    P.s[0] = (f32x2){bflo(P.sk[0]), bflo(P.sk[2])};
  } else if constexpr (k == 8) {
    P.s[1] = (f32x2){bfhi(P.sk[0]), bfhi(P.sk[2])};
    P.s[2] = (f32x2){bflo(P.sk[1]), bflo(P.sk[3])}; P.s[3] = (f32x2){bfhi(P.sk[1]), bfhi(P.sk[3])};
    asm volatile("" : "+v"(P.s[0]), "+v"(P.s[1]), "+v"(P.s[2]), "+v"(P.s[3]));
  } else if constexpr (k == 9) {
    P.k2[0] = unpk2(P.u[0]); P.k2[1] = unpk2(P.u[1]); P.k2[2] = unpk2(P.u[2]);
    asm volatile("" : "+v"(P.k2[0]), "+v"(P.k2[1]), "+v"(P.k2[2]));
  } else if constexpr (k < 73) {
    constexpr int i = (k - 10) / 7, st = (k - 10) % 7;
    auto mul = [](f32x2 x, f32x2 y) { f32x2 r = {x[0] * y[0], x[1] * y[1]}; return r; };
    if constexpr (st == 0) { P.a = mul(P.k2[i & 3], P.s[3]); P.p = mul(P.k2[(i + 1) & 3], P.s[2]); P.vn = *(LAS unsigned*)(ldsp(P.lp) + K1_PV + 144 * i); }
    else if constexpr (st == 1) { P.a = rb2(P.a); P.p = rb2(P.p); }
    else if constexpr (st == 2) { P.a += P.p; P.p = mul(P.k2[(i + 2) & 3], P.s[1]); P.k2[(i + 3) & 3] = unpk2(P.kn); }
    else if constexpr (st == 3) {
      P.a = rb2(P.a); P.p = rb2(P.p);
      if constexpr (i < 8) P.kn = *(LAS unsigned*)(ldsp(P.lp) + 144 * (i + 4));
    }
    else if constexpr (st == 4) { P.a += P.p; P.p = mul(P.k2[(i + 3) & 3], P.s[0]); P.q = unpk2(P.vn); }
    else if constexpr (st == 5) { P.a = rb2(P.a); P.p = rb2(P.p); }
    else { P.a = mul(rb2(P.a + P.p), P.q); P.u[i] = pk2(P.a[0], P.a[1]); }
    if constexpr (st == 6) asm volatile("" : "+v"(P.u[i]));
    else if constexpr (st == 2) asm volatile("" : "+v"(P.a), "+v"(P.p), "+v"(P.k2[(i + 3) & 3]));
    else if constexpr (st == 4) asm volatile("" : "+v"(P.a), "+v"(P.p), "+v"(P.q));
    else asm volatile("" : "+v"(P.a), "+v"(P.p));
  } else if constexpr (k == 73) {
    const int l = lane_of(c);
    const int dp = 8 * c.w + (l >> 3);
    P.o0 = dp < 36 ? (unsigned)((72 * (c.b & 7) + 2 * dp) * 2 * S_ + (1984 - 64 * (c.b >> 3)) + 8 * (l & 7)) * 2 : OOB;
  } else {
    constexpr int ch = (k - 74) >> 1, cp = (k - 74) & 1;
    constexpr unsigned sel = ch ? 0x07060302u : 0x05040100u;
    u32x4 o;
#pragma unroll
    for (int e = 0; e < 4; ++e) o[e] = __builtin_amdgcn_perm(P.u[7 + cp - 2 * e - 1], P.u[7 + cp - 2 * e], sel);
    st128(o, c.rs, P.o0, WS_UR + (ch * 2 + cp) * S_ * 2, AUX_SC1);
  }
}
template <int NT, int e> __device__ __forceinline__ void k1_epi_step(const Ctx& c, const f32x16* acc, K1Epi& E, int ft0) {
  constexpr int pr = e / 3, st = e % 3, t = pr / 4, i = 2 * (pr % 4);
  if constexpr (st == 0) {
    if constexpr (pr > 0) { E.h[0] = E.pq[0] * E.r[0]; E.h[1] = E.pq[1] * E.r[1]; }
    if constexpr (pr < 4 * NT) {
      E.m[0] = acc[t][i] * -1.44269504088896f; E.m[1] = acc[t][i + 1] * -1.44269504088896f;
      E.p[0] = acc[t][i] * acc[t][i + 8]; E.p[1] = acc[t][i + 1] * acc[t][i + 9];
      asm volatile("" : "+v"(E.p[0]), "+v"(E.p[1]));
    }
  } else if constexpr (st == 1) {
    if constexpr (pr < 4 * NT) { E.m[0] = __builtin_amdgcn_exp2f(E.m[0]); E.m[1] = __builtin_amdgcn_exp2f(E.m[1]); }
    if constexpr (pr > 0) {
      E.ho[(pr - 1) % 4] = pk2(E.h[0], E.h[1]);
      if constexpr (pr % 4 == 0) st128(E.ho, c.rs, c.lane16, c.hb + (ft0 + (pr - 1) / 4) * 1024, AUX_SC1);
    }
  } else {
    if constexpr (pr < 4 * NT) {
      float d0 = 1.0f + E.m[0], d1 = 1.0f + E.m[1];
      E.r[0] = __builtin_amdgcn_rcpf(d0); E.r[1] = __builtin_amdgcn_rcpf(d1);
      E.pq[0] = E.p[0]; E.pq[1] = E.p[1];
    }
  }
}

template <int ch, int n, int e = 0> __device__ __forceinline__ void k1_epi_at(const Ctx& c, const f32x16* acc, K1Epi& E) {
  if constexpr (n >= K1_EPI0[ch] && n <= k1_epi_slot(ch, 49)) {
    if constexpr (k1_epi_slot(ch, e) == n) k1_epi_step<4, e>(c, acc, E, 8 * c.nl + 4 * ch);
    if constexpr (e < 49 && k1_epi_slot(ch, e < 49 ? e + 1 : e) <= n) k1_epi_at<ch, n, e + 1>(c, acc, E);
  }
}
template <int i> __device__ __forceinline__ void warm_touch(Ctx& c, const u32x4& sw2, const u32x4& ssk) {
  constexpr unsigned long long m = i < 4 ? (0x3full << (6 * (i >> 1))) : (i < 8 ? (0x3full << (16 + 6 * (i - 4))) : (0x1full << (48 + 5 * (i - 8))));
  const u32x4& srd = i < 4 ? ((i & 1) ? c.sw3 : c.sw1) : (i < 8 ? sw2 : ssk);
  asm volatile("s_mov_b32 exec_lo, %3\n\ts_mov_b32 exec_hi, %4\n\tbuffer_load_dword %0, %1, %2, 0 offen\n\ts_mov_b64 exec, -1"
               : "+v"(c.touch) : "v"(c.warm), "s"(srd), "n"((int)(unsigned)m), "n"((int)(unsigned)(m >> 32)) : "memory");
}
__device__ __forceinline__ int g2_ft(int s, int i8) {
  return s >= 8 ? 64 + i8 : (s < 4 ? 16 * s + 8 * (i8 >> 2) + (i8 & 3) : 32 * (s & 1) + 8 * (i8 >> 1) + (i8 & 1) + 4 + (s & 2));
}
template <int s, int i> __device__ __forceinline__ void g2_issue_w(const Ctx& c) {
  const unsigned n16 = 64u * i + (s < 4 ? (unsigned)lane_of(c) : c.xl0);
  const unsigned vo = ((n16 * 171u) >> 11) * (D_ * 2 - 192) + n16 * 16u;
  dma16<K1_XOFF + (s % G2_NSLOT) * G2_SLOT + i * 1024, 0>(vo, c.s2, 16 * g2_ft(s, c.w) * (D_ * 2) + 192 * c.nl, c.w * 3072);
}
template <int i> __device__ __forceinline__ u32x4 g2_load_h(const Ctx& c) {
  return __builtin_amdgcn_raw_buffer_load_b128(c.rs, c.lane16, c.hb + g2_ft(i / 8, i % 8) * 1024, AUX_SC1);
}
__device__ __forceinline__ unsigned g2_out_base(const Ctx& c) {
  return WS_PART + g2_wg(c.mt, c.nl, c.w >> 1) * 49152 + (c.fq * 2 + (c.w & 1)) * 6144;
}
__device__ __forceinline__ unsigned cv_base(int ch) { return ch < 7 ? K1_XOFF + ch * CV_CH : (ch == 7 ? k1_wbase(17) : k1_wbase(18)); }
template <int i> __device__ __forceinline__ void cv_issue(const Ctx& c, int ch) {
  const int d = 9 * cv_id(c) + ch;
  if constexpr (i < 4) dma16<CV_K + 64 + i * 1024, i * 1024>(c.lane16, c.s2, d * (S_ * 2), cv_base(ch));
  else dma16<(i < 8 ? CV_U0 + (i - 4) * 1024 : CV_U1 + (i - 8) * 1024), WS_UR + (i - 4) * 1024>(c.lane16, c.sws, d * (4 * S_), cv_base(ch));
}
typedef unsigned u32x2a4 __attribute__((ext_vector_type(2), aligned(4)));
__device__ __forceinline__ bf16x8 cv_read_a(LAS char* p) {
  u32x2 lo = *(LAS u32x2a4*)p;
  u32x2 hi = *(LAS u32x2a4*)(p + 8);
  u32x4 v = {lo[0], lo[1], hi[0], hi[1]};
  return __builtin_bit_cast(bf16x8, v);
}
__device__ __forceinline__ int cv_zwg(const Ctx& c, int lane, int& slot) {
  const int cw = cv_id(c);
  const int n0 = (9 * cw) / 96, n1 = (9 * cw + 8) / 96;
  const int s0 = 16 + cw - (96 * n0) / 9, s1 = n1 != n0 ? 16 + cw - (96 * n1) / 9 : -1;
  slot = lane < 32 ? s0 : s1;
  return g2_wg((lane >> 2) & 7, lane < 32 ? n0 : n1, lane & 3);
}

template <int n, int ROLE>
__device__ __forceinline__ void k1_slot(Ctx& c, bf16x8 (&xf)[36], bf16x8 (&wf)[K1_RD + 1], f32x16 (&acc)[4], K1Epi& E, K1Prep& P, u32x4 (&hw)[24],
                                        CvRegs& V) {
  constexpr const K1Sched& S = K1S[ROLE];
  constexpr int sg = S.seg[n];
  if constexpr (n + K1_RD == 288) {
    const unsigned l = lane_of(c);
    unsigned mat = (l >> 4) & 1, h = l >> 5, q = (l & 15) >> 2, p = l & 3;
    unsigned base = mat * 8192 + (8 * h + q) * 32 + p * 8;
    c.rc0 = base + (mat ? 128 : 0);
    c.rc1 = base + (mat ? 0 : 128);
  }
  if constexpr (n + K1_RD < K1_NS) wf[(n + K1_RD) % (K1_RD + 1)] = k1_read_w<n + K1_RD>(c);
  SCHED_FENCE();
  if constexpr (n + 4 < 144) {
    constexpr int sn = S.seg[n + 4];
    if constexpr (S.tile[n + 4] == 0 && sn >= 1) xf[4 * sn + S.j[n + 4]] = k1_read_x<sn, S.j[n + 4]>(c);
  }
  {
    constexpr int t = S.tile[n];
    constexpr int xi = sg < 18 ? 4 * (sg % 9) + S.j[n] : n - 288;
    constexpr bool first = (sg < 18) ? (sg % 9 == 0 && S.j[n] == 0) : (n == 288);
    if constexpr (first) acc[t] = __builtin_amdgcn_mfma_f32_32x32x16_bf16(wf[n % (K1_RD + 1)], xf[xi], (f32x16){}, 0, 0, 0);
    else acc[t] = __builtin_amdgcn_mfma_f32_32x32x16_bf16(wf[n % (K1_RD + 1)], xf[xi], acc[t], 0, 0, 0);
  }
  SCHED_FENCE();
  {
    constexpr int ws0 = [] { for (int s = 0; s < K1_NSEG; ++s) if (K1S[0].wissue[s] == n) return s; return -1; }();
    constexpr int ws1 = [] { for (int s = 0; s < K1_NSEG; ++s) if (K1S[0].wissue[s] >= 0 && K1S[0].wissue[s] + 2 == n) return s; return -1; }();
    constexpr int xs = [] { for (int p = 0; p < 9; ++p) if (K1S[0].xissue[p] >= 0 && n >= K1S[0].xissue[p] && n < K1S[0].xissue[p] + 4) return p; return -1; }();
    if constexpr (ws0 >= 0) k1_issue_w<ws0, 0>(c);
    if constexpr (ws1 >= 0) k1_issue_w<ws1, 1>(c);
    if constexpr (xs >= 0) k1_issue_x<xs, n - S.xissue[xs]>(c);
  }
  if constexpr (n >= K1_PREP0 && n < K1_PREP0 + K1_PREP_UNITS) k1_prep_unit<n - K1_PREP0>(c, P);
  k1_epi_at<0, n>(c, acc, E);
  k1_epi_at<1, n>(c, acc, E);
  {
    constexpr int wi = [] { for (int i = 0; i < WARM_N; ++i) if (WARM_SLOT[i] == n) return i; return -1; }();
    if constexpr (wi >= 0) warm_touch<wi>(c, ROLE == 0 ? c.s2 : c.s3, ROLE == 0 ? c.s3 : c.s2);
    if constexpr (n == WARM_DONE + 1) asm volatile("" : "+v"(c.touch)::"memory");
  }
  if constexpr (n == EV_PUB_U) {
    if (c.w == 1) { const int l = lane_of(c); flag_store(c, l < 8, cv_wg(8 * (c.b & 7) + l), c.b >> 3, c.ev); }
  }
  if constexpr (n == EV_PUB_A) {
    if (c.w == 1) publish_h(c, 0);
  }
  if constexpr (n == EV_PUB_B1) {
    if (c.w == 1) publish_h(c, 8);
  }
  if constexpr (ROLE == 0) {
    if constexpr (n >= EV_SENT0 && n < EV_SENT0 + 6 * EV_SENT_STEP && (n - EV_SENT0) % EV_SENT_STEP == 0)
      st128(sent4(), c.rs, c.lane16, (c.w >> 1) == c.fq ? OOB : g2_out_base(c) + (n - EV_SENT0) / EV_SENT_STEP * 1024, AUX_SC1);
    if constexpr (n == EV_PUB_S) {
      if (c.w == 2) { const int l = lane_of(c); flag_store(c, (l & 60) == 16, g2_wg(c.mt, c.nl, l & 3), 32 + c.fq, c.ev); }
    }
    if constexpr (n >= EV_PRE0 && n < EV_PRE0 + 12 * EV_PRE_STEP && (n - EV_PRE0) % EV_PRE_STEP == 0)
      g2_issue_w<(n - EV_PRE0) / EV_PRE_STEP / 3, (n - EV_PRE0) / EV_PRE_STEP % 3>(c);
    if constexpr (n == EV_G2_POLL) poll_issue(c);
    if constexpr (n == EV_G2_CHECK) {
      POLL_LANDED(S.ops_after(EV_G2_POLL, EV_G2_CHECK));
      if (c.w == 0) poll_wait(c, lane_of(c) < 8);
    }
    if constexpr (n >= EV_G2_H0 && (n - EV_G2_H0) % 2 == 0 && (n - EV_G2_H0) / 2 < G2_WIN) hw[(n - EV_G2_H0) / 2] = g2_load_h<(n - EV_G2_H0) / 2>(c);
  } else {
    if constexpr (n == EV_CV_POLL) poll_issue(c);
    if constexpr (n == EV_CV_CHECK) {
      POLL_LANDED(K1S[1].ops_after(EV_CV_POLL, EV_CV_CHECK));
      if (c.w == 0) {
        poll_wait(c, lane_of(c) < 32);
        asm volatile("buffer_inv sc1" ::: "memory");
        __builtin_amdgcn_raw_buffer_store_b32(c.ev + 1u, c.rs, lane_of(c) == 14 ? 0u : OOB, WS_EPOCH + c.b * 4, AUX_SC1);
      }
    }
    if constexpr (n == EV_CV_INVW) { if (c.w == 0) wait_vm<S.ops_after(EV_CV_CHECK, EV_CV_INVW)>(); }
    if constexpr (n >= EV_PRE0 && n < EV_PRE0 + 12 * EV_PRE_STEP && (n - EV_PRE0) % EV_PRE_STEP == 0) {
      if (c.w < 7) cv_issue<(n - EV_PRE0) / EV_PRE_STEP>(c, c.w);
    }
    if constexpr (n >= EV_CV_PRE7 && n < EV_CV_PRE7 + 12) {
      if (c.w == 7) cv_issue<n - EV_CV_PRE7>(c, 7);
    }
    if constexpr (n == EV_CV_PRE7 + 1) {
      const int l = lane_of(c);
      const unsigned cb = cv_base(c.w);
      if (l < 60) {
        int o = l < 4 ? CV_K + l * 16 : (l < 32 ? CV_U0 + 4096 + (l - 4) * 16 : CV_U1 + 4096 + (l - 32) * 16);
        *(LAS u32x4*)(ldsp(cb) + o) = zero4();
      }
    }
    if constexpr (n == EV_CV_PRE7 + 4) {
      c.dptr = karg(5);
      asm volatile("" : "+s"(c.dptr));
      const int l = lane_of(c);
      const unsigned cb = cv_base(c.w);
      const int rho = l & 15, q = l >> 4, pi = (l >> 3) & 1, ii = l & 7;
      V.bp = cb + CV_U0 + pi * 4640 + 2 * (2016 - 32 * ii + 8 * q) - 64 * 56;
#pragma unroll
      for (int i = 0; i < 4; ++i) { V.ap[i] = cb + CV_K + 4 + 4 * rho + 16 * q + 1024 * i; asm volatile("" : "+v"(V.ap[i])); }
    }
    if constexpr (n == EV_CV_PRE8) {
      dma16<k1_wbase(18), WS_UR>(c.lane16, c.sws, (9 * cv_id(c) + 8) * (4 * S_) + c.w * 1024, c.w < 4 ? CV_U0 + c.w * 1024 : CV_U1 + (c.w - 4) * 1024);
    }
    if constexpr (n == EV_CV_PRE8 + 1) {
      dma16<k1_wbase(18) + CV_K + 64, 0>(c.lane16, c.s2, (9 * cv_id(c) + 8) * (S_ * 2) + (c.w >> 1) * 1024, (c.w >> 1) * 1024);
      if (c.w == 6) {
        const int l = lane_of(c);
        if (l < 60) {
          int o = l < 4 ? CV_K + l * 16 : (l < 32 ? CV_U0 + 4096 + (l - 4) * 16 : CV_U1 + 4096 + (l - 32) * 16);
          *(LAS u32x4*)(ldsp(k1_wbase(18)) + o) = zero4();
        }
      }
    }
    if constexpr (n == 319) { if (c.w == 7) wait_vm<K1S[2].ops_after(EV_CV_PRE7 + 11, 319)>(); }
    if constexpr (n >= 320) {
#pragma unroll
      for (int f = 2 * (n - 320); f < 2 * (n - 320) + 2; ++f) {
        V.b0[f] = *(LAS bf16x8*)(ldsp(V.bp) + 64 * (56 - 8 * f));
        V.a0[f] = cv_read_a(ldsp(V.ap[f >> 1]) + 512 * (f & 1));
      }
    }
  }
  SCHED_FENCE();
  if constexpr (S.bar[n]) {
    if constexpr (ROLE == 1 && K1S[1].wait[n] != K1S[2].wait[n]) {
      if (c.w == 7) wait_vm<K1S[2].wait[n]>();
      else wait_vm<K1S[1].wait[n]>();
      wait_vm_barrier<63>();
    } else {
      wait_vm_barrier<S.wait[n]>();
    }
    SCHED_FENCE();
  }
  if constexpr (n + 1 < K1_NS) k1_slot<n + 1, ROLE>(c, xf, wf, acc, E, P, hw, V);
}

constexpr int g2_ks(int n) { return n < 168 ? n / 3 : 56 + (n - 168) % 16; }
constexpr int g2_tl(int n) { return n < 168 ? n % 3 : (n - 168) / 16; }
constexpr int G2_HW = 24;
template <int n> __device__ __forceinline__ bf16x8 g2_read_w(unsigned rd) {
  constexpr int i = g2_ks(n), t = g2_tl(n), s = i / 8, j = i % 8;
  LAS char* p = ldsp(rd) + (s % G2_NSLOT) * G2_SLOT + (16 * j) * 192 + t * 64;
  return tr_frag(p, p + 8 * 192);
}
static_assert(k1_epi_slot(1, 49) < K1_NS, "chunk B epilogue ends in phase 1");
constexpr int G2_TAIL0 = 2, G2_TAILST = G2_TAIL0 + 13;
constexpr int G2_PUBB = 19, G2_PUBC = 43;
constexpr int G2_CHECK0 = 39, G2_LEAD = 18;
constexpr bool g2_check(int n) { return n >= G2_CHECK0 && n <= G2_CHECK0 + 120 && (n - G2_CHECK0) % 24 == 0; }
constexpr int G2_PST = 186;
constexpr int G2_PRE3 = 206;
constexpr int g2_hslot(int i) {
  return i < 64 ? 3 * (i - G2_WIN) + 2 : 144 + (i - 64) + (i - 64) / 2;
}
constexpr int g2_hload_at(int n) { for (int i = G2_WIN; i < 72; ++i) if (g2_hslot(i) == n) return i; return -1; }
constexpr bool g2_bar(int n) { return (n % 24 == 24 - G2_PF - 1 && n / 24 < 7) || n == 176 - G2_PF - 1; }
constexpr int g2_ops_in_slot(int n) {
  int k = 0;
  for (int s = 1; s + 3 < 9; ++s) for (int i = 0; i < 3; ++i) if (n == 24 * s + 24 - G2_PF + 1 + 3 * i) ++k;
  if (g2_hload_at(n) >= 0) ++k;
  if (n == G2_TAILST) ++k;
  if (n == G2_PST || n == G2_PST + 2 || n == G2_PST + 16 || n == G2_PST + 18) ++k;
  return k;
}
constexpr int g2_ops_after(int a, int b) { int k = 0; for (int n = a + 1; n <= b; ++n) k += g2_ops_in_slot(n); return k; }
static_assert([] { for (int i = G2_WIN; i + 1 < 72; ++i) if (g2_hslot(i) >= g2_hslot(i + 1) || (i >= G2_HW && g2_hslot(i) < 3 * (i - G2_HW) + 2)) return false; return g2_hslot(64) > G2_CHECK0 + 100; }(), "h load slots");
static_assert(g2_bar(G2_PUBB) && g2_bar(G2_PUBC) && g2_bar(G2_CHECK0 + 4) && g2_hslot(32) > G2_CHECK0 + 4 && G2_PRE3 > G2_CHECK0 + 124 && G2_LEAD < 23, "events");
struct G2Fin { u32x4 zz[2], qq[2]; float g; };
__device__ __forceinline__ void g2_send(const Ctx& c, const f32x16& a, int t, int p2) {
  const bool own = (c.w >> 1) == c.fq;
  const u32x4 o = {pk2(a[8 * p2], a[8 * p2 + 1]), pk2(a[8 * p2 + 2], a[8 * p2 + 3]), pk2(a[8 * p2 + 4], a[8 * p2 + 5]), pk2(a[8 * p2 + 6], a[8 * p2 + 7])};
  st128(o, c.rs, c.lane16, own ? OOB : g2_out_base(c) + (t * 2 + p2) * 1024, AUX_SC1);
  if (own) {
#pragma unroll
    for (int qd = 2 * p2; qd < 2 * p2 + 2; ++qd)
      *(LAS f32x4*)(ldsp(c.lane16) + ((c.w & 1) * 12 + t * 4 + qd) * 1024) = (f32x4){a[4 * qd], a[4 * qd + 1], a[4 * qd + 2], a[4 * qd + 3]};
  }
}
__device__ __forceinline__ void g2_fin_load(const Ctx& c, G2Fin& Fn, bool all) {
  const int lane = lane_of(c);
  const int uh = (c.w / 3) & 1, ut = c.w % 3;
  const int t0 = 256 * c.mt + 64 * c.fq + 32 * uh, d0 = 96 * c.nl + 32 * ut;
#pragma unroll
  for (int i = 0; i < 2; ++i)
    Fn.zz[i] = __builtin_amdgcn_raw_buffer_load_b128(c.rs, ((16 * i + (lane >> 2)) * S_) * 2 + (lane & 3) * 16, WS_Z + (d0 * S_ + t0) * 2, AUX_SC1);
  if (all) {
    const char* qptr = karg(0);
#pragma unroll
    for (int i = 0; i < 2; ++i) Fn.qq[i] = *(const u32x4*)(qptr + ((size_t)(t0 + (lane & 31)) * D_ + d0 + 16 * (lane >> 5) + 8 * i) * 2);
    Fn.g = __builtin_bit_cast(float, __builtin_amdgcn_raw_buffer_load_b32(c.rs, (lane & 31) * 4, WS_G + t0 * 4, AUX_SC1));
  }
}
template <int n>
__device__ __forceinline__ void g2_slot(Ctx& c, unsigned rd, u32x4 (&hw)[24], bf16x8 (&wf)[G2_PF + 1], f32x16 (&acc)[3], const f32x16* tailacc, K1Epi& E,
                                        G2Fin& Fn) {
  constexpr int i = g2_ks(n), t = g2_tl(n), s = n / 24;
  if constexpr (n + G2_PF < G2_NS) wf[(n + G2_PF) % (G2_PF + 1)] = g2_read_w<n + G2_PF>(rd);
  if constexpr (i == 0) acc[t] = __builtin_amdgcn_mfma_f32_32x32x16_bf16(wf[n % (G2_PF + 1)], __builtin_bit_cast(bf16x8, hw[i % G2_HW]), (f32x16){}, 0, 0, 0);
  else acc[t] = __builtin_amdgcn_mfma_f32_32x32x16_bf16(wf[n % (G2_PF + 1)], __builtin_bit_cast(bf16x8, hw[i % G2_HW]), acc[t], 0, 0, 0);
  SCHED_FENCE();
  if constexpr (n >= G2_TAIL0 && n <= G2_TAILST) k1_epi_step<1, n - G2_TAIL0>(c, tailacc, E, 64 + c.nl);
  {
    constexpr int m = n - (24 - G2_PF + 1);
    if constexpr (m >= 24 && m % 24 < 9 && m % 3 == 0 && m / 24 + 3 < 9) g2_issue_w<m / 24 + 3, (m % 24) / 3>(c);
  }
  {
    constexpr int hl = g2_hload_at(n);
    if constexpr (hl >= 0) hw[hl % G2_HW] = g2_load_h<hl>(c);
  }
  if constexpr (n == G2_PST || n == G2_PST + 2) g2_send(c, acc[0], 0, (n - G2_PST) / 2);
  if constexpr (n == G2_PST + 16 || n == G2_PST + 18) g2_send(c, acc[1], 1, (n - G2_PST - 16) / 2);
  if constexpr (n == G2_PRE3) {
    g2_fin_load(c, Fn, true);
  }
  if constexpr (g2_check(n + G2_LEAD)) poll_issue(c);
  if constexpr (g2_check(n)) {
    constexpr int k = (n - G2_CHECK0) / 24;
    POLL_LANDED(g2_ops_after(n - G2_LEAD, n));
    if (c.w == 0) {
      const int l = lane_of(c);
      if constexpr (k == 0) poll_wait<1>(c, (l & 60) == 8);
      if constexpr (k == 1) poll_wait<2>(c, (l & 60) == 12);
      if constexpr (k == 2) poll_wait<3>(c, (l & 60) == 48);
      if constexpr (k == 3) poll_wait<4>(c, (l & 60) == 52);
      if constexpr (k == 4) poll_wait<7>(c, (l & 56) == 40);
      if constexpr (k == 5) {
        poll_wait<5>(c, (l >= 16 && l <= 16 + (96 * c.nl + 95) / 9 - (96 * c.nl) / 9) || (l & 60) == 32);
        __builtin_amdgcn_raw_buffer_store_b32(c.ev + 1u, c.rs, l == 14 ? 0u : OOB, WS_EPOCH + c.b * 4, AUX_SC1);
      }
    }
  }
  SCHED_FENCE();
  if constexpr (g2_bar(n)) {
    constexpr int wt = n == G2_PUBB ? K1S[0].ops_after(k1_epi_slot(1, 49), K1_NS - 1) + g2_ops_after(-1, n)
                                    : (n == G2_PUBC ? g2_ops_after(G2_TAILST, n) : (s + 1 < 4 ? 63 : g2_ops_after(24 * (s - 2) + 24 - G2_PF + 1 + 6, n)));
    wait_vm_barrier<cmin(63, wt)>();
    if constexpr (n == G2_PUBB) { if (c.w == 1) publish_h(c, 48); }
    if constexpr (n == G2_PUBC) { if (c.w == 1) publish_h(c, 40); }
    SCHED_FENCE();
  }
  if constexpr (n + 1 < G2_NS) g2_slot<n + 1>(c, rd, hw, wf, acc, tailacc, E, Fn);
}
__device__ __forceinline__ unsigned umax4(const u32x4& v) {
  unsigned a = v[0] > v[1] ? v[0] : v[1], b = v[2] > v[3] ? v[2] : v[3];
  return a > b ? a : b;
}

__device__ __forceinline__ void g2_phase(Ctx& c, u32x4 (&hw)[24], const f32x16* tailacc, K1Epi& E) {
  const int lane = lane_of(c);
  const int w = c.w, ks = c.fq, nt = c.nl, mt = c.mt;
  c.xl0 = lane;
  unsigned rd;
  {
    int gq = lane >> 4, chf = gq & 1, h = gq >> 1, q = (lane & 15) >> 2, p = lane & 3;
    rd = K1_XOFF + (4 * h + q) * 192 + chf * 32 + p * 8;
  }
  bf16x8 wf[G2_PF + 1];
  f32x16 acc[3];
  wf[0] = g2_read_w<0>(rd); wf[1] = g2_read_w<1>(rd); wf[2] = g2_read_w<2>(rd); wf[3] = g2_read_w<3>(rd);
  SCHED_FENCE();
  G2Fin Fn;
  g2_slot<0>(c, rd, hw, wf, acc, tailacc, E, Fn);
  g2_send(c, acc[2], 2, 0);
  g2_send(c, acc[2], 2, 1);
  const int uh = w / 3, ut = w % 3;
  const int qp = lane >> 5, cc = lane & 31;
  lds_barrier();
  STAMP(33);
  if (w < 6) {
    char* optr = (char*)karg(11);
    u32x4 pp[3][2];
    float ga[16], og = 0.f;
    f32x4 own[2][2];
    bool zdone = false;
    unsigned spin = 0;
#pragma unroll 1
    for (; spin < SPIN_CAP; ++spin) {
      asm volatile("" ::: "memory");
      const bool zok = __all(umax4(Fn.zz[0]) != SENT && umax4(Fn.zz[1]) != SENT) || spin == SPIN_CAP - 1;
      if (!zok) g2_fin_load(c, Fn, false);
#pragma unroll
      for (int s = 0; s < 3; ++s) {
        const int src = s + (s >= ks ? 1 : 0);
#pragma unroll
        for (int h = 0; h < 2; ++h)
          pp[s][h] = __builtin_amdgcn_raw_buffer_load_b128(c.rs, qp * 1024 + (cc + 32 * h) * 16, WS_PART + c.b * 49152 + (src * 2 + uh) * 6144 + ut * 2048, AUX_SC1);
      }
      SCHED_FENCE();
      if (zok && !zdone) {
        zdone = true;
        LAS char* zt = ldsp(24576 + w * 2560);
#pragma unroll
        for (int i = 0; i < 2; ++i) *(LAS u32x4*)(zt + (16 * i + (lane >> 2)) * 80 + (lane & 3) * 16) = Fn.zz[i];
        unsigned zv[16];
#pragma unroll
        for (int j = 0; j < 16; ++j) zv[j] = *(LAS u16*)(zt + (16 * qp + j) * 80 + cc * 2);
#pragma unroll
        for (int o = 0; o < 2; ++o)
#pragma unroll
          for (int h = 0; h < 2; ++h) own[o][h] = *(LAS f32x4*)(ldsp((cc + 32 * h) * 16) + (uh * 12 + ut * 4 + 2 * qp + o) * 1024);
        og = rbf(1.0f - Fn.g);
#pragma unroll
        for (int j = 0; j < 16; j += 2) {
          const unsigned qw = Fn.qq[j >> 3][(j >> 1) & 3];
          const unsigned a2 = pk2(bflo(qw) * bflo(zv[j]), bfhi(qw) * bflo(zv[j + 1]));
          ga[j] = Fn.g * bflo(a2); ga[j + 1] = Fn.g * bfhi(a2);
        }
      }
      SCHED_FENCE();
      u32x4 mx = {umax4(pp[0][0]), umax4(pp[0][1]), umax4(pp[1][0]), umax4(pp[1][1])};
      if (zdone && __all(umax4(mx) != SENT && umax4(pp[2][0]) != SENT && umax4(pp[2][1]) != SENT)) break;
    }
    DBGCNT(46, spin);
    STAMP(34);
#pragma unroll
    for (int o = 0; o < 2; ++o) {
      float res[8];
#pragma unroll
      for (int h = 0; h < 2; ++h)
#pragma unroll
        for (int k = 0; k < 4; k += 2) {
          f32x2 bs = {own[o][h][k], own[o][h][k + 1]};
#pragma unroll
          for (int s = 0; s < 3; ++s) bs += unpk2(pp[s][h][2 * o + (k >> 1)]);
          const f32x2 b2 = unpk2(pk2(bs[0], bs[1]));
          res[4 * h + k] = radd(ga[8 * o + 4 * h + k], og * b2[0]);
          res[4 * h + k + 1] = radd(ga[8 * o + 4 * h + k + 1], og * b2[1]);
        }
      u32x4 ov = {pk2(res[0], res[1]), pk2(res[2], res[3]), pk2(res[4], res[5]), pk2(res[6], res[7])};
      *(u32x4*)(optr + ((size_t)(256 * mt + 64 * ks + 32 * uh + cc) * D_ + 96 * nt + 32 * ut + 16 * qp + 8 * o) * 2) = ov;
    }
  }
  STAMP(35);
}

template <bool PREF, bool FIRST, class Hook>
__device__ __forceinline__ void cv_block(f32x4 (&acc)[8], const bf16x8 (&af)[8], const bf16x8 (&bf)[8], bf16x8 (&naf)[8], bf16x8 (&nbf)[8],
                                         const unsigned (&ap)[4], unsigned bp, int roff, Hook&& hook) {
  auto step = [&](auto K) {
    constexpr int k = decltype(K)::value;
    constexpr int p = [] { int r = k, q = 0; while (r >= 8 - q) { r -= 8 - q; ++q; } return q; }();
    constexpr int dl = [] { int r = k, q = 0; while (r >= 8 - q) { r -= 8 - q; ++q; } return r; }();
    if constexpr (PREF && (k & 1) == 0 && k < 32) {
      constexpr int f = k >> 2;
      if constexpr ((k & 2) != 0) naf[f] = cv_read_a(ldsp(ap[f >> 1]) + 512 * (f & 1) + roff);
      else nbf[f] = *(LAS bf16x8*)(ldsp(bp) + 64 * (56 - 8 * f) + roff);
    }
    if constexpr (FIRST && p == 0) asm volatile("v_mfma_f32_16x16x32_bf16 %0, %1, %2, 0" : "=&v"(acc[dl]) : "v"(af[p]), "v"(bf[dl]));
    else asm volatile("v_mfma_f32_16x16x32_bf16 %0, %1, %2, %0" : "+v"(acc[p + dl]) : "v"(af[p]), "v"(bf[dl]));
    if constexpr (k & 1) { SCHED_FENCE(); hook(K); SCHED_FENCE(); }
  };
  [&]<int... Ks>(std::integer_sequence<int, Ks...>) { (step(std::integral_constant<int, Ks>{}), ...); }(std::make_integer_sequence<int, 36>{});
}
__device__ __forceinline__ u32x4 cv_u(LAS char* cl, int g, int lane) {
  const int q = lane >> 4, pi = (lane >> 3) & 1, ii = lane & 7;
  return *(LAS u32x4*)(cl + (pi ? CV_U1 : CV_U0) + 2 * (2040 - (256 * g + 32 * ii + 8 * q)));
}
__device__ __forceinline__ u32x2 cv_z(const f32x4& y, const u32x4& uu, int lane, float Dv) {
  const int pi = (lane >> 3) & 1;
  float z[4];
#pragma unroll
  for (int k = 0; k < 4; ++k) z[k] = radd(y[k], bflo(uu[3 - k]) * Dv);
  unsigned p01 = pk2(z[0], z[1]), p23 = pk2(z[2], z[3]);
  unsigned snd = pi ? p01 : p23;
  unsigned rcv = __builtin_amdgcn_update_dpp(0u, snd, 0x128, 0xf, 0xf, false);
  unsigned Ev = pi ? rcv : p01, Od = pi ? p23 : rcv;
  return (u32x2){__builtin_amdgcn_perm(Od, Ev, 0x05040100u), __builtin_amdgcn_perm(Od, Ev, 0x07060302u)};
}
__device__ __forceinline__ void cv_phase(Ctx& c, const f32x16* tailacc, K1Epi& E, CvRegs& V) {
  const int w = c.w;
  f32x4 acc[8], acc8[8];
  bf16x8 a1[8], b1[8];
  auto none = [](auto) {};
  cv_block<true, true>(acc, V.a0, V.b0, a1, b1, V.ap, V.bp, 64, [&](auto K) {
    constexpr int k = decltype(K)::value / 2;
    if constexpr (k >= G2_TAIL0 && k <= G2_TAILST) k1_epi_step<1, k - G2_TAIL0>(c, tailacc, E, 64 + c.nl);
  });
  STAMP(24);
  wait_vm_barrier<1>();
  if (w == 1) publish_h(c, 48);
  const int lane = lane_of(c);
  const unsigned zb = WS_Z + 9 * cv_id(c) * (S_ * 2);
  const unsigned cb = cv_base(w), cb8 = cv_base(8);
  int zsl, zwg;
  unsigned zev, dd, ap8[4];
  SCHED_FENCE();
  cv_block<true, false>(acc, a1, b1, V.a0, V.b0, V.ap, V.bp, 128, [&](auto K) {
    constexpr int k = decltype(K)::value / 2;
    if constexpr (k == 1 || k == 5 || k == 9 || k == 13) st128(sent4(), c.rs, c.lane16, zb + w * (S_ * 2) + (k / 4) * 1024, AUX_SC1);
    if constexpr (k == 17) __builtin_amdgcn_raw_buffer_store_b64(sent2(), c.rs, lane * 8, zb + 8 * (S_ * 2) + 512 * w, AUX_SC1);
    if constexpr (k == 3) { zwg = cv_zwg(c, lane, zsl); zev = __builtin_amdgcn_raw_buffer_load_b32(c.rs, zwg * 4, WS_EPOCH, 0); }
    if constexpr (k == 7) dd = *(const u16*)(c.dptr + (9 * cv_id(c) + ((lane & 1) ? 8 : w)) * 2);
    if constexpr (k == 11) {
#pragma unroll
      for (int i = 0; i < 4; ++i) { ap8[i] = V.ap[i] + (cb8 - cb + 64 * w); asm volatile("" : "+v"(ap8[i])); }
    }
  });
  STAMP(25);
  cv_block<true, false>(acc, V.a0, V.b0, a1, b1, ap8, V.bp + (cb8 - cb + 64 * w), 0, none);
  STAMP(26);
  cv_block<true, true>(acc8, a1, b1, V.a0, V.b0, V.ap, V.bp, 192, none);
  STAMP(27);
  LAS char* sc = ldsp(((w & 2) ? k1_wbase(20) : k1_wbase(19)) + (w & 1) * 8192 + lane * 16);
  cv_block<true, false>(acc, V.a0, V.b0, a1, b1, V.ap, V.bp, 256, [&](auto K) {
    constexpr int k = decltype(K)::value / 2;
    if constexpr (k >= 4 && k < 12) { if (w >= 4) *(LAS f32x4*)(sc + (k - 4) * 1024) = acc8[k - 4]; }
  });
  STAMP(28);
  asm volatile("s_waitcnt vmcnt(0) lgkmcnt(0)\n\ts_barrier" ::: "memory");
  if (w == 5) publish_h(c, 40);
  if (w == 6) flag_store(c, zsl >= 0, zwg, zsl, zev);
  SCHED_FENCE();
  f32x4 o8[2];
  cv_block<true, false>(acc, a1, b1, V.a0, V.b0, V.ap, V.bp, 320, [&](auto K) {
    constexpr int k = decltype(K)::value / 2;
    if (w < 4) {
      if constexpr (k >= 2 && k < 18 && (k & 1) == 0) *(LAS f32x4*)(sc + (k / 2 - 1) * 1024) = acc8[k / 2 - 1] + o8[(k / 2 - 1) & 1];
      if constexpr (k < 16 && (k & 1) == 0) o8[(k / 2) & 1] = *(LAS f32x4*)(sc + (k / 2) * 1024);
    }
  });
  STAMP(29);
  lds_barrier();
  const float Dv = bflo((unsigned)__builtin_amdgcn_readlane((int)dd, 0)), Dv8 = bflo((unsigned)__builtin_amdgcn_readlane((int)dd, 1));
  const int zo = (32 * (lane & 7) + 8 * (lane >> 4) + 4 * ((lane >> 3) & 1)) * 2;
  f32x4 y4[4];
  u32x4 uu[8];
  SCHED_FENCE();
  cv_block<true, false>(acc, V.a0, V.b0, a1, b1, V.ap, V.bp, 384, [&](auto K) {
    constexpr int k = decltype(K)::value / 2;
    LAS char* s0 = ldsp(w * 1024 + lane * 16);
    if constexpr (k < 4) y4[k] = *(LAS f32x4*)(s0 + ((k & 2) ? k1_wbase(20) : k1_wbase(19)) + (k & 1) * 8192);
    if constexpr (k == 4) uu[0] = cv_u(ldsp(cb8), w, lane);
    if constexpr (k == 8) { y4[0] = (y4[0] + y4[1]) + (y4[2] + y4[3]); asm volatile("" : "+v"(y4[0])); }
    if constexpr (k == 12) __builtin_amdgcn_raw_buffer_store_b64(cv_z(y4[0], uu[0], lane, Dv8), c.rs, zo, zb + 8 * (S_ * 2) + 512 * w, AUX_SC1);
  });
  STAMP(30);
  cv_block<true, false>(acc, a1, b1, V.a0, V.b0, V.ap, V.bp, 448, none);
  STAMP(31);
  cv_block<false, false>(acc, V.a0, V.b0, a1, b1, V.ap, V.bp, 0, [&](auto K) {
    constexpr int k = decltype(K)::value;
    if constexpr (k < 8) { uu[k - 1] = cv_u(ldsp(cb), k - 1, lane); uu[k] = cv_u(ldsp(cb), k, lane); }
    constexpr int g = k == 9 ? 0 : (k == 13 ? 1 : (k == 19 ? 2 : (k == 25 ? 3 : (k == 31 ? 4 : (k == 35 ? 5 : -1)))));
    if constexpr (g >= 0) {
      asm volatile("s_nop 3" : "+v"(acc[g]));
      __builtin_amdgcn_raw_buffer_store_b64(cv_z(acc[g], uu[g], lane, Dv), c.rs, zo, zb + w * (S_ * 2) + 512 * g, AUX_SC1);
    }
  });
  STAMP(32);
  asm volatile("s_nop 7\n\ts_nop 1" : "+v"(acc[6]), "+v"(acc[7]));
#pragma unroll
  for (int g = 6; g < 8; ++g) __builtin_amdgcn_raw_buffer_store_b64(cv_z(acc[g], uu[g], lane, Dv), c.rs, zo, zb + w * (S_ * 2) + 512 * g, AUX_SC1);
  STAMP(35);
}

template <int ROLE>
__device__ __forceinline__ void fused_body(const int w, const unsigned long long pc) {
  const int b = (blockIdx.x + 192) & 255, xcd = b & 7, r = b >> 3;
  const int ml = r & 3, nl = r >> 2, fq = xcd >> 1;
  const int mt = 4 * (xcd & 1) + ml;
  const int lane = __builtin_amdgcn_mbcnt_hi(~0u, __builtin_amdgcn_mbcnt_lo(~0u, 0u));
  Ctx c;
  RSTAMP(37);
  STAMP(0);
  c.w = w; c.nl = nl; c.ml = ml; c.b = b; c.mt = mt; c.fq = fq;
  const char* ka[11];
  {
    typedef const char* cptr;
    const __attribute__((address_space(4))) cptr* p = (const __attribute__((address_space(4))) cptr*)__builtin_amdgcn_kernarg_segment_ptr();
    asm volatile("" : "+s"(p));
#pragma unroll
    for (int i = 0; i < 11; ++i) ka[i] = p[i];
    asm volatile("" : "+s"(ka[0]), "+s"(ka[1]), "+s"(ka[2]), "+s"(ka[3]), "+s"(ka[4]), "+s"(ka[6]), "+s"(ka[7]), "+s"(ka[8]), "+s"(ka[9]), "+s"(ka[10]));
  }
  c.rot = (nl + ml) % 3;
  c.lane16 = lane * 16;
  asm volatile("" : "+v"(c.lane16));
  c.sw1 = make_srd(ka[6], (1152 * fq + 128 * nl) * 2, D_ * F_ * 2);
  c.sw3 = make_srd(ka[7], (1152 * fq + 128 * nl) * 2, D_ * F_ * 2);
  c.sx = make_srd(ka[0], (256 * mt + 32 * w) * (D_ * 2), S_ * D_ * 2);
#pragma unroll
  for (int i = 0; i < 3; ++i) { const int k = kslice(i, c.rot); c.wo[i] = k * (64 * F_ * 2); c.xo[i] = k * 128; }
  {
    int row = lane >> 3, pos = lane & 7;
    c.voff_w = (8 * w + row) * (F_ * 2) + ((pos ^ (2 * ((row >> 1) & 1))) * 16);
    c.voff_x = row * (D_ * 2) + ((pos ^ (lane >> 4)) * 16);
  }
  k1_issue_w<0, 0>(c); k1_issue_w<0, 1>(c);
  k1_issue_x<0, 0>(c); k1_issue_x<0, 1>(c); k1_issue_x<0, 2>(c); k1_issue_x<0, 3>(c);
  {
    unsigned e0, e1;
    asm volatile("s_mov_b32 %0, .Lgm_code_end0-.Lgm_code_start\n\ts_mov_b32 %1, .Lgm_code_end1-.Lgm_code_start" : "=s"(e0), "=s"(e1));
    const u32x4 sc = make_srd((const char*)pc, 0, e0 > e1 ? e0 : e1);
    const unsigned vo = lane < 3 ? (unsigned)(8 * r + w + 256 * lane) * 128 : OOB;
    asm volatile("buffer_load_dword %0, %1, %2, 0 offen" : "=v"(c.touch) : "v"(vo), "s"(sc) : "memory");
  }
  k1_issue_w<1, 0>(c); k1_issue_w<1, 1>(c);
  SCHED_FENCE();
  c.sws = make_srd(ka[10], 0, WS_END);
  c.rs = __builtin_amdgcn_make_buffer_rsrc((void*)ka[10], 0, WS_END, 0x00020000);
  c.hb = WS_H + ((8 * mt + w) * 288 + 72 * fq) * 1024;
  {
    int gq = lane >> 4, mat = gq & 1, h = gq >> 1, q = (lane & 15) >> 2, p = lane & 3;
    int X = 2 * (q >> 1) + 4 * mat;
#pragma unroll
    for (int t = 0; t < 4; ++t) c.rd[t] = mat * 8192 + (8 * h + q) * 128 + (((2 * t) ^ X) + (p >> 1)) * 16 + (p & 1) * 8;
    int cc = lane & 31, hh = lane >> 5;
    c.xl0 = K1_XOFF + w * (K1_XD * 4096) + cc * 128 + ((hh ^ ((cc >> 1) & 7)) * 16);
  }
  {
    const u32x4 sw2 = make_srd(ka[8], 1152 * fq * D_ * 2, F_ * D_ * 2), ssk = make_srd(ka[4], 0, D_ * S_ * 2);
    c.s2 = ROLE == 0 ? sw2 : ssk;
    c.s3 = ROLE == 0 ? ssk : sw2;
    const unsigned mh = xcd & 1, i = 8 * r + w + 256 * (lane < 16 ? lane : (lane < 48 ? lane - 16 : lane - 48));
    const unsigned r1 = (i * 6554u) >> 16, c1 = i - 10 * r1;
    const int o1 = (int)((2 * r1 + mh) * (F_ * 2) + (c1 < 8 ? 2 * c1 + 1 : c1 + 8) * 128) - 256 * nl;
    const unsigned r2 = (i * 7282u) >> 16, o2 = (2 * r2 + mh) * (D_ * 2) + (i - 9 * r2) * 128;
    unsigned o = 72 * xcd * (S_ * 2) + i * 128;
    o = lane < 48 ? (i < 5184 ? o2 : OOB) : o;
    o = lane < 16 ? ((i < 2880 && o1 >= 0) ? (unsigned)o1 : OOB) : o;
    c.warm = lane < 57 ? o : OOB;
    asm volatile("" : "+v"(c.warm));
  }
  K1Prep P;
  {
    const u32x4 sk_ = make_srd(ka[1], 0, S_ * D_ * 2), sv_ = make_srd(ka[2], 0, S_ * D_ * 2);
    auto voff = [&](int blk, int halo, bool on) {
      const unsigned pc = (unsigned)(64 * blk + lane), row = (pc * 7282u) >> 16;
      const int t = 64 * r - halo + (int)row;
      const unsigned o = (unsigned)t * (D_ * 2) + (pc - 9 * row) * 16 + 144 * xcd;
      return (on && t >= 0 && (int)row < 64 + halo) ? o : OOB;
    };
    const bool isv = (w & 2) != 0;
    const u32x4 sd = {isv ? sv_[0] : sk_[0], isv ? sv_[1] : sk_[1], sk_[2], sk_[3]};
    unsigned v0 = voff(w, 4, true), v1 = voff(w, 1, true), v2 = voff(8 + (w & 1), isv ? 1 : 4, w < 4);
    const int e0 = cv_wg(8 * xcd + lane), e1 = g2_wg(mt, lane - 8, fq), e2 = g2_wg(mt, nl < 6 ? nl : 0, lane & 3);
    int ewg = b;
    ewg = (lane & 60) == 16 ? e2 : ewg;
    ewg = lane < 14 ? e1 : ewg;
    ewg = (lane < 8 ? e0 : ewg) * 4;
    asm volatile("" : "+v"(v0), "+v"(v1), "+v"(v2), "+v"(ewg), "+v"(c.rd[0]), "+v"(c.rd[1]), "+v"(c.rd[2]), "+v"(c.rd[3]), "+v"(c.xl0));
    SCHED_FENCE();
    dma16<K1_POFF, 0>(v0, sk_, 0u, w * 1024);
    dma16<K1_POFF + K1_PV, 0>(v1, sv_, 0u, w * 1024);
    dma16<K1_POFF, 0>(v2, sd, 0u, w < 4 ? (8 + (w & 1)) * 1024 + (isv ? K1_PV : 0) : 20480 + w * 1024);
    const unsigned so = WS_EPOCH;
    asm volatile("buffer_load_dword %0, %1, %2, %3 offen" : "=v"(c.ev) : "v"(ewg), "s"(c.sws), "s"(so) : "memory");
    const char* gx = ka[0] + (size_t)(256 * mt + 64 * fq + 8 * nl + w) * (D_ * 2);
    const int dp = 8 * w + (lane >> 3), dc = dp < 36 ? dp : 35;
    P.lp = K1_POFF + 8 * (7 - (lane & 7)) * 144 + dc * 4;
    asm volatile("" : "+v"(P.lp));
    GLD4(P.sk, (unsigned)(72 * xcd + 2 * dc) * 8, ka[3]);
    GLD4(P.ga0, c.lane16, gx);
    GLD4(P.ga1, 1024 + (c.lane16 & 127), gx);
    GLD4(P.gw0, c.lane16, ka[9]);
    GLD4(P.gw1, 1024 + (c.lane16 & 127), ka[9]);
  }
  bf16x8 xf[36];
  bf16x8 wf[K1_RD + 1];
  f32x16 acc[4];
  u32x4 hw[24];
  wait_vm_barrier<12>();
  STAMP(1);
  k1_issue_x<1, 0>(c); k1_issue_x<1, 1>(c); k1_issue_x<1, 2>(c); k1_issue_x<1, 3>(c);
  xf[0] = k1_read_x<0, 0>(c); wf[0] = k1_read_w<0>(c);
  xf[1] = k1_read_x<0, 1>(c); wf[1] = k1_read_w<1>(c);
  xf[2] = k1_read_x<0, 2>(c); wf[2] = k1_read_w<2>(c);
  xf[3] = k1_read_x<0, 3>(c);
  static_assert(K1_RD == 3, "prologue reads");
  SCHED_FENCE();
  STAMP(2);
  K1Epi E;
  CvRegs V;
  k1_slot<0, ROLE>(c, xf, wf, acc, E, P, hw, V);
  STAMP(23);
  if constexpr (ROLE == 1) cv_phase(c, acc, E, V);
  else {
    g2_phase(c, hw, acc, E);
    __builtin_amdgcn_s_waitcnt(0);
  }
  if constexpr (ROLE == 0) asm volatile(".Lgm_code_end0:");
  else asm volatile(".Lgm_code_end1:");
}

__global__ __launch_bounds__(512, 2) void fused_kernel(const void* x, const void* kk, const void* vv, const void* shift_k, const void* ssm_k,
                                                       const void* ssm_D, const void* w1, const void* w3, const void* w2, const void* gate_w, void* ws,
                                                       void* out) {
  const int w = __builtin_amdgcn_readfirstlane(threadIdx.x >> 6);
  unsigned long long pc;
  asm volatile("s_getpc_b64 %0\n.Lgm_code_start:" : "=s"(pc));
  if (blockIdx.x < 64) fused_body<1>(w, pc);
  else fused_body<0>(w, pc);
}

extern "C" void kernel_launch(void* const* d_in, const int* in_sizes, int n_in, void* d_out, int out_size, void* d_ws, size_t ws_size,
                              hipStream_t stream) {
  (void)hipFuncSetAttribute((const void*)fused_kernel, hipFuncAttributeMaxDynamicSharedMemorySize, LDS_BYTES);
  fused_kernel<<<256, 512, LDS_BYTES, stream>>>(d_in[0], d_in[1], d_in[2], d_in[3], d_in[4], d_in[5], d_in[7], d_in[8], d_in[9], d_in[10], d_ws, d_out);
}
```

```cpp
#include <hip/hip_runtime.h>
#include <hip/hip_bf16.h>

#define LAS __attribute__((address_space(3)))
#define GAS __attribute__((address_space(1)))
typedef unsigned short u16;
typedef __bf16 bf16x2 __attribute__((ext_vector_type(2)));
typedef __bf16 bf16x4 __attribute__((ext_vector_type(4)));
typedef __bf16 bf16x8 __attribute__((ext_vector_type(8)));
typedef float f32x2 __attribute__((ext_vector_type(2)));
typedef float f32x4 __attribute__((ext_vector_type(4)));
typedef float f32x16 __attribute__((ext_vector_type(16)));
typedef unsigned u32x2 __attribute__((ext_vector_type(2)));
typedef unsigned u32x4 __attribute__((ext_vector_type(4)));

constexpr int S_ = 2048, D_ = 576, F_ = 4608;
constexpr unsigned WS_H = 0;
constexpr unsigned WS_PART = WS_H + 64 * 288 * 1024;
constexpr unsigned WS_UR = WS_PART + 256 * 49152;
constexpr unsigned WS_Z = WS_UR + D_ * 2 * S_ * 2;
constexpr unsigned WS_G = WS_Z + D_ * S_ * 2;
constexpr unsigned WS_FLAG = WS_G + S_ * 4;
constexpr unsigned WS_EPOCH = WS_FLAG + 256 * 512;
constexpr unsigned WS_END = WS_EPOCH + 1024;
constexpr unsigned FLAG_MAGIC = 0x7c3a91e5u, SENT = 0xffffffffu;
constexpr int AUX_SC1 = 16;
constexpr unsigned OOB = 0x7ff00000u;
constexpr unsigned SPIN_CAP = 1u << 16;

#define STAMP(i)
#define RSTAMP(i)
#define STDUMP()
#define DBGCNT(i, x)

__device__ __forceinline__ float bflo(unsigned v) { return __builtin_bit_cast(float, v << 16); }
__device__ __forceinline__ float bfhi(unsigned v) { return __builtin_bit_cast(float, v & 0xffff0000u); }
__device__ __forceinline__ float rbf(float x) { return (float)(__bf16)x; }
__device__ __forceinline__ unsigned pk2(float lo, float hi) {
  f32x2 v = {lo, hi};
  return __builtin_bit_cast(unsigned, __builtin_convertvector(v, bf16x2));
}
__device__ __forceinline__ f32x2 unpk2(unsigned v) { return (f32x2){bflo(v), bfhi(v)}; }
__device__ __forceinline__ float radd(float a, float b) { const unsigned t = pk2(a, b); return bflo(t) + bfhi(t); }
#define SCHED_FENCE() __builtin_amdgcn_sched_barrier(0)
__device__ __forceinline__ void st128(u32x4 v, __amdgpu_buffer_rsrc_t rs, unsigned voff, unsigned soff, int) {
  __builtin_amdgcn_raw_buffer_store_b128(v, rs, voff, soff, AUX_SC1);
  asm volatile("s_nop 1" ::"v"(v));
}

template <int LC, int SC> __device__ __forceinline__ void dma16(unsigned voff, const u32x4& srd, unsigned so, unsigned lb) {
  unsigned t;
  asm volatile("s_add_u32 m0, %4, %5\n\ts_add_u32 %0, %3, %6\n\tbuffer_load_dwordx4 %1, %2, %0 offen lds"
               : "=&s"(t) : "v"(voff), "s"(srd), "s"(so), "s"(lb), "n"(LC), "n"(SC) : "memory", "scc");
}
template <int N> __device__ __forceinline__ void wait_vm_barrier() {
  asm volatile("s_waitcnt vmcnt(%0)\n\ts_barrier" ::"n"(N) : "memory");
}
template <int N> __device__ __forceinline__ void wait_vm() { asm volatile("s_waitcnt vmcnt(%0)" ::"n"(N) : "memory"); }
__device__ __forceinline__ void lds_barrier() { asm volatile("s_waitcnt lgkmcnt(0)\n\ts_barrier" ::: "memory"); }
__device__ __forceinline__ LAS char* ldsp(unsigned a) { return (LAS char*)(size_t)a; }
__device__ __forceinline__ bf16x8 tr_frag(LAS char* p0, LAS char* p1) {
  bf16x4 a = __builtin_amdgcn_ds_read_tr16_b64_v4bf16((LAS bf16x4*)p0);
  bf16x4 b = __builtin_amdgcn_ds_read_tr16_b64_v4bf16((LAS bf16x4*)p1);
  return __builtin_shufflevector(a, b, 0, 1, 2, 3, 4, 5, 6, 7);
}
constexpr int cmin(int a, int b) { return a < b ? a : b; }
__device__ __forceinline__ const char* karg(int i) {
  typedef const char* cptr;
  const __attribute__((address_space(4))) cptr* p = (const __attribute__((address_space(4))) cptr*)__builtin_amdgcn_kernarg_segment_ptr();
  asm volatile("" : "+s"(p));
  return p[i];
}
__device__ __forceinline__ u32x4 make_srd(const char* base, unsigned off, unsigned total) {
  const unsigned long long a = (unsigned long long)base + off;
  return (u32x4){(unsigned)a, (unsigned)(a >> 32) & 0xffffu, total - off, 0x00020000u};
}

constexpr int K1_SLOT = 16384, K1_XOFF = 65536, K1_XD = 2, K1_POFF = 131072, K1_PV = 10240;
constexpr int LDS_BYTES = 163840;
constexpr int K1_PF = 4, K1_RD = 3, K1_NS = 324, K1_NSEG = 21;
constexpr int K1_PREP0 = 28, K1_PREP_UNITS = 8 + 2 + 63 + 1 + 4;
constexpr int G2_SLOT = 24576, G2_NSLOT = 4, G2_WIN = 16, G2_PF = 4, G2_NS = 216;
constexpr int CV_K = 0, CV_U0 = 4160, CV_U1 = CV_U0 + 4640, CV_CH = 13440;
constexpr int k1_wbase(int s) { return s == 11 ? K1_XOFF : (s == 12 ? K1_XOFF + K1_SLOT : ((s < 11 ? s : s - 2) % 4) * K1_SLOT); }
constexpr int k1_wprev(int s) { return s < 4 || s == 11 || s == 12 ? -1 : (s < 11 ? s - 4 : (s < 17 ? s - 6 : s - 4)); }
constexpr int EV_PUB_U = 140;
constexpr int EV_PUB_A = 204;
constexpr int EV_PRE0 = 221, EV_PRE_STEP = 4;
constexpr int EV_CV_POLL = 176, EV_CV_CHECK = 190, EV_CV_INVW = 219;
constexpr int EV_CV_PRE7 = 300;
constexpr int EV_CV_PRE8 = 316;
constexpr int EV_SENT0 = 177, EV_SENT_STEP = 7, EV_PUB_S = 236;
constexpr int EV_G2_POLL = 264, EV_G2_CHECK = 280, EV_G2_H0 = 289;
constexpr int WARM_N = 10;
constexpr int WARM_SLOT[WARM_N] = {8, 24, 40, 56, 72, 88, 119, 151, 175, 191};
constexpr int WARM_DONE = 235;
constexpr int EV_PUB_B1 = 316;
constexpr int K1_EPI0[2] = {125, 269};
constexpr int k1_epi_slot(int ch, int e) { return K1_EPI0[ch] + (ch == 0 ? e - e / 12 : e); }

struct K1Sched {
  int seg[K1_NS], tile[K1_NS], j[K1_NS];
  int first_use[K1_NSEG], last_use[K1_NSEG];
  int wissue[K1_NSEG];
  int xissue[9];
  bool bar[K1_NS];
  int ops[K1_NS];
  int wait[K1_NS];
  int done[K1_NS];
  constexpr int ops_after(int a, int b) const { int k = 0; for (int q = a + 1; q <= b; ++q) k += ops[q]; return k; }
};
constexpr K1Sched k1_make(int role) {
  K1Sched S{};
  int n = 0;
  for (int s = 0; s < 18;) {
    if (s == 7 || s == 9 || s == 16) {
      const int tt[8] = {0, 1, 0, 1, 2, 2, 3, 3}, hh[8] = {0, 0, 1, 1, 0, 1, 0, 1};
      for (int q = 0; q < 8; ++q) for (int j = 0; j < 4; ++j) { S.seg[n] = s + hh[q]; S.tile[n] = tt[q]; S.j[n] = j; ++n; }
      s += 2;
    } else {
      for (int t = 0; t < 4; ++t) for (int j = 0; j < 4; ++j) { S.seg[n] = s; S.tile[n] = t; S.j[n] = j; ++n; }
      s += 1;
    }
  }
  for (int i = 0; i < 36; ++i) { S.seg[n] = 18 + i / 16; S.tile[n] = 0; S.j[n] = i % 16; ++n; }
  for (int s = 0; s < K1_NSEG; ++s) { S.first_use[s] = 100000; S.last_use[s] = -1; }
  for (int i = 0; i < K1_NS; ++i) { int s = S.seg[i]; if (i < S.first_use[s]) S.first_use[s] = i; if (i > S.last_use[s]) S.last_use[s] = i; }
  for (int s = 1; s < K1_NSEG; ++s) S.bar[S.first_use[s] - K1_PF - 1] = true;
  S.wissue[0] = S.wissue[1] = -1;
  S.wissue[2] = 0; S.wissue[3] = 12;
  int busy = 0;
  for (int s = 4; s < K1_NSEG; ++s) {
    int q = k1_wprev(s) >= 0 ? S.last_use[k1_wprev(s)] : S.first_use[8] + 4;
    while (!S.bar[q]) ++q;
    int is = q + 1;
    if (is < busy) is = busy;
    S.wissue[s] = is;
    busy = is + (s >= 11 ? 8 : 4);
  }
  S.xissue[0] = S.xissue[1] = -1;
  for (int p = 2; p < 9; ++p) S.xissue[p] = S.first_use[p - 2] + 4;
  for (int s = 0; s < K1_NSEG; ++s) if (S.wissue[s] >= 0) { S.ops[S.wissue[s]]++; S.ops[S.wissue[s] + 2]++; }
  for (int p = 0; p < 9; ++p) if (S.xissue[p] >= 0) for (int i = 0; i < 4; ++i) S.ops[S.xissue[p] + i]++;
  S.ops[K1_PREP0 + 7]++;
  for (int i = 0; i < WARM_N; ++i) S.ops[WARM_SLOT[i]]++;
  for (int i = 0; i < 4; ++i) S.ops[K1_PREP0 + K1_PREP_UNITS - 4 + i]++;
  for (int c = 0; c < 2; ++c) for (int t = 0; t < 4; ++t) if (k1_epi_slot(c, 12 * t + 13) < K1_NS) S.ops[k1_epi_slot(c, 12 * t + 13)]++;
  if (role == 0) {
    for (int i = 0; i < G2_WIN; ++i) S.ops[EV_G2_H0 + 2 * i]++;
    for (int i = 0; i < 6; ++i) S.ops[EV_SENT0 + EV_SENT_STEP * i]++;
  } else {
    S.ops[EV_CV_PRE8]++; S.ops[EV_CV_PRE8 + 1]++;
    S.ops[EV_CV_PRE7]++; S.ops[EV_CV_PRE7 + 1]++;
  }
  for (int i = 0; i < 12; ++i) S.ops[EV_PRE0 + EV_PRE_STEP * i]++;
  for (int i = 0; i < K1_NS; ++i) {
    S.wait[i] = 63;
    S.done[i] = -2;
    if (!S.bar[i]) continue;
    int youngest = -2;
    for (int s = 1; s < K1_NSEG; ++s)
      if (S.first_use[s] - K1_PF - 1 == i) {
        int y = S.wissue[s] < 0 ? -1 : S.wissue[s] + 2;
        if (s < 9) { int yx = S.xissue[s] < 0 ? -1 : S.xissue[s] + 3; if (yx > y) y = yx; }
        if (y > youngest) youngest = y;
      }
    if (i == EV_PUB_A - 1 && youngest < k1_epi_slot(0, 49)) youngest = k1_epi_slot(0, 49);
    if (i == EV_PUB_S - 1 && youngest < EV_SENT0 + 5 * EV_SENT_STEP) youngest = EV_SENT0 + 5 * EV_SENT_STEP;
    if (i == WARM_DONE && youngest < WARM_SLOT[WARM_N - 1]) youngest = WARM_SLOT[WARM_N - 1];
    if (i == EV_PUB_B1 - 1 && youngest < k1_epi_slot(1, 25)) youngest = k1_epi_slot(1, 25);
    if (role == 1 && i == 315 && youngest < EV_CV_PRE7 + 1) youngest = EV_CV_PRE7 + 1;
    if (i == 299 && youngest < EV_PRE0 + 11 * EV_PRE_STEP) youngest = EV_PRE0 + 11 * EV_PRE_STEP;
    S.wait[i] = cmin(63, S.ops_after(youngest, i));
    S.done[i] = youngest;
  }
  return S;
}
constexpr K1Sched K1S[2] = {k1_make(0), k1_make(1)};
static_assert(K1_PREP0 + K1_PREP_UNITS - 1 <= K1S[0].done[EV_PUB_U - 1] && K1S[0].bar[EV_PUB_U - 1], "u stores complete before publish");
static_assert(K1S[0].bar[EV_PUB_A - 1] && K1S[0].bar[EV_PUB_S - 1] && K1S[0].bar[299] && K1S[0].bar[315], "events");
static_assert(K1S[0].bar[EV_G2_CHECK + 3] && K1S[0].bar[EV_CV_INVW] && K1S[0].bar[EV_CV_PRE7 - 1] && K1S[0].last_use[17] < EV_CV_PRE7, "events");
static_assert(K1S[0].bar[EV_CV_PRE8 - 1] && K1S[0].last_use[18] < EV_CV_PRE8 && EV_G2_H0 + 2 * G2_WIN <= K1_NS + 1, "events");
static_assert(EV_PRE0 > EV_CV_INVW && K1S[0].last_use[12] <= EV_CV_INVW, "events");
static_assert([] {
  for (int t = 0; t < 4; ++t) {
    int last = 0, next = 1000;
    for (int i = 0; i < 144; ++i) if (K1S[0].tile[i] == t) last = i;
    for (int i = 287; i >= 144; --i) if (K1S[0].tile[i] == t) next = i;
    if (k1_epi_slot(0, 12 * t) < last + 2 || k1_epi_slot(0, 12 * t + 9) >= next) return false;
  }
  return true;
}(), "epilogue windows");

struct Ctx {
  u32x4 sw1, sw3, sx, s2, s3, sws;
  __amdgpu_buffer_rsrc_t rs;
  unsigned wo[3], xo[3];
  unsigned hb;
  unsigned voff_w, voff_x;
  unsigned rd[4];
  unsigned xl0, lane16;
  unsigned ev;
  u32x2 poll;
  unsigned touch, warm;
  const char* dptr;
  unsigned rc0, rc1;
  int w, rot, nl, ml, b, mt, fq;
};
struct K1Prep {
  u32x4 sk, ga0, ga1, gw0, gw1;
  unsigned lp, kn, vn, o0;
  f32x2 s[4], k2[4], a, p, q; unsigned u[9]; float gs;
};
struct K1Epi { float m[2], p[2], pq[2], r[2], h[2]; u32x4 ho; };
struct CvRegs { unsigned ap[4], bp; bf16x8 a0[8], b0[8]; };

__device__ __forceinline__ int lane_of(const Ctx& c) {
  unsigned l = c.lane16;
  asm volatile("" : "+v"(l));
  return (int)(l >> 4);
}
__device__ __forceinline__ u32x4 sent4() {
  u32x4 v = {SENT, SENT, SENT, SENT};
  asm volatile("" : "+v"(v));
  return v;
}
__device__ __forceinline__ u32x2 sent2() {
  u32x2 v = {SENT, SENT};
  asm volatile("" : "+v"(v));
  return v;
}
__device__ __forceinline__ u32x4 zero4() {
  u32x4 v = {0u, 0u, 0u, 0u};
  asm volatile("" : "+v"(v));
  return v;
}
__device__ __forceinline__ int g2_wg(int mt, int nt, int ks) { return ((nt * 4 + (mt & 3)) << 3) | (ks * 2 + (mt >> 2)); }
__device__ __forceinline__ int cv_wg(int cw) { return ((24 + (cw & 7)) << 3) | (cw >> 3); }
__device__ __forceinline__ int cv_id(const Ctx& c) { return (c.b & 7) * 8 + (c.b >> 3) - 24; }
__device__ __forceinline__ void flag_store(const Ctx& c, bool act, int wg, int slot, unsigned epoch) {
  asm volatile("" : "+v"(epoch));
  __builtin_amdgcn_raw_buffer_store_b64((u32x2){epoch + 1u, FLAG_MAGIC}, c.rs, act ? wg * 512 + slot * 8 : OOB, WS_FLAG, AUX_SC1);
}
__device__ __forceinline__ void publish_h(const Ctx& c, int slot0) {
  const int l = lane_of(c);
  flag_store(c, l >= 8 && l < 14, g2_wg(c.mt, l - 8, c.fq), slot0 + c.nl, c.ev);
}
__device__ __forceinline__ void poll_issue(Ctx& c) {
  const unsigned vo = lane_of(c) * 8;
  const unsigned so = WS_FLAG + c.b * 512;
  asm volatile("s_cmp_lg_u32 %4, 0\n\ts_cbranch_scc1 1f\n\tbuffer_load_dwordx2 %0, %1, %2, %3 offen sc1\n1:"
               : "=v"(c.poll) : "v"(vo), "s"(c.sws), "s"(so), "s"(c.w) : "memory", "scc");
}
template <int DBG = 0> __device__ __forceinline__ void poll_wait(Ctx& c, bool need) {
  const unsigned want = (unsigned)__builtin_amdgcn_readlane((int)c.ev, 14) + 1u;
  u32x2 v = c.poll;
  unsigned spin = 0;
#pragma unroll 1
  for (; spin < SPIN_CAP; ++spin) {
    if (__all(!need || (v[0] == want && v[1] == FLAG_MAGIC))) break;
    const unsigned vo = lane_of(c) * 8, so = WS_FLAG + c.b * 512;
    asm volatile("s_sleep 1\n\tbuffer_load_dwordx2 %0, %1, %2, %3 offen sc1\n\ts_waitcnt vmcnt(0)" : "=&v"(v) : "v"(vo), "s"(c.sws), "s"(so) : "memory");
  }
  DBGCNT(40 + DBG, spin);
  asm volatile("" ::: "memory");
}
#define POLL_LANDED(N) asm volatile("s_cmp_lg_u32 %2, 0\n\ts_cbranch_scc1 1f\n\ts_waitcnt vmcnt(%1)\n1:" : "+v"(c.poll) : "n"(N), "s"(c.w) : "memory", "scc")

__device__ __forceinline__ int kslice(int p, int r) {
  const int g = (p >= 6) ? 6 : (p >= 3 ? 3 : 0);
  const int v = p - g + r;
  return g + (v >= 3 ? v - 3 : v);
}
template <int sg, int half> __device__ __forceinline__ void k1_issue_w(const Ctx& c) {
  constexpr int dst = k1_wbase(sg) + half * 8192;
  if constexpr (sg < 18) {
    constexpr int p = sg % 9;
    dma16<dst, (p / 3 * 3) * 64 * F_ * 2 + 128 * (sg / 9)>(half ? (c.voff_w ^ 64u) : c.voff_w, half ? c.sw3 : c.sw1, c.wo[p % 3], c.w * 1024);
  } else {
    const unsigned l = lane_of(c);
    const unsigned rp = l >> 1;
    unsigned vo = (half ? (rp ^ 4u) : rp) * (F_ * 2) + (l & 1u) * 16;
    const int col = (1024 + 16 * c.nl - 128 * c.nl) * 2;
    if constexpr (sg < 20) {
      const int ksl = kslice(4 * (sg - 18) + (c.w >> 1), c.rot);
      dma16<dst, 0>(vo, half ? c.sw3 : c.sw1, (64 * ksl + 32 * (c.w & 1)) * (F_ * 2) + col, c.w * 1024);
    } else {
      if (l >= 16) vo = OOB;
      dma16<dst, 6 * 64 * F_ * 2>(vo, half ? c.sw3 : c.sw1, c.wo[2] + col + 8 * c.w * (F_ * 2), c.w * 1024);
    }
  }
}
template <int p, int i> __device__ __forceinline__ void k1_issue_x(const Ctx& c) {
  dma16<K1_XOFF + (p % K1_XD) * 4096 + i * 1024, 8 * i * D_ * 2 + (p / 3 * 3) * 128>((i & 1) ? (c.voff_x ^ 64u) : c.voff_x, c.sx, c.xo[p % 3],
                                                                                     c.w * (K1_XD * 4096));
}
template <int p, int j> __device__ __forceinline__ bf16x8 k1_read_x(const Ctx& c) {
  return *(LAS bf16x8*)(ldsp(c.xl0 ^ (32 * j)) + (p % K1_XD) * 4096);
}
template <int n> __device__ __forceinline__ bf16x8 k1_read_w(Ctx& c) {
  constexpr int sg = K1S[0].seg[n], t = K1S[0].tile[n], j = K1S[0].j[n];
  if constexpr (sg < 18) {
    if constexpr ((sg == 11 || sg == 13) && j == 0) {
      c.rd[t] += sg == 11 ? K1_XOFF : -K1_XOFF;
      asm volatile("" : "+v"(c.rd[t]));
    }
    LAS char* p = ldsp(c.rd[t]) + k1_wbase(sg) % K1_XOFF + (16 * j) * 128;
    return tr_frag(p, p + 4 * 128);
  } else if constexpr (sg < 20) {
    const int off = k1_wbase(sg) + (16 * j) * 32;
    return tr_frag(ldsp(c.rc0) + off, ldsp(c.rc1) + off);
  } else {
    const unsigned ex = (lane_of(c) >> 5) * 768u;
    const int off = k1_wbase(sg) + j * 2048;
    return tr_frag(ldsp(c.rc0 + ex) + off, ldsp(c.rc1 + ex) + off);
  }
}
#define GLD1(dst, voff, sb) asm volatile("global_load_dword %0, %1, %2" : "=v"(dst) : "v"(voff), "s"(sb) : "memory")
#define GLD4(dst, voff, sb) asm volatile("global_load_dwordx4 %0, %1, %2" : "=v"(dst) : "v"(voff), "s"(sb) : "memory")
template <int ctrl> __device__ __forceinline__ float dpp_add(float v) {
  return v + __builtin_bit_cast(float, __builtin_amdgcn_update_dpp(0, __builtin_bit_cast(int, v), ctrl, 0xf, 0xf, true));
}
__device__ __forceinline__ float rb1(float x) { return __builtin_bit_cast(float, pk2(0.f, x)); }
__device__ __forceinline__ f32x2 rb2(f32x2 v) { return (f32x2){rb1(v[0]), rb1(v[1])}; }
template <int k> __device__ __forceinline__ void k1_prep_unit(Ctx& c, K1Prep& P) {
  if constexpr (k == 0) {
    asm volatile("" : "+v"(P.sk), "+v"(P.ga0), "+v"(P.ga1), "+v"(P.gw0), "+v"(P.gw1), "+v"(c.ev)::"memory");
    P.gs = 0.f;
  }
  if constexpr (k < 4) {
    const u32x4 &a = k < 2 ? P.ga1 : P.ga0, &g = k < 2 ? P.gw1 : P.gw0;
#pragma unroll
    for (int i = 2 * (k & 1); i < 2 * (k & 1) + 2; ++i) P.gs = __builtin_fmaf(bfhi(a[i]), bfhi(g[i]), __builtin_fmaf(bflo(a[i]), bflo(g[i]), P.gs));
    if constexpr (k == 1) P.gs = lane_of(c) < 8 ? P.gs : 0.f;
    asm volatile("" : "+v"(P.gs));
  } else if constexpr (k == 4) {
    P.gs = dpp_add<0x111>(P.gs); P.gs = dpp_add<0x112>(P.gs);
    P.u[0] = *(LAS unsigned*)(ldsp(P.lp)); P.u[1] = *(LAS unsigned*)(ldsp(P.lp) + 144);
  } else if constexpr (k == 5) {
    P.gs = dpp_add<0x114>(P.gs); P.gs = dpp_add<0x118>(P.gs);
    P.u[2] = *(LAS unsigned*)(ldsp(P.lp) + 288); P.kn = *(LAS unsigned*)(ldsp(P.lp) + 432);
  } else if constexpr (k == 6) {
    int v = __builtin_bit_cast(int, P.gs);
    float a = __builtin_bit_cast(float, __builtin_amdgcn_readlane(v, 15)), b = __builtin_bit_cast(float, __builtin_amdgcn_readlane(v, 31));
    float d = __builtin_bit_cast(float, __builtin_amdgcn_readlane(v, 47)), e = __builtin_bit_cast(float, __builtin_amdgcn_readlane(v, 63));
    P.gs = __builtin_amdgcn_exp2f(rb1((a + b) + (d + e)) * -1.44269504088896f);
  } else if constexpr (k == 7) {
    float g = rb1(__builtin_amdgcn_rcpf(1.0f + P.gs));
    __builtin_amdgcn_raw_buffer_store_b32(__builtin_bit_cast(unsigned, g), c.rs, lane_of(c) == 0 ? 0u : OOB,
                                          WS_G + (256 * c.mt + 64 * c.fq + 8 * c.nl + c.w) * 4, AUX_SC1);
    P.s[0] = (f32x2){bflo(P.sk[0]), bflo(P.sk[2])};
  } else if constexpr (k == 8) {
    P.s[1] = (f32x2){bfhi(P.sk[0]), bfhi(P.sk[2])};
    P.s[2] = (f32x2){bflo(P.sk[1]), bflo(P.sk[3])}; P.s[3] = (f32x2){bfhi(P.sk[1]), bfhi(P.sk[3])};
    asm volatile("" : "+v"(P.s[0]), "+v"(P.s[1]), "+v"(P.s[2]), "+v"(P.s[3]));
  } else if constexpr (k == 9) {
    P.k2[0] = unpk2(P.u[0]); P.k2[1] = unpk2(P.u[1]); P.k2[2] = unpk2(P.u[2]);
    asm volatile("" : "+v"(P.k2[0]), "+v"(P.k2[1]), "+v"(P.k2[2]));
  } else if constexpr (k < 73) {
    constexpr int i = (k - 10) / 7, st = (k - 10) % 7;
    auto mul = [](f32x2 x, f32x2 y) { f32x2 r = {x[0] * y[0], x[1] * y[1]}; return r; };
    if constexpr (st == 0) { P.a = mul(P.k2[i & 3], P.s[3]); P.p = mul(P.k2[(i + 1) & 3], P.s[2]); P.vn = *(LAS unsigned*)(ldsp(P.lp) + K1_PV + 144 * i); }
    else if constexpr (st == 1) { P.a = rb2(P.a); P.p = rb2(P.p); }
    else if constexpr (st == 2) { P.a += P.p; P.p = mul(P.k2[(i + 2) & 3], P.s[1]); P.k2[(i + 3) & 3] = unpk2(P.kn); }
    else if constexpr (st == 3) {
      P.a = rb2(P.a); P.p = rb2(P.p);
      if constexpr (i < 8) P.kn = *(LAS unsigned*)(ldsp(P.lp) + 144 * (i + 4));
    }
    else if constexpr (st == 4) { P.a += P.p; P.p = mul(P.k2[(i + 3) & 3], P.s[0]); P.q = unpk2(P.vn); }
    else if constexpr (st == 5) { P.a = rb2(P.a); P.p = rb2(P.p); }
    else { P.a = mul(rb2(P.a + P.p), P.q); P.u[i] = pk2(P.a[0], P.a[1]); }
    if constexpr (st == 6) asm volatile("" : "+v"(P.u[i]));
    else if constexpr (st == 2) asm volatile("" : "+v"(P.a), "+v"(P.p), "+v"(P.k2[(i + 3) & 3]));
    else if constexpr (st == 4) asm volatile("" : "+v"(P.a), "+v"(P.p), "+v"(P.q));
    else asm volatile("" : "+v"(P.a), "+v"(P.p));
  } else if constexpr (k == 73) {
    const int l = lane_of(c);
    const int dp = 8 * c.w + (l >> 3);
    P.o0 = dp < 36 ? (unsigned)((72 * (c.b & 7) + 2 * dp) * 2 * S_ + (1984 - 64 * (c.b >> 3)) + 8 * (l & 7)) * 2 : OOB;
  } else {
    constexpr int ch = (k - 74) >> 1, cp = (k - 74) & 1;
    constexpr unsigned sel = ch ? 0x07060302u : 0x05040100u;
    u32x4 o;
#pragma unroll
    for (int e = 0; e < 4; ++e) o[e] = __builtin_amdgcn_perm(P.u[7 + cp - 2 * e - 1], P.u[7 + cp - 2 * e], sel);
    st128(o, c.rs, P.o0, WS_UR + (ch * 2 + cp) * S_ * 2, AUX_SC1);
  }
}
template <int NT, int e> __device__ __forceinline__ void k1_epi_step(const Ctx& c, const f32x16* acc, K1Epi& E, int ft0) {
  constexpr int pr = e / 3, st = e % 3, t = pr / 4, i = 2 * (pr % 4);
  if constexpr (st == 0) {
    if constexpr (pr > 0) { E.h[0] = E.pq[0] * E.r[0]; E.h[1] = E.pq[1] * E.r[1]; }
    if constexpr (pr < 4 * NT) {
      E.m[0] = acc[t][i] * -1.44269504088896f; E.m[1] = acc[t][i + 1] * -1.44269504088896f;
      E.p[0] = acc[t][i] * acc[t][i + 8]; E.p[1] = acc[t][i + 1] * acc[t][i + 9];
      asm volatile("" : "+v"(E.p[0]), "+v"(E.p[1]));
    }
  } else if constexpr (st == 1) {
    if constexpr (pr < 4 * NT) { E.m[0] = __builtin_amdgcn_exp2f(E.m[0]); E.m[1] = __builtin_amdgcn_exp2f(E.m[1]); }
    if constexpr (pr > 0) {
      E.ho[(pr - 1) % 4] = pk2(E.h[0], E.h[1]);
      if constexpr (pr % 4 == 0) st128(E.ho, c.rs, c.lane16, c.hb + (ft0 + (pr - 1) / 4) * 1024, AUX_SC1);
    }
  } else {
    if constexpr (pr < 4 * NT) {
      float d0 = 1.0f + E.m[0], d1 = 1.0f + E.m[1];
      E.r[0] = __builtin_amdgcn_rcpf(d0); E.r[1] = __builtin_amdgcn_rcpf(d1);
      E.pq[0] = E.p[0]; E.pq[1] = E.p[1];
    }
  }
}

template <int ch, int n, int e = 0> __device__ __forceinline__ void k1_epi_at(const Ctx& c, const f32x16* acc, K1Epi& E) {
  if constexpr (n >= K1_EPI0[ch] && n <= k1_epi_slot(ch, 49)) {
    if constexpr (k1_epi_slot(ch, e) == n) k1_epi_step<4, e>(c, acc, E, 8 * c.nl + 4 * ch);
    if constexpr (e < 49 && k1_epi_slot(ch, e < 49 ? e + 1 : e) <= n) k1_epi_at<ch, n, e + 1>(c, acc, E);
  }
}
template <int i> __device__ __forceinline__ void warm_touch(Ctx& c, const u32x4& sw2, const u32x4& ssk) {
  constexpr unsigned long long m = i < 4 ? (0x3full << (6 * (i >> 1))) : (i < 8 ? (0x3full << (16 + 6 * (i - 4))) : (0x1full << (48 + 5 * (i - 8))));
  const u32x4& srd = i < 4 ? ((i & 1) ? c.sw3 : c.sw1) : (i < 8 ? sw2 : ssk);
  asm volatile("s_mov_b32 exec_lo, %3\n\ts_mov_b32 exec_hi, %4\n\tbuffer_load_dword %0, %1, %2, 0 offen\n\ts_mov_b64 exec, -1"
               : "+v"(c.touch) : "v"(c.warm), "s"(srd), "n"((int)(unsigned)m), "n"((int)(unsigned)(m >> 32)) : "memory");
}
__device__ __forceinline__ int g2_ft(int s, int i8) {
  return s >= 8 ? 64 + i8 : (s < 4 ? 16 * s + 8 * (i8 >> 2) + (i8 & 3) : 32 * (s & 1) + 8 * (i8 >> 1) + (i8 & 1) + 4 + (s & 2));
}
template <int s, int i> __device__ __forceinline__ void g2_issue_w(const Ctx& c) {
  const unsigned n16 = 64u * i + (s < 4 ? (unsigned)lane_of(c) : c.xl0);
  const unsigned vo = ((n16 * 171u) >> 11) * (D_ * 2 - 192) + n16 * 16u;
  dma16<K1_XOFF + (s % G2_NSLOT) * G2_SLOT + i * 1024, 0>(vo, c.s2, 16 * g2_ft(s, c.w) * (D_ * 2) + 192 * c.nl, c.w * 3072);
}
template <int i> __device__ __forceinline__ u32x4 g2_load_h(const Ctx& c) {
  return __builtin_amdgcn_raw_buffer_load_b128(c.rs, c.lane16, c.hb + g2_ft(i / 8, i % 8) * 1024, AUX_SC1);
}
__device__ __forceinline__ unsigned g2_out_base(const Ctx& c) {
  return WS_PART + g2_wg(c.mt, c.nl, c.w >> 1) * 49152 + (c.fq * 2 + (c.w & 1)) * 6144;
}
__device__ __forceinline__ unsigned cv_base(int ch) { return ch < 7 ? K1_XOFF + ch * CV_CH : (ch == 7 ? k1_wbase(17) : k1_wbase(18)); }
template <int i> __device__ __forceinline__ void cv_issue(const Ctx& c) {
  const int d = 9 * cv_id(c) + c.w;
  constexpr int lo = i < 4 ? CV_K + 64 + i * 1024 : (i < 8 ? CV_U0 + (i - 4) * 1024 : CV_U1 + (i - 8) * 1024);
  const unsigned lb = c.w < 7 ? K1_XOFF + c.w * CV_CH : LDS_BYTES - 1024 - lo;
  if constexpr (i < 4) dma16<lo, i * 1024>(c.lane16, c.s2, c.w < 7 ? d * (S_ * 2) : OOB, lb);
  else dma16<lo, WS_UR + (i - 4) * 1024>(c.lane16, c.sws, c.w < 7 ? d * (4 * S_) : OOB, lb);
}
template <int ch, int part> __device__ __forceinline__ void cv_issue_shared(const Ctx& c) {
  const int d = 9 * cv_id(c) + ch;
  if constexpr (part == 0) dma16<k1_wbase(10 + ch), WS_UR>(c.lane16, c.sws, d * (4 * S_) + c.w * 1024, c.w < 4 ? CV_U0 + c.w * 1024 : CV_U1 + (c.w - 4) * 1024);
  else dma16<k1_wbase(10 + ch) + CV_K + 64, 0>(c.lane16, c.s2, d * (S_ * 2) + (c.w >> 1) * 1024, (c.w >> 1) * 1024);
}
static_assert(K1_XOFF + 7 * CV_CH <= LDS_BYTES - 1024, "dummy target");
typedef unsigned u32x2a4 __attribute__((ext_vector_type(2), aligned(4)));
__device__ __forceinline__ bf16x8 cv_read_a(LAS char* p) {
  u32x2 lo = *(LAS u32x2a4*)p;
  u32x2 hi = *(LAS u32x2a4*)(p + 8);
  u32x4 v = {lo[0], lo[1], hi[0], hi[1]};
  return __builtin_bit_cast(bf16x8, v);
}
__device__ __forceinline__ int cv_zwg(const Ctx& c, int lane, int& slot) {
  const int cw = cv_id(c);
  const int n0 = (9 * cw) / 96, n1 = (9 * cw + 8) / 96;
  const int s0 = 16 + cw - (96 * n0) / 9, s1 = n1 != n0 ? 16 + cw - (96 * n1) / 9 : -1;
  slot = lane < 32 ? s0 : s1;
  return g2_wg((lane >> 2) & 7, lane < 32 ? n0 : n1, lane & 3);
}

template <int n, int ROLE>
__device__ __forceinline__ void k1_slot(Ctx& c, bf16x8 (&xf)[36], bf16x8 (&wf)[K1_RD + 1], f32x16 (&acc)[4], K1Epi& E, K1Prep& P, u32x4 (&hw)[24],
                                        CvRegs& V) {
  constexpr const K1Sched& S = K1S[ROLE];
  constexpr int sg = S.seg[n];
  if constexpr (n + K1_RD == 288) {
    const unsigned l = lane_of(c);
    unsigned mat = (l >> 4) & 1, h = l >> 5, q = (l & 15) >> 2, p = l & 3;
    unsigned base = mat * 8192 + (8 * h + q) * 32 + p * 8;
    c.rc0 = base + (mat ? 128 : 0);
    c.rc1 = base + (mat ? 0 : 128);
  }
  if constexpr (n + K1_RD < K1_NS) wf[(n + K1_RD) % (K1_RD + 1)] = k1_read_w<n + K1_RD>(c);
  SCHED_FENCE();
  if constexpr (n + 4 < 144) {
    constexpr int sn = S.seg[n + 4];
    if constexpr (S.tile[n + 4] == 0 && sn >= 1) xf[4 * sn + S.j[n + 4]] = k1_read_x<sn, S.j[n + 4]>(c);
  }
  {
    constexpr int t = S.tile[n];
    constexpr int xi = sg < 18 ? 4 * (sg % 9) + S.j[n] : n - 288;
    constexpr bool first = (sg < 18) ? (sg % 9 == 0 && S.j[n] == 0) : (n == 288);
    if constexpr (first) acc[t] = __builtin_amdgcn_mfma_f32_32x32x16_bf16(wf[n % (K1_RD + 1)], xf[xi], (f32x16){}, 0, 0, 0);
    else acc[t] = __builtin_amdgcn_mfma_f32_32x32x16_bf16(wf[n % (K1_RD + 1)], xf[xi], acc[t], 0, 0, 0);
  }
  SCHED_FENCE();
  {
    constexpr int ws0 = [] { for (int s = 0; s < K1_NSEG; ++s) if (K1S[0].wissue[s] == n) return s; return -1; }();
    constexpr int ws1 = [] { for (int s = 0; s < K1_NSEG; ++s) if (K1S[0].wissue[s] >= 0 && K1S[0].wissue[s] + 2 == n) return s; return -1; }();
    constexpr int xs = [] { for (int p = 0; p < 9; ++p) if (K1S[0].xissue[p] >= 0 && n >= K1S[0].xissue[p] && n < K1S[0].xissue[p] + 4) return p; return -1; }();
    if constexpr (ws0 >= 0) k1_issue_w<ws0, 0>(c);
    if constexpr (ws1 >= 0) k1_issue_w<ws1, 1>(c);
    if constexpr (xs >= 0) k1_issue_x<xs, n - S.xissue[xs]>(c);
  }
  if constexpr (n >= K1_PREP0 && n < K1_PREP0 + K1_PREP_UNITS) k1_prep_unit<n - K1_PREP0>(c, P);
  k1_epi_at<0, n>(c, acc, E);
  k1_epi_at<1, n>(c, acc, E);
  {
    constexpr int wi = [] { for (int i = 0; i < WARM_N; ++i) if (WARM_SLOT[i] == n) return i; return -1; }();
    if constexpr (wi >= 0) warm_touch<wi>(c, ROLE == 0 ? c.s2 : c.s3, ROLE == 0 ? c.s3 : c.s2);
    if constexpr (n == WARM_DONE + 1) asm volatile("" : "+v"(c.touch)::"memory");
  }
  if constexpr (n == EV_PUB_U) {
    if (c.w == 1) { const int l = lane_of(c); flag_store(c, l < 8, cv_wg(8 * (c.b & 7) + l), c.b >> 3, c.ev); }
  }
  if constexpr (n == EV_PUB_A) {
    if (c.w == 1) publish_h(c, 0);
  }
  if constexpr (n == EV_PUB_B1) {
    if (c.w == 1) publish_h(c, 8);
  }
  if constexpr (ROLE == 0) {
    if constexpr (n >= EV_SENT0 && n < EV_SENT0 + 6 * EV_SENT_STEP && (n - EV_SENT0) % EV_SENT_STEP == 0)
      st128(sent4(), c.rs, c.lane16, (c.w >> 1) == c.fq ? OOB : g2_out_base(c) + (n - EV_SENT0) / EV_SENT_STEP * 1024, AUX_SC1);
    if constexpr (n == EV_PUB_S) {
      if (c.w == 2) { const int l = lane_of(c); flag_store(c, (l & 60) == 16, g2_wg(c.mt, c.nl, l & 3), 32 + c.fq, c.ev); }
    }
    if constexpr (n >= EV_PRE0 && n < EV_PRE0 + 12 * EV_PRE_STEP && (n - EV_PRE0) % EV_PRE_STEP == 0)
      g2_issue_w<(n - EV_PRE0) / EV_PRE_STEP / 3, (n - EV_PRE0) / EV_PRE_STEP % 3>(c);
    if constexpr (n == EV_G2_POLL) poll_issue(c);
    if constexpr (n == EV_G2_CHECK) {
      POLL_LANDED(S.ops_after(EV_G2_POLL, EV_G2_CHECK));
      if (c.w == 0) poll_wait(c, lane_of(c) < 8);
    }
    if constexpr (n >= EV_G2_H0 && (n - EV_G2_H0) % 2 == 0 && (n - EV_G2_H0) / 2 < G2_WIN) hw[(n - EV_G2_H0) / 2] = g2_load_h<(n - EV_G2_H0) / 2>(c);
  } else {
    if constexpr (n == EV_CV_POLL) poll_issue(c);
    if constexpr (n == EV_CV_CHECK) {
      POLL_LANDED(K1S[1].ops_after(EV_CV_POLL, EV_CV_CHECK));
      if (c.w == 0) {
        poll_wait(c, lane_of(c) < 32);
        asm volatile("buffer_inv sc1" ::: "memory");
        __builtin_amdgcn_raw_buffer_store_b32(c.ev + 1u, c.rs, lane_of(c) == 14 ? 0u : OOB, WS_EPOCH + c.b * 4, AUX_SC1);
      }
    }
    if constexpr (n == EV_CV_INVW) { if (c.w == 0) wait_vm<S.ops_after(EV_CV_CHECK, EV_CV_INVW)>(); }
    if constexpr (n >= EV_PRE0 && n < EV_PRE0 + 12 * EV_PRE_STEP && (n - EV_PRE0) % EV_PRE_STEP == 0) {
      cv_issue<(n - EV_PRE0) / EV_PRE_STEP>(c);
    }
    if constexpr (n == EV_CV_PRE7 || n == EV_CV_PRE7 + 1) cv_issue_shared<7, n - EV_CV_PRE7>(c);
    if constexpr (n == EV_CV_PRE7 + 3) {
      const int l = lane_of(c);
      const unsigned cb = cv_base(c.w);
      if (l < 60) {
        int o = l < 4 ? CV_K + l * 16 : (l < 32 ? CV_U0 + 4096 + (l - 4) * 16 : CV_U1 + 4096 + (l - 32) * 16);
        *(LAS u32x4*)(ldsp(cb) + o) = zero4();
      }
    }
    if constexpr (n == EV_CV_PRE7 + 8) {
      c.dptr = karg(5);
      asm volatile("" : "+s"(c.dptr));
      const int l = lane_of(c);
      const unsigned cb = cv_base(c.w);
      const int rho = l & 15, q = l >> 4, pi = (l >> 3) & 1, ii = l & 7;
      V.bp = cb + CV_U0 + pi * 4640 + 2 * (2016 - 32 * ii + 8 * q) - 64 * 56;
#pragma unroll
      for (int i = 0; i < 4; ++i) { V.ap[i] = cb + CV_K + 4 + 4 * rho + 16 * q + 1024 * i; asm volatile("" : "+v"(V.ap[i])); }
    }
    if constexpr (n == EV_CV_PRE8 || n == EV_CV_PRE8 + 1) cv_issue_shared<8, n - EV_CV_PRE8>(c);
    if constexpr (n == EV_CV_PRE8 + 1) {
      if (c.w == 6) {
        const int l = lane_of(c);
        if (l < 60) {
          int o = l < 4 ? CV_K + l * 16 : (l < 32 ? CV_U0 + 4096 + (l - 4) * 16 : CV_U1 + 4096 + (l - 32) * 16);
          *(LAS u32x4*)(ldsp(k1_wbase(18)) + o) = zero4();
        }
      }
    }
    if constexpr (n >= 320) {
#pragma unroll
      for (int f = 2 * (n - 320); f < 2 * (n - 320) + 2; ++f) {
        V.b0[f] = *(LAS bf16x8*)(ldsp(V.bp) + 64 * (56 - 8 * f));
        V.a0[f] = cv_read_a(ldsp(V.ap[f >> 1]) + 512 * (f & 1));
      }
    }
  }
  SCHED_FENCE();
  if constexpr (S.bar[n]) {
    wait_vm_barrier<S.wait[n]>();
    SCHED_FENCE();
  }
  if constexpr (n + 1 < K1_NS) k1_slot<n + 1, ROLE>(c, xf, wf, acc, E, P, hw, V);
}

constexpr int g2_ks(int n) { return n < 168 ? n / 3 : 56 + (n - 168) % 16; }
constexpr int g2_tl(int n) { return n < 168 ? n % 3 : (n - 168) / 16; }
constexpr int G2_HW = 24;
template <int n> __device__ __forceinline__ bf16x8 g2_read_w(unsigned rd) {
  constexpr int i = g2_ks(n), t = g2_tl(n), s = i / 8, j = i % 8;
  LAS char* p = ldsp(rd) + (s % G2_NSLOT) * G2_SLOT + (16 * j) * 192 + t * 64;
  return tr_frag(p, p + 8 * 192);
}
static_assert(k1_epi_slot(1, 49) < K1_NS, "chunk B epilogue ends in phase 1");
constexpr int G2_TAIL0 = 2, G2_TAILST = G2_TAIL0 + 13;
constexpr int G2_PUBB = 19, G2_PUBC = 43;
constexpr int G2_CHECK0 = 39, G2_LEAD = 18;
constexpr bool g2_check(int n) { return n >= G2_CHECK0 && n <= G2_CHECK0 + 120 && (n - G2_CHECK0) % 24 == 0; }
constexpr int G2_PST = 186;
constexpr int G2_PRE3 = 206;
constexpr int g2_hslot(int i) {
  return i < 64 ? 3 * (i - G2_WIN) + 2 : 144 + (i - 64) + (i - 64) / 2;
}
constexpr int g2_hload_at(int n) { for (int i = G2_WIN; i < 72; ++i) if (g2_hslot(i) == n) return i; return -1; }
constexpr bool g2_bar(int n) { return (n % 24 == 24 - G2_PF - 1 && n / 24 < 7) || n == 176 - G2_PF - 1; }
constexpr int g2_ops_in_slot(int n) {
  int k = 0;
  for (int s = 1; s + 3 < 9; ++s) for (int i = 0; i < 3; ++i) if (n == 24 * s + 24 - G2_PF + 1 + 3 * i) ++k;
  if (g2_hload_at(n) >= 0) ++k;
  if (n == G2_TAILST) ++k;
  if (n == G2_PST || n == G2_PST + 2 || n == G2_PST + 16 || n == G2_PST + 18) ++k;
  return k;
}
constexpr int g2_ops_after(int a, int b) { int k = 0; for (int n = a + 1; n <= b; ++n) k += g2_ops_in_slot(n); return k; }
static_assert([] { for (int i = G2_WIN; i + 1 < 72; ++i) if (g2_hslot(i) >= g2_hslot(i + 1) || (i >= G2_HW && g2_hslot(i) < 3 * (i - G2_HW) + 2)) return false; return g2_hslot(64) > G2_CHECK0 + 100; }(), "h load slots");
static_assert(g2_bar(G2_PUBB) && g2_bar(G2_PUBC) && g2_bar(G2_CHECK0 + 4) && g2_hslot(32) > G2_CHECK0 + 4 && G2_PRE3 > G2_CHECK0 + 124 && G2_LEAD < 23, "events");
struct G2Fin { u32x4 zz[2], qq[2]; float g; };
__device__ __forceinline__ void g2_send(const Ctx& c, const f32x16& a, int t, int p2) {
  const bool own = (c.w >> 1) == c.fq;
  const u32x4 o = {pk2(a[8 * p2], a[8 * p2 + 1]), pk2(a[8 * p2 + 2], a[8 * p2 + 3]), pk2(a[8 * p2 + 4], a[8 * p2 + 5]), pk2(a[8 * p2 + 6], a[8 * p2 + 7])};
  st128(o, c.rs, c.lane16, own ? OOB : g2_out_base(c) + (t * 2 + p2) * 1024, AUX_SC1);
  if (own) {
#pragma unroll
    for (int qd = 2 * p2; qd < 2 * p2 + 2; ++qd)
      *(LAS f32x4*)(ldsp(c.lane16) + ((c.w & 1) * 12 + t * 4 + qd) * 1024) = (f32x4){a[4 * qd], a[4 * qd + 1], a[4 * qd + 2], a[4 * qd + 3]};
  }
}
__device__ __forceinline__ void g2_fin_load(const Ctx& c, G2Fin& Fn, bool all) {
  const int lane = lane_of(c);
  const int uh = (c.w / 3) & 1, ut = c.w % 3;
  const int t0 = 256 * c.mt + 64 * c.fq + 32 * uh, d0 = 96 * c.nl + 32 * ut;
#pragma unroll
  for (int i = 0; i < 2; ++i)
    Fn.zz[i] = __builtin_amdgcn_raw_buffer_load_b128(c.rs, ((16 * i + (lane >> 2)) * S_) * 2 + (lane & 3) * 16, WS_Z + (d0 * S_ + t0) * 2, AUX_SC1);
  if (all) {
    const char* qptr = karg(0);
#pragma unroll
    for (int i = 0; i < 2; ++i) Fn.qq[i] = *(const u32x4*)(qptr + ((size_t)(t0 + (lane & 31)) * D_ + d0 + 16 * (lane >> 5) + 8 * i) * 2);
    Fn.g = __builtin_bit_cast(float, __builtin_amdgcn_raw_buffer_load_b32(c.rs, (lane & 31) * 4, WS_G + t0 * 4, AUX_SC1));
  }
}
template <int n>
__device__ __forceinline__ void g2_slot(Ctx& c, unsigned rd, u32x4 (&hw)[24], bf16x8 (&wf)[G2_PF + 1], f32x16 (&acc)[3], const f32x16* tailacc, K1Epi& E,
                                        G2Fin& Fn) {
  constexpr int i = g2_ks(n), t = g2_tl(n), s = n / 24;
  if constexpr (n + G2_PF < G2_NS) wf[(n + G2_PF) % (G2_PF + 1)] = g2_read_w<n + G2_PF>(rd);
  if constexpr (i == 0) acc[t] = __builtin_amdgcn_mfma_f32_32x32x16_bf16(wf[n % (G2_PF + 1)], __builtin_bit_cast(bf16x8, hw[i % G2_HW]), (f32x16){}, 0, 0, 0);
  else acc[t] = __builtin_amdgcn_mfma_f32_32x32x16_bf16(wf[n % (G2_PF + 1)], __builtin_bit_cast(bf16x8, hw[i % G2_HW]), acc[t], 0, 0, 0);
  SCHED_FENCE();
  if constexpr (n >= G2_TAIL0 && n <= G2_TAILST) k1_epi_step<1, n - G2_TAIL0>(c, tailacc, E, 64 + c.nl);
  {
    constexpr int m = n - (24 - G2_PF + 1);
    if constexpr (m >= 24 && m % 24 < 9 && m % 3 == 0 && m / 24 + 3 < 9) g2_issue_w<m / 24 + 3, (m % 24) / 3>(c);
  }
  {
    constexpr int hl = g2_hload_at(n);
    if constexpr (hl >= 0) hw[hl % G2_HW] = g2_load_h<hl>(c);
  }
  if constexpr (n == G2_PST || n == G2_PST + 2) g2_send(c, acc[0], 0, (n - G2_PST) / 2);
  if constexpr (n == G2_PST + 16 || n == G2_PST + 18) g2_send(c, acc[1], 1, (n - G2_PST - 16) / 2);
  if constexpr (n == G2_PRE3) {
    g2_fin_load(c, Fn, true);
  }
  if constexpr (g2_check(n + G2_LEAD)) poll_issue(c);
  if constexpr (g2_check(n)) {
    constexpr int k = (n - G2_CHECK0) / 24;
    POLL_LANDED(g2_ops_after(n - G2_LEAD, n));
    if (c.w == 0) {
      const int l = lane_of(c);
      if constexpr (k == 0) poll_wait<1>(c, (l & 60) == 8);
      if constexpr (k == 1) poll_wait<2>(c, (l & 60) == 12);
      if constexpr (k == 2) poll_wait<3>(c, (l & 60) == 48);
      if constexpr (k == 3) poll_wait<4>(c, (l & 60) == 52);
      if constexpr (k == 4) poll_wait<7>(c, (l & 56) == 40);
      if constexpr (k == 5) {
        poll_wait<5>(c, (l >= 16 && l <= 16 + (96 * c.nl + 95) / 9 - (96 * c.nl) / 9) || (l & 60) == 32);
        __builtin_amdgcn_raw_buffer_store_b32(c.ev + 1u, c.rs, l == 14 ? 0u : OOB, WS_EPOCH + c.b * 4, AUX_SC1);
      }
    }
  }
  SCHED_FENCE();
  if constexpr (g2_bar(n)) {
    constexpr int wt = n == G2_PUBB ? K1S[0].ops_after(k1_epi_slot(1, 49), K1_NS - 1) + g2_ops_after(-1, n)
                                    : (n == G2_PUBC ? g2_ops_after(G2_TAILST, n) : (s + 1 < 4 ? 63 : g2_ops_after(24 * (s - 2) + 24 - G2_PF + 1 + 6, n)));
    wait_vm_barrier<cmin(63, wt)>();
    if constexpr (n == G2_PUBB) { if (c.w == 1) publish_h(c, 48); }
    if constexpr (n == G2_PUBC) { if (c.w == 1) publish_h(c, 40); }
    SCHED_FENCE();
  }
  if constexpr (n + 1 < G2_NS) g2_slot<n + 1>(c, rd, hw, wf, acc, tailacc, E, Fn);
}
__device__ __forceinline__ unsigned umax4(const u32x4& v) {
  unsigned a = v[0] > v[1] ? v[0] : v[1], b = v[2] > v[3] ? v[2] : v[3];
  return a > b ? a : b;
}

__device__ __forceinline__ void g2_phase(Ctx& c, u32x4 (&hw)[24], const f32x16* tailacc, K1Epi& E) {
  const int lane = lane_of(c);
  const int w = c.w, ks = c.fq, nt = c.nl, mt = c.mt;
  c.xl0 = lane;
  unsigned rd;
  {
    int gq = lane >> 4, chf = gq & 1, h = gq >> 1, q = (lane & 15) >> 2, p = lane & 3;
    rd = K1_XOFF + (4 * h + q) * 192 + chf * 32 + p * 8;
  }
  bf16x8 wf[G2_PF + 1];
  f32x16 acc[3];
  wf[0] = g2_read_w<0>(rd); wf[1] = g2_read_w<1>(rd); wf[2] = g2_read_w<2>(rd); wf[3] = g2_read_w<3>(rd);
  SCHED_FENCE();
  G2Fin Fn;
  g2_slot<0>(c, rd, hw, wf, acc, tailacc, E, Fn);
  g2_send(c, acc[2], 2, 0);
  g2_send(c, acc[2], 2, 1);
  const int uh = w / 3, ut = w % 3;
  const int qp = lane >> 5, cc = lane & 31;
  lds_barrier();
  STAMP(33);
  if (w < 6) {
    char* optr = (char*)karg(11);
    u32x4 pp[3][2];
    float ga[16], og = 0.f;
    f32x4 own[2][2];
    bool zdone = false;
    unsigned spin = 0;
#pragma unroll 1
    for (; spin < SPIN_CAP; ++spin) {
      asm volatile("" ::: "memory");
      const bool zok = __all(umax4(Fn.zz[0]) != SENT && umax4(Fn.zz[1]) != SENT) || spin == SPIN_CAP - 1;
      if (!zok) g2_fin_load(c, Fn, false);
#pragma unroll
      for (int s = 0; s < 3; ++s) {
        const int src = s + (s >= ks ? 1 : 0);
#pragma unroll
        for (int h = 0; h < 2; ++h)
          pp[s][h] = __builtin_amdgcn_raw_buffer_load_b128(c.rs, qp * 1024 + (cc + 32 * h) * 16, WS_PART + c.b * 49152 + (src * 2 + uh) * 6144 + ut * 2048, AUX_SC1);
      }
      SCHED_FENCE();
      if (zok && !zdone) {
        zdone = true;
        LAS char* zt = ldsp(24576 + w * 2560);
#pragma unroll
        for (int i = 0; i < 2; ++i) *(LAS u32x4*)(zt + (16 * i + (lane >> 2)) * 80 + (lane & 3) * 16) = Fn.zz[i];
        unsigned zv[16];
#pragma unroll
        for (int j = 0; j < 16; ++j) zv[j] = *(LAS u16*)(zt + (16 * qp + j) * 80 + ((cc & 24) + 4 * (cc & 1) + ((cc & 7) >> 1)) * 2);
#pragma unroll
        for (int o = 0; o < 2; ++o)
#pragma unroll
          for (int h = 0; h < 2; ++h) own[o][h] = *(LAS f32x4*)(ldsp((cc + 32 * h) * 16) + (uh * 12 + ut * 4 + 2 * qp + o) * 1024);
        og = rbf(1.0f - Fn.g);
#pragma unroll
        for (int j = 0; j < 16; j += 2) {
          const unsigned qw = Fn.qq[j >> 3][(j >> 1) & 3];
          const unsigned a2 = pk2(bflo(qw) * bflo(zv[j]), bfhi(qw) * bflo(zv[j + 1]));
          ga[j] = Fn.g * bflo(a2); ga[j + 1] = Fn.g * bfhi(a2);
        }
      }
      SCHED_FENCE();
      u32x4 mx = {umax4(pp[0][0]), umax4(pp[0][1]), umax4(pp[1][0]), umax4(pp[1][1])};
      if (zdone && __all(umax4(mx) != SENT && umax4(pp[2][0]) != SENT && umax4(pp[2][1]) != SENT)) break;
    }
    DBGCNT(46, spin);
    STAMP(34);
#pragma unroll
    for (int o = 0; o < 2; ++o) {
      float res[8];
#pragma unroll
      for (int h = 0; h < 2; ++h)
#pragma unroll
        for (int k = 0; k < 4; k += 2) {
          f32x2 bs = {own[o][h][k], own[o][h][k + 1]};
#pragma unroll
          for (int s = 0; s < 3; ++s) bs += unpk2(pp[s][h][2 * o + (k >> 1)]);
          const f32x2 b2 = unpk2(pk2(bs[0], bs[1]));
          res[4 * h + k] = radd(ga[8 * o + 4 * h + k], og * b2[0]);
          res[4 * h + k + 1] = radd(ga[8 * o + 4 * h + k + 1], og * b2[1]);
        }
      u32x4 ov = {pk2(res[0], res[1]), pk2(res[2], res[3]), pk2(res[4], res[5]), pk2(res[6], res[7])};
      *(u32x4*)(optr + ((size_t)(256 * mt + 64 * ks + 32 * uh + cc) * D_ + 96 * nt + 32 * ut + 16 * qp + 8 * o) * 2) = ov;
    }
  }
  STAMP(35);
}

template <bool PREF, bool FIRST, class Hook>
__device__ __forceinline__ void cv_block(f32x4 (&acc)[8], const bf16x8 (&af)[8], const bf16x8 (&bf)[8], bf16x8 (&naf)[8], bf16x8 (&nbf)[8],
                                         const unsigned (&ap)[4], unsigned bp, int roff, Hook&& hook) {
  auto step = [&](auto K) {
    constexpr int k = decltype(K)::value;
    constexpr int p = [] { int r = k, q = 0; while (r >= 8 - q) { r -= 8 - q; ++q; } return q; }();
    constexpr int dl = [] { int r = k, q = 0; while (r >= 8 - q) { r -= 8 - q; ++q; } return r; }();
    if constexpr (PREF && (k & 1) == 0 && k < 32) {
      constexpr int f = k >> 2;
      if constexpr ((k & 2) != 0) naf[f] = cv_read_a(ldsp(ap[f >> 1]) + 512 * (f & 1) + roff);
      else nbf[f] = *(LAS bf16x8*)(ldsp(bp) + 64 * (56 - 8 * f) + roff);
    }
    if constexpr (FIRST && p == 0) asm volatile("v_mfma_f32_16x16x32_bf16 %0, %1, %2, 0" : "=&v"(acc[dl]) : "v"(af[p]), "v"(bf[dl]));
    else asm volatile("v_mfma_f32_16x16x32_bf16 %0, %1, %2, %0" : "+v"(acc[p + dl]) : "v"(af[p]), "v"(bf[dl]));
    if constexpr (k & 1) { SCHED_FENCE(); hook(K); SCHED_FENCE(); }
  };
  [&]<int... Ks>(std::integer_sequence<int, Ks...>) { (step(std::integral_constant<int, Ks>{}), ...); }(std::make_integer_sequence<int, 36>{});
}
__device__ __forceinline__ u32x4 cv_u(LAS char* cl, int g, int lane) {
  const int q = lane >> 4, pi = (lane >> 3) & 1, ii = lane & 7;
  return *(LAS u32x4*)(cl + (pi ? CV_U1 : CV_U0) + 2 * (2040 - (256 * g + 32 * ii + 8 * q)));
}
__device__ __forceinline__ f32x4 cv_ud(const u32x4& uu, float Dv) {
  return (f32x4){rb1(bflo(uu[3]) * Dv), rb1(bflo(uu[2]) * Dv), rb1(bflo(uu[1]) * Dv), rb1(bflo(uu[0]) * Dv)};
}
__device__ __forceinline__ u32x2 cv_z(const f32x4& y, const f32x4& ud) {
  return (u32x2){pk2(rb1(y[0]) + ud[0], rb1(y[1]) + ud[1]), pk2(rb1(y[2]) + ud[2], rb1(y[3]) + ud[3])};
}
__device__ __forceinline__ void cv_phase(Ctx& c, const f32x16* tailacc, K1Epi& E, CvRegs& V) {
  const int w = c.w;
  f32x4 acc[8], acc8[8];
  bf16x8 a1[8], b1[8];
  auto none = [](auto) {};
  cv_block<true, true>(acc, V.a0, V.b0, a1, b1, V.ap, V.bp, 64, [&](auto K) {
    constexpr int k = decltype(K)::value / 2;
    if constexpr (k >= G2_TAIL0 && k <= G2_TAILST) k1_epi_step<1, k - G2_TAIL0>(c, tailacc, E, 64 + c.nl);
  });
  STAMP(24);
  wait_vm_barrier<1>();
  if (w == 1) publish_h(c, 48);
  const int lane = lane_of(c);
  const unsigned zb = WS_Z + 9 * cv_id(c) * (S_ * 2);
  const unsigned cb = cv_base(w), cb8 = cv_base(8);
  int zsl, zwg;
  unsigned zev, dd, ap8[4];
  SCHED_FENCE();
  cv_block<true, false>(acc, a1, b1, V.a0, V.b0, V.ap, V.bp, 128, [&](auto K) {
    constexpr int k = decltype(K)::value / 2;
    if constexpr (k == 1 || k == 5 || k == 9 || k == 13) st128(sent4(), c.rs, c.lane16, zb + w * (S_ * 2) + (k / 4) * 1024, AUX_SC1);
    if constexpr (k == 17) __builtin_amdgcn_raw_buffer_store_b64(sent2(), c.rs, lane * 8, zb + 8 * (S_ * 2) + 512 * w, AUX_SC1);
    if constexpr (k == 3) { zwg = cv_zwg(c, lane, zsl); zev = __builtin_amdgcn_raw_buffer_load_b32(c.rs, zwg * 4, WS_EPOCH, 0); }
    if constexpr (k == 7) dd = *(const u16*)(c.dptr + (9 * cv_id(c) + ((lane & 1) ? 8 : w)) * 2);
    if constexpr (k == 11) {
#pragma unroll
      for (int i = 0; i < 4; ++i) { ap8[i] = V.ap[i] + (cb8 - cb + 64 * w); asm volatile("" : "+v"(ap8[i])); }
    }
  });
  STAMP(25);
  cv_block<true, false>(acc, V.a0, V.b0, a1, b1, ap8, V.bp + (cb8 - cb + 64 * w), 0, none);
  STAMP(26);
  cv_block<true, true>(acc8, a1, b1, V.a0, V.b0, V.ap, V.bp, 192, none);
  STAMP(27);
  LAS char* sc = ldsp(((w & 2) ? k1_wbase(20) : k1_wbase(19)) + (w & 1) * 8192 + lane * 16);
  cv_block<true, false>(acc, V.a0, V.b0, a1, b1, V.ap, V.bp, 256, [&](auto K) {
    constexpr int k = decltype(K)::value / 2;
    if constexpr (k >= 4 && k < 12) { if (w >= 4) *(LAS f32x4*)(sc + (k - 4) * 1024) = acc8[k - 4]; }
  });
  STAMP(28);
  asm volatile("s_waitcnt vmcnt(0) lgkmcnt(0)\n\ts_barrier" ::: "memory");
  if (w == 5) publish_h(c, 40);
  if (w == 6) flag_store(c, zsl >= 0, zwg, zsl, zev);
  SCHED_FENCE();
  f32x4 o8[2];
  cv_block<true, false>(acc, a1, b1, V.a0, V.b0, V.ap, V.bp, 320, [&](auto K) {
    constexpr int k = decltype(K)::value / 2;
    if (w < 4) {
      if constexpr (k >= 2 && k < 18 && (k & 1) == 0) *(LAS f32x4*)(sc + (k / 2 - 1) * 1024) = acc8[k / 2 - 1] + o8[(k / 2 - 1) & 1];
      if constexpr (k < 16 && (k & 1) == 0) o8[(k / 2) & 1] = *(LAS f32x4*)(sc + (k / 2) * 1024);
    }
  });
  STAMP(29);
  lds_barrier();
  const float Dv = bflo((unsigned)__builtin_amdgcn_readlane((int)dd, 0)), Dv8 = bflo((unsigned)__builtin_amdgcn_readlane((int)dd, 1));
  const int zo = (32 * (lane & 7) + 8 * (lane >> 4) + 4 * ((lane >> 3) & 1)) * 2;
  f32x4 y4[4], ud[8];
  u32x4 uu[2];
  SCHED_FENCE();
  cv_block<true, false>(acc, V.a0, V.b0, a1, b1, V.ap, V.bp, 384, [&](auto K) {
    constexpr int k = decltype(K)::value / 2;
    LAS char* s0 = ldsp(w * 1024 + lane * 16);
    if constexpr (k < 4) y4[k] = *(LAS f32x4*)(s0 + ((k & 2) ? k1_wbase(20) : k1_wbase(19)) + (k & 1) * 8192);
    if constexpr (k == 4) uu[0] = cv_u(ldsp(cb8), w, lane);
    if constexpr (k == 8) { y4[0] = (y4[0] + y4[1]) + (y4[2] + y4[3]); asm volatile("" : "+v"(y4[0])); }
    if constexpr (k == 12) __builtin_amdgcn_raw_buffer_store_b64(cv_z(y4[0], cv_ud(uu[0], Dv8)), c.rs, zo, zb + 8 * (S_ * 2) + 512 * w, AUX_SC1);
  });
  STAMP(30);
  cv_block<true, false>(acc, a1, b1, V.a0, V.b0, V.ap, V.bp, 448, [&](auto K) {
    constexpr int k = decltype(K)::value / 2;
    if constexpr (k >= 2 && (k & 1) == 0) { ud[k / 2 - 1] = cv_ud(uu[(k / 2 - 1) & 1], Dv); asm volatile("" : "+v"(ud[k / 2 - 1])); }
    if constexpr (k < 16 && (k & 1) == 0) uu[(k / 2) & 1] = cv_u(ldsp(cb), k / 2, lane);
  });
  STAMP(31);
  cv_block<false, false>(acc, V.a0, V.b0, a1, b1, V.ap, V.bp, 0, [&](auto K) {
    constexpr int k = decltype(K)::value;
    constexpr int g = k == 5 ? 0 : (k == 13 ? 1 : (k == 19 ? 2 : (k == 25 ? 3 : (k == 31 ? 4 : (k == 35 ? 5 : -1)))));
    if constexpr (g >= 0) {
      asm volatile("s_nop 3" : "+v"(acc[g]));
      __builtin_amdgcn_raw_buffer_store_b64(cv_z(acc[g], ud[g]), c.rs, zo, zb + w * (S_ * 2) + 512 * g, AUX_SC1);
    }
  });
  STAMP(32);
  asm volatile("s_nop 7\n\ts_nop 1" : "+v"(acc[6]), "+v"(acc[7]));
#pragma unroll
  for (int g = 6; g < 8; ++g) __builtin_amdgcn_raw_buffer_store_b64(cv_z(acc[g], ud[g]), c.rs, zo, zb + w * (S_ * 2) + 512 * g, AUX_SC1);
  STAMP(35);
}

template <int ROLE>
__device__ __forceinline__ void fused_body(const int w, const unsigned long long pc) {
  const int b = (blockIdx.x + 192) & 255, xcd = b & 7, r = b >> 3;
  const int ml = r & 3, nl = r >> 2, fq = xcd >> 1;
  const int mt = 4 * (xcd & 1) + ml;
  const int lane = __builtin_amdgcn_mbcnt_hi(~0u, __builtin_amdgcn_mbcnt_lo(~0u, 0u));
  Ctx c;
  RSTAMP(37);
  STAMP(0);
  c.w = w; c.nl = nl; c.ml = ml; c.b = b; c.mt = mt; c.fq = fq;
  const char* ka[11];
  {
    typedef const char* cptr;
    const __attribute__((address_space(4))) cptr* p = (const __attribute__((address_space(4))) cptr*)__builtin_amdgcn_kernarg_segment_ptr();
    asm volatile("" : "+s"(p));
#pragma unroll
    for (int i = 0; i < 11; ++i) ka[i] = p[i];
    asm volatile("" : "+s"(ka[0]), "+s"(ka[1]), "+s"(ka[2]), "+s"(ka[3]), "+s"(ka[4]), "+s"(ka[6]), "+s"(ka[7]), "+s"(ka[8]), "+s"(ka[9]), "+s"(ka[10]));
  }
  c.rot = (nl + ml) % 3;
  c.lane16 = lane * 16;
  asm volatile("" : "+v"(c.lane16));
  c.sw1 = make_srd(ka[6], (1152 * fq + 128 * nl) * 2, D_ * F_ * 2);
  c.sw3 = make_srd(ka[7], (1152 * fq + 128 * nl) * 2, D_ * F_ * 2);
  c.sx = make_srd(ka[0], (256 * mt + 32 * w) * (D_ * 2), S_ * D_ * 2);
#pragma unroll
  for (int i = 0; i < 3; ++i) { const int k = kslice(i, c.rot); c.wo[i] = k * (64 * F_ * 2); c.xo[i] = k * 128; }
  {
    int row = lane >> 3, pos = lane & 7;
    c.voff_w = (8 * w + row) * (F_ * 2) + ((pos ^ (2 * ((row >> 1) & 1))) * 16);
    c.voff_x = row * (D_ * 2) + ((pos ^ (lane >> 4)) * 16);
  }
  k1_issue_w<0, 0>(c); k1_issue_w<0, 1>(c);
  k1_issue_x<0, 0>(c); k1_issue_x<0, 1>(c); k1_issue_x<0, 2>(c); k1_issue_x<0, 3>(c);
  {
    unsigned e0, e1;
    asm volatile("s_mov_b32 %0, .Lgm_code_end0-.Lgm_code_start\n\ts_mov_b32 %1, .Lgm_code_end1-.Lgm_code_start" : "=s"(e0), "=s"(e1));
    const u32x4 sc = make_srd((const char*)pc, 0, e0 > e1 ? e0 : e1);
    const unsigned vo = lane < 3 ? (unsigned)(8 * r + w + 256 * lane) * 128 : OOB;
    asm volatile("buffer_load_dword %0, %1, %2, 0 offen" : "=v"(c.touch) : "v"(vo), "s"(sc) : "memory");
  }
  k1_issue_w<1, 0>(c); k1_issue_w<1, 1>(c);
  SCHED_FENCE();
  c.sws = make_srd(ka[10], 0, WS_END);
  c.rs = __builtin_amdgcn_make_buffer_rsrc((void*)ka[10], 0, WS_END, 0x00020000);
  c.hb = WS_H + ((8 * mt + w) * 288 + 72 * fq) * 1024;
  {
    int gq = lane >> 4, mat = gq & 1, h = gq >> 1, q = (lane & 15) >> 2, p = lane & 3;
    int X = 2 * (q >> 1) + 4 * mat;
#pragma unroll
    for (int t = 0; t < 4; ++t) c.rd[t] = mat * 8192 + (8 * h + q) * 128 + (((2 * t) ^ X) + (p >> 1)) * 16 + (p & 1) * 8;
    int cc = lane & 31, hh = lane >> 5;
    c.xl0 = K1_XOFF + w * (K1_XD * 4096) + cc * 128 + ((hh ^ ((cc >> 1) & 7)) * 16);
  }
  {
    const u32x4 sw2 = make_srd(ka[8], 1152 * fq * D_ * 2, F_ * D_ * 2), ssk = make_srd(ka[4], 0, D_ * S_ * 2);
    c.s2 = ROLE == 0 ? sw2 : ssk;
    c.s3 = ROLE == 0 ? ssk : sw2;
    const unsigned mh = xcd & 1, i = 8 * r + w + 256 * (lane < 16 ? lane : (lane < 48 ? lane - 16 : lane - 48));
    const unsigned r1 = (i * 6554u) >> 16, c1 = i - 10 * r1;
    const int o1 = (int)((2 * r1 + mh) * (F_ * 2) + (c1 < 8 ? 2 * c1 + 1 : c1 + 8) * 128) - 256 * nl;
    const unsigned r2 = (i * 7282u) >> 16, o2 = (2 * r2 + mh) * (D_ * 2) + (i - 9 * r2) * 128;
    unsigned o = 72 * xcd * (S_ * 2) + i * 128;
    o = lane < 48 ? (i < 5184 ? o2 : OOB) : o;
    o = lane < 16 ? ((i < 2880 && o1 >= 0) ? (unsigned)o1 : OOB) : o;
    c.warm = lane < 57 ? o : OOB;
    asm volatile("" : "+v"(c.warm));
  }
  K1Prep P;
  {
    const u32x4 sk_ = make_srd(ka[1], 0, S_ * D_ * 2), sv_ = make_srd(ka[2], 0, S_ * D_ * 2);
    auto voff = [&](int blk, int halo, bool on) {
      const unsigned pc = (unsigned)(64 * blk + lane), row = (pc * 7282u) >> 16;
      const int t = 64 * r - halo + (int)row;
      const unsigned o = (unsigned)t * (D_ * 2) + (pc - 9 * row) * 16 + 144 * xcd;
      return (on && t >= 0 && (int)row < 64 + halo) ? o : OOB;
    };
    const bool isv = (w & 2) != 0;
    const u32x4 sd = {isv ? sv_[0] : sk_[0], isv ? sv_[1] : sk_[1], sk_[2], sk_[3]};
    unsigned v0 = voff(w, 4, true), v1 = voff(w, 1, true), v2 = voff(8 + (w & 1), isv ? 1 : 4, w < 4);
    const int e0 = cv_wg(8 * xcd + lane), e1 = g2_wg(mt, lane - 8, fq), e2 = g2_wg(mt, nl < 6 ? nl : 0, lane & 3);
    int ewg = b;
    ewg = (lane & 60) == 16 ? e2 : ewg;
    ewg = lane < 14 ? e1 : ewg;
    ewg = (lane < 8 ? e0 : ewg) * 4;
    asm volatile("" : "+v"(v0), "+v"(v1), "+v"(v2), "+v"(ewg), "+v"(c.rd[0]), "+v"(c.rd[1]), "+v"(c.rd[2]), "+v"(c.rd[3]), "+v"(c.xl0));
    SCHED_FENCE();
    dma16<K1_POFF, 0>(v0, sk_, 0u, w * 1024);
    dma16<K1_POFF + K1_PV, 0>(v1, sv_, 0u, w * 1024);
    dma16<K1_POFF, 0>(v2, sd, 0u, w < 4 ? (8 + (w & 1)) * 1024 + (isv ? K1_PV : 0) : 20480 + w * 1024);
    const unsigned so = WS_EPOCH;
    asm volatile("buffer_load_dword %0, %1, %2, %3 offen" : "=v"(c.ev) : "v"(ewg), "s"(c.sws), "s"(so) : "memory");
    const char* gx = ka[0] + (size_t)(256 * mt + 64 * fq + 8 * nl + w) * (D_ * 2);
    const int dp = 8 * w + (lane >> 3), dc = dp < 36 ? dp : 35;
    P.lp = K1_POFF + 8 * (7 - (lane & 7)) * 144 + dc * 4;
    asm volatile("" : "+v"(P.lp));
    GLD4(P.sk, (unsigned)(72 * xcd + 2 * dc) * 8, ka[3]);
    GLD4(P.ga0, c.lane16, gx);
    GLD4(P.ga1, 1024 + (c.lane16 & 127), gx);
    GLD4(P.gw0, c.lane16, ka[9]);
    GLD4(P.gw1, 1024 + (c.lane16 & 127), ka[9]);
  }
  bf16x8 xf[36];
  bf16x8 wf[K1_RD + 1];
  f32x16 acc[4];
  u32x4 hw[24];
  wait_vm_barrier<12>();
  STAMP(1);
  k1_issue_x<1, 0>(c); k1_issue_x<1, 1>(c); k1_issue_x<1, 2>(c); k1_issue_x<1, 3>(c);
  xf[0] = k1_read_x<0, 0>(c); wf[0] = k1_read_w<0>(c);
  xf[1] = k1_read_x<0, 1>(c); wf[1] = k1_read_w<1>(c);
  xf[2] = k1_read_x<0, 2>(c); wf[2] = k1_read_w<2>(c);
  xf[3] = k1_read_x<0, 3>(c);
  static_assert(K1_RD == 3, "prologue reads");
  SCHED_FENCE();
  STAMP(2);
  K1Epi E;
  CvRegs V;
  k1_slot<0, ROLE>(c, xf, wf, acc, E, P, hw, V);
  STAMP(23);
  if constexpr (ROLE == 1) cv_phase(c, acc, E, V);
  else {
    g2_phase(c, hw, acc, E);
    __builtin_amdgcn_s_waitcnt(0);
  }
  if constexpr (ROLE == 0) asm volatile(".Lgm_code_end0:");
  else asm volatile(".Lgm_code_end1:");
}

__global__ __launch_bounds__(512, 2) void fused_kernel(const void* x, const void* kk, const void* vv, const void* shift_k, const void* ssm_k,
                                                       const void* ssm_D, const void* w1, const void* w3, const void* w2, const void* gate_w, void* ws,
                                                       void* out) {
  const int w = __builtin_amdgcn_readfirstlane(threadIdx.x >> 6);
  unsigned long long pc;
  asm volatile("s_getpc_b64 %0\n.Lgm_code_start:" : "=s"(pc));
  if (blockIdx.x < 64) fused_body<1>(w, pc);
  else fused_body<0>(w, pc);
}

extern "C" void kernel_launch(void* const* d_in, const int* in_sizes, int n_in, void* d_out, int out_size, void* d_ws, size_t ws_size,
                              hipStream_t stream) {
  (void)hipFuncSetAttribute((const void*)fused_kernel, hipFuncAttributeMaxDynamicSharedMemorySize, LDS_BYTES);
  fused_kernel<<<256, 512, LDS_BYTES, stream>>>(d_in[0], d_in[1], d_in[2], d_in[3], d_in[4], d_in[5], d_in[7], d_in[8], d_in[9], d_in[10], d_ws, d_out);
}
```

```cpp
#include <hip/hip_runtime.h>
#include <hip/hip_bf16.h>
#include <utility>

#define LAS __attribute__((address_space(3)))
#define GAS __attribute__((address_space(1)))
typedef unsigned short u16;
typedef __bf16 bf16x2 __attribute__((ext_vector_type(2)));
typedef __bf16 bf16x4 __attribute__((ext_vector_type(4)));
typedef __bf16 bf16x8 __attribute__((ext_vector_type(8)));
typedef float f32x2 __attribute__((ext_vector_type(2)));
typedef float f32x4 __attribute__((ext_vector_type(4)));
typedef float f32x16 __attribute__((ext_vector_type(16)));
typedef unsigned u32x2 __attribute__((ext_vector_type(2)));
typedef unsigned u32x4 __attribute__((ext_vector_type(4)));

constexpr int S_ = 2048, D_ = 576, F_ = 4608;
constexpr unsigned WS_H = 0;
constexpr unsigned WS_PART = WS_H + 64 * 288 * 1024;
constexpr unsigned WS_UR = WS_PART + 256 * 49152;
constexpr unsigned WS_Z = WS_UR + D_ * 2 * S_ * 2;
constexpr unsigned WS_G = WS_Z + D_ * S_ * 2;
constexpr unsigned WS_FLAG = WS_G + S_ * 4;
constexpr unsigned WS_EPOCH = WS_FLAG + 256 * 512;
constexpr unsigned WS_END = WS_EPOCH + 1024;
constexpr unsigned FLAG_MAGIC = 0x7c3a91e5u, SENT = 0xffffffffu;
constexpr int AUX_SC1 = 16;
constexpr unsigned OOB = 0x7ff00000u;
constexpr unsigned SPIN_CAP = 1u << 16;

__device__ __forceinline__ float bflo(unsigned v) { return __builtin_bit_cast(float, v << 16); }
__device__ __forceinline__ float bfhi(unsigned v) { return __builtin_bit_cast(float, v & 0xffff0000u); }
__device__ __forceinline__ float rbf(float x) { return (float)(__bf16)x; }
__device__ __forceinline__ unsigned pk2(float lo, float hi) {
  f32x2 v = {lo, hi};
  return __builtin_bit_cast(unsigned, __builtin_convertvector(v, bf16x2));
}
__device__ __forceinline__ f32x2 unpk2(unsigned v) { return (f32x2){bflo(v), bfhi(v)}; }
__device__ __forceinline__ float radd(float a, float b) { const unsigned t = pk2(a, b); return bflo(t) + bfhi(t); }
#define SCHED_FENCE() __builtin_amdgcn_sched_barrier(0)
__device__ __forceinline__ void st128(u32x4 v, __amdgpu_buffer_rsrc_t rs, unsigned voff, unsigned soff, int) {
  __builtin_amdgcn_raw_buffer_store_b128(v, rs, voff, soff, AUX_SC1);
  asm volatile("s_nop 1" ::"v"(v));
}

template <int LC, int SC> __device__ __forceinline__ void dma16(unsigned voff, const u32x4& srd, unsigned so, unsigned lb) {
  unsigned t;
  asm volatile("s_add_u32 m0, %4, %5\n\ts_add_u32 %0, %3, %6\n\tbuffer_load_dwordx4 %1, %2, %0 offen lds"
               : "=&s"(t) : "v"(voff), "s"(srd), "s"(so), "s"(lb), "n"(LC), "n"(SC) : "memory", "scc");
}
template <int N> __device__ __forceinline__ void wait_vm_barrier() {
  asm volatile("s_waitcnt vmcnt(%0)\n\ts_barrier" ::"n"(N) : "memory");
}
template <int N> __device__ __forceinline__ void wait_vm() { asm volatile("s_waitcnt vmcnt(%0)" ::"n"(N) : "memory"); }
__device__ __forceinline__ void lds_barrier() { asm volatile("s_waitcnt lgkmcnt(0)\n\ts_barrier" ::: "memory"); }
__device__ __forceinline__ LAS char* ldsp(unsigned a) { return (LAS char*)(size_t)a; }
__device__ __forceinline__ bf16x8 tr_frag(LAS char* p0, LAS char* p1) {
  bf16x4 a = __builtin_amdgcn_ds_read_tr16_b64_v4bf16((LAS bf16x4*)p0);
  bf16x4 b = __builtin_amdgcn_ds_read_tr16_b64_v4bf16((LAS bf16x4*)p1);
  return __builtin_shufflevector(a, b, 0, 1, 2, 3, 4, 5, 6, 7);
}
constexpr int cmin(int a, int b) { return a < b ? a : b; }
__device__ __forceinline__ const char* karg(int i) {
  typedef const char* cptr;
  const __attribute__((address_space(4))) cptr* p = (const __attribute__((address_space(4))) cptr*)__builtin_amdgcn_kernarg_segment_ptr();
  asm volatile("" : "+s"(p));
  return p[i];
}
__device__ __forceinline__ u32x4 make_srd(const char* base, unsigned off, unsigned total) {
  const unsigned long long a = (unsigned long long)base + off;
  return (u32x4){(unsigned)a, (unsigned)(a >> 32) & 0xffffu, total - off, 0x00020000u};
}

constexpr int K1_SLOT = 16384, K1_XOFF = 65536, K1_XD = 2, K1_POFF = 131072, K1_PV = 10240;
constexpr int LDS_BYTES = 163840;
constexpr int K1_PF = 4, K1_RD = 3, K1_NS = 324, K1_NSEG = 21;
constexpr int K1_PREP0 = 28, K1_PREP_UNITS = 8 + 2 + 63 + 1 + 4;
constexpr int G2_SLOT = 24576, G2_NSLOT = 4, G2_WIN = 16, G2_PF = 4, G2_NS = 216;
constexpr int CV_K = 0, CV_U0 = 4160, CV_U1 = CV_U0 + 4640, CV_CH = 13440;
constexpr int k1_wbase(int s) { return s == 11 ? K1_XOFF : (s == 12 ? K1_XOFF + K1_SLOT : ((s < 11 ? s : s - 2) % 4) * K1_SLOT); }
constexpr int k1_wprev(int s) { return s < 4 || s == 11 || s == 12 ? -1 : (s < 11 ? s - 4 : (s < 17 ? s - 6 : s - 4)); }
constexpr int EV_PUB_U = 140;
constexpr int EV_PUB_A = 204;
constexpr int EV_PRE0 = 221, EV_PRE_STEP = 4;
constexpr int EV_CV_POLL = 176, EV_CV_CHECK = 190, EV_CV_INVW = 219;
constexpr int EV_CV_PRE7 = 300;
constexpr int EV_CV_PRE8 = 316;
constexpr int EV_SENT0 = 177, EV_SENT_STEP = 7, EV_PUB_S = 236;
constexpr int EV_G2_POLL = 264, EV_G2_CHECK = 280, EV_G2_H0 = 289;
constexpr int WARM_N = 10;
constexpr int WARM_SLOT[WARM_N] = {8, 24, 40, 56, 72, 88, 119, 151, 175, 191};
constexpr int WARM_DONE = 235;
constexpr int EV_PUB_B1 = 316;
constexpr int K1_EPI0[2] = {125, 269};
constexpr int k1_epi_slot(int ch, int e) { return K1_EPI0[ch] + (ch == 0 ? e - e / 12 : e); }

struct K1Sched {
  int seg[K1_NS], tile[K1_NS], j[K1_NS];
  int first_use[K1_NSEG], last_use[K1_NSEG];
  int wissue[K1_NSEG];
  int xissue[9];
  bool bar[K1_NS];
  int ops[K1_NS];
  int wait[K1_NS];
  int done[K1_NS];
  constexpr int ops_after(int a, int b) const { int k = 0; for (int q = a + 1; q <= b; ++q) k += ops[q]; return k; }
};
constexpr K1Sched k1_make(int role) {
  K1Sched S{};
  int n = 0;
  for (int s = 0; s < 18;) {
    if (s == 7 || s == 9 || s == 16) {
      const int tt[8] = {0, 1, 0, 1, 2, 2, 3, 3}, hh[8] = {0, 0, 1, 1, 0, 1, 0, 1};
      for (int q = 0; q < 8; ++q) for (int j = 0; j < 4; ++j) { S.seg[n] = s + hh[q]; S.tile[n] = tt[q]; S.j[n] = j; ++n; }
      s += 2;
    } else {
      for (int t = 0; t < 4; ++t) for (int j = 0; j < 4; ++j) { S.seg[n] = s; S.tile[n] = t; S.j[n] = j; ++n; }
      s += 1;
    }
  }
  for (int i = 0; i < 36; ++i) { S.seg[n] = 18 + i / 16; S.tile[n] = 0; S.j[n] = i % 16; ++n; }
  for (int s = 0; s < K1_NSEG; ++s) { S.first_use[s] = 100000; S.last_use[s] = -1; }
  for (int i = 0; i < K1_NS; ++i) { int s = S.seg[i]; if (i < S.first_use[s]) S.first_use[s] = i; if (i > S.last_use[s]) S.last_use[s] = i; }
  for (int s = 1; s < K1_NSEG; ++s) S.bar[S.first_use[s] - K1_PF - 1] = true;
  S.wissue[0] = S.wissue[1] = -1;
  S.wissue[2] = 0; S.wissue[3] = 12;
  int busy = 0;
  for (int s = 4; s < K1_NSEG; ++s) {
    int q = k1_wprev(s) >= 0 ? S.last_use[k1_wprev(s)] : S.first_use[8] + 4;
    while (!S.bar[q]) ++q;
    int is = q + 1;
    if (is < busy) is = busy;
    S.wissue[s] = is;
    busy = is + (s >= 11 ? 8 : 4);
  }
  S.xissue[0] = S.xissue[1] = -1;
  for (int p = 2; p < 9; ++p) S.xissue[p] = S.first_use[p - 2] + 4;
  for (int s = 0; s < K1_NSEG; ++s) if (S.wissue[s] >= 0) { S.ops[S.wissue[s]]++; S.ops[S.wissue[s] + 2]++; }
  for (int p = 0; p < 9; ++p) if (S.xissue[p] >= 0) for (int i = 0; i < 4; ++i) S.ops[S.xissue[p] + i]++;
  S.ops[K1_PREP0 + 7]++;
  for (int i = 0; i < WARM_N; ++i) S.ops[WARM_SLOT[i]]++;
  for (int i = 0; i < 4; ++i) S.ops[K1_PREP0 + K1_PREP_UNITS - 4 + i]++;
  for (int c = 0; c < 2; ++c) for (int t = 0; t < 4; ++t) if (k1_epi_slot(c, 12 * t + 13) < K1_NS) S.ops[k1_epi_slot(c, 12 * t + 13)]++;
  if (role == 0) {
    for (int i = 0; i < G2_WIN; ++i) S.ops[EV_G2_H0 + 2 * i]++;
    for (int i = 0; i < 6; ++i) S.ops[EV_SENT0 + EV_SENT_STEP * i]++;
  } else {
    S.ops[EV_CV_PRE8]++; S.ops[EV_CV_PRE8 + 1]++;
    S.ops[EV_CV_PRE7]++; S.ops[EV_CV_PRE7 + 1]++;
  }
  for (int i = 0; i < 12; ++i) S.ops[EV_PRE0 + EV_PRE_STEP * i]++;
  for (int i = 0; i < K1_NS; ++i) {
    S.wait[i] = 63;
    S.done[i] = -2;
    if (!S.bar[i]) continue;
    int youngest = -2;
    for (int s = 1; s < K1_NSEG; ++s)
      if (S.first_use[s] - K1_PF - 1 == i) {
        int y = S.wissue[s] < 0 ? -1 : S.wissue[s] + 2;
        if (s < 9) { int yx = S.xissue[s] < 0 ? -1 : S.xissue[s] + 3; if (yx > y) y = yx; }
        if (y > youngest) youngest = y;
      }
    if (i == EV_PUB_A - 1 && youngest < k1_epi_slot(0, 49)) youngest = k1_epi_slot(0, 49);
    if (i == EV_PUB_S - 1 && youngest < EV_SENT0 + 5 * EV_SENT_STEP) youngest = EV_SENT0 + 5 * EV_SENT_STEP;
    if (i == WARM_DONE && youngest < WARM_SLOT[WARM_N - 1]) youngest = WARM_SLOT[WARM_N - 1];
    if (i == EV_PUB_B1 - 1 && youngest < k1_epi_slot(1, 25)) youngest = k1_epi_slot(1, 25);
    if (role == 1 && i == 315 && youngest < EV_CV_PRE7 + 1) youngest = EV_CV_PRE7 + 1;
    if (i == 299 && youngest < EV_PRE0 + 11 * EV_PRE_STEP) youngest = EV_PRE0 + 11 * EV_PRE_STEP;
    S.wait[i] = cmin(63, S.ops_after(youngest, i));
    S.done[i] = youngest;
  }
  return S;
}
constexpr K1Sched K1S[2] = {k1_make(0), k1_make(1)};
static_assert(K1_PREP0 + K1_PREP_UNITS - 1 <= K1S[0].done[EV_PUB_U - 1] && K1S[0].bar[EV_PUB_U - 1], "u stores complete before publish");
static_assert(K1S[0].bar[EV_PUB_A - 1] && K1S[0].bar[EV_PUB_S - 1] && K1S[0].bar[299] && K1S[0].bar[315], "events");
static_assert(K1S[0].bar[EV_G2_CHECK + 3] && K1S[0].bar[EV_CV_INVW] && K1S[0].bar[EV_CV_PRE7 - 1] && K1S[0].last_use[17] < EV_CV_PRE7, "events");
static_assert(K1S[0].bar[EV_CV_PRE8 - 1] && K1S[0].last_use[18] < EV_CV_PRE8 && EV_G2_H0 + 2 * G2_WIN <= K1_NS + 1, "events");
static_assert(EV_PRE0 > EV_CV_INVW && K1S[0].last_use[12] <= EV_CV_INVW, "events");
static_assert([] {
  for (int t = 0; t < 4; ++t) {
    int last = 0, next = 1000;
    for (int i = 0; i < 144; ++i) if (K1S[0].tile[i] == t) last = i;
    for (int i = 287; i >= 144; --i) if (K1S[0].tile[i] == t) next = i;
    if (k1_epi_slot(0, 12 * t) < last + 2 || k1_epi_slot(0, 12 * t + 9) >= next) return false;
  }
  return true;
}(), "epilogue windows");

struct Ctx {
  u32x4 sw1, sw3, sx, s2, s3, sws;
  __amdgpu_buffer_rsrc_t rs;
  unsigned wo[3], xo[3];
  unsigned hb;
  unsigned voff_w, voff_x;
  unsigned rd[4];
  unsigned xl0, lane16;
  unsigned ev;
  u32x2 poll;
  unsigned touch, warm;
  const char* dptr;
  unsigned rc0, rc1;
  int w, rot, nl, ml, b, mt, fq;
};
struct K1Prep {
  u32x4 sk, ga0, ga1, gw0, gw1;
  unsigned lp, kn, vn, o0;
  f32x2 s[4], k2[4], a, p, q; unsigned u[9]; float gs;
};
struct K1Epi { float m[2], p[2], pq[2], r[2], h[2]; u32x4 ho; };
struct CvRegs { unsigned ap[4], bp; bf16x8 a0[8], b0[8]; };

__device__ __forceinline__ int lane_of(const Ctx& c) {
  unsigned l = c.lane16;
  asm volatile("" : "+v"(l));
  return (int)(l >> 4);
}
__device__ __forceinline__ u32x4 sent4() {
  u32x4 v = {SENT, SENT, SENT, SENT};
  asm volatile("" : "+v"(v));
  return v;
}
__device__ __forceinline__ u32x2 sent2() {
  u32x2 v = {SENT, SENT};
  asm volatile("" : "+v"(v));
  return v;
}
__device__ __forceinline__ u32x4 zero4() {
  u32x4 v = {0u, 0u, 0u, 0u};
  asm volatile("" : "+v"(v));
  return v;
}
__device__ __forceinline__ int g2_wg(int mt, int nt, int ks) { return ((nt * 4 + (mt & 3)) << 3) | (ks * 2 + (mt >> 2)); }
__device__ __forceinline__ int cv_wg(int cw) { return ((24 + (cw & 7)) << 3) | (cw >> 3); }
__device__ __forceinline__ int cv_id(const Ctx& c) { return (c.b & 7) * 8 + (c.b >> 3) - 24; }
__device__ __forceinline__ void flag_store(const Ctx& c, bool act, int wg, int slot, unsigned epoch) {
  asm volatile("" : "+v"(epoch));
  __builtin_amdgcn_raw_buffer_store_b64((u32x2){epoch + 1u, FLAG_MAGIC}, c.rs, act ? wg * 512 + slot * 8 : OOB, WS_FLAG, AUX_SC1);
}
__device__ __forceinline__ void publish_h(const Ctx& c, int slot0) {
  const int l = lane_of(c);
  flag_store(c, l >= 8 && l < 14, g2_wg(c.mt, l - 8, c.fq), slot0 + c.nl, c.ev);
}
__device__ __forceinline__ void poll_issue(Ctx& c) {
  const unsigned vo = lane_of(c) * 8;
  const unsigned so = WS_FLAG + c.b * 512;
  asm volatile("s_cmp_lg_u32 %4, 0\n\ts_cbranch_scc1 1f\n\tbuffer_load_dwordx2 %0, %1, %2, %3 offen sc1\n1:"
               : "=v"(c.poll) : "v"(vo), "s"(c.sws), "s"(so), "s"(c.w) : "memory", "scc");
}
__device__ __forceinline__ void poll_wait(Ctx& c, bool need) {
  const unsigned want = (unsigned)__builtin_amdgcn_readlane((int)c.ev, 14) + 1u;
  u32x2 v = c.poll;
  unsigned spin = 0;
#pragma unroll 1
  for (; spin < SPIN_CAP; ++spin) {
    if (__all(!need || (v[0] == want && v[1] == FLAG_MAGIC))) break;
    const unsigned vo = lane_of(c) * 8, so = WS_FLAG + c.b * 512;
    asm volatile("s_sleep 1\n\tbuffer_load_dwordx2 %0, %1, %2, %3 offen sc1\n\ts_waitcnt vmcnt(0)" : "=&v"(v) : "v"(vo), "s"(c.sws), "s"(so) : "memory");
  }
  asm volatile("" ::: "memory");
}
#define POLL_LANDED(N) asm volatile("s_cmp_lg_u32 %2, 0\n\ts_cbranch_scc1 1f\n\ts_waitcnt vmcnt(%1)\n1:" : "+v"(c.poll) : "n"(N), "s"(c.w) : "memory", "scc")

__device__ __forceinline__ int kslice(int p, int r) {
  const int g = (p >= 6) ? 6 : (p >= 3 ? 3 : 0);
  const int v = p - g + r;
  return g + (v >= 3 ? v - 3 : v);
}
template <int sg, int half> __device__ __forceinline__ void k1_issue_w(const Ctx& c) {
  constexpr int dst = k1_wbase(sg) + half * 8192;
  if constexpr (sg < 18) {
    constexpr int p = sg % 9;
    dma16<dst, (p / 3 * 3) * 64 * F_ * 2 + 128 * (sg / 9)>(half ? (c.voff_w ^ 64u) : c.voff_w, half ? c.sw3 : c.sw1, c.wo[p % 3], c.w * 1024);
  } else {
    const unsigned l = lane_of(c);
    const unsigned rp = l >> 1;
    unsigned vo = (half ? (rp ^ 4u) : rp) * (F_ * 2) + (l & 1u) * 16;
    const int col = (1024 + 16 * c.nl - 128 * c.nl) * 2;
    if constexpr (sg < 20) {
      const int ksl = kslice(4 * (sg - 18) + (c.w >> 1), c.rot);
      dma16<dst, 0>(vo, half ? c.sw3 : c.sw1, (64 * ksl + 32 * (c.w & 1)) * (F_ * 2) + col, c.w * 1024);
    } else {
      if (l >= 16) vo = OOB;
      dma16<dst, 6 * 64 * F_ * 2>(vo, half ? c.sw3 : c.sw1, c.wo[2] + col + 8 * c.w * (F_ * 2), c.w * 1024);
    }
  }
}
template <int p, int i> __device__ __forceinline__ void k1_issue_x(const Ctx& c) {
  dma16<K1_XOFF + (p % K1_XD) * 4096 + i * 1024, 8 * i * D_ * 2 + (p / 3 * 3) * 128>((i & 1) ? (c.voff_x ^ 64u) : c.voff_x, c.sx, c.xo[p % 3],
                                                                                     c.w * (K1_XD * 4096));
}
template <int p, int j> __device__ __forceinline__ bf16x8 k1_read_x(const Ctx& c) {
  return *(LAS bf16x8*)(ldsp(c.xl0 ^ (32 * j)) + (p % K1_XD) * 4096);
}
template <int n> __device__ __forceinline__ bf16x8 k1_read_w(Ctx& c) {
  constexpr int sg = K1S[0].seg[n], t = K1S[0].tile[n], j = K1S[0].j[n];
  if constexpr (sg < 18) {
    if constexpr ((sg == 11 || sg == 13) && j == 0) {
      c.rd[t] += sg == 11 ? K1_XOFF : -K1_XOFF;
      asm volatile("" : "+v"(c.rd[t]));
    }
    LAS char* p = ldsp(c.rd[t]) + k1_wbase(sg) % K1_XOFF + (16 * j) * 128;
    return tr_frag(p, p + 4 * 128);
  } else if constexpr (sg < 20) {
    const int off = k1_wbase(sg) + (16 * j) * 32;
    return tr_frag(ldsp(c.rc0) + off, ldsp(c.rc1) + off);
  } else {
    const unsigned ex = (lane_of(c) >> 5) * 768u;
    const int off = k1_wbase(sg) + j * 2048;
    return tr_frag(ldsp(c.rc0 + ex) + off, ldsp(c.rc1 + ex) + off);
  }
}
#define GLD1(dst, voff, sb) asm volatile("global_load_dword %0, %1, %2" : "=v"(dst) : "v"(voff), "s"(sb) : "memory")
#define GLD4(dst, voff, sb) asm volatile("global_load_dwordx4 %0, %1, %2" : "=v"(dst) : "v"(voff), "s"(sb) : "memory")
template <int ctrl> __device__ __forceinline__ float dpp_add(float v) {
  return v + __builtin_bit_cast(float, __builtin_amdgcn_update_dpp(0, __builtin_bit_cast(int, v), ctrl, 0xf, 0xf, true));
}
__device__ __forceinline__ float rb1(float x) { return __builtin_bit_cast(float, pk2(0.f, x)); }
__device__ __forceinline__ f32x2 rb2(f32x2 v) { return (f32x2){rb1(v[0]), rb1(v[1])}; }
template <int k> __device__ __forceinline__ void k1_prep_unit(Ctx& c, K1Prep& P) {
  if constexpr (k == 0) {
    asm volatile("" : "+v"(P.sk), "+v"(P.ga0), "+v"(P.ga1), "+v"(P.gw0), "+v"(P.gw1), "+v"(c.ev)::"memory");
    P.gs = 0.f;
  }
  if constexpr (k < 4) {
    const u32x4 &a = k < 2 ? P.ga1 : P.ga0, &g = k < 2 ? P.gw1 : P.gw0;
#pragma unroll
    for (int i = 2 * (k & 1); i < 2 * (k & 1) + 2; ++i) P.gs = __builtin_fmaf(bfhi(a[i]), bfhi(g[i]), __builtin_fmaf(bflo(a[i]), bflo(g[i]), P.gs));
    if constexpr (k == 1) P.gs = lane_of(c) < 8 ? P.gs : 0.f;
    asm volatile("" : "+v"(P.gs));
  } else if constexpr (k == 4) {
    P.gs = dpp_add<0x111>(P.gs); P.gs = dpp_add<0x112>(P.gs);
    P.u[0] = *(LAS unsigned*)(ldsp(P.lp)); P.u[1] = *(LAS unsigned*)(ldsp(P.lp) + 144);
  } else if constexpr (k == 5) {
    P.gs = dpp_add<0x114>(P.gs); P.gs = dpp_add<0x118>(P.gs);
    P.u[2] = *(LAS unsigned*)(ldsp(P.lp) + 288); P.kn = *(LAS unsigned*)(ldsp(P.lp) + 432);
  } else if constexpr (k == 6) {
    int v = __builtin_bit_cast(int, P.gs);
    float a = __builtin_bit_cast(float, __builtin_amdgcn_readlane(v, 15)), b = __builtin_bit_cast(float, __builtin_amdgcn_readlane(v, 31));
    float d = __builtin_bit_cast(float, __builtin_amdgcn_readlane(v, 47)), e = __builtin_bit_cast(float, __builtin_amdgcn_readlane(v, 63));
    P.gs = __builtin_amdgcn_exp2f(rb1((a + b) + (d + e)) * -1.44269504088896f);
  } else if constexpr (k == 7) {
    float g = rb1(__builtin_amdgcn_rcpf(1.0f + P.gs));
    __builtin_amdgcn_raw_buffer_store_b32(__builtin_bit_cast(unsigned, g), c.rs, lane_of(c) == 0 ? 0u : OOB,
                                          WS_G + (256 * c.mt + 64 * c.fq + 8 * c.nl + c.w) * 4, AUX_SC1);
    P.s[0] = (f32x2){bflo(P.sk[0]), bflo(P.sk[2])};
  } else if constexpr (k == 8) {
    P.s[1] = (f32x2){bfhi(P.sk[0]), bfhi(P.sk[2])};
    P.s[2] = (f32x2){bflo(P.sk[1]), bflo(P.sk[3])}; P.s[3] = (f32x2){bfhi(P.sk[1]), bfhi(P.sk[3])};
    asm volatile("" : "+v"(P.s[0]), "+v"(P.s[1]), "+v"(P.s[2]), "+v"(P.s[3]));
  } else if constexpr (k == 9) {
    P.k2[0] = unpk2(P.u[0]); P.k2[1] = unpk2(P.u[1]); P.k2[2] = unpk2(P.u[2]);
    asm volatile("" : "+v"(P.k2[0]), "+v"(P.k2[1]), "+v"(P.k2[2]));
  } else if constexpr (k < 73) {
    constexpr int i = (k - 10) / 7, st = (k - 10) % 7;
    auto mul = [](f32x2 x, f32x2 y) { f32x2 r = {x[0] * y[0], x[1] * y[1]}; return r; };
    if constexpr (st == 0) { P.a = mul(P.k2[i & 3], P.s[3]); P.p = mul(P.k2[(i + 1) & 3], P.s[2]); P.vn = *(LAS unsigned*)(ldsp(P.lp) + K1_PV + 144 * i); }
    else if constexpr (st == 1) { P.a = rb2(P.a); P.p = rb2(P.p); }
    else if constexpr (st == 2) { P.a += P.p; P.p = mul(P.k2[(i + 2) & 3], P.s[1]); P.k2[(i + 3) & 3] = unpk2(P.kn); }
    else if constexpr (st == 3) {
      P.a = rb2(P.a); P.p = rb2(P.p);
      if constexpr (i < 8) P.kn = *(LAS unsigned*)(ldsp(P.lp) + 144 * (i + 4));
    }
    else if constexpr (st == 4) { P.a += P.p; P.p = mul(P.k2[(i + 3) & 3], P.s[0]); P.q = unpk2(P.vn); }
    else if constexpr (st == 5) { P.a = rb2(P.a); P.p = rb2(P.p); }
    else { P.a = mul(rb2(P.a + P.p), P.q); P.u[i] = pk2(P.a[0], P.a[1]); }
    if constexpr (st == 6) asm volatile("" : "+v"(P.u[i]));
    else if constexpr (st == 2) asm volatile("" : "+v"(P.a), "+v"(P.p), "+v"(P.k2[(i + 3) & 3]));
    else if constexpr (st == 4) asm volatile("" : "+v"(P.a), "+v"(P.p), "+v"(P.q));
    else asm volatile("" : "+v"(P.a), "+v"(P.p));
  } else if constexpr (k == 73) {
    const int l = lane_of(c);
    const int dp = 8 * c.w + (l >> 3);
    P.o0 = dp < 36 ? (unsigned)((72 * (c.b & 7) + 2 * dp) * 2 * S_ + (1984 - 64 * (c.b >> 3)) + 8 * (l & 7)) * 2 : OOB;
  } else {
    constexpr int ch = (k - 74) >> 1, cp = (k - 74) & 1;
    constexpr unsigned sel = ch ? 0x07060302u : 0x05040100u;
    u32x4 o;
#pragma unroll
    for (int e = 0; e < 4; ++e) o[e] = __builtin_amdgcn_perm(P.u[7 + cp - 2 * e - 1], P.u[7 + cp - 2 * e], sel);
    st128(o, c.rs, P.o0, WS_UR + (ch * 2 + cp) * S_ * 2, AUX_SC1);
  }
}
template <int NT, int e> __device__ __forceinline__ void k1_epi_step(const Ctx& c, const f32x16* acc, K1Epi& E, int ft0) {
  constexpr int pr = e / 3, st = e % 3, t = pr / 4, i = 2 * (pr % 4);
  if constexpr (st == 0) {
    if constexpr (pr > 0) { E.h[0] = E.pq[0] * E.r[0]; E.h[1] = E.pq[1] * E.r[1]; }
    if constexpr (pr < 4 * NT) {
      E.m[0] = acc[t][i] * -1.44269504088896f; E.m[1] = acc[t][i + 1] * -1.44269504088896f;
      E.p[0] = acc[t][i] * acc[t][i + 8]; E.p[1] = acc[t][i + 1] * acc[t][i + 9];
      asm volatile("" : "+v"(E.p[0]), "+v"(E.p[1]));
    }
  } else if constexpr (st == 1) {
    if constexpr (pr < 4 * NT) { E.m[0] = __builtin_amdgcn_exp2f(E.m[0]); E.m[1] = __builtin_amdgcn_exp2f(E.m[1]); }
    if constexpr (pr > 0) {
      E.ho[(pr - 1) % 4] = pk2(E.h[0], E.h[1]);
      if constexpr (pr % 4 == 0) st128(E.ho, c.rs, c.lane16, c.hb + (ft0 + (pr - 1) / 4) * 1024, AUX_SC1);
    }
  } else {
    if constexpr (pr < 4 * NT) {
      float d0 = 1.0f + E.m[0], d1 = 1.0f + E.m[1];
      E.r[0] = __builtin_amdgcn_rcpf(d0); E.r[1] = __builtin_amdgcn_rcpf(d1);
      E.pq[0] = E.p[0]; E.pq[1] = E.p[1];
    }
  }
}

template <int ch, int n, int e = 0> __device__ __forceinline__ void k1_epi_at(const Ctx& c, const f32x16* acc, K1Epi& E) {
  if constexpr (n >= K1_EPI0[ch] && n <= k1_epi_slot(ch, 49)) {
    if constexpr (k1_epi_slot(ch, e) == n) k1_epi_step<4, e>(c, acc, E, 8 * c.nl + 4 * ch);
    if constexpr (e < 49 && k1_epi_slot(ch, e < 49 ? e + 1 : e) <= n) k1_epi_at<ch, n, e + 1>(c, acc, E);
  }
}
template <int i> __device__ __forceinline__ void warm_touch(Ctx& c, const u32x4& sw2, const u32x4& ssk) {
  constexpr unsigned long long m = i < 4 ? (0x3full << (6 * (i >> 1))) : (i < 8 ? (0x3full << (16 + 6 * (i - 4))) : (0x1full << (48 + 5 * (i - 8))));
  const u32x4& srd = i < 4 ? ((i & 1) ? c.sw3 : c.sw1) : (i < 8 ? sw2 : ssk);
  asm volatile("s_mov_b32 exec_lo, %3\n\ts_mov_b32 exec_hi, %4\n\tbuffer_load_dword %0, %1, %2, 0 offen\n\ts_mov_b64 exec, -1"
               : "+v"(c.touch) : "v"(c.warm), "s"(srd), "n"((int)(unsigned)m), "n"((int)(unsigned)(m >> 32)) : "memory");
}
__device__ __forceinline__ int g2_ft(int s, int i8) {
  return s >= 8 ? 64 + i8 : (s < 4 ? 16 * s + 8 * (i8 >> 2) + (i8 & 3) : 32 * (s & 1) + 8 * (i8 >> 1) + (i8 & 1) + 4 + (s & 2));
}
template <int s, int i> __device__ __forceinline__ void g2_issue_w(const Ctx& c) {
  const unsigned n16 = 64u * i + (s < 4 ? (unsigned)lane_of(c) : c.xl0);
  const unsigned vo = ((n16 * 171u) >> 11) * (D_ * 2 - 192) + n16 * 16u;
  dma16<K1_XOFF + (s % G2_NSLOT) * G2_SLOT + i * 1024, 0>(vo, c.s2, 16 * g2_ft(s, c.w) * (D_ * 2) + 192 * c.nl, c.w * 3072);
}
template <int i> __device__ __forceinline__ u32x4 g2_load_h(const Ctx& c) {
  return __builtin_amdgcn_raw_buffer_load_b128(c.rs, c.lane16, c.hb + g2_ft(i / 8, i % 8) * 1024, AUX_SC1);
}
__device__ __forceinline__ unsigned g2_out_base(const Ctx& c) {
  return WS_PART + g2_wg(c.mt, c.nl, c.w >> 1) * 49152 + (c.fq * 2 + (c.w & 1)) * 6144;
}
__device__ __forceinline__ unsigned cv_base(int ch) { return ch < 7 ? K1_XOFF + ch * CV_CH : (ch == 7 ? k1_wbase(17) : k1_wbase(18)); }
template <int i> __device__ __forceinline__ void cv_issue(const Ctx& c) {
  const int d = 9 * cv_id(c) + c.w;
  constexpr int lo = i < 4 ? CV_K + 64 + i * 1024 : (i < 8 ? CV_U0 + (i - 4) * 1024 : CV_U1 + (i - 8) * 1024);
  const unsigned lb = c.w < 7 ? K1_XOFF + c.w * CV_CH : LDS_BYTES - 1024 - lo;
  if constexpr (i < 4) dma16<lo, i * 1024>(c.lane16, c.s2, c.w < 7 ? d * (S_ * 2) : OOB, lb);
  else dma16<lo, WS_UR + (i - 4) * 1024>(c.lane16, c.sws, c.w < 7 ? d * (4 * S_) : OOB, lb);
}
template <int ch, int part> __device__ __forceinline__ void cv_issue_shared(const Ctx& c) {
  const int d = 9 * cv_id(c) + ch;
  if constexpr (part == 0) dma16<k1_wbase(10 + ch), WS_UR>(c.lane16, c.sws, d * (4 * S_) + c.w * 1024, c.w < 4 ? CV_U0 + c.w * 1024 : CV_U1 + (c.w - 4) * 1024);
  else dma16<k1_wbase(10 + ch) + CV_K + 64, 0>(c.lane16, c.s2, d * (S_ * 2) + (c.w >> 1) * 1024, (c.w >> 1) * 1024);
}
static_assert(K1_XOFF + 7 * CV_CH <= LDS_BYTES - 1024, "dummy target");
typedef unsigned u32x2a4 __attribute__((ext_vector_type(2), aligned(4)));
__device__ __forceinline__ bf16x8 cv_read_a(LAS char* p) {
  u32x2 lo = *(LAS u32x2a4*)p;
  u32x2 hi = *(LAS u32x2a4*)(p + 8);
  u32x4 v = {lo[0], lo[1], hi[0], hi[1]};
  return __builtin_bit_cast(bf16x8, v);
}
__device__ __forceinline__ int cv_zwg(const Ctx& c, int lane, int& slot) {
  const int cw = cv_id(c);
  const int n0 = (9 * cw) / 96, n1 = (9 * cw + 8) / 96;
  const int s0 = 16 + cw - (96 * n0) / 9, s1 = n1 != n0 ? 16 + cw - (96 * n1) / 9 : -1;
  slot = lane < 32 ? s0 : s1;
  return g2_wg((lane >> 2) & 7, lane < 32 ? n0 : n1, lane & 3);
}

template <int n, int ROLE>
__device__ __forceinline__ void k1_slot(Ctx& c, bf16x8 (&xf)[36], bf16x8 (&wf)[K1_RD + 1], f32x16 (&acc)[4], K1Epi& E, K1Prep& P, u32x4 (&hw)[24],
                                        CvRegs& V) {
  constexpr const K1Sched& S = K1S[ROLE];
  constexpr int sg = S.seg[n];
  if constexpr (n + K1_RD == 288) {
    const unsigned l = lane_of(c);
    unsigned mat = (l >> 4) & 1, h = l >> 5, q = (l & 15) >> 2, p = l & 3;
    unsigned base = mat * 8192 + (8 * h + q) * 32 + p * 8;
    c.rc0 = base + (mat ? 128 : 0);
    c.rc1 = base + (mat ? 0 : 128);
  }
  if constexpr (n + K1_RD < K1_NS) wf[(n + K1_RD) % (K1_RD + 1)] = k1_read_w<n + K1_RD>(c);
  SCHED_FENCE();
  if constexpr (n + 4 < 144) {
    constexpr int sn = S.seg[n + 4];
    if constexpr (S.tile[n + 4] == 0 && sn >= 1) xf[4 * sn + S.j[n + 4]] = k1_read_x<sn, S.j[n + 4]>(c);
  }
  {
    constexpr int t = S.tile[n];
    constexpr int xi = sg < 18 ? 4 * (sg % 9) + S.j[n] : n - 288;
    constexpr bool first = (sg < 18) ? (sg % 9 == 0 && S.j[n] == 0) : (n == 288);
    if constexpr (first) acc[t] = __builtin_amdgcn_mfma_f32_32x32x16_bf16(wf[n % (K1_RD + 1)], xf[xi], (f32x16){}, 0, 0, 0);
    else acc[t] = __builtin_amdgcn_mfma_f32_32x32x16_bf16(wf[n % (K1_RD + 1)], xf[xi], acc[t], 0, 0, 0);
  }
  SCHED_FENCE();
  {
    constexpr int ws0 = [] { for (int s = 0; s < K1_NSEG; ++s) if (K1S[0].wissue[s] == n) return s; return -1; }();
    constexpr int ws1 = [] { for (int s = 0; s < K1_NSEG; ++s) if (K1S[0].wissue[s] >= 0 && K1S[0].wissue[s] + 2 == n) return s; return -1; }();
    constexpr int xs = [] { for (int p = 0; p < 9; ++p) if (K1S[0].xissue[p] >= 0 && n >= K1S[0].xissue[p] && n < K1S[0].xissue[p] + 4) return p; return -1; }();
    if constexpr (ws0 >= 0) k1_issue_w<ws0, 0>(c);
    if constexpr (ws1 >= 0) k1_issue_w<ws1, 1>(c);
    if constexpr (xs >= 0) k1_issue_x<xs, n - S.xissue[xs]>(c);
  }
  if constexpr (n >= K1_PREP0 && n < K1_PREP0 + K1_PREP_UNITS) k1_prep_unit<n - K1_PREP0>(c, P);
  k1_epi_at<0, n>(c, acc, E);
  k1_epi_at<1, n>(c, acc, E);
  {
    constexpr int wi = [] { for (int i = 0; i < WARM_N; ++i) if (WARM_SLOT[i] == n) return i; return -1; }();
    if constexpr (wi >= 0) warm_touch<wi>(c, ROLE == 0 ? c.s2 : c.s3, ROLE == 0 ? c.s3 : c.s2);
    if constexpr (n == WARM_DONE + 1) asm volatile("" : "+v"(c.touch)::"memory");
  }
  if constexpr (n == EV_PUB_U) {
    if (c.w == 1) { const int l = lane_of(c); flag_store(c, l < 8, cv_wg(8 * (c.b & 7) + l), c.b >> 3, c.ev); }
  }
  if constexpr (n == EV_PUB_A) {
    if (c.w == 1) publish_h(c, 0);
  }
  if constexpr (n == EV_PUB_B1) {
    if (c.w == 1) publish_h(c, 8);
  }
  if constexpr (ROLE == 0) {
    if constexpr (n >= EV_SENT0 && n < EV_SENT0 + 6 * EV_SENT_STEP && (n - EV_SENT0) % EV_SENT_STEP == 0)
      st128(sent4(), c.rs, c.lane16, (c.w >> 1) == c.fq ? OOB : g2_out_base(c) + (n - EV_SENT0) / EV_SENT_STEP * 1024, AUX_SC1);
    if constexpr (n == EV_PUB_S) {
      if (c.w == 2) { const int l = lane_of(c); flag_store(c, (l & 60) == 16, g2_wg(c.mt, c.nl, l & 3), 32 + c.fq, c.ev); }
    }
    if constexpr (n >= EV_PRE0 && n < EV_PRE0 + 12 * EV_PRE_STEP && (n - EV_PRE0) % EV_PRE_STEP == 0)
      g2_issue_w<(n - EV_PRE0) / EV_PRE_STEP / 3, (n - EV_PRE0) / EV_PRE_STEP % 3>(c);
    if constexpr (n == EV_G2_POLL) poll_issue(c);
    if constexpr (n == EV_G2_CHECK) {
      POLL_LANDED(S.ops_after(EV_G2_POLL, EV_G2_CHECK));
      if (c.w == 0) poll_wait(c, lane_of(c) < 8);
    }
    if constexpr (n >= EV_G2_H0 && (n - EV_G2_H0) % 2 == 0 && (n - EV_G2_H0) / 2 < G2_WIN) hw[(n - EV_G2_H0) / 2] = g2_load_h<(n - EV_G2_H0) / 2>(c);
  } else {
    if constexpr (n == EV_CV_POLL) poll_issue(c);
    if constexpr (n == EV_CV_CHECK) {
      POLL_LANDED(K1S[1].ops_after(EV_CV_POLL, EV_CV_CHECK));
      if (c.w == 0) {
        poll_wait(c, lane_of(c) < 32);
        asm volatile("buffer_inv sc1" ::: "memory");
        __builtin_amdgcn_raw_buffer_store_b32(c.ev + 1u, c.rs, lane_of(c) == 14 ? 0u : OOB, WS_EPOCH + c.b * 4, AUX_SC1);
      }
    }
    if constexpr (n == EV_CV_INVW) { if (c.w == 0) wait_vm<S.ops_after(EV_CV_CHECK, EV_CV_INVW)>(); }
    if constexpr (n >= EV_PRE0 && n < EV_PRE0 + 12 * EV_PRE_STEP && (n - EV_PRE0) % EV_PRE_STEP == 0) {
      cv_issue<(n - EV_PRE0) / EV_PRE_STEP>(c);
    }
    if constexpr (n == EV_CV_PRE7 || n == EV_CV_PRE7 + 1) cv_issue_shared<7, n - EV_CV_PRE7>(c);
    if constexpr (n == EV_CV_PRE7 + 3) {
      const int l = lane_of(c);
      const unsigned cb = cv_base(c.w);
      if (l < 60) {
        int o = l < 4 ? CV_K + l * 16 : (l < 32 ? CV_U0 + 4096 + (l - 4) * 16 : CV_U1 + 4096 + (l - 32) * 16);
        *(LAS u32x4*)(ldsp(cb) + o) = zero4();
      }
    }
    if constexpr (n == EV_CV_PRE7 + 8) {
      c.dptr = karg(5);
      asm volatile("" : "+s"(c.dptr));
      const int l = lane_of(c);
      const unsigned cb = cv_base(c.w);
      const int rho = l & 15, q = l >> 4, pi = (l >> 3) & 1, ii = l & 7;
      V.bp = cb + CV_U0 + pi * 4640 + 2 * (2016 - 32 * ii + 8 * q) - 64 * 56;
#pragma unroll
      for (int i = 0; i < 4; ++i) { V.ap[i] = cb + CV_K + 4 + 4 * rho + 16 * q + 1024 * i; asm volatile("" : "+v"(V.ap[i])); }
    }
    if constexpr (n == EV_CV_PRE8 || n == EV_CV_PRE8 + 1) cv_issue_shared<8, n - EV_CV_PRE8>(c);
    if constexpr (n == EV_CV_PRE8 + 1) {
      if (c.w == 6) {
        const int l = lane_of(c);
        if (l < 60) {
          int o = l < 4 ? CV_K + l * 16 : (l < 32 ? CV_U0 + 4096 + (l - 4) * 16 : CV_U1 + 4096 + (l - 32) * 16);
          *(LAS u32x4*)(ldsp(k1_wbase(18)) + o) = zero4();
        }
      }
    }
    if constexpr (n >= 320) {
#pragma unroll
      for (int f = 2 * (n - 320); f < 2 * (n - 320) + 2; ++f) {
        V.b0[f] = *(LAS bf16x8*)(ldsp(V.bp) + 64 * (56 - 8 * f));
        V.a0[f] = cv_read_a(ldsp(V.ap[f >> 1]) + 512 * (f & 1));
      }
    }
  }
  SCHED_FENCE();
  if constexpr (S.bar[n]) {
    wait_vm_barrier<S.wait[n]>();
    SCHED_FENCE();
  }
  if constexpr (n + 1 < K1_NS) k1_slot<n + 1, ROLE>(c, xf, wf, acc, E, P, hw, V);
}

constexpr int g2_ks(int n) { return n < 168 ? n / 3 : 56 + (n - 168) % 16; }
constexpr int g2_tl(int n) { return n < 168 ? n % 3 : (n - 168) / 16; }
constexpr int G2_HW = 24;
template <int n> __device__ __forceinline__ bf16x8 g2_read_w(unsigned rd) {
  constexpr int i = g2_ks(n), t = g2_tl(n), s = i / 8, j = i % 8;
  LAS char* p = ldsp(rd) + (s % G2_NSLOT) * G2_SLOT + (16 * j) * 192 + t * 64;
  return tr_frag(p, p + 8 * 192);
}
static_assert(k1_epi_slot(1, 49) < K1_NS, "chunk B epilogue ends in phase 1");
constexpr int G2_TAIL0 = 2, G2_TAILST = G2_TAIL0 + 13;
constexpr int G2_PUBB = 19, G2_PUBC = 43;
constexpr int G2_CHECK0 = 39, G2_LEAD = 18;
constexpr bool g2_check(int n) { return n >= G2_CHECK0 && n <= G2_CHECK0 + 120 && (n - G2_CHECK0) % 24 == 0; }
constexpr int G2_PST = 186;
constexpr int G2_PRE3 = 206;
constexpr int g2_hslot(int i) {
  return i < 64 ? 3 * (i - G2_WIN) + 2 : 144 + (i - 64) + (i - 64) / 2;
}
constexpr int g2_hload_at(int n) { for (int i = G2_WIN; i < 72; ++i) if (g2_hslot(i) == n) return i; return -1; }
constexpr bool g2_bar(int n) { return (n % 24 == 24 - G2_PF - 1 && n / 24 < 7) || n == 176 - G2_PF - 1; }
constexpr int g2_ops_in_slot(int n) {
  int k = 0;
  for (int s = 1; s + 3 < 9; ++s) for (int i = 0; i < 3; ++i) if (n == 24 * s + 24 - G2_PF + 1 + 3 * i) ++k;
  if (g2_hload_at(n) >= 0) ++k;
  if (n == G2_TAILST) ++k;
  if (n == G2_PST || n == G2_PST + 2 || n == G2_PST + 16 || n == G2_PST + 18) ++k;
  return k;
}
constexpr int g2_ops_after(int a, int b) { int k = 0; for (int n = a + 1; n <= b; ++n) k += g2_ops_in_slot(n); return k; }
static_assert([] { for (int i = G2_WIN; i + 1 < 72; ++i) if (g2_hslot(i) >= g2_hslot(i + 1) || (i >= G2_HW && g2_hslot(i) < 3 * (i - G2_HW) + 2)) return false; return g2_hslot(64) > G2_CHECK0 + 100; }(), "h load slots");
static_assert(g2_bar(G2_PUBB) && g2_bar(G2_PUBC) && g2_bar(G2_CHECK0 + 4) && g2_hslot(32) > G2_CHECK0 + 4 && G2_PRE3 > G2_CHECK0 + 124 && G2_LEAD < 23, "events");
struct G2Fin { u32x4 zz[2], qq[2]; float g; };
__device__ __forceinline__ void g2_send(const Ctx& c, const f32x16& a, int t, int p2) {
  const bool own = (c.w >> 1) == c.fq;
  const u32x4 o = {pk2(a[8 * p2], a[8 * p2 + 1]), pk2(a[8 * p2 + 2], a[8 * p2 + 3]), pk2(a[8 * p2 + 4], a[8 * p2 + 5]), pk2(a[8 * p2 + 6], a[8 * p2 + 7])};
  st128(o, c.rs, c.lane16, own ? OOB : g2_out_base(c) + (t * 2 + p2) * 1024, AUX_SC1);
  if (own) {
#pragma unroll
    for (int qd = 2 * p2; qd < 2 * p2 + 2; ++qd)
      *(LAS f32x4*)(ldsp(c.lane16) + ((c.w & 1) * 12 + t * 4 + qd) * 1024) = (f32x4){a[4 * qd], a[4 * qd + 1], a[4 * qd + 2], a[4 * qd + 3]};
  }
}
__device__ __forceinline__ void g2_fin_load(const Ctx& c, G2Fin& Fn, bool all) {
  const int lane = lane_of(c);
  const int uh = (c.w / 3) & 1, ut = c.w % 3;
  const int t0 = 256 * c.mt + 64 * c.fq + 32 * uh, d0 = 96 * c.nl + 32 * ut;
#pragma unroll
  for (int i = 0; i < 2; ++i)
    Fn.zz[i] = __builtin_amdgcn_raw_buffer_load_b128(c.rs, ((16 * i + (lane >> 2)) * S_) * 2 + (lane & 3) * 16, WS_Z + (d0 * S_ + t0) * 2, AUX_SC1);
  if (all) {
    const char* qptr = karg(0);
#pragma unroll
    for (int i = 0; i < 2; ++i) Fn.qq[i] = *(const u32x4*)(qptr + ((size_t)(t0 + (lane & 31)) * D_ + d0 + 16 * (lane >> 5) + 8 * i) * 2);
    Fn.g = __builtin_bit_cast(float, __builtin_amdgcn_raw_buffer_load_b32(c.rs, (lane & 31) * 4, WS_G + t0 * 4, AUX_SC1));
  }
}
template <int n>
__device__ __forceinline__ void g2_slot(Ctx& c, unsigned rd, u32x4 (&hw)[24], bf16x8 (&wf)[G2_PF + 1], f32x16 (&acc)[3], const f32x16* tailacc, K1Epi& E,
                                        G2Fin& Fn) {
  constexpr int i = g2_ks(n), t = g2_tl(n), s = n / 24;
  if constexpr (n + G2_PF < G2_NS) wf[(n + G2_PF) % (G2_PF + 1)] = g2_read_w<n + G2_PF>(rd);
  if constexpr (i == 0) acc[t] = __builtin_amdgcn_mfma_f32_32x32x16_bf16(wf[n % (G2_PF + 1)], __builtin_bit_cast(bf16x8, hw[i % G2_HW]), (f32x16){}, 0, 0, 0);
  else acc[t] = __builtin_amdgcn_mfma_f32_32x32x16_bf16(wf[n % (G2_PF + 1)], __builtin_bit_cast(bf16x8, hw[i % G2_HW]), acc[t], 0, 0, 0);
  SCHED_FENCE();
  if constexpr (n >= G2_TAIL0 && n <= G2_TAILST) k1_epi_step<1, n - G2_TAIL0>(c, tailacc, E, 64 + c.nl);
  {
    constexpr int m = n - (24 - G2_PF + 1);
    if constexpr (m >= 24 && m % 24 < 9 && m % 3 == 0 && m / 24 + 3 < 9) g2_issue_w<m / 24 + 3, (m % 24) / 3>(c);
  }
  {
    constexpr int hl = g2_hload_at(n);
    if constexpr (hl >= 0) hw[hl % G2_HW] = g2_load_h<hl>(c);
  }
  if constexpr (n == G2_PST || n == G2_PST + 2) g2_send(c, acc[0], 0, (n - G2_PST) / 2);
  if constexpr (n == G2_PST + 16 || n == G2_PST + 18) g2_send(c, acc[1], 1, (n - G2_PST - 16) / 2);
  if constexpr (n == G2_PRE3) {
    g2_fin_load(c, Fn, true);
  }
  if constexpr (g2_check(n + G2_LEAD)) poll_issue(c);
  if constexpr (g2_check(n)) {
    constexpr int k = (n - G2_CHECK0) / 24;
    POLL_LANDED(g2_ops_after(n - G2_LEAD, n));
    if (c.w == 0) {
      const int l = lane_of(c);
      if constexpr (k == 0) poll_wait(c, (l & 60) == 8);
      if constexpr (k == 1) poll_wait(c, (l & 60) == 12);
      if constexpr (k == 2) poll_wait(c, (l & 60) == 48);
      if constexpr (k == 3) poll_wait(c, (l & 60) == 52);
      if constexpr (k == 4) poll_wait(c, (l & 56) == 40);
      if constexpr (k == 5) {
        poll_wait(c, (l >= 16 && l <= 16 + (96 * c.nl + 95) / 9 - (96 * c.nl) / 9) || (l & 60) == 32);
        __builtin_amdgcn_raw_buffer_store_b32(c.ev + 1u, c.rs, l == 14 ? 0u : OOB, WS_EPOCH + c.b * 4, AUX_SC1);
      }
    }
  }
  SCHED_FENCE();
  if constexpr (g2_bar(n)) {
    constexpr int wt = n == G2_PUBB ? K1S[0].ops_after(k1_epi_slot(1, 49), K1_NS - 1) + g2_ops_after(-1, n)
                                    : (n == G2_PUBC ? g2_ops_after(G2_TAILST, n) : (s + 1 < 4 ? 63 : g2_ops_after(24 * (s - 2) + 24 - G2_PF + 1 + 6, n)));
    wait_vm_barrier<cmin(63, wt)>();
    if constexpr (n == G2_PUBB) { if (c.w == 1) publish_h(c, 48); }
    if constexpr (n == G2_PUBC) { if (c.w == 1) publish_h(c, 40); }
    SCHED_FENCE();
  }
  if constexpr (n + 1 < G2_NS) g2_slot<n + 1>(c, rd, hw, wf, acc, tailacc, E, Fn);
}
__device__ __forceinline__ unsigned umax4(const u32x4& v) {
  unsigned a = v[0] > v[1] ? v[0] : v[1], b = v[2] > v[3] ? v[2] : v[3];
  return a > b ? a : b;
}

__device__ __forceinline__ void g2_phase(Ctx& c, u32x4 (&hw)[24], const f32x16* tailacc, K1Epi& E) {
  const int lane = lane_of(c);
  const int w = c.w, ks = c.fq, nt = c.nl, mt = c.mt;
  c.xl0 = lane;
  unsigned rd;
  {
    int gq = lane >> 4, chf = gq & 1, h = gq >> 1, q = (lane & 15) >> 2, p = lane & 3;
    rd = K1_XOFF + (4 * h + q) * 192 + chf * 32 + p * 8;
  }
  bf16x8 wf[G2_PF + 1];
  f32x16 acc[3];
  wf[0] = g2_read_w<0>(rd); wf[1] = g2_read_w<1>(rd); wf[2] = g2_read_w<2>(rd); wf[3] = g2_read_w<3>(rd);
  SCHED_FENCE();
  G2Fin Fn;
  g2_slot<0>(c, rd, hw, wf, acc, tailacc, E, Fn);
  g2_send(c, acc[2], 2, 0);
  g2_send(c, acc[2], 2, 1);
  const int uh = w / 3, ut = w % 3;
  const int qp = lane >> 5, cc = lane & 31;
  lds_barrier();
  if (w < 6) {
    char* optr = (char*)karg(11);
    u32x4 pp[3][2];
    float ga[16], og = 0.f;
    f32x4 own[2][2];
    bool zdone = false;
    unsigned spin = 0;
#pragma unroll 1
    for (; spin < SPIN_CAP; ++spin) {
      asm volatile("" ::: "memory");
      const bool zok = __all(umax4(Fn.zz[0]) != SENT && umax4(Fn.zz[1]) != SENT) || spin == SPIN_CAP - 1;
      if (!zok) g2_fin_load(c, Fn, false);
#pragma unroll
      for (int s = 0; s < 3; ++s) {
        const int src = s + (s >= ks ? 1 : 0);
#pragma unroll
        for (int h = 0; h < 2; ++h)
          pp[s][h] = __builtin_amdgcn_raw_buffer_load_b128(c.rs, qp * 1024 + (cc + 32 * h) * 16, WS_PART + c.b * 49152 + (src * 2 + uh) * 6144 + ut * 2048, AUX_SC1);
      }
      SCHED_FENCE();
      if (zok && !zdone) {
        zdone = true;
        LAS char* zt = ldsp(24576 + w * 2560);
#pragma unroll
        for (int i = 0; i < 2; ++i) *(LAS u32x4*)(zt + (16 * i + (lane >> 2)) * 80 + (lane & 3) * 16) = Fn.zz[i];
        unsigned zv[16];
#pragma unroll
        for (int j = 0; j < 16; ++j) zv[j] = *(LAS u16*)(zt + (16 * qp + j) * 80 + ((cc & 24) + 4 * (cc & 1) + ((cc & 7) >> 1)) * 2);
#pragma unroll
        for (int o = 0; o < 2; ++o)
#pragma unroll
          for (int h = 0; h < 2; ++h) own[o][h] = *(LAS f32x4*)(ldsp((cc + 32 * h) * 16) + (uh * 12 + ut * 4 + 2 * qp + o) * 1024);
        og = rbf(1.0f - Fn.g);
#pragma unroll
        for (int j = 0; j < 16; j += 2) {
          const unsigned qw = Fn.qq[j >> 3][(j >> 1) & 3];
          const unsigned a2 = pk2(bflo(qw) * bflo(zv[j]), bfhi(qw) * bflo(zv[j + 1]));
          ga[j] = Fn.g * bflo(a2); ga[j + 1] = Fn.g * bfhi(a2);
        }
      }
      SCHED_FENCE();
      u32x4 mx = {umax4(pp[0][0]), umax4(pp[0][1]), umax4(pp[1][0]), umax4(pp[1][1])};
      if (zdone && __all(umax4(mx) != SENT && umax4(pp[2][0]) != SENT && umax4(pp[2][1]) != SENT)) break;
    }
#pragma unroll
    for (int o = 0; o < 2; ++o) {
      float res[8];
#pragma unroll
      for (int h = 0; h < 2; ++h)
#pragma unroll
        for (int k = 0; k < 4; k += 2) {
          f32x2 bs = {own[o][h][k], own[o][h][k + 1]};
#pragma unroll
          for (int s = 0; s < 3; ++s) bs += unpk2(pp[s][h][2 * o + (k >> 1)]);
          const f32x2 b2 = unpk2(pk2(bs[0], bs[1]));
          res[4 * h + k] = radd(ga[8 * o + 4 * h + k], og * b2[0]);
          res[4 * h + k + 1] = radd(ga[8 * o + 4 * h + k + 1], og * b2[1]);
        }
      u32x4 ov = {pk2(res[0], res[1]), pk2(res[2], res[3]), pk2(res[4], res[5]), pk2(res[6], res[7])};
      __builtin_amdgcn_raw_buffer_store_b128(ov, __builtin_amdgcn_make_buffer_rsrc(optr, 0, S_ * D_ * 2, 0x00020000),
                                             ((256 * mt + 64 * ks + 32 * uh + cc) * D_ + 96 * nt + 32 * ut + 16 * qp + 8 * o) * 2, 0, 17);
      asm volatile("s_nop 1" ::"v"(ov));
    }
  }
}

template <bool PREF, bool FIRST, class Hook>
__device__ __forceinline__ void cv_block(f32x4 (&acc)[8], const bf16x8 (&af)[8], const bf16x8 (&bf)[8], bf16x8 (&naf)[8], bf16x8 (&nbf)[8],
                                         const unsigned (&ap)[4], unsigned bp, int roff, Hook&& hook) {
  auto step = [&](auto K) {
    constexpr int k = decltype(K)::value;
    constexpr int p = [] { int r = k, q = 0; while (r >= 8 - q) { r -= 8 - q; ++q; } return q; }();
    constexpr int dl = [] { int r = k, q = 0; while (r >= 8 - q) { r -= 8 - q; ++q; } return r; }();
    if constexpr (PREF && (k & 1) == 0 && k < 32) {
      constexpr int f = k >> 2;
      if constexpr ((k & 2) != 0) naf[f] = cv_read_a(ldsp(ap[f >> 1]) + 512 * (f & 1) + roff);
      else nbf[f] = *(LAS bf16x8*)(ldsp(bp) + 64 * (56 - 8 * f) + roff);
    }
    if constexpr (FIRST && p == 0) asm volatile("v_mfma_f32_16x16x32_bf16 %0, %1, %2, 0" : "=&v"(acc[dl]) : "v"(af[p]), "v"(bf[dl]));
    else asm volatile("v_mfma_f32_16x16x32_bf16 %0, %1, %2, %0" : "+v"(acc[p + dl]) : "v"(af[p]), "v"(bf[dl]));
    if constexpr (k & 1) { SCHED_FENCE(); hook(K); SCHED_FENCE(); }
  };
  [&]<int... Ks>(std::integer_sequence<int, Ks...>) { (step(std::integral_constant<int, Ks>{}), ...); }(std::make_integer_sequence<int, 36>{});
}
__device__ __forceinline__ u32x4 cv_u(LAS char* cl, int g, int lane) {
  const int q = lane >> 4, pi = (lane >> 3) & 1, ii = lane & 7;
  return *(LAS u32x4*)(cl + (pi ? CV_U1 : CV_U0) + 2 * (2040 - (256 * g + 32 * ii + 8 * q)));
}
__device__ __forceinline__ f32x4 cv_ud(const u32x4& uu, float Dv) {
  return (f32x4){rb1(bflo(uu[3]) * Dv), rb1(bflo(uu[2]) * Dv), rb1(bflo(uu[1]) * Dv), rb1(bflo(uu[0]) * Dv)};
}
__device__ __forceinline__ u32x2 cv_z(const f32x4& y, const f32x4& ud) {
  return (u32x2){pk2(rb1(y[0]) + ud[0], rb1(y[1]) + ud[1]), pk2(rb1(y[2]) + ud[2], rb1(y[3]) + ud[3])};
}
__device__ __forceinline__ void cv_phase(Ctx& c, const f32x16* tailacc, K1Epi& E, CvRegs& V) {
  const int w = c.w;
  f32x4 acc[8], acc8[8];
  bf16x8 a1[8], b1[8];
  auto none = [](auto) {};
  cv_block<true, true>(acc, V.a0, V.b0, a1, b1, V.ap, V.bp, 64, [&](auto K) {
    constexpr int k = decltype(K)::value / 2;
    if constexpr (k >= G2_TAIL0 && k <= G2_TAILST) k1_epi_step<1, k - G2_TAIL0>(c, tailacc, E, 64 + c.nl);
  });
  wait_vm_barrier<1>();
  if (w == 1) publish_h(c, 48);
  const int lane = lane_of(c);
  const unsigned zb = WS_Z + 9 * cv_id(c) * (S_ * 2);
  const unsigned cb = cv_base(w), cb8 = cv_base(8);
  int zsl, zwg;
  unsigned zev, dd, ap8[4];
  SCHED_FENCE();
  cv_block<true, false>(acc, a1, b1, V.a0, V.b0, V.ap, V.bp, 128, [&](auto K) {
    constexpr int k = decltype(K)::value / 2;
    if constexpr (k == 1 || k == 5 || k == 9 || k == 13) st128(sent4(), c.rs, c.lane16, zb + w * (S_ * 2) + (k / 4) * 1024, AUX_SC1);
    if constexpr (k == 17) __builtin_amdgcn_raw_buffer_store_b64(sent2(), c.rs, lane * 8, zb + 8 * (S_ * 2) + 512 * w, AUX_SC1);
    if constexpr (k == 3) { zwg = cv_zwg(c, lane, zsl); zev = __builtin_amdgcn_raw_buffer_load_b32(c.rs, zwg * 4, WS_EPOCH, 0); }
    if constexpr (k == 7) dd = *(const u16*)(c.dptr + (9 * cv_id(c) + ((lane & 1) ? 8 : w)) * 2);
    if constexpr (k == 11) {
#pragma unroll
      for (int i = 0; i < 4; ++i) { ap8[i] = V.ap[i] + (cb8 - cb + 64 * w); asm volatile("" : "+v"(ap8[i])); }
    }
  });
  cv_block<true, false>(acc, V.a0, V.b0, a1, b1, ap8, V.bp + (cb8 - cb + 64 * w), 0, none);
  cv_block<true, true>(acc8, a1, b1, V.a0, V.b0, V.ap, V.bp, 192, none);
  LAS char* sc = ldsp(((w & 2) ? k1_wbase(20) : k1_wbase(19)) + (w & 1) * 8192 + lane * 16);
  cv_block<true, false>(acc, V.a0, V.b0, a1, b1, V.ap, V.bp, 256, [&](auto K) {
    constexpr int k = decltype(K)::value / 2;
    if constexpr (k >= 4 && k < 12) { if (w >= 4) *(LAS f32x4*)(sc + (k - 4) * 1024) = acc8[k - 4]; }
  });
  asm volatile("s_waitcnt vmcnt(0) lgkmcnt(0)\n\ts_barrier" ::: "memory");
  if (w == 5) publish_h(c, 40);
  if (w == 6) flag_store(c, zsl >= 0, zwg, zsl, zev);
  SCHED_FENCE();
  f32x4 o8[2];
  cv_block<true, false>(acc, a1, b1, V.a0, V.b0, V.ap, V.bp, 320, [&](auto K) {
    constexpr int k = decltype(K)::value / 2;
    if (w < 4) {
      if constexpr (k >= 2 && k < 18 && (k & 1) == 0) *(LAS f32x4*)(sc + (k / 2 - 1) * 1024) = acc8[k / 2 - 1] + o8[(k / 2 - 1) & 1];
      if constexpr (k < 16 && (k & 1) == 0) o8[(k / 2) & 1] = *(LAS f32x4*)(sc + (k / 2) * 1024);
    }
  });
  lds_barrier();
  const float Dv = bflo((unsigned)__builtin_amdgcn_readlane((int)dd, 0)), Dv8 = bflo((unsigned)__builtin_amdgcn_readlane((int)dd, 1));
  const int zo = (32 * (lane & 7) + 8 * (lane >> 4) + 4 * ((lane >> 3) & 1)) * 2;
  f32x4 y4[4], ud[8];
  u32x4 uu[2];
  SCHED_FENCE();
  cv_block<true, false>(acc, V.a0, V.b0, a1, b1, V.ap, V.bp, 384, [&](auto K) {
    constexpr int k = decltype(K)::value / 2;
    LAS char* s0 = ldsp(w * 1024 + lane * 16);
    if constexpr (k < 4) y4[k] = *(LAS f32x4*)(s0 + ((k & 2) ? k1_wbase(20) : k1_wbase(19)) + (k & 1) * 8192);
    if constexpr (k == 4) uu[0] = cv_u(ldsp(cb8), w, lane);
    if constexpr (k == 8) { y4[0] = (y4[0] + y4[1]) + (y4[2] + y4[3]); asm volatile("" : "+v"(y4[0])); }
    if constexpr (k == 12) __builtin_amdgcn_raw_buffer_store_b64(cv_z(y4[0], cv_ud(uu[0], Dv8)), c.rs, zo, zb + 8 * (S_ * 2) + 512 * w, AUX_SC1);
  });
  cv_block<true, false>(acc, a1, b1, V.a0, V.b0, V.ap, V.bp, 448, [&](auto K) {
    constexpr int k = decltype(K)::value / 2;
    if constexpr (k >= 2 && (k & 1) == 0) { ud[k / 2 - 1] = cv_ud(uu[(k / 2 - 1) & 1], Dv); asm volatile("" : "+v"(ud[k / 2 - 1])); }
    if constexpr (k < 16 && (k & 1) == 0) uu[(k / 2) & 1] = cv_u(ldsp(cb), k / 2, lane);
  });
  cv_block<false, false>(acc, V.a0, V.b0, a1, b1, V.ap, V.bp, 0, [&](auto K) {
    constexpr int k = decltype(K)::value;
    constexpr int g = k == 5 ? 0 : (k == 13 ? 1 : (k == 19 ? 2 : (k == 25 ? 3 : (k == 31 ? 4 : (k == 35 ? 5 : -1)))));
    if constexpr (g >= 0) {
      asm volatile("s_nop 3" : "+v"(acc[g]));
      __builtin_amdgcn_raw_buffer_store_b64(cv_z(acc[g], ud[g]), c.rs, zo, zb + w * (S_ * 2) + 512 * g, AUX_SC1);
    }
  });
  asm volatile("s_nop 7\n\ts_nop 1" : "+v"(acc[6]), "+v"(acc[7]));
#pragma unroll
  for (int g = 6; g < 8; ++g) __builtin_amdgcn_raw_buffer_store_b64(cv_z(acc[g], ud[g]), c.rs, zo, zb + w * (S_ * 2) + 512 * g, AUX_SC1);
}

template <int ROLE>
__device__ __forceinline__ void fused_body(const int w, const unsigned long long pc) {
  const int b = (blockIdx.x + 192) & 255, xcd = b & 7, r = b >> 3;
  const int ml = r & 3, nl = r >> 2, fq = xcd >> 1;
  const int mt = 4 * (xcd & 1) + ml;
  const int lane = __builtin_amdgcn_mbcnt_hi(~0u, __builtin_amdgcn_mbcnt_lo(~0u, 0u));
  Ctx c;
  c.w = w; c.nl = nl; c.ml = ml; c.b = b; c.mt = mt; c.fq = fq;
  const char* ka[11];
  {
    typedef const char* cptr;
    const __attribute__((address_space(4))) cptr* p = (const __attribute__((address_space(4))) cptr*)__builtin_amdgcn_kernarg_segment_ptr();
    asm volatile("" : "+s"(p));
#pragma unroll
    for (int i = 0; i < 11; ++i) ka[i] = p[i];
    asm volatile("" : "+s"(ka[0]), "+s"(ka[1]), "+s"(ka[2]), "+s"(ka[3]), "+s"(ka[4]), "+s"(ka[6]), "+s"(ka[7]), "+s"(ka[8]), "+s"(ka[9]), "+s"(ka[10]));
  }
  c.rot = (nl + ml) % 3;
  c.lane16 = lane * 16;
  asm volatile("" : "+v"(c.lane16));
  c.sw1 = make_srd(ka[6], (1152 * fq + 128 * nl) * 2, D_ * F_ * 2);
  c.sw3 = make_srd(ka[7], (1152 * fq + 128 * nl) * 2, D_ * F_ * 2);
  c.sx = make_srd(ka[0], (256 * mt + 32 * w) * (D_ * 2), S_ * D_ * 2);
#pragma unroll
  for (int i = 0; i < 3; ++i) { const int k = kslice(i, c.rot); c.wo[i] = k * (64 * F_ * 2); c.xo[i] = k * 128; }
  {
    int row = lane >> 3, pos = lane & 7;
    c.voff_w = (8 * w + row) * (F_ * 2) + ((pos ^ (2 * ((row >> 1) & 1))) * 16);
    c.voff_x = row * (D_ * 2) + ((pos ^ (lane >> 4)) * 16);
  }
  k1_issue_w<0, 0>(c); k1_issue_w<0, 1>(c);
  k1_issue_x<0, 0>(c); k1_issue_x<0, 1>(c); k1_issue_x<0, 2>(c); k1_issue_x<0, 3>(c);
  {
    unsigned e0, e1;
    asm volatile("s_mov_b32 %0, .Lgm_code_end0-.Lgm_code_start\n\ts_mov_b32 %1, .Lgm_code_end1-.Lgm_code_start" : "=s"(e0), "=s"(e1));
    const u32x4 sc = make_srd((const char*)pc, 0, e0 > e1 ? e0 : e1);
    const unsigned vo = lane < 3 ? (unsigned)(8 * r + w + 256 * lane) * 128 : OOB;
    asm volatile("buffer_load_dword %0, %1, %2, 0 offen" : "=v"(c.touch) : "v"(vo), "s"(sc) : "memory");
  }
  k1_issue_w<1, 0>(c); k1_issue_w<1, 1>(c);
  SCHED_FENCE();
  c.sws = make_srd(ka[10], 0, WS_END);
  c.rs = __builtin_amdgcn_make_buffer_rsrc((void*)ka[10], 0, WS_END, 0x00020000);
  c.hb = WS_H + ((8 * mt + w) * 288 + 72 * fq) * 1024;
  {
    int gq = lane >> 4, mat = gq & 1, h = gq >> 1, q = (lane & 15) >> 2, p = lane & 3;
    int X = 2 * (q >> 1) + 4 * mat;
#pragma unroll
    for (int t = 0; t < 4; ++t) c.rd[t] = mat * 8192 + (8 * h + q) * 128 + (((2 * t) ^ X) + (p >> 1)) * 16 + (p & 1) * 8;
    int cc = lane & 31, hh = lane >> 5;
    c.xl0 = K1_XOFF + w * (K1_XD * 4096) + cc * 128 + ((hh ^ ((cc >> 1) & 7)) * 16);
  }
  {
    const u32x4 sw2 = make_srd(ka[8], 1152 * fq * D_ * 2, F_ * D_ * 2), ssk = make_srd(ka[4], 0, D_ * S_ * 2);
    c.s2 = ROLE == 0 ? sw2 : ssk;
    c.s3 = ROLE == 0 ? ssk : sw2;
    const unsigned mh = xcd & 1, i = 8 * r + w + 256 * (lane < 16 ? lane : (lane < 48 ? lane - 16 : lane - 48));
    const unsigned r1 = (i * 6554u) >> 16, c1 = i - 10 * r1;
    const int o1 = (int)((2 * r1 + mh) * (F_ * 2) + (c1 < 8 ? 2 * c1 + 1 : c1 + 8) * 128) - 256 * nl;
    const unsigned r2 = (i * 7282u) >> 16, o2 = (2 * r2 + mh) * (D_ * 2) + (i - 9 * r2) * 128;
    unsigned o = 72 * xcd * (S_ * 2) + i * 128;
    o = lane < 48 ? (i < 5184 ? o2 : OOB) : o;
    o = lane < 16 ? ((i < 2880 && o1 >= 0) ? (unsigned)o1 : OOB) : o;
    c.warm = lane < 57 ? o : OOB;
    asm volatile("" : "+v"(c.warm));
  }
  K1Prep P;
  {
    const u32x4 sk_ = make_srd(ka[1], 0, S_ * D_ * 2), sv_ = make_srd(ka[2], 0, S_ * D_ * 2);
    auto voff = [&](int blk, int halo, bool on) {
      const unsigned pc = (unsigned)(64 * blk + lane), row = (pc * 7282u) >> 16;
      const int t = 64 * r - halo + (int)row;
      const unsigned o = (unsigned)t * (D_ * 2) + (pc - 9 * row) * 16 + 144 * xcd;
      return (on && t >= 0 && (int)row < 64 + halo) ? o : OOB;
    };
    const bool isv = (w & 2) != 0;
    const u32x4 sd = {isv ? sv_[0] : sk_[0], isv ? sv_[1] : sk_[1], sk_[2], sk_[3]};
    unsigned v0 = voff(w, 4, true), v1 = voff(w, 1, true), v2 = voff(8 + (w & 1), isv ? 1 : 4, w < 4);
    const int e0 = cv_wg(8 * xcd + lane), e1 = g2_wg(mt, lane - 8, fq), e2 = g2_wg(mt, nl < 6 ? nl : 0, lane & 3);
    int ewg = b;
    ewg = (lane & 60) == 16 ? e2 : ewg;
    ewg = lane < 14 ? e1 : ewg;
    ewg = (lane < 8 ? e0 : ewg) * 4;
    asm volatile("" : "+v"(v0), "+v"(v1), "+v"(v2), "+v"(ewg), "+v"(c.rd[0]), "+v"(c.rd[1]), "+v"(c.rd[2]), "+v"(c.rd[3]), "+v"(c.xl0));
    SCHED_FENCE();
    dma16<K1_POFF, 0>(v0, sk_, 0u, w * 1024);
    dma16<K1_POFF + K1_PV, 0>(v1, sv_, 0u, w * 1024);
    dma16<K1_POFF, 0>(v2, sd, 0u, w < 4 ? (8 + (w & 1)) * 1024 + (isv ? K1_PV : 0) : 20480 + w * 1024);
    const unsigned so = WS_EPOCH;
    asm volatile("buffer_load_dword %0, %1, %2, %3 offen" : "=v"(c.ev) : "v"(ewg), "s"(c.sws), "s"(so) : "memory");
    const char* gx = ka[0] + (size_t)(256 * mt + 64 * fq + 8 * nl + w) * (D_ * 2);
    const int dp = 8 * w + (lane >> 3), dc = dp < 36 ? dp : 35;
    P.lp = K1_POFF + 8 * (7 - (lane & 7)) * 144 + dc * 4;
    asm volatile("" : "+v"(P.lp));
    GLD4(P.sk, (unsigned)(72 * xcd + 2 * dc) * 8, ka[3]);
    GLD4(P.ga0, c.lane16, gx);
    GLD4(P.ga1, 1024 + (c.lane16 & 127), gx);
    GLD4(P.gw0, c.lane16, ka[9]);
    GLD4(P.gw1, 1024 + (c.lane16 & 127), ka[9]);
  }
  bf16x8 xf[36];
  bf16x8 wf[K1_RD + 1];
  f32x16 acc[4];
  u32x4 hw[24];
  wait_vm_barrier<12>();
  k1_issue_x<1, 0>(c); k1_issue_x<1, 1>(c); k1_issue_x<1, 2>(c); k1_issue_x<1, 3>(c);
  xf[0] = k1_read_x<0, 0>(c); wf[0] = k1_read_w<0>(c);
  xf[1] = k1_read_x<0, 1>(c); wf[1] = k1_read_w<1>(c);
  xf[2] = k1_read_x<0, 2>(c); wf[2] = k1_read_w<2>(c);
  xf[3] = k1_read_x<0, 3>(c);
  static_assert(K1_RD == 3, "prologue reads");
  SCHED_FENCE();
  K1Epi E;
  CvRegs V;
  k1_slot<0, ROLE>(c, xf, wf, acc, E, P, hw, V);
  if constexpr (ROLE == 1) cv_phase(c, acc, E, V);
  else {
    g2_phase(c, hw, acc, E);
    __builtin_amdgcn_s_waitcnt(0);
  }
  if constexpr (ROLE == 0) asm volatile(".Lgm_code_end0:");
  else asm volatile(".Lgm_code_end1:");
}

__global__ __launch_bounds__(512, 2) void fused_kernel(const void* x, const void* kk, const void* vv, const void* shift_k, const void* ssm_k,
                                                       const void* ssm_D, const void* w1, const void* w3, const void* w2, const void* gate_w, void* ws,
                                                       void* out) {
  const int w = __builtin_amdgcn_readfirstlane(threadIdx.x >> 6);
  unsigned long long pc;
  asm volatile("s_getpc_b64 %0\n.Lgm_code_start:" : "=s"(pc));
  if (blockIdx.x < 64) fused_body<1>(w, pc);
  else fused_body<0>(w, pc);
}

extern "C" void kernel_launch(void* const* d_in, const int* in_sizes, int n_in, void* d_out, int out_size, void* d_ws, size_t ws_size,
                              hipStream_t stream) {
  (void)hipFuncSetAttribute((const void*)fused_kernel, hipFuncAttributeMaxDynamicSharedMemorySize, LDS_BYTES);
  fused_kernel<<<256, 512, LDS_BYTES, stream>>>(d_in[0], d_in[1], d_in[2], d_in[3], d_in[4], d_in[5], d_in[7], d_in[8], d_in[9], d_in[10], d_ws, d_out);
}
```

```cpp
#include <hip/hip_runtime.h>
#include <hip/hip_bf16.h>
#include <utility>

#define LAS __attribute__((address_space(3)))
#define GAS __attribute__((address_space(1)))
typedef unsigned short u16;
typedef __bf16 bf16x2 __attribute__((ext_vector_type(2)));
typedef __bf16 bf16x4 __attribute__((ext_vector_type(4)));
typedef __bf16 bf16x8 __attribute__((ext_vector_type(8)));
typedef float f32x2 __attribute__((ext_vector_type(2)));
typedef float f32x4 __attribute__((ext_vector_type(4)));
typedef float f32x16 __attribute__((ext_vector_type(16)));
typedef unsigned u32x2 __attribute__((ext_vector_type(2)));
typedef unsigned u32x4 __attribute__((ext_vector_type(4)));

constexpr int S_ = 2048, D_ = 576, F_ = 4608;
constexpr unsigned WS_H = 0;
constexpr unsigned WS_PART = WS_H + 64 * 288 * 1024;
constexpr unsigned WS_UR = WS_PART + 256 * 49152;
constexpr unsigned WS_Z = WS_UR + D_ * 2 * S_ * 2;
constexpr unsigned WS_G = WS_Z + D_ * S_ * 2;
constexpr unsigned WS_FLAG = WS_G + S_ * 4;
constexpr unsigned WS_EPOCH = WS_FLAG + 256 * 512;
constexpr unsigned WS_END = WS_EPOCH + 1024;
constexpr unsigned FLAG_MAGIC = 0x7c3a91e5u, SENT = 0xffffffffu;
constexpr int AUX_SC1 = 16;
constexpr unsigned OOB = 0x7ff00000u;
constexpr unsigned SPIN_CAP = 1u << 16;

__device__ __forceinline__ float bflo(unsigned v) { return __builtin_bit_cast(float, v << 16); }
__device__ __forceinline__ float bfhi(unsigned v) { return __builtin_bit_cast(float, v & 0xffff0000u); }
__device__ __forceinline__ float rbf(float x) { return (float)(__bf16)x; }
__device__ __forceinline__ unsigned pk2(float lo, float hi) {
  f32x2 v = {lo, hi};
  return __builtin_bit_cast(unsigned, __builtin_convertvector(v, bf16x2));
}
__device__ __forceinline__ f32x2 unpk2(unsigned v) { return (f32x2){bflo(v), bfhi(v)}; }
__device__ __forceinline__ float radd(float a, float b) { const unsigned t = pk2(a, b); return bflo(t) + bfhi(t); }
#define SCHED_FENCE() __builtin_amdgcn_sched_barrier(0)
__device__ __forceinline__ void st128(u32x4 v, __amdgpu_buffer_rsrc_t rs, unsigned voff, unsigned soff, int) {
  __builtin_amdgcn_raw_buffer_store_b128(v, rs, voff, soff, AUX_SC1);
  asm volatile("s_nop 1" ::"v"(v));
}

template <int LC, int SC> __device__ __forceinline__ void dma16(unsigned voff, const u32x4& srd, unsigned so, unsigned lb) {
  unsigned t;
  asm volatile("s_add_u32 m0, %4, %5\n\ts_add_u32 %0, %3, %6\n\tbuffer_load_dwordx4 %1, %2, %0 offen lds"
               : "=&s"(t) : "v"(voff), "s"(srd), "s"(so), "s"(lb), "n"(LC), "n"(SC) : "memory", "scc");
}
template <int N> __device__ __forceinline__ void wait_vm_barrier() {
  asm volatile("s_waitcnt vmcnt(%0)\n\ts_barrier" ::"n"(N) : "memory");
}
template <int N> __device__ __forceinline__ void wait_vm() { asm volatile("s_waitcnt vmcnt(%0)" ::"n"(N) : "memory"); }
__device__ __forceinline__ void lds_barrier() { asm volatile("s_waitcnt lgkmcnt(0)\n\ts_barrier" ::: "memory"); }
__device__ __forceinline__ LAS char* ldsp(unsigned a) { return (LAS char*)(size_t)a; }
__device__ __forceinline__ bf16x8 tr_frag(LAS char* p0, LAS char* p1) {
  bf16x4 a = __builtin_amdgcn_ds_read_tr16_b64_v4bf16((LAS bf16x4*)p0);
  bf16x4 b = __builtin_amdgcn_ds_read_tr16_b64_v4bf16((LAS bf16x4*)p1);
  return __builtin_shufflevector(a, b, 0, 1, 2, 3, 4, 5, 6, 7);
}
constexpr int cmin(int a, int b) { return a < b ? a : b; }
__device__ __forceinline__ const char* karg(int i) {
  typedef const char* cptr;
  const __attribute__((address_space(4))) cptr* p = (const __attribute__((address_space(4))) cptr*)__builtin_amdgcn_kernarg_segment_ptr();
  asm volatile("" : "+s"(p));
  return p[i];
}
__device__ __forceinline__ u32x4 make_srd(const char* base, unsigned off, unsigned total) {
  const unsigned long long a = (unsigned long long)base + off;
  return (u32x4){(unsigned)a, (unsigned)(a >> 32) & 0xffffu, total - off, 0x00020000u};
}

constexpr int K1_SLOT = 16384, K1_XOFF = 65536, K1_XD = 2, K1_POFF = 131072, K1_PV = 10240;
constexpr int LDS_BYTES = 163840;
constexpr int K1_PF = 4, K1_RD = 3, K1_NS = 324, K1_NSEG = 21;
constexpr int K1_PREP0 = 28, K1_PREP_UNITS = 8 + 2 + 63 + 1 + 4;
constexpr int G2_SLOT = 24576, G2_NSLOT = 4, G2_WIN = 16, G2_PF = 4, G2_NS = 216;
constexpr int CV_K = 0, CV_U0 = 4160, CV_U1 = CV_U0 + 4640, CV_CH = 13440;
constexpr int k1_wbase(int s) { return s == 11 ? K1_XOFF : (s == 12 ? K1_XOFF + K1_SLOT : ((s < 11 ? s : s - 2) % 4) * K1_SLOT); }
constexpr int k1_wprev(int s) { return s < 4 || s == 11 || s == 12 ? -1 : (s < 11 ? s - 4 : (s < 17 ? s - 6 : s - 4)); }
constexpr int EV_PUB_U = 140;
constexpr int EV_PUB_A = 204;
constexpr int EV_PRE0 = 221, EV_PRE_STEP = 4;
constexpr int EV_CV_POLL = 176, EV_CV_CHECK = 190, EV_CV_INVW = 219;
constexpr int EV_CV_PRE7 = 300;
constexpr int EV_CV_PRE8 = 316;
constexpr int EV_SENT0 = 177, EV_SENT_STEP = 7, EV_PUB_S = 236;
constexpr int EV_G2_POLL = 264, EV_G2_CHECK = 280, EV_G2_H0 = 289;
constexpr int SMALL_SLOT[6] = {1, 3, 9, 10, 13, 15};
constexpr int WARM_N = 10;
constexpr int WARM_SLOT[WARM_N] = {17, 24, 40, 56, 72, 88, 119, 151, 175, 191};
constexpr int WARM_DONE = 235;
constexpr int EV_PUB_B1 = 316;
constexpr int K1_EPI0[2] = {125, 269};
constexpr int k1_epi_slot(int ch, int e) { return K1_EPI0[ch] + (ch == 0 ? e - e / 12 : e); }

struct K1Sched {
  int seg[K1_NS], tile[K1_NS], j[K1_NS];
  int first_use[K1_NSEG], last_use[K1_NSEG];
  int wissue[K1_NSEG];
  int xissue[9];
  bool bar[K1_NS];
  int ops[K1_NS];
  int wait[K1_NS];
  int done[K1_NS];
  constexpr int ops_after(int a, int b) const { int k = 0; for (int q = a + 1; q <= b; ++q) k += ops[q]; return k; }
};
constexpr K1Sched k1_make(int role) {
  K1Sched S{};
  int n = 0;
  for (int s = 0; s < 18;) {
    if (s == 7 || s == 9 || s == 16) {
      const int tt[8] = {0, 1, 0, 1, 2, 2, 3, 3}, hh[8] = {0, 0, 1, 1, 0, 1, 0, 1};
      for (int q = 0; q < 8; ++q) for (int j = 0; j < 4; ++j) { S.seg[n] = s + hh[q]; S.tile[n] = tt[q]; S.j[n] = j; ++n; }
      s += 2;
    } else {
      for (int t = 0; t < 4; ++t) for (int j = 0; j < 4; ++j) { S.seg[n] = s; S.tile[n] = t; S.j[n] = j; ++n; }
      s += 1;
    }
  }
  for (int i = 0; i < 36; ++i) { S.seg[n] = 18 + i / 16; S.tile[n] = 0; S.j[n] = i % 16; ++n; }
  for (int s = 0; s < K1_NSEG; ++s) { S.first_use[s] = 100000; S.last_use[s] = -1; }
  for (int i = 0; i < K1_NS; ++i) { int s = S.seg[i]; if (i < S.first_use[s]) S.first_use[s] = i; if (i > S.last_use[s]) S.last_use[s] = i; }
  for (int s = 1; s < K1_NSEG; ++s) S.bar[S.first_use[s] - K1_PF - 1] = true;
  S.wissue[0] = S.wissue[1] = -1;
  S.wissue[2] = 0; S.wissue[3] = 12;
  int busy = 0;
  for (int s = 4; s < K1_NSEG; ++s) {
    int q = k1_wprev(s) >= 0 ? S.last_use[k1_wprev(s)] : S.first_use[8] + 4;
    while (!S.bar[q]) ++q;
    int is = q + 1;
    if (is < busy) is = busy;
    S.wissue[s] = is;
    busy = is + (s >= 11 ? 8 : 4);
  }
  S.xissue[0] = S.xissue[1] = -1;
  for (int p = 2; p < 9; ++p) S.xissue[p] = S.first_use[p - 2] + 4;
  for (int s = 0; s < K1_NSEG; ++s) if (S.wissue[s] >= 0) { S.ops[S.wissue[s]]++; S.ops[S.wissue[s] + 2]++; }
  for (int p = 0; p < 9; ++p) if (S.xissue[p] >= 0) for (int i = 0; i < 4; ++i) S.ops[S.xissue[p] + i]++;
  S.ops[K1_PREP0 + 7]++;
  for (int i = 0; i < 6; ++i) S.ops[SMALL_SLOT[i]]++;
  for (int i = 0; i < WARM_N; ++i) S.ops[WARM_SLOT[i]]++;
  for (int i = 0; i < 4; ++i) S.ops[K1_PREP0 + K1_PREP_UNITS - 4 + i]++;
  for (int c = 0; c < 2; ++c) for (int t = 0; t < 4; ++t) if (k1_epi_slot(c, 12 * t + 13) < K1_NS) S.ops[k1_epi_slot(c, 12 * t + 13)]++;
  if (role == 0) {
    for (int i = 0; i < G2_WIN; ++i) S.ops[EV_G2_H0 + 2 * i]++;
    for (int i = 0; i < 6; ++i) S.ops[EV_SENT0 + EV_SENT_STEP * i]++;
  } else {
    S.ops[EV_CV_PRE8]++; S.ops[EV_CV_PRE8 + 1]++;
    S.ops[EV_CV_PRE7]++; S.ops[EV_CV_PRE7 + 1]++;
  }
  for (int i = 0; i < 12; ++i) S.ops[EV_PRE0 + EV_PRE_STEP * i]++;
  for (int i = 0; i < K1_NS; ++i) {
    S.wait[i] = 63;
    S.done[i] = -2;
    if (!S.bar[i]) continue;
    int youngest = -2;
    for (int s = 1; s < K1_NSEG; ++s)
      if (S.first_use[s] - K1_PF - 1 == i) {
        int y = S.wissue[s] < 0 ? -1 : S.wissue[s] + 2;
        if (s < 9) { int yx = S.xissue[s] < 0 ? -1 : S.xissue[s] + 3; if (yx > y) y = yx; }
        if (y > youngest) youngest = y;
      }
    if (i == EV_PUB_A - 1 && youngest < k1_epi_slot(0, 49)) youngest = k1_epi_slot(0, 49);
    if (i == EV_PUB_S - 1 && youngest < EV_SENT0 + 5 * EV_SENT_STEP) youngest = EV_SENT0 + 5 * EV_SENT_STEP;
    if (i == WARM_DONE && youngest < WARM_SLOT[WARM_N - 1]) youngest = WARM_SLOT[WARM_N - 1];
    if (i == EV_PUB_B1 - 1 && youngest < k1_epi_slot(1, 25)) youngest = k1_epi_slot(1, 25);
    if (role == 1 && i == 315 && youngest < EV_CV_PRE7 + 1) youngest = EV_CV_PRE7 + 1;
    if (i == K1_PREP0 - 1 && youngest < SMALL_SLOT[5]) youngest = SMALL_SLOT[5];
    if (i == 299 && youngest < EV_PRE0 + 11 * EV_PRE_STEP) youngest = EV_PRE0 + 11 * EV_PRE_STEP;
    S.wait[i] = cmin(63, S.ops_after(youngest, i));
    S.done[i] = youngest;
  }
  return S;
}
constexpr K1Sched K1S[2] = {k1_make(0), k1_make(1)};
static_assert(K1_PREP0 + K1_PREP_UNITS - 1 <= K1S[0].done[EV_PUB_U - 1] && K1S[0].bar[EV_PUB_U - 1], "u stores complete before publish");
static_assert(K1S[0].bar[EV_PUB_A - 1] && K1S[0].bar[EV_PUB_S - 1] && K1S[0].bar[299] && K1S[0].bar[315], "events");
static_assert(K1S[0].bar[EV_G2_CHECK + 3] && K1S[0].bar[EV_CV_INVW] && K1S[0].bar[EV_CV_PRE7 - 1] && K1S[0].last_use[17] < EV_CV_PRE7, "events");
static_assert(K1S[0].bar[EV_CV_PRE8 - 1] && K1S[0].last_use[18] < EV_CV_PRE8 && EV_G2_H0 + 2 * G2_WIN <= K1_NS + 1, "events");
static_assert(EV_PRE0 > EV_CV_INVW && K1S[0].last_use[12] <= EV_CV_INVW, "events");
static_assert([] {
  for (int t = 0; t < 4; ++t) {
    int last = 0, next = 1000;
    for (int i = 0; i < 144; ++i) if (K1S[0].tile[i] == t) last = i;
    for (int i = 287; i >= 144; --i) if (K1S[0].tile[i] == t) next = i;
    if (k1_epi_slot(0, 12 * t) < last + 2 || k1_epi_slot(0, 12 * t + 9) >= next) return false;
  }
  return true;
}(), "epilogue windows");

struct Ctx {
  u32x4 sw1, sw3, sx, s2, s3, sws;
  __amdgpu_buffer_rsrc_t rs;
  unsigned wo[3], xo[3];
  unsigned hb;
  unsigned voff_w, voff_x;
  unsigned rd[4];
  unsigned xl0, lane16;
  unsigned ev;
  u32x2 poll;
  unsigned touch, warm;
  const char* dptr;
  unsigned rc0, rc1;
  int w, rot, nl, ml, b, mt, fq;
};
struct K1Prep {
  u32x4 sk, ga0, ga1, gw0, gw1;
  unsigned lp, kn, vn, o0, psk, pev;
  const char *skp, *gw, *gx;
  f32x2 s[4], k2[4], a, p, q; unsigned u[9]; float gs;
};
struct K1Epi { float m[2], p[2], pq[2], r[2], h[2]; u32x4 ho; };
struct CvRegs { unsigned ap[4], bp; bf16x8 a0[8], b0[8]; };

__device__ __forceinline__ int lane_of(const Ctx& c) {
  unsigned l = c.lane16;
  asm volatile("" : "+v"(l));
  return (int)(l >> 4);
}
__device__ __forceinline__ u32x4 sent4() {
  u32x4 v = {SENT, SENT, SENT, SENT};
  asm volatile("" : "+v"(v));
  return v;
}
__device__ __forceinline__ u32x2 sent2() {
  u32x2 v = {SENT, SENT};
  asm volatile("" : "+v"(v));
  return v;
}
__device__ __forceinline__ u32x4 zero4() {
  u32x4 v = {0u, 0u, 0u, 0u};
  asm volatile("" : "+v"(v));
  return v;
}
__device__ __forceinline__ int g2_wg(int mt, int nt, int ks) { return ((nt * 4 + (mt & 3)) << 3) | (ks * 2 + (mt >> 2)); }
__device__ __forceinline__ int cv_wg(int cw) { return ((24 + (cw & 7)) << 3) | (cw >> 3); }
__device__ __forceinline__ int prep_dg(int b) { return (b >> 3) & 7; }
__device__ __forceinline__ int prep_tt(int b) { return ((b >> 6) << 3) | (b & 7); }
__device__ __forceinline__ int cv_id(const Ctx& c) { return (c.b & 7) * 8 + (c.b >> 3) - 24; }
__device__ __forceinline__ void flag_store(const Ctx& c, bool act, int wg, int slot, unsigned epoch) {
  asm volatile("" : "+v"(epoch));
  __builtin_amdgcn_raw_buffer_store_b64((u32x2){epoch + 1u, FLAG_MAGIC}, c.rs, act ? wg * 512 + slot * 8 : OOB, WS_FLAG, AUX_SC1);
}
__device__ __forceinline__ void publish_h(const Ctx& c, int slot0) {
  const int l = lane_of(c);
  flag_store(c, l >= 8 && l < 14, g2_wg(c.mt, l - 8, c.fq), slot0 + c.nl, c.ev);
}
__device__ __forceinline__ void poll_issue(Ctx& c) {
  const unsigned vo = lane_of(c) * 8;
  const unsigned so = WS_FLAG + c.b * 512;
  asm volatile("s_cmp_lg_u32 %4, 0\n\ts_cbranch_scc1 1f\n\tbuffer_load_dwordx2 %0, %1, %2, %3 offen sc1\n1:"
               : "=v"(c.poll) : "v"(vo), "s"(c.sws), "s"(so), "s"(c.w) : "memory", "scc");
}
__device__ __forceinline__ void poll_wait(Ctx& c, bool need) {
  const unsigned want = (unsigned)__builtin_amdgcn_readlane((int)c.ev, 14) + 1u;
  u32x2 v = c.poll;
  unsigned spin = 0;
#pragma unroll 1
  for (; spin < SPIN_CAP; ++spin) {
    if (__all(!need || (v[0] == want && v[1] == FLAG_MAGIC))) break;
    const unsigned vo = lane_of(c) * 8, so = WS_FLAG + c.b * 512;
    asm volatile("s_sleep 1\n\tbuffer_load_dwordx2 %0, %1, %2, %3 offen sc1\n\ts_waitcnt vmcnt(0)" : "=&v"(v) : "v"(vo), "s"(c.sws), "s"(so) : "memory");
  }
  asm volatile("" ::: "memory");
}
#define POLL_LANDED(N) asm volatile("s_cmp_lg_u32 %2, 0\n\ts_cbranch_scc1 1f\n\ts_waitcnt vmcnt(%1)\n1:" : "+v"(c.poll) : "n"(N), "s"(c.w) : "memory", "scc")

__device__ __forceinline__ int kslice(int p, int r) {
  const int g = (p >= 6) ? 6 : (p >= 3 ? 3 : 0);
  const int v = p - g + r;
  return g + (v >= 3 ? v - 3 : v);
}
template <int sg, int half> __device__ __forceinline__ void k1_issue_w(const Ctx& c) {
  constexpr int dst = k1_wbase(sg) + half * 8192;
  if constexpr (sg < 18) {
    constexpr int p = sg % 9;
    dma16<dst, (p / 3 * 3) * 64 * F_ * 2 + 128 * (sg / 9)>(half ? (c.voff_w ^ 64u) : c.voff_w, half ? c.sw3 : c.sw1, c.wo[p % 3], c.w * 1024);
  } else {
    const unsigned l = lane_of(c);
    const unsigned rp = l >> 1;
    unsigned vo = (half ? (rp ^ 4u) : rp) * (F_ * 2) + (l & 1u) * 16;
    const int col = (1024 + 16 * c.nl - 128 * c.nl) * 2;
    if constexpr (sg < 20) {
      const int ksl = kslice(4 * (sg - 18) + (c.w >> 1), c.rot);
      dma16<dst, 0>(vo, half ? c.sw3 : c.sw1, (64 * ksl + 32 * (c.w & 1)) * (F_ * 2) + col, c.w * 1024);
    } else {
      if (l >= 16) vo = OOB;
      dma16<dst, 6 * 64 * F_ * 2>(vo, half ? c.sw3 : c.sw1, c.wo[2] + col + 8 * c.w * (F_ * 2), c.w * 1024);
    }
  }
}
template <int p, int i> __device__ __forceinline__ void k1_issue_x(const Ctx& c) {
  dma16<K1_XOFF + (p % K1_XD) * 4096 + i * 1024, 8 * i * D_ * 2 + (p / 3 * 3) * 128>((i & 1) ? (c.voff_x ^ 64u) : c.voff_x, c.sx, c.xo[p % 3],
                                                                                     c.w * (K1_XD * 4096));
}
template <int p, int j> __device__ __forceinline__ bf16x8 k1_read_x(const Ctx& c) {
  return *(LAS bf16x8*)(ldsp(c.xl0 ^ (32 * j)) + (p % K1_XD) * 4096);
}
template <int n> __device__ __forceinline__ bf16x8 k1_read_w(Ctx& c) {
  constexpr int sg = K1S[0].seg[n], t = K1S[0].tile[n], j = K1S[0].j[n];
  if constexpr (sg < 18) {
    if constexpr ((sg == 11 || sg == 13) && j == 0) {
      c.rd[t] += sg == 11 ? K1_XOFF : -K1_XOFF;
      asm volatile("" : "+v"(c.rd[t]));
    }
    LAS char* p = ldsp(c.rd[t]) + k1_wbase(sg) % K1_XOFF + (16 * j) * 128;
    return tr_frag(p, p + 4 * 128);
  } else if constexpr (sg < 20) {
    const int off = k1_wbase(sg) + (16 * j) * 32;
    return tr_frag(ldsp(c.rc0) + off, ldsp(c.rc1) + off);
  } else {
    const unsigned ex = (lane_of(c) >> 5) * 768u;
    const int off = k1_wbase(sg) + j * 2048;
    return tr_frag(ldsp(c.rc0 + ex) + off, ldsp(c.rc1 + ex) + off);
  }
}
#define GLD1(dst, voff, sb) asm volatile("global_load_dword %0, %1, %2" : "=v"(dst) : "v"(voff), "s"(sb) : "memory")
#define GLD4(dst, voff, sb) asm volatile("global_load_dwordx4 %0, %1, %2" : "=v"(dst) : "v"(voff), "s"(sb) : "memory")
template <int ctrl> __device__ __forceinline__ float dpp_add(float v) {
  return v + __builtin_bit_cast(float, __builtin_amdgcn_update_dpp(0, __builtin_bit_cast(int, v), ctrl, 0xf, 0xf, true));
}
__device__ __forceinline__ float rb1(float x) { return __builtin_bit_cast(float, pk2(0.f, x)); }
__device__ __forceinline__ f32x2 rb2(f32x2 v) { return (f32x2){rb1(v[0]), rb1(v[1])}; }
template <int k> __device__ __forceinline__ void k1_prep_unit(Ctx& c, K1Prep& P) {
  if constexpr (k == 0) {
    asm volatile("" : "+v"(P.sk), "+v"(P.ga0), "+v"(P.ga1), "+v"(P.gw0), "+v"(P.gw1), "+v"(c.ev)::"memory");
    P.gs = 0.f;
  }
  if constexpr (k < 4) {
    const u32x4 &a = k < 2 ? P.ga1 : P.ga0, &g = k < 2 ? P.gw1 : P.gw0;
#pragma unroll
    for (int i = 2 * (k & 1); i < 2 * (k & 1) + 2; ++i) P.gs = __builtin_fmaf(bfhi(a[i]), bfhi(g[i]), __builtin_fmaf(bflo(a[i]), bflo(g[i]), P.gs));
    if constexpr (k == 1) P.gs = lane_of(c) < 8 ? P.gs : 0.f;
    asm volatile("" : "+v"(P.gs));
  } else if constexpr (k == 4) {
    P.gs = dpp_add<0x111>(P.gs); P.gs = dpp_add<0x112>(P.gs);
    P.u[0] = *(LAS unsigned*)(ldsp(P.lp)); P.u[1] = *(LAS unsigned*)(ldsp(P.lp) + 144);
  } else if constexpr (k == 5) {
    P.gs = dpp_add<0x114>(P.gs); P.gs = dpp_add<0x118>(P.gs);
    P.u[2] = *(LAS unsigned*)(ldsp(P.lp) + 288); P.kn = *(LAS unsigned*)(ldsp(P.lp) + 432);
  } else if constexpr (k == 6) {
    int v = __builtin_bit_cast(int, P.gs);
    float a = __builtin_bit_cast(float, __builtin_amdgcn_readlane(v, 15)), b = __builtin_bit_cast(float, __builtin_amdgcn_readlane(v, 31));
    float d = __builtin_bit_cast(float, __builtin_amdgcn_readlane(v, 47)), e = __builtin_bit_cast(float, __builtin_amdgcn_readlane(v, 63));
    P.gs = __builtin_amdgcn_exp2f(rb1((a + b) + (d + e)) * -1.44269504088896f);
  } else if constexpr (k == 7) {
    float g = rb1(__builtin_amdgcn_rcpf(1.0f + P.gs));
    __builtin_amdgcn_raw_buffer_store_b32(__builtin_bit_cast(unsigned, g), c.rs, lane_of(c) == 0 ? 0u : OOB,
                                          WS_G + (256 * c.mt + 64 * c.fq + 8 * c.nl + c.w) * 4, AUX_SC1);
    P.s[0] = (f32x2){bflo(P.sk[0]), bflo(P.sk[2])};
  } else if constexpr (k == 8) {
    P.s[1] = (f32x2){bfhi(P.sk[0]), bfhi(P.sk[2])};
    P.s[2] = (f32x2){bflo(P.sk[1]), bflo(P.sk[3])}; P.s[3] = (f32x2){bfhi(P.sk[1]), bfhi(P.sk[3])};
    asm volatile("" : "+v"(P.s[0]), "+v"(P.s[1]), "+v"(P.s[2]), "+v"(P.s[3]));
  } else if constexpr (k == 9) {
    P.k2[0] = unpk2(P.u[0]); P.k2[1] = unpk2(P.u[1]); P.k2[2] = unpk2(P.u[2]);
    asm volatile("" : "+v"(P.k2[0]), "+v"(P.k2[1]), "+v"(P.k2[2]));
  } else if constexpr (k < 73) {
    constexpr int i = (k - 10) / 7, st = (k - 10) % 7;
    auto mul = [](f32x2 x, f32x2 y) { f32x2 r = {x[0] * y[0], x[1] * y[1]}; return r; };
    if constexpr (st == 0) { P.a = mul(P.k2[i & 3], P.s[3]); P.p = mul(P.k2[(i + 1) & 3], P.s[2]); P.vn = *(LAS unsigned*)(ldsp(P.lp) + K1_PV + 144 * i); }
    else if constexpr (st == 1) { P.a = rb2(P.a); P.p = rb2(P.p); }
    else if constexpr (st == 2) { P.a += P.p; P.p = mul(P.k2[(i + 2) & 3], P.s[1]); P.k2[(i + 3) & 3] = unpk2(P.kn); }
    else if constexpr (st == 3) {
      P.a = rb2(P.a); P.p = rb2(P.p);
      if constexpr (i < 8) P.kn = *(LAS unsigned*)(ldsp(P.lp) + 144 * (i + 4));
    }
    else if constexpr (st == 4) { P.a += P.p; P.p = mul(P.k2[(i + 3) & 3], P.s[0]); P.q = unpk2(P.vn); }
    else if constexpr (st == 5) { P.a = rb2(P.a); P.p = rb2(P.p); }
    else { P.a = mul(rb2(P.a + P.p), P.q); P.u[i] = pk2(P.a[0], P.a[1]); }
    if constexpr (st == 6) asm volatile("" : "+v"(P.u[i]));
    else if constexpr (st == 2) asm volatile("" : "+v"(P.a), "+v"(P.p), "+v"(P.k2[(i + 3) & 3]));
    else if constexpr (st == 4) asm volatile("" : "+v"(P.a), "+v"(P.p), "+v"(P.q));
    else asm volatile("" : "+v"(P.a), "+v"(P.p));
  } else if constexpr (k == 73) {
    const int l = lane_of(c);
    const int dp = 8 * c.w + (l >> 3);
    P.o0 = dp < 36 ? (unsigned)((72 * prep_dg(c.b) + 2 * dp) * 2 * S_ + (1984 - 64 * prep_tt(c.b)) + 8 * (l & 7)) * 2 : OOB;
  } else {
    constexpr int ch = (k - 74) >> 1, cp = (k - 74) & 1;
    constexpr unsigned sel = ch ? 0x07060302u : 0x05040100u;
    u32x4 o;
#pragma unroll
    for (int e = 0; e < 4; ++e) o[e] = __builtin_amdgcn_perm(P.u[7 + cp - 2 * e - 1], P.u[7 + cp - 2 * e], sel);
    st128(o, c.rs, P.o0, WS_UR + (ch * 2 + cp) * S_ * 2, AUX_SC1);
  }
}
template <int NT, int e> __device__ __forceinline__ void k1_epi_step(const Ctx& c, const f32x16* acc, K1Epi& E, int ft0) {
  constexpr int pr = e / 3, st = e % 3, t = pr / 4, i = 2 * (pr % 4);
  if constexpr (st == 0) {
    if constexpr (pr > 0) { E.h[0] = E.pq[0] * E.r[0]; E.h[1] = E.pq[1] * E.r[1]; }
    if constexpr (pr < 4 * NT) {
      E.m[0] = acc[t][i] * -1.44269504088896f; E.m[1] = acc[t][i + 1] * -1.44269504088896f;
      E.p[0] = acc[t][i] * acc[t][i + 8]; E.p[1] = acc[t][i + 1] * acc[t][i + 9];
      asm volatile("" : "+v"(E.p[0]), "+v"(E.p[1]));
    }
  } else if constexpr (st == 1) {
    if constexpr (pr < 4 * NT) { E.m[0] = __builtin_amdgcn_exp2f(E.m[0]); E.m[1] = __builtin_amdgcn_exp2f(E.m[1]); }
    if constexpr (pr > 0) {
      E.ho[(pr - 1) % 4] = pk2(E.h[0], E.h[1]);
      if constexpr (pr % 4 == 0) st128(E.ho, c.rs, c.lane16, c.hb + (ft0 + (pr - 1) / 4) * 1024, AUX_SC1);
    }
  } else {
    if constexpr (pr < 4 * NT) {
      float d0 = 1.0f + E.m[0], d1 = 1.0f + E.m[1];
      E.r[0] = __builtin_amdgcn_rcpf(d0); E.r[1] = __builtin_amdgcn_rcpf(d1);
      E.pq[0] = E.p[0]; E.pq[1] = E.p[1];
    }
  }
}

template <int ch, int n, int e = 0> __device__ __forceinline__ void k1_epi_at(const Ctx& c, const f32x16* acc, K1Epi& E) {
  if constexpr (n >= K1_EPI0[ch] && n <= k1_epi_slot(ch, 49)) {
    if constexpr (k1_epi_slot(ch, e) == n) k1_epi_step<4, e>(c, acc, E, 8 * c.nl + 4 * ch);
    if constexpr (e < 49 && k1_epi_slot(ch, e < 49 ? e + 1 : e) <= n) k1_epi_at<ch, n, e + 1>(c, acc, E);
  }
}
template <int i> __device__ __forceinline__ void warm_touch(Ctx& c, const u32x4& sw2, const u32x4& ssk) {
  constexpr unsigned long long m = i < 4 ? (0x3full << (6 * (i >> 1))) : (i < 8 ? (0x3full << (16 + 6 * (i - 4))) : (0x1full << (48 + 5 * (i - 8))));
  const u32x4& srd = i < 4 ? ((i & 1) ? c.sw3 : c.sw1) : (i < 8 ? sw2 : ssk);
  asm volatile("s_mov_b32 exec_lo, %3\n\ts_mov_b32 exec_hi, %4\n\tbuffer_load_dword %0, %1, %2, 0 offen\n\ts_mov_b64 exec, -1"
               : "+v"(c.touch) : "v"(c.warm), "s"(srd), "n"((int)(unsigned)m), "n"((int)(unsigned)(m >> 32)) : "memory");
}
__device__ __forceinline__ int g2_ft(int s, int i8) {
  return s >= 8 ? 64 + i8 : (s < 4 ? 16 * s + 8 * (i8 >> 2) + (i8 & 3) : 32 * (s & 1) + 8 * (i8 >> 1) + (i8 & 1) + 4 + (s & 2));
}
template <int s, int i> __device__ __forceinline__ void g2_issue_w(const Ctx& c) {
  const unsigned n16 = 64u * i + (s < 4 ? (unsigned)lane_of(c) : c.xl0);
  const unsigned vo = ((n16 * 171u) >> 11) * (D_ * 2 - 192) + n16 * 16u;
  dma16<K1_XOFF + (s % G2_NSLOT) * G2_SLOT + i * 1024, 0>(vo, c.s2, 16 * g2_ft(s, c.w) * (D_ * 2) + 192 * c.nl, c.w * 3072);
}
template <int i> __device__ __forceinline__ u32x4 g2_load_h(const Ctx& c) {
  return __builtin_amdgcn_raw_buffer_load_b128(c.rs, c.lane16, c.hb + g2_ft(i / 8, i % 8) * 1024, AUX_SC1);
}
__device__ __forceinline__ unsigned g2_out_base(const Ctx& c) {
  return WS_PART + g2_wg(c.mt, c.nl, c.w >> 1) * 49152 + (c.fq * 2 + (c.w & 1)) * 6144;
}
__device__ __forceinline__ unsigned cv_base(int ch) { return ch < 7 ? K1_XOFF + ch * CV_CH : (ch == 7 ? k1_wbase(17) : k1_wbase(18)); }
template <int i> __device__ __forceinline__ void cv_issue(const Ctx& c) {
  const int d = 9 * cv_id(c) + c.w;
  constexpr int lo = i < 4 ? CV_K + 64 + i * 1024 : (i < 8 ? CV_U0 + (i - 4) * 1024 : CV_U1 + (i - 8) * 1024);
  const unsigned lb = c.w < 7 ? K1_XOFF + c.w * CV_CH : LDS_BYTES - 1024 - lo;
  if constexpr (i < 4) dma16<lo, i * 1024>(c.lane16, c.s2, c.w < 7 ? d * (S_ * 2) : OOB, lb);
  else dma16<lo, WS_UR + (i - 4) * 1024>(c.lane16, c.sws, c.w < 7 ? d * (4 * S_) : OOB, lb);
}
template <int ch, int part> __device__ __forceinline__ void cv_issue_shared(const Ctx& c) {
  const int d = 9 * cv_id(c) + ch;
  if constexpr (part == 0) dma16<k1_wbase(10 + ch), WS_UR>(c.lane16, c.sws, d * (4 * S_) + c.w * 1024, c.w < 4 ? CV_U0 + c.w * 1024 : CV_U1 + (c.w - 4) * 1024);
  else dma16<k1_wbase(10 + ch) + CV_K + 64, 0>(c.lane16, c.s2, d * (S_ * 2) + (c.w >> 1) * 1024, (c.w >> 1) * 1024);
}
static_assert(K1_XOFF + 7 * CV_CH <= LDS_BYTES - 1024, "dummy target");
typedef unsigned u32x2a4 __attribute__((ext_vector_type(2), aligned(4)));
__device__ __forceinline__ bf16x8 cv_read_a(LAS char* p) {
  u32x2 lo = *(LAS u32x2a4*)p;
  u32x2 hi = *(LAS u32x2a4*)(p + 8);
  u32x4 v = {lo[0], lo[1], hi[0], hi[1]};
  return __builtin_bit_cast(bf16x8, v);
}
__device__ __forceinline__ int cv_zwg(const Ctx& c, int lane, int& slot) {
  const int cw = cv_id(c);
  const int n0 = (9 * cw) / 96, n1 = (9 * cw + 8) / 96;
  const int s0 = 16 + cw - (96 * n0) / 9, s1 = n1 != n0 ? 16 + cw - (96 * n1) / 9 : -1;
  slot = lane < 32 ? s0 : s1;
  return g2_wg((lane >> 2) & 7, lane < 32 ? n0 : n1, lane & 3);
}

template <int n, int ROLE>
__device__ __forceinline__ void k1_slot(Ctx& c, bf16x8 (&xf)[36], bf16x8 (&wf)[K1_RD + 1], f32x16 (&acc)[4], K1Epi& E, K1Prep& P, u32x4 (&hw)[24],
                                        CvRegs& V) {
  constexpr const K1Sched& S = K1S[ROLE];
  constexpr int sg = S.seg[n];
  if constexpr (n + K1_RD == 288) {
    const unsigned l = lane_of(c);
    unsigned mat = (l >> 4) & 1, h = l >> 5, q = (l & 15) >> 2, p = l & 3;
    unsigned base = mat * 8192 + (8 * h + q) * 32 + p * 8;
    c.rc0 = base + (mat ? 128 : 0);
    c.rc1 = base + (mat ? 0 : 128);
  }
  if constexpr (n + K1_RD < K1_NS) wf[(n + K1_RD) % (K1_RD + 1)] = k1_read_w<n + K1_RD>(c);
  SCHED_FENCE();
  if constexpr (n + 4 < 144) {
    constexpr int sn = S.seg[n + 4];
    if constexpr (S.tile[n + 4] == 0 && sn >= 1) xf[4 * sn + S.j[n + 4]] = k1_read_x<sn, S.j[n + 4]>(c);
  }
  {
    constexpr int t = S.tile[n];
    constexpr int xi = sg < 18 ? 4 * (sg % 9) + S.j[n] : n - 288;
    constexpr bool first = (sg < 18) ? (sg % 9 == 0 && S.j[n] == 0) : (n == 288);
    if constexpr (first) acc[t] = __builtin_amdgcn_mfma_f32_32x32x16_bf16(wf[n % (K1_RD + 1)], xf[xi], (f32x16){}, 0, 0, 0);
    else acc[t] = __builtin_amdgcn_mfma_f32_32x32x16_bf16(wf[n % (K1_RD + 1)], xf[xi], acc[t], 0, 0, 0);
  }
  SCHED_FENCE();
  {
    constexpr int ws0 = [] { for (int s = 0; s < K1_NSEG; ++s) if (K1S[0].wissue[s] == n) return s; return -1; }();
    constexpr int ws1 = [] { for (int s = 0; s < K1_NSEG; ++s) if (K1S[0].wissue[s] >= 0 && K1S[0].wissue[s] + 2 == n) return s; return -1; }();
    constexpr int xs = [] { for (int p = 0; p < 9; ++p) if (K1S[0].xissue[p] >= 0 && n >= K1S[0].xissue[p] && n < K1S[0].xissue[p] + 4) return p; return -1; }();
    if constexpr (ws0 >= 0) k1_issue_w<ws0, 0>(c);
    if constexpr (ws1 >= 0) k1_issue_w<ws1, 1>(c);
    if constexpr (xs >= 0) k1_issue_x<xs, n - S.xissue[xs]>(c);
  }
  if constexpr (n == SMALL_SLOT[0]) GLD4(P.ga0, c.lane16, P.gx);
  if constexpr (n == SMALL_SLOT[1]) GLD4(P.ga1, 1024 + (c.lane16 & 127), P.gx);
  if constexpr (n == SMALL_SLOT[2]) GLD4(P.gw0, c.lane16, P.gw);
  if constexpr (n == SMALL_SLOT[3]) GLD4(P.gw1, 1024 + (c.lane16 & 127), P.gw);
  if constexpr (n == SMALL_SLOT[4]) GLD4(P.sk, P.psk, P.skp);
  if constexpr (n == SMALL_SLOT[5]) {
    const unsigned so = WS_EPOCH;
    asm volatile("buffer_load_dword %0, %1, %2, %3 offen" : "=v"(c.ev) : "v"(P.pev), "s"(c.sws), "s"(so) : "memory");
  }
  if constexpr (n >= K1_PREP0 && n < K1_PREP0 + K1_PREP_UNITS) k1_prep_unit<n - K1_PREP0>(c, P);
  k1_epi_at<0, n>(c, acc, E);
  k1_epi_at<1, n>(c, acc, E);
  {
    constexpr int wi = [] { for (int i = 0; i < WARM_N; ++i) if (WARM_SLOT[i] == n) return i; return -1; }();
    if constexpr (wi >= 0) warm_touch<wi>(c, ROLE == 0 ? c.s2 : c.s3, ROLE == 0 ? c.s3 : c.s2);
    if constexpr (n == WARM_DONE + 1) asm volatile("" : "+v"(c.touch)::"memory");
  }
  if constexpr (n == EV_PUB_U) {
    if (c.w == 1) { const int l = lane_of(c); flag_store(c, l < 8, cv_wg(8 * prep_dg(c.b) + l), prep_tt(c.b), c.ev); }
  }
  if constexpr (n == EV_PUB_A) {
    if (c.w == 1) publish_h(c, 0);
  }
  if constexpr (n == EV_PUB_B1) {
    if (c.w == 1) publish_h(c, 8);
  }
  if constexpr (ROLE == 0) {
    if constexpr (n >= EV_SENT0 && n < EV_SENT0 + 6 * EV_SENT_STEP && (n - EV_SENT0) % EV_SENT_STEP == 0)
      st128(sent4(), c.rs, c.lane16, (c.w >> 1) == c.fq ? OOB : g2_out_base(c) + (n - EV_SENT0) / EV_SENT_STEP * 1024, AUX_SC1);
    if constexpr (n == EV_PUB_S) {
      if (c.w == 2) { const int l = lane_of(c); flag_store(c, (l & 60) == 16, g2_wg(c.mt, c.nl, l & 3), 32 + c.fq, c.ev); }
    }
    if constexpr (n >= EV_PRE0 && n < EV_PRE0 + 12 * EV_PRE_STEP && (n - EV_PRE0) % EV_PRE_STEP == 0)
      g2_issue_w<(n - EV_PRE0) / EV_PRE_STEP / 3, (n - EV_PRE0) / EV_PRE_STEP % 3>(c);
    if constexpr (n == EV_G2_POLL) poll_issue(c);
    if constexpr (n == EV_G2_CHECK) {
      POLL_LANDED(S.ops_after(EV_G2_POLL, EV_G2_CHECK));
      if (c.w == 0) poll_wait(c, lane_of(c) < 8);
    }
    if constexpr (n >= EV_G2_H0 && (n - EV_G2_H0) % 2 == 0 && (n - EV_G2_H0) / 2 < G2_WIN) hw[(n - EV_G2_H0) / 2] = g2_load_h<(n - EV_G2_H0) / 2>(c);
  } else {
    if constexpr (n == EV_CV_POLL) poll_issue(c);
    if constexpr (n == EV_CV_CHECK) {
      POLL_LANDED(K1S[1].ops_after(EV_CV_POLL, EV_CV_CHECK));
      if (c.w == 0) {
        poll_wait(c, lane_of(c) < 32);
        asm volatile("buffer_inv sc1" ::: "memory");
        __builtin_amdgcn_raw_buffer_store_b32(c.ev + 1u, c.rs, lane_of(c) == 14 ? 0u : OOB, WS_EPOCH + c.b * 4, AUX_SC1);
      }
    }
    if constexpr (n == EV_CV_INVW) { if (c.w == 0) wait_vm<S.ops_after(EV_CV_CHECK, EV_CV_INVW)>(); }
    if constexpr (n >= EV_PRE0 && n < EV_PRE0 + 12 * EV_PRE_STEP && (n - EV_PRE0) % EV_PRE_STEP == 0) {
      cv_issue<(n - EV_PRE0) / EV_PRE_STEP>(c);
    }
    if constexpr (n == EV_CV_PRE7 || n == EV_CV_PRE7 + 1) cv_issue_shared<7, n - EV_CV_PRE7>(c);
    if constexpr (n == EV_CV_PRE7 + 3) {
      const int l = lane_of(c);
      const unsigned cb = cv_base(c.w);
      if (l < 60) {
        int o = l < 4 ? CV_K + l * 16 : (l < 32 ? CV_U0 + 4096 + (l - 4) * 16 : CV_U1 + 4096 + (l - 32) * 16);
        *(LAS u32x4*)(ldsp(cb) + o) = zero4();
      }
    }
    if constexpr (n == EV_CV_PRE7 + 8) {
      c.dptr = karg(5);
      asm volatile("" : "+s"(c.dptr));
      const int l = lane_of(c);
      const unsigned cb = cv_base(c.w);
      const int rho = l & 15, q = l >> 4, pi = (l >> 3) & 1, ii = l & 7;
      V.bp = cb + CV_U0 + pi * 4640 + 2 * (2016 - 32 * ii + 8 * q) - 64 * 56;
#pragma unroll
      for (int i = 0; i < 4; ++i) { V.ap[i] = cb + CV_K + 4 + 4 * rho + 16 * q + 1024 * i; asm volatile("" : "+v"(V.ap[i])); }
    }
    if constexpr (n == EV_CV_PRE8 || n == EV_CV_PRE8 + 1) cv_issue_shared<8, n - EV_CV_PRE8>(c);
    if constexpr (n == EV_CV_PRE8 + 1) {
      if (c.w == 6) {
        const int l = lane_of(c);
        if (l < 60) {
          int o = l < 4 ? CV_K + l * 16 : (l < 32 ? CV_U0 + 4096 + (l - 4) * 16 : CV_U1 + 4096 + (l - 32) * 16);
          *(LAS u32x4*)(ldsp(k1_wbase(18)) + o) = zero4();
        }
      }
    }
    if constexpr (n >= 320) {
#pragma unroll
      for (int f = 2 * (n - 320); f < 2 * (n - 320) + 2; ++f) {
        V.b0[f] = *(LAS bf16x8*)(ldsp(V.bp) + 64 * (56 - 8 * f));
        V.a0[f] = cv_read_a(ldsp(V.ap[f >> 1]) + 512 * (f & 1));
      }
    }
  }
  SCHED_FENCE();
  if constexpr (S.bar[n]) {
    wait_vm_barrier<S.wait[n]>();
    SCHED_FENCE();
  }
  if constexpr (n + 1 < K1_NS) k1_slot<n + 1, ROLE>(c, xf, wf, acc, E, P, hw, V);
}

constexpr int g2_ks(int n) { return n < 168 ? n / 3 : 56 + (n - 168) % 16; }
constexpr int g2_tl(int n) { return n < 168 ? n % 3 : (n - 168) / 16; }
constexpr int G2_HW = 24;
template <int n> __device__ __forceinline__ bf16x8 g2_read_w(unsigned rd) {
  constexpr int i = g2_ks(n), t = g2_tl(n), s = i / 8, j = i % 8;
  LAS char* p = ldsp(rd) + (s % G2_NSLOT) * G2_SLOT + (16 * j) * 192 + t * 64;
  return tr_frag(p, p + 8 * 192);
}
static_assert(k1_epi_slot(1, 49) < K1_NS, "chunk B epilogue ends in phase 1");
constexpr int G2_TAIL0 = 2, G2_TAILST = G2_TAIL0 + 13;
constexpr int G2_PUBB = 19, G2_PUBC = 43;
constexpr int G2_CHECK0 = 39, G2_LEAD = 18;
constexpr bool g2_check(int n) { return n >= G2_CHECK0 && n <= G2_CHECK0 + 120 && (n - G2_CHECK0) % 24 == 0; }
constexpr int G2_PST = 186;
constexpr int G2_PRE3 = 206;
constexpr int g2_hslot(int i) {
  return i < 64 ? 3 * (i - G2_WIN) + 2 : 144 + (i - 64) + (i - 64) / 2;
}
constexpr int g2_hload_at(int n) { for (int i = G2_WIN; i < 72; ++i) if (g2_hslot(i) == n) return i; return -1; }
constexpr bool g2_bar(int n) { return (n % 24 == 24 - G2_PF - 1 && n / 24 < 7) || n == 176 - G2_PF - 1; }
constexpr int g2_ops_in_slot(int n) {
  int k = 0;
  for (int s = 1; s + 3 < 9; ++s) for (int i = 0; i < 3; ++i) if (n == 24 * s + 24 - G2_PF + 1 + 3 * i) ++k;
  if (g2_hload_at(n) >= 0) ++k;
  if (n == G2_TAILST) ++k;
  if (n == G2_PST || n == G2_PST + 2 || n == G2_PST + 16 || n == G2_PST + 18) ++k;
  return k;
}
constexpr int g2_ops_after(int a, int b) { int k = 0; for (int n = a + 1; n <= b; ++n) k += g2_ops_in_slot(n); return k; }
static_assert([] { for (int i = G2_WIN; i + 1 < 72; ++i) if (g2_hslot(i) >= g2_hslot(i + 1) || (i >= G2_HW && g2_hslot(i) < 3 * (i - G2_HW) + 2)) return false; return g2_hslot(64) > G2_CHECK0 + 100; }(), "h load slots");
static_assert(g2_bar(G2_PUBB) && g2_bar(G2_PUBC) && g2_bar(G2_CHECK0 + 4) && g2_hslot(32) > G2_CHECK0 + 4 && G2_PRE3 > G2_CHECK0 + 124 && G2_LEAD < 23, "events");
struct G2Fin { u32x4 zz[2], qq[2]; float g; };
__device__ __forceinline__ void g2_send(const Ctx& c, const f32x16& a, int t, int p2) {
  const bool own = (c.w >> 1) == c.fq;
  const u32x4 o = {pk2(a[8 * p2], a[8 * p2 + 1]), pk2(a[8 * p2 + 2], a[8 * p2 + 3]), pk2(a[8 * p2 + 4], a[8 * p2 + 5]), pk2(a[8 * p2 + 6], a[8 * p2 + 7])};
  st128(o, c.rs, c.lane16, own ? OOB : g2_out_base(c) + (t * 2 + p2) * 1024, AUX_SC1);
  if (own) {
#pragma unroll
    for (int qd = 2 * p2; qd < 2 * p2 + 2; ++qd)
      *(LAS f32x4*)(ldsp(c.lane16) + ((c.w & 1) * 12 + t * 4 + qd) * 1024) = (f32x4){a[4 * qd], a[4 * qd + 1], a[4 * qd + 2], a[4 * qd + 3]};
  }
}
__device__ __forceinline__ void g2_fin_load(const Ctx& c, G2Fin& Fn, bool all) {
  const int lane = lane_of(c);
  const int uh = (c.w / 3) & 1, ut = c.w % 3;
  const int t0 = 256 * c.mt + 64 * c.fq + 32 * uh, d0 = 96 * c.nl + 32 * ut;
#pragma unroll
  for (int i = 0; i < 2; ++i)
    Fn.zz[i] = __builtin_amdgcn_raw_buffer_load_b128(c.rs, ((16 * i + (lane >> 2)) * S_) * 2 + (lane & 3) * 16, WS_Z + (d0 * S_ + t0) * 2, AUX_SC1);
  if (all) {
    const char* qptr = karg(0);
#pragma unroll
    for (int i = 0; i < 2; ++i) Fn.qq[i] = *(const u32x4*)(qptr + ((size_t)(t0 + (lane & 31)) * D_ + d0 + 16 * (lane >> 5) + 8 * i) * 2);
    Fn.g = __builtin_bit_cast(float, __builtin_amdgcn_raw_buffer_load_b32(c.rs, (lane & 31) * 4, WS_G + t0 * 4, AUX_SC1));
  }
}
template <int n>
__device__ __forceinline__ void g2_slot(Ctx& c, unsigned rd, u32x4 (&hw)[24], bf16x8 (&wf)[G2_PF + 1], f32x16 (&acc)[3], const f32x16* tailacc, K1Epi& E,
                                        G2Fin& Fn) {
  constexpr int i = g2_ks(n), t = g2_tl(n), s = n / 24;
  if constexpr (n + G2_PF < G2_NS) wf[(n + G2_PF) % (G2_PF + 1)] = g2_read_w<n + G2_PF>(rd);
  if constexpr (i == 0) acc[t] = __builtin_amdgcn_mfma_f32_32x32x16_bf16(wf[n % (G2_PF + 1)], __builtin_bit_cast(bf16x8, hw[i % G2_HW]), (f32x16){}, 0, 0, 0);
  else acc[t] = __builtin_amdgcn_mfma_f32_32x32x16_bf16(wf[n % (G2_PF + 1)], __builtin_bit_cast(bf16x8, hw[i % G2_HW]), acc[t], 0, 0, 0);
  SCHED_FENCE();
  if constexpr (n >= G2_TAIL0 && n <= G2_TAILST) k1_epi_step<1, n - G2_TAIL0>(c, tailacc, E, 64 + c.nl);
  {
    constexpr int m = n - (24 - G2_PF + 1);
    if constexpr (m >= 24 && m % 24 < 9 && m % 3 == 0 && m / 24 + 3 < 9) g2_issue_w<m / 24 + 3, (m % 24) / 3>(c);
  }
  {
    constexpr int hl = g2_hload_at(n);
    if constexpr (hl >= 0) hw[hl % G2_HW] = g2_load_h<hl>(c);
  }
  if constexpr (n == G2_PST || n == G2_PST + 2) g2_send(c, acc[0], 0, (n - G2_PST) / 2);
  if constexpr (n == G2_PST + 16 || n == G2_PST + 18) g2_send(c, acc[1], 1, (n - G2_PST - 16) / 2);
  if constexpr (n == G2_PRE3) {
    g2_fin_load(c, Fn, true);
  }
  if constexpr (g2_check(n + G2_LEAD)) poll_issue(c);
  if constexpr (g2_check(n)) {
    constexpr int k = (n - G2_CHECK0) / 24;
    POLL_LANDED(g2_ops_after(n - G2_LEAD, n));
    if (c.w == 0) {
      const int l = lane_of(c);
      if constexpr (k == 0) poll_wait(c, (l & 60) == 8);
      if constexpr (k == 1) poll_wait(c, (l & 60) == 12);
      if constexpr (k == 2) poll_wait(c, (l & 60) == 48);
      if constexpr (k == 3) poll_wait(c, (l & 60) == 52);
      if constexpr (k == 4) poll_wait(c, (l & 56) == 40);
      if constexpr (k == 5) {
        poll_wait(c, (l >= 16 && l <= 16 + (96 * c.nl + 95) / 9 - (96 * c.nl) / 9) || (l & 60) == 32);
        __builtin_amdgcn_raw_buffer_store_b32(c.ev + 1u, c.rs, l == 14 ? 0u : OOB, WS_EPOCH + c.b * 4, AUX_SC1);
      }
    }
  }
  SCHED_FENCE();
  if constexpr (g2_bar(n)) {
    constexpr int wt = n == G2_PUBB ? K1S[0].ops_after(k1_epi_slot(1, 49), K1_NS - 1) + g2_ops_after(-1, n)
                                    : (n == G2_PUBC ? g2_ops_after(G2_TAILST, n) : (s + 1 < 4 ? 63 : g2_ops_after(24 * (s - 2) + 24 - G2_PF + 1 + 6, n)));
    wait_vm_barrier<cmin(63, wt)>();
    if constexpr (n == G2_PUBB) { if (c.w == 1) publish_h(c, 48); }
    if constexpr (n == G2_PUBC) { if (c.w == 1) publish_h(c, 40); }
    SCHED_FENCE();
  }
  if constexpr (n + 1 < G2_NS) g2_slot<n + 1>(c, rd, hw, wf, acc, tailacc, E, Fn);
}
__device__ __forceinline__ unsigned umax4(const u32x4& v) {
  unsigned a = v[0] > v[1] ? v[0] : v[1], b = v[2] > v[3] ? v[2] : v[3];
  return a > b ? a : b;
}

__device__ __forceinline__ void g2_phase(Ctx& c, u32x4 (&hw)[24], const f32x16* tailacc, K1Epi& E) {
  const int lane = lane_of(c);
  const int w = c.w, ks = c.fq, nt = c.nl, mt = c.mt;
  c.xl0 = lane;
  unsigned rd;
  {
    int gq = lane >> 4, chf = gq & 1, h = gq >> 1, q = (lane & 15) >> 2, p = lane & 3;
    rd = K1_XOFF + (4 * h + q) * 192 + chf * 32 + p * 8;
  }
  bf16x8 wf[G2_PF + 1];
  f32x16 acc[3];
  wf[0] = g2_read_w<0>(rd); wf[1] = g2_read_w<1>(rd); wf[2] = g2_read_w<2>(rd); wf[3] = g2_read_w<3>(rd);
  SCHED_FENCE();
  G2Fin Fn;
  g2_slot<0>(c, rd, hw, wf, acc, tailacc, E, Fn);
  g2_send(c, acc[2], 2, 0);
  g2_send(c, acc[2], 2, 1);
  const int uh = w / 3, ut = w % 3;
  const int qp = lane >> 5, cc = lane & 31;
  lds_barrier();
  if (w < 6) {
    char* optr = (char*)karg(11);
    u32x4 pp[3][2];
    float ga[16], og = 0.f;
    f32x4 own[2][2];
    bool zdone = false;
    unsigned spin = 0;
#pragma unroll 1
    for (; spin < SPIN_CAP; ++spin) {
      asm volatile("" ::: "memory");
      const bool zok = __all(umax4(Fn.zz[0]) != SENT && umax4(Fn.zz[1]) != SENT) || spin == SPIN_CAP - 1;
      if (!zok) g2_fin_load(c, Fn, false);
#pragma unroll
      for (int s = 0; s < 3; ++s) {
        const int src = s + (s >= ks ? 1 : 0);
#pragma unroll
        for (int h = 0; h < 2; ++h)
          pp[s][h] = __builtin_amdgcn_raw_buffer_load_b128(c.rs, qp * 1024 + (cc + 32 * h) * 16, WS_PART + c.b * 49152 + (src * 2 + uh) * 6144 + ut * 2048, AUX_SC1);
      }
      SCHED_FENCE();
      if (zok && !zdone) {
        zdone = true;
        LAS char* zt = ldsp(24576 + w * 2560);
#pragma unroll
        for (int i = 0; i < 2; ++i) *(LAS u32x4*)(zt + (16 * i + (lane >> 2)) * 80 + (lane & 3) * 16) = Fn.zz[i];
        unsigned zv[16];
#pragma unroll
        for (int j = 0; j < 16; ++j) zv[j] = *(LAS u16*)(zt + (16 * qp + j) * 80 + ((cc & 24) + 4 * (cc & 1) + ((cc & 7) >> 1)) * 2);
#pragma unroll
        for (int o = 0; o < 2; ++o)
#pragma unroll
          for (int h = 0; h < 2; ++h) own[o][h] = *(LAS f32x4*)(ldsp((cc + 32 * h) * 16) + (uh * 12 + ut * 4 + 2 * qp + o) * 1024);
        og = rbf(1.0f - Fn.g);
#pragma unroll
        for (int j = 0; j < 16; j += 2) {
          const unsigned qw = Fn.qq[j >> 3][(j >> 1) & 3];
          const unsigned a2 = pk2(bflo(qw) * bflo(zv[j]), bfhi(qw) * bflo(zv[j + 1]));
          ga[j] = Fn.g * bflo(a2); ga[j + 1] = Fn.g * bfhi(a2);
        }
      }
      SCHED_FENCE();
      u32x4 mx = {umax4(pp[0][0]), umax4(pp[0][1]), umax4(pp[1][0]), umax4(pp[1][1])};
      if (zdone && __all(umax4(mx) != SENT && umax4(pp[2][0]) != SENT && umax4(pp[2][1]) != SENT)) break;
    }
#pragma unroll
    for (int o = 0; o < 2; ++o) {
      float res[8];
#pragma unroll
      for (int h = 0; h < 2; ++h)
#pragma unroll
        for (int k = 0; k < 4; k += 2) {
          f32x2 bs = {own[o][h][k], own[o][h][k + 1]};
#pragma unroll
          for (int s = 0; s < 3; ++s) bs += unpk2(pp[s][h][2 * o + (k >> 1)]);
          const f32x2 b2 = unpk2(pk2(bs[0], bs[1]));
          res[4 * h + k] = radd(ga[8 * o + 4 * h + k], og * b2[0]);
          res[4 * h + k + 1] = radd(ga[8 * o + 4 * h + k + 1], og * b2[1]);
        }
      u32x4 ov = {pk2(res[0], res[1]), pk2(res[2], res[3]), pk2(res[4], res[5]), pk2(res[6], res[7])};
      *(u32x4*)(optr + ((size_t)(256 * mt + 64 * ks + 32 * uh + cc) * D_ + 96 * nt + 32 * ut + 16 * qp + 8 * o) * 2) = ov;
    }
  }
}

template <bool PREF, bool FIRST, class Hook>
__device__ __forceinline__ void cv_block(f32x4 (&acc)[8], const bf16x8 (&af)[8], const bf16x8 (&bf)[8], bf16x8 (&naf)[8], bf16x8 (&nbf)[8],
                                         const unsigned (&ap)[4], unsigned bp, int roff, Hook&& hook) {
  auto step = [&](auto K) {
    constexpr int k = decltype(K)::value;
    constexpr int p = [] { int r = k, q = 0; while (r >= 8 - q) { r -= 8 - q; ++q; } return q; }();
    constexpr int dl = [] { int r = k, q = 0; while (r >= 8 - q) { r -= 8 - q; ++q; } return r; }();
    if constexpr (PREF && (k & 1) == 0 && k < 32) {
      constexpr int f = k >> 2;
      if constexpr ((k & 2) != 0) naf[f] = cv_read_a(ldsp(ap[f >> 1]) + 512 * (f & 1) + roff);
      else nbf[f] = *(LAS bf16x8*)(ldsp(bp) + 64 * (56 - 8 * f) + roff);
    }
    if constexpr (FIRST && p == 0) asm volatile("v_mfma_f32_16x16x32_bf16 %0, %1, %2, 0" : "=&v"(acc[dl]) : "v"(af[p]), "v"(bf[dl]));
    else asm volatile("v_mfma_f32_16x16x32_bf16 %0, %1, %2, %0" : "+v"(acc[p + dl]) : "v"(af[p]), "v"(bf[dl]));
    if constexpr (k & 1) { SCHED_FENCE(); hook(K); SCHED_FENCE(); }
  };
  [&]<int... Ks>(std::integer_sequence<int, Ks...>) { (step(std::integral_constant<int, Ks>{}), ...); }(std::make_integer_sequence<int, 36>{});
}
__device__ __forceinline__ u32x4 cv_u(LAS char* cl, int g, int lane) {
  const int q = lane >> 4, pi = (lane >> 3) & 1, ii = lane & 7;
  return *(LAS u32x4*)(cl + (pi ? CV_U1 : CV_U0) + 2 * (2040 - (256 * g + 32 * ii + 8 * q)));
}
__device__ __forceinline__ f32x4 cv_ud(const u32x4& uu, float Dv) {
  return (f32x4){rb1(bflo(uu[3]) * Dv), rb1(bflo(uu[2]) * Dv), rb1(bflo(uu[1]) * Dv), rb1(bflo(uu[0]) * Dv)};
}
__device__ __forceinline__ u32x2 cv_z(const f32x4& y, const f32x4& ud) {
  return (u32x2){pk2(rb1(y[0]) + ud[0], rb1(y[1]) + ud[1]), pk2(rb1(y[2]) + ud[2], rb1(y[3]) + ud[3])};
}
__device__ __forceinline__ void cv_phase(Ctx& c, const f32x16* tailacc, K1Epi& E, CvRegs& V) {
  const int w = c.w;
  f32x4 acc[8], acc8[8];
  bf16x8 a1[8], b1[8];
  auto none = [](auto) {};
  cv_block<true, true>(acc, V.a0, V.b0, a1, b1, V.ap, V.bp, 64, [&](auto K) {
    constexpr int k = decltype(K)::value / 2;
    if constexpr (k >= G2_TAIL0 && k <= G2_TAILST) k1_epi_step<1, k - G2_TAIL0>(c, tailacc, E, 64 + c.nl);
  });
  wait_vm_barrier<1>();
  if (w == 1) publish_h(c, 48);
  const int lane = lane_of(c);
  const unsigned zb = WS_Z + 9 * cv_id(c) * (S_ * 2);
  const unsigned cb = cv_base(w), cb8 = cv_base(8);
  int zsl, zwg;
  unsigned zev, dd, ap8[4];
  SCHED_FENCE();
  cv_block<true, false>(acc, a1, b1, V.a0, V.b0, V.ap, V.bp, 128, [&](auto K) {
    constexpr int k = decltype(K)::value / 2;
    if constexpr (k == 1 || k == 5 || k == 9 || k == 13) st128(sent4(), c.rs, c.lane16, zb + w * (S_ * 2) + (k / 4) * 1024, AUX_SC1);
    if constexpr (k == 17) __builtin_amdgcn_raw_buffer_store_b64(sent2(), c.rs, lane * 8, zb + 8 * (S_ * 2) + 512 * w, AUX_SC1);
    if constexpr (k == 3) { zwg = cv_zwg(c, lane, zsl); zev = __builtin_amdgcn_raw_buffer_load_b32(c.rs, zwg * 4, WS_EPOCH, 0); }
    if constexpr (k == 7) dd = *(const u16*)(c.dptr + (9 * cv_id(c) + ((lane & 1) ? 8 : w)) * 2);
    if constexpr (k == 11) {
#pragma unroll
      for (int i = 0; i < 4; ++i) { ap8[i] = V.ap[i] + (cb8 - cb + 64 * w); asm volatile("" : "+v"(ap8[i])); }
    }
  });
  cv_block<true, false>(acc, V.a0, V.b0, a1, b1, ap8, V.bp + (cb8 - cb + 64 * w), 0, none);
  cv_block<true, true>(acc8, a1, b1, V.a0, V.b0, V.ap, V.bp, 192, none);
  LAS char* sc = ldsp(((w & 2) ? k1_wbase(20) : k1_wbase(19)) + (w & 1) * 8192 + lane * 16);
  cv_block<true, false>(acc, V.a0, V.b0, a1, b1, V.ap, V.bp, 256, [&](auto K) {
    constexpr int k = decltype(K)::value / 2;
    if constexpr (k >= 4 && k < 12) { if (w >= 4) *(LAS f32x4*)(sc + (k - 4) * 1024) = acc8[k - 4]; }
  });
  asm volatile("s_waitcnt vmcnt(0) lgkmcnt(0)\n\ts_barrier" ::: "memory");
  if (w == 5) publish_h(c, 40);
  if (w == 6) flag_store(c, zsl >= 0, zwg, zsl, zev);
  SCHED_FENCE();
  f32x4 o8[2];
  cv_block<true, false>(acc, a1, b1, V.a0, V.b0, V.ap, V.bp, 320, [&](auto K) {
    constexpr int k = decltype(K)::value / 2;
    if (w < 4) {
      if constexpr (k >= 2 && k < 18 && (k & 1) == 0) *(LAS f32x4*)(sc + (k / 2 - 1) * 1024) = acc8[k / 2 - 1] + o8[(k / 2 - 1) & 1];
      if constexpr (k < 16 && (k & 1) == 0) o8[(k / 2) & 1] = *(LAS f32x4*)(sc + (k / 2) * 1024);
    }
  });
  lds_barrier();
  const float Dv = bflo((unsigned)__builtin_amdgcn_readlane((int)dd, 0)), Dv8 = bflo((unsigned)__builtin_amdgcn_readlane((int)dd, 1));
  const int zo = (32 * (lane & 7) + 8 * (lane >> 4) + 4 * ((lane >> 3) & 1)) * 2;
  f32x4 y4[4], ud[8];
  u32x4 uu[2];
  SCHED_FENCE();
  cv_block<true, false>(acc, V.a0, V.b0, a1, b1, V.ap, V.bp, 384, [&](auto K) {
    constexpr int k = decltype(K)::value / 2;
    LAS char* s0 = ldsp(w * 1024 + lane * 16);
    if constexpr (k < 4) y4[k] = *(LAS f32x4*)(s0 + ((k & 2) ? k1_wbase(20) : k1_wbase(19)) + (k & 1) * 8192);
    if constexpr (k == 4) uu[0] = cv_u(ldsp(cb8), w, lane);
    if constexpr (k == 8) { y4[0] = (y4[0] + y4[1]) + (y4[2] + y4[3]); asm volatile("" : "+v"(y4[0])); }
    if constexpr (k == 12) __builtin_amdgcn_raw_buffer_store_b64(cv_z(y4[0], cv_ud(uu[0], Dv8)), c.rs, zo, zb + 8 * (S_ * 2) + 512 * w, AUX_SC1);
  });
  cv_block<true, false>(acc, a1, b1, V.a0, V.b0, V.ap, V.bp, 448, [&](auto K) {
    constexpr int k = decltype(K)::value / 2;
    if constexpr (k >= 2 && (k & 1) == 0) { ud[k / 2 - 1] = cv_ud(uu[(k / 2 - 1) & 1], Dv); asm volatile("" : "+v"(ud[k / 2 - 1])); }
    if constexpr (k < 16 && (k & 1) == 0) uu[(k / 2) & 1] = cv_u(ldsp(cb), k / 2, lane);
  });
  cv_block<false, false>(acc, V.a0, V.b0, a1, b1, V.ap, V.bp, 0, [&](auto K) {
    constexpr int k = decltype(K)::value;
    constexpr int g = k == 5 ? 0 : (k == 13 ? 1 : (k == 19 ? 2 : (k == 25 ? 3 : (k == 31 ? 4 : (k == 35 ? 5 : -1)))));
    if constexpr (g >= 0) {
      asm volatile("s_nop 3" : "+v"(acc[g]));
      __builtin_amdgcn_raw_buffer_store_b64(cv_z(acc[g], ud[g]), c.rs, zo, zb + w * (S_ * 2) + 512 * g, AUX_SC1);
    }
  });
  asm volatile("s_nop 7\n\ts_nop 1" : "+v"(acc[6]), "+v"(acc[7]));
#pragma unroll
  for (int g = 6; g < 8; ++g) __builtin_amdgcn_raw_buffer_store_b64(cv_z(acc[g], ud[g]), c.rs, zo, zb + w * (S_ * 2) + 512 * g, AUX_SC1);
}

template <int ROLE>
__device__ __forceinline__ void fused_body(const int w, const unsigned long long pc) {
  const int b = (blockIdx.x + 192) & 255, xcd = b & 7, r = b >> 3;
  const int ml = r & 3, nl = r >> 2, fq = xcd >> 1;
  const int mt = 4 * (xcd & 1) + ml;
  const int lane = __builtin_amdgcn_mbcnt_hi(~0u, __builtin_amdgcn_mbcnt_lo(~0u, 0u));
  Ctx c;
  c.w = w; c.nl = nl; c.ml = ml; c.b = b; c.mt = mt; c.fq = fq;
  const char* ka[11];
  {
    typedef const char* cptr;
    const __attribute__((address_space(4))) cptr* p = (const __attribute__((address_space(4))) cptr*)__builtin_amdgcn_kernarg_segment_ptr();
    asm volatile("" : "+s"(p));
#pragma unroll
    for (int i = 0; i < 11; ++i) ka[i] = p[i];
    SCHED_FENCE();
  }
  c.rot = (nl + ml) % 3;
  c.lane16 = lane * 16;
  asm volatile("" : "+v"(c.lane16));
#pragma unroll
  for (int i = 0; i < 3; ++i) { const int k = kslice(i, c.rot); c.wo[i] = k * (64 * F_ * 2); c.xo[i] = k * 128; }
  {
    int row = lane >> 3, pos = lane & 7;
    c.voff_w = (8 * w + row) * (F_ * 2) + ((pos ^ (2 * ((row >> 1) & 1))) * 16);
    c.voff_x = row * (D_ * 2) + ((pos ^ (lane >> 4)) * 16);
  }
  asm volatile("" : "+v"(c.voff_w), "+v"(c.voff_x), "+s"(c.wo[0]), "+s"(c.xo[0]), "+s"(c.wo[1]));
  SCHED_FENCE();
  asm volatile("" : "+s"(ka[0]), "+s"(ka[1]), "+s"(ka[2]), "+s"(ka[3]), "+s"(ka[4]), "+s"(ka[6]), "+s"(ka[7]), "+s"(ka[8]), "+s"(ka[9]), "+s"(ka[10]));
  c.sw1 = make_srd(ka[6], (1152 * fq + 128 * nl) * 2, D_ * F_ * 2);
  c.sw3 = make_srd(ka[7], (1152 * fq + 128 * nl) * 2, D_ * F_ * 2);
  c.sx = make_srd(ka[0], (256 * mt + 32 * w) * (D_ * 2), S_ * D_ * 2);
  k1_issue_w<0, 0>(c); k1_issue_w<0, 1>(c);
  k1_issue_x<0, 0>(c); k1_issue_x<0, 1>(c); k1_issue_x<0, 2>(c); k1_issue_x<0, 3>(c);
  {
    unsigned e0, e1;
    asm volatile("s_mov_b32 %0, .Lgm_code_end0-.Lgm_code_start\n\ts_mov_b32 %1, .Lgm_code_end1-.Lgm_code_start" : "=s"(e0), "=s"(e1));
    const u32x4 sc = make_srd((const char*)pc, 0, e0 > e1 ? e0 : e1);
    const unsigned vo = lane < 3 ? (unsigned)(8 * r + w + 256 * lane) * 128 : OOB;
    asm volatile("buffer_load_dword %0, %1, %2, 0 offen" : "=v"(c.touch) : "v"(vo), "s"(sc) : "memory");
  }
  k1_issue_w<1, 0>(c); k1_issue_w<1, 1>(c);
  SCHED_FENCE();
  c.sws = make_srd(ka[10], 0, WS_END);
  c.rs = __builtin_amdgcn_make_buffer_rsrc((void*)ka[10], 0, WS_END, 0x00020000);
  c.hb = WS_H + ((8 * mt + w) * 288 + 72 * fq) * 1024;
  {
    int gq = lane >> 4, mat = gq & 1, h = gq >> 1, q = (lane & 15) >> 2, p = lane & 3;
    int X = 2 * (q >> 1) + 4 * mat;
#pragma unroll
    for (int t = 0; t < 4; ++t) c.rd[t] = mat * 8192 + (8 * h + q) * 128 + (((2 * t) ^ X) + (p >> 1)) * 16 + (p & 1) * 8;
    int cc = lane & 31, hh = lane >> 5;
    c.xl0 = K1_XOFF + w * (K1_XD * 4096) + cc * 128 + ((hh ^ ((cc >> 1) & 7)) * 16);
  }
  {
    const u32x4 sw2 = make_srd(ka[8], 1152 * fq * D_ * 2, F_ * D_ * 2), ssk = make_srd(ka[4], 0, D_ * S_ * 2);
    c.s2 = ROLE == 0 ? sw2 : ssk;
    c.s3 = ROLE == 0 ? ssk : sw2;
    const unsigned mh = xcd & 1, i = 8 * r + w + 256 * (lane < 16 ? lane : (lane < 48 ? lane - 16 : lane - 48));
    const unsigned r1 = (i * 6554u) >> 16, c1 = i - 10 * r1;
    const int o1 = (int)((2 * r1 + mh) * (F_ * 2) + (c1 < 8 ? 2 * c1 + 1 : c1 + 8) * 128) - 256 * nl;
    const unsigned r2 = (i * 7282u) >> 16, o2 = (2 * r2 + mh) * (D_ * 2) + (i - 9 * r2) * 128;
    unsigned o = 72 * xcd * (S_ * 2) + i * 128;
    o = lane < 48 ? (i < 5184 ? o2 : OOB) : o;
    o = lane < 16 ? ((i < 2880 && o1 >= 0) ? (unsigned)o1 : OOB) : o;
    c.warm = lane < 57 ? o : OOB;
    asm volatile("" : "+v"(c.warm));
  }
  K1Prep P;
  {
    const u32x4 sk_ = make_srd(ka[1], 0, S_ * D_ * 2), sv_ = make_srd(ka[2], 0, S_ * D_ * 2);
    auto voff = [&](int blk, int halo, bool on) {
      const unsigned pc = (unsigned)(64 * blk + lane), row = (pc * 7282u) >> 16;
      const int t = 64 * prep_tt(b) - halo + (int)row;
      const unsigned o = (unsigned)t * (D_ * 2) + (pc - 9 * row) * 16 + 144 * prep_dg(b);
      return (on && t >= 0 && (int)row < 64 + halo) ? o : OOB;
    };
    const bool isv = (w & 2) != 0;
    const u32x4 sd = {isv ? sv_[0] : sk_[0], isv ? sv_[1] : sk_[1], sk_[2], sk_[3]};
    unsigned v0 = voff(w, 4, true), v1 = voff(w, 1, true), v2 = voff(8 + (w & 1), isv ? 1 : 4, w < 4);
    const int e0 = cv_wg(8 * prep_dg(b) + lane), e1 = g2_wg(mt, lane - 8, fq), e2 = g2_wg(mt, nl < 6 ? nl : 0, lane & 3);
    int ewg = b;
    ewg = (lane & 60) == 16 ? e2 : ewg;
    ewg = lane < 14 ? e1 : ewg;
    ewg = (lane < 8 ? e0 : ewg) * 4;
    asm volatile("" : "+v"(v0), "+v"(v1), "+v"(v2), "+v"(ewg), "+v"(c.rd[0]), "+v"(c.rd[1]), "+v"(c.rd[2]), "+v"(c.rd[3]), "+v"(c.xl0));
    SCHED_FENCE();
    dma16<K1_POFF, 0>(v0, sk_, 0u, w * 1024);
    dma16<K1_POFF + K1_PV, 0>(v1, sv_, 0u, w * 1024);
    dma16<K1_POFF, 0>(v2, sd, 0u, w < 4 ? (8 + (w & 1)) * 1024 + (isv ? K1_PV : 0) : 20480 + w * 1024);
    P.pev = ewg;
    const char* gx = ka[0] + (size_t)(256 * mt + 64 * fq + 8 * nl + w) * (D_ * 2);
    const int dp = 8 * w + (lane >> 3), dc = dp < 36 ? dp : 35;
    P.lp = K1_POFF + 8 * (7 - (lane & 7)) * 144 + dc * 4;
    asm volatile("" : "+v"(P.lp));
    P.psk = (unsigned)(72 * prep_dg(b) + 2 * dc) * 8; P.skp = ka[3]; P.gw = ka[9]; P.gx = gx;
  }
  bf16x8 xf[36];
  bf16x8 wf[K1_RD + 1];
  f32x16 acc[4];
  u32x4 hw[24];
  wait_vm_barrier<6>();
  k1_issue_x<1, 0>(c); k1_issue_x<1, 1>(c); k1_issue_x<1, 2>(c); k1_issue_x<1, 3>(c);
  xf[0] = k1_read_x<0, 0>(c); wf[0] = k1_read_w<0>(c);
  xf[1] = k1_read_x<0, 1>(c); wf[1] = k1_read_w<1>(c);
  xf[2] = k1_read_x<0, 2>(c); wf[2] = k1_read_w<2>(c);
  xf[3] = k1_read_x<0, 3>(c);
  static_assert(K1_RD == 3, "prologue reads");
  SCHED_FENCE();
  K1Epi E;
  CvRegs V;
  k1_slot<0, ROLE>(c, xf, wf, acc, E, P, hw, V);
  if constexpr (ROLE == 1) cv_phase(c, acc, E, V);
  else {
    g2_phase(c, hw, acc, E);
    __builtin_amdgcn_s_waitcnt(0);
  }
  if constexpr (ROLE == 0) asm volatile(".Lgm_code_end0:");
  else asm volatile(".Lgm_code_end1:");
}

__global__ __launch_bounds__(512, 2) void fused_kernel(const void* x, const void* kk, const void* vv, const void* shift_k, const void* ssm_k,
                                                       const void* ssm_D, const void* w1, const void* w3, const void* w2, const void* gate_w, void* ws,
                                                       void* out) {
  const int w = __builtin_amdgcn_readfirstlane(threadIdx.x >> 6);
  unsigned long long pc;
  asm volatile("s_getpc_b64 %0\n.Lgm_code_start:" : "=s"(pc));
  if (blockIdx.x < 64) fused_body<1>(w, pc);
  else fused_body<0>(w, pc);
}

extern "C" void kernel_launch(void* const* d_in, const int* in_sizes, int n_in, void* d_out, int out_size, void* d_ws, size_t ws_size,
                              hipStream_t stream) {
  (void)hipFuncSetAttribute((const void*)fused_kernel, hipFuncAttributeMaxDynamicSharedMemorySize, LDS_BYTES);
  fused_kernel<<<256, 512, LDS_BYTES, stream>>>(d_in[0], d_in[1], d_in[2], d_in[3], d_in[4], d_in[5], d_in[7], d_in[8], d_in[9], d_in[10], d_ws, d_out);
}
```

```cpp
#include <hip/hip_runtime.h>
#include <hip/hip_bf16.h>
#include <utility>

#define LAS __attribute__((address_space(3)))
#define GAS __attribute__((address_space(1)))
typedef unsigned short u16;
typedef __bf16 bf16x2 __attribute__((ext_vector_type(2)));
typedef __bf16 bf16x4 __attribute__((ext_vector_type(4)));
typedef __bf16 bf16x8 __attribute__((ext_vector_type(8)));
typedef float f32x2 __attribute__((ext_vector_type(2)));
typedef float f32x4 __attribute__((ext_vector_type(4)));
typedef float f32x16 __attribute__((ext_vector_type(16)));
typedef unsigned u32x2 __attribute__((ext_vector_type(2)));
typedef unsigned u32x4 __attribute__((ext_vector_type(4)));

constexpr int S_ = 2048, D_ = 576, F_ = 4608;
constexpr unsigned WS_H = 0;
constexpr unsigned WS_PART = WS_H + 64 * 288 * 1024;
constexpr unsigned WS_UR = WS_PART + 256 * 49152;
constexpr unsigned WS_Z = WS_UR + D_ * 2 * S_ * 2;
constexpr unsigned WS_G = WS_Z + D_ * S_ * 2;
constexpr unsigned WS_FLAG = WS_G + S_ * 4;
constexpr unsigned WS_EPOCH = WS_FLAG + 256 * 512;
constexpr unsigned WS_END = WS_EPOCH + 1024;
constexpr unsigned FLAG_MAGIC = 0x7c3a91e5u, SENT = 0xffffffffu;
constexpr int AUX_SC1 = 16;
constexpr unsigned OOB = 0x7ff00000u;
constexpr unsigned SPIN_CAP = 1u << 16;

__device__ __forceinline__ float bflo(unsigned v) { return __builtin_bit_cast(float, v << 16); }
__device__ __forceinline__ float bfhi(unsigned v) { return __builtin_bit_cast(float, v & 0xffff0000u); }
__device__ __forceinline__ float rbf(float x) { return (float)(__bf16)x; }
__device__ __forceinline__ unsigned pk2(float lo, float hi) {
  f32x2 v = {lo, hi};
  return __builtin_bit_cast(unsigned, __builtin_convertvector(v, bf16x2));
}
__device__ __forceinline__ f32x2 unpk2(unsigned v) { return (f32x2){bflo(v), bfhi(v)}; }
__device__ __forceinline__ float radd(float a, float b) { const unsigned t = pk2(a, b); return bflo(t) + bfhi(t); }
#define SCHED_FENCE() __builtin_amdgcn_sched_barrier(0)
__device__ __forceinline__ void st128(u32x4 v, __amdgpu_buffer_rsrc_t rs, unsigned voff, unsigned soff, int) {
  __builtin_amdgcn_raw_buffer_store_b128(v, rs, voff, soff, AUX_SC1);
  asm volatile("s_nop 1" ::"v"(v));
}

template <int LC, int SC> __device__ __forceinline__ void dma16(unsigned voff, const u32x4& srd, unsigned so, unsigned lb) {
  unsigned t;
  asm volatile("s_add_u32 m0, %4, %5\n\ts_add_u32 %0, %3, %6\n\tbuffer_load_dwordx4 %1, %2, %0 offen lds"
               : "=&s"(t) : "v"(voff), "s"(srd), "s"(so), "s"(lb), "n"(LC), "n"(SC) : "memory", "scc");
}
template <int N> __device__ __forceinline__ void wait_vm_barrier() {
  asm volatile("s_waitcnt vmcnt(%0)\n\ts_barrier" ::"n"(N) : "memory");
}
template <int N> __device__ __forceinline__ void wait_vm() { asm volatile("s_waitcnt vmcnt(%0)" ::"n"(N) : "memory"); }
__device__ __forceinline__ void lds_barrier() { asm volatile("s_waitcnt lgkmcnt(0)\n\ts_barrier" ::: "memory"); }
__device__ __forceinline__ LAS char* ldsp(unsigned a) { return (LAS char*)(size_t)a; }
__device__ __forceinline__ bf16x8 tr_frag(LAS char* p0, LAS char* p1) {
  bf16x4 a = __builtin_amdgcn_ds_read_tr16_b64_v4bf16((LAS bf16x4*)p0);
  bf16x4 b = __builtin_amdgcn_ds_read_tr16_b64_v4bf16((LAS bf16x4*)p1);
  return __builtin_shufflevector(a, b, 0, 1, 2, 3, 4, 5, 6, 7);
}
constexpr int cmin(int a, int b) { return a < b ? a : b; }
__device__ __forceinline__ const char* karg(int i) {
  typedef const char* cptr;
  const __attribute__((address_space(4))) cptr* p = (const __attribute__((address_space(4))) cptr*)__builtin_amdgcn_kernarg_segment_ptr();
  asm volatile("" : "+s"(p));
  return p[i];
}
__device__ __forceinline__ u32x4 make_srd(const char* base, unsigned off, unsigned total) {
  const unsigned long long a = (unsigned long long)base + off;
  return (u32x4){(unsigned)a, (unsigned)(a >> 32) & 0xffffu, total - off, 0x00020000u};
}

constexpr int K1_SLOT = 16384, K1_XOFF = 65536, K1_XD = 2, K1_POFF = 131072, K1_PV = 10240;
constexpr int LDS_BYTES = 163840;
constexpr int K1_PF = 4, K1_RD = 3, K1_NS = 324, K1_NSEG = 21;
constexpr int K1_PREP0 = 28, K1_PREP_UNITS = 8 + 2 + 63 + 3 + 4;
constexpr int G2_SLOT = 24576, G2_NSLOT = 4, G2_WIN = 16, G2_PF = 4, G2_NS = 216;
constexpr int CV_K = 0, CV_U0 = 4160, CV_U1 = CV_U0 + 4640, CV_CH = 13440;
constexpr int k1_xstep(int p) { return p == 8 ? 1 : 2; }
constexpr int k1_wgap(int s) { return s == 18 || s == 19 ? 5 : 2; }
constexpr int k1_wbase(int s) { return s == 11 ? K1_XOFF : (s == 12 ? K1_XOFF + K1_SLOT : ((s < 11 ? s : s - 2) % 4) * K1_SLOT); }
constexpr int k1_wprev(int s) { return s < 4 || s == 11 || s == 12 ? -1 : (s < 11 ? s - 4 : (s < 17 ? s - 6 : s - 4)); }
constexpr int EV_PUB_U = 140;
constexpr int EV_PUB_A = 204;
constexpr int EV_PRE0 = 221, EV_PRE_STEP = 4;
constexpr int EV_CV_POLL = 176, EV_CV_CHECK = 190, EV_CV_INVW = 219;
constexpr int EV_CV_PRE7 = 300;
constexpr int EV_CV_PRE8 = 316;
constexpr int EV_SENT0 = 177, EV_SENT_STEP = 7, EV_PUB_S = 236;
constexpr int EV_G2_POLL = 264, EV_G2_CHECK = 280, EV_G2_H0 = 289;
constexpr int SMALL_SLOT[6] = {1, 3, 5, 7, 9, 13};
constexpr int WARM_N = 10;
constexpr int WARM_SLOT[WARM_N] = {17, 27, 43, 59, 75, 91, 119, 151, 175, 191};
constexpr int WARM_DONE = 235;
constexpr int EV_PUB_B1 = 316;
constexpr int K1_EPI0[2] = {125, 269};
constexpr int k1_epi_slot(int ch, int e) { return K1_EPI0[ch] + (ch == 0 ? e - e / 12 : e); }

struct K1Sched {
  int seg[K1_NS], tile[K1_NS], j[K1_NS];
  int first_use[K1_NSEG], last_use[K1_NSEG];
  int wissue[K1_NSEG];
  int xissue[9];
  bool bar[K1_NS];
  int ops[K1_NS];
  int wait[K1_NS];
  int done[K1_NS];
  constexpr int ops_after(int a, int b) const { int k = 0; for (int q = a + 1; q <= b; ++q) k += ops[q]; return k; }
};
constexpr K1Sched k1_make(int role) {
  K1Sched S{};
  int n = 0;
  for (int s = 0; s < 18;) {
    if (s == 7 || s == 9 || s == 16) {
      const int tt[8] = {0, 1, 0, 1, 2, 2, 3, 3}, hh[8] = {0, 0, 1, 1, 0, 1, 0, 1};
      for (int q = 0; q < 8; ++q) for (int j = 0; j < 4; ++j) { S.seg[n] = s + hh[q]; S.tile[n] = tt[q]; S.j[n] = j; ++n; }
      s += 2;
    } else {
      for (int t = 0; t < 4; ++t) for (int j = 0; j < 4; ++j) { S.seg[n] = s; S.tile[n] = t; S.j[n] = j; ++n; }
      s += 1;
    }
  }
  for (int i = 0; i < 36; ++i) { S.seg[n] = 18 + i / 16; S.tile[n] = 0; S.j[n] = i % 16; ++n; }
  for (int s = 0; s < K1_NSEG; ++s) { S.first_use[s] = 100000; S.last_use[s] = -1; }
  for (int i = 0; i < K1_NS; ++i) { int s = S.seg[i]; if (i < S.first_use[s]) S.first_use[s] = i; if (i > S.last_use[s]) S.last_use[s] = i; }
  for (int s = 1; s < K1_NSEG; ++s) S.bar[S.first_use[s] - K1_PF - 1] = true;
  S.wissue[0] = S.wissue[1] = -1;
  S.wissue[2] = 0; S.wissue[3] = 12;
  int busy = 0;
  for (int s = 4; s < K1_NSEG; ++s) {
    int q = k1_wprev(s) >= 0 ? S.last_use[k1_wprev(s)] : S.first_use[8] + 4;
    while (!S.bar[q]) ++q;
    int is = q + 1;
    if (is < busy) is = busy;
    S.wissue[s] = is;
    busy = is + (s >= 18 ? 10 : (s >= 11 ? 8 : 4));
  }
  S.xissue[0] = S.xissue[1] = -1;
  for (int p = 2; p < 9; ++p) S.xissue[p] = S.first_use[p - 2] + 4;
  for (int s = 0; s < K1_NSEG; ++s) if (S.wissue[s] >= 0) { S.ops[S.wissue[s]]++; S.ops[S.wissue[s] + k1_wgap(s)]++; }
  for (int p = 0; p < 9; ++p) if (S.xissue[p] >= 0) for (int i = 0; i < 4; ++i) S.ops[S.xissue[p] + k1_xstep(p) * i]++;
  S.ops[K1_PREP0 + 7]++;
  for (int i = 0; i < 6; ++i) S.ops[SMALL_SLOT[i]]++;
  for (int i = 0; i < WARM_N; ++i) S.ops[WARM_SLOT[i]]++;
  for (int i = 0; i < 4; ++i) S.ops[K1_PREP0 + K1_PREP_UNITS - 4 + i]++;
  for (int c = 0; c < 2; ++c) for (int t = 0; t < 4; ++t) if (k1_epi_slot(c, 12 * t + 13) < K1_NS) S.ops[k1_epi_slot(c, 12 * t + 13)]++;
  if (role == 0) {
    for (int i = 0; i < G2_WIN; ++i) S.ops[EV_G2_H0 + 2 * i]++;
    for (int i = 0; i < 6; ++i) S.ops[EV_SENT0 + EV_SENT_STEP * i]++;
  } else {
    S.ops[EV_CV_PRE8]++; S.ops[EV_CV_PRE8 + 1]++;
    S.ops[EV_CV_PRE7]++; S.ops[EV_CV_PRE7 + 1]++;
  }
  for (int i = 0; i < 12; ++i) S.ops[EV_PRE0 + EV_PRE_STEP * i]++;
  for (int i = 0; i < K1_NS; ++i) {
    S.wait[i] = 63;
    S.done[i] = -2;
    if (!S.bar[i]) continue;
    int youngest = -2;
    for (int s = 1; s < K1_NSEG; ++s)
      if (S.first_use[s] - K1_PF - 1 == i) {
        int y = S.wissue[s] < 0 ? -1 : S.wissue[s] + k1_wgap(s);
        if (s < 9) { int yx = S.xissue[s] < 0 ? -1 : S.xissue[s] + 3 * k1_xstep(s); if (yx > y) y = yx; }
        if (y > youngest) youngest = y;
      }
    if (i == EV_PUB_A - 1 && youngest < k1_epi_slot(0, 49)) youngest = k1_epi_slot(0, 49);
    if (i == EV_PUB_S - 1 && youngest < EV_SENT0 + 5 * EV_SENT_STEP) youngest = EV_SENT0 + 5 * EV_SENT_STEP;
    if (i == WARM_DONE && youngest < WARM_SLOT[WARM_N - 1]) youngest = WARM_SLOT[WARM_N - 1];
    if (i == EV_PUB_B1 - 1 && youngest < k1_epi_slot(1, 25)) youngest = k1_epi_slot(1, 25);
    if (role == 1 && i == 315 && youngest < EV_CV_PRE7 + 1) youngest = EV_CV_PRE7 + 1;
    if (i == K1_PREP0 - 1 && youngest < SMALL_SLOT[5]) youngest = SMALL_SLOT[5];
    if (i == 299 && youngest < EV_PRE0 + 11 * EV_PRE_STEP) youngest = EV_PRE0 + 11 * EV_PRE_STEP;
    S.wait[i] = cmin(63, S.ops_after(youngest, i));
    S.done[i] = youngest;
  }
  return S;
}
constexpr K1Sched K1S[2] = {k1_make(0), k1_make(1)};
static_assert(K1_PREP0 + K1_PREP_UNITS - 1 <= K1S[0].done[EV_PUB_U - 1] && K1S[0].bar[EV_PUB_U - 1], "u stores complete before publish");
static_assert(K1S[0].bar[EV_PUB_A - 1] && K1S[0].bar[EV_PUB_S - 1] && K1S[0].bar[299] && K1S[0].bar[315], "events");
static_assert(K1S[0].bar[EV_G2_CHECK + 3] && K1S[0].bar[EV_CV_INVW] && K1S[0].bar[EV_CV_PRE7 - 1] && K1S[0].last_use[17] < EV_CV_PRE7, "events");
static_assert(K1S[0].bar[EV_CV_PRE8 - 1] && K1S[0].last_use[18] < EV_CV_PRE8 && EV_G2_H0 + 2 * G2_WIN <= K1_NS + 1, "events");
static_assert(EV_PRE0 > EV_CV_INVW && K1S[0].last_use[12] <= EV_CV_INVW, "events");
static_assert([] {
  for (int t = 0; t < 4; ++t) {
    int last = 0, next = 1000;
    for (int i = 0; i < 144; ++i) if (K1S[0].tile[i] == t) last = i;
    for (int i = 287; i >= 144; --i) if (K1S[0].tile[i] == t) next = i;
    if (k1_epi_slot(0, 12 * t) < last + 2 || k1_epi_slot(0, 12 * t + 9) >= next) return false;
  }
  return true;
}(), "epilogue windows");

struct Ctx {
  u32x4 sw1, sw3, sx, s2, s3, sws;
  __amdgpu_buffer_rsrc_t rs;
  unsigned wo[3], xo[3];
  unsigned hb;
  unsigned voff_w, voff_x;
  unsigned rd[4];
  unsigned xl0, lane16;
  unsigned ev;
  u32x2 poll;
  unsigned touch, warm;
  const char* dptr;
  unsigned rc0, rc1;
  int w, rot, nl, ml, b, mt, fq;
};
struct K1Prep {
  u32x4 sk, ga0, ga1, gw0, gw1;
  unsigned lp, kn, vn, o0, psk, pev;
  const char *skp, *gw, *gx;
  f32x2 s[4], k2[4], a, p, q; unsigned u[9]; float gs;
};
struct K1Epi { float m[2], p[2], pq[2], r[2], h[2]; u32x4 ho; };
struct CvRegs { unsigned ap[4], bp; bf16x8 a0[8], b0[8]; };

__device__ __forceinline__ int lane_of(const Ctx& c) {
  unsigned l = c.lane16;
  asm volatile("" : "+v"(l));
  return (int)(l >> 4);
}
__device__ __forceinline__ u32x4 sent4() {
  u32x4 v = {SENT, SENT, SENT, SENT};
  asm volatile("" : "+v"(v));
  return v;
}
__device__ __forceinline__ u32x2 sent2() {
  u32x2 v = {SENT, SENT};
  asm volatile("" : "+v"(v));
  return v;
}
__device__ __forceinline__ u32x4 zero4() {
  u32x4 v = {0u, 0u, 0u, 0u};
  asm volatile("" : "+v"(v));
  return v;
}
__device__ __forceinline__ int g2_wg(int mt, int nt, int ks) { return ((nt * 4 + (mt & 3)) << 3) | (ks * 2 + (mt >> 2)); }
__device__ __forceinline__ int cv_wg(int cw) { return ((24 + (cw & 7)) << 3) | (cw >> 3); }
__device__ __forceinline__ int prep_dg(int b) { return (b >> 3) & 7; }
__device__ __forceinline__ int prep_tt(int b) { return ((b >> 6) << 3) | (b & 7); }
__device__ __forceinline__ int cv_id(const Ctx& c) { return (c.b & 7) * 8 + (c.b >> 3) - 24; }
__device__ __forceinline__ void flag_store(const Ctx& c, bool act, int wg, int slot, unsigned epoch) {
  asm volatile("" : "+v"(epoch));
  __builtin_amdgcn_raw_buffer_store_b64((u32x2){epoch + 1u, FLAG_MAGIC}, c.rs, act ? wg * 512 + slot * 8 : OOB, WS_FLAG, AUX_SC1);
}
__device__ __forceinline__ void publish_h(const Ctx& c, int slot0) {
  const int l = lane_of(c);
  flag_store(c, l >= 8 && l < 14, g2_wg(c.mt, l - 8, c.fq), slot0 + c.nl, c.ev);
}
__device__ __forceinline__ void poll_issue(Ctx& c) {
  const unsigned vo = lane_of(c) * 8;
  const unsigned so = WS_FLAG + c.b * 512;
  asm volatile("s_cmp_lg_u32 %4, 0\n\ts_cbranch_scc1 1f\n\tbuffer_load_dwordx2 %0, %1, %2, %3 offen sc1\n1:"
               : "=v"(c.poll) : "v"(vo), "s"(c.sws), "s"(so), "s"(c.w) : "memory", "scc");
}
__device__ __forceinline__ void poll_wait(Ctx& c, bool need) {
  const unsigned want = (unsigned)__builtin_amdgcn_readlane((int)c.ev, 14) + 1u;
  u32x2 v = c.poll;
  unsigned spin = 0;
#pragma unroll 1
  for (; spin < SPIN_CAP; ++spin) {
    if (__all(!need || (v[0] == want && v[1] == FLAG_MAGIC))) break;
    const unsigned vo = lane_of(c) * 8, so = WS_FLAG + c.b * 512;
    asm volatile("s_sleep 1\n\tbuffer_load_dwordx2 %0, %1, %2, %3 offen sc1\n\ts_waitcnt vmcnt(0)" : "=&v"(v) : "v"(vo), "s"(c.sws), "s"(so) : "memory");
  }
  asm volatile("" ::: "memory");
}
#define POLL_LANDED(N) asm volatile("s_cmp_lg_u32 %2, 0\n\ts_cbranch_scc1 1f\n\ts_waitcnt vmcnt(%1)\n1:" : "+v"(c.poll) : "n"(N), "s"(c.w) : "memory", "scc")

__device__ __forceinline__ int kslice(int p, int r) {
  const int g = (p >= 6) ? 6 : (p >= 3 ? 3 : 0);
  const int v = p - g + r;
  return g + (v >= 3 ? v - 3 : v);
}
template <int sg, int half> __device__ __forceinline__ void k1_issue_w(const Ctx& c) {
  constexpr int dst = k1_wbase(sg) + half * 8192;
  if constexpr (sg < 18) {
    constexpr int p = sg % 9;
    dma16<dst, (p / 3 * 3) * 64 * F_ * 2 + 128 * (sg / 9)>(half ? (c.voff_w ^ 64u) : c.voff_w, half ? c.sw3 : c.sw1, c.wo[p % 3], c.w * 1024);
  } else {
    const unsigned l = lane_of(c);
    const unsigned rp = l >> 1;
    unsigned vo = (half ? (rp ^ 4u) : rp) * (F_ * 2) + (l & 1u) * 16;
    const int col = (1024 + 16 * c.nl - 128 * c.nl) * 2;
    if constexpr (sg < 20) {
      const int ksl = kslice(4 * (sg - 18) + (c.w >> 1), c.rot);
      dma16<dst, 0>(vo, half ? c.sw3 : c.sw1, (64 * ksl + 32 * (c.w & 1)) * (F_ * 2) + col, c.w * 1024);
    } else {
      if (l >= 16) vo = OOB;
      dma16<dst, 6 * 64 * F_ * 2>(vo, half ? c.sw3 : c.sw1, c.wo[2] + col + 8 * c.w * (F_ * 2), c.w * 1024);
    }
  }
}
template <int p, int i> __device__ __forceinline__ void k1_issue_x(const Ctx& c) {
  dma16<K1_XOFF + (p % K1_XD) * 4096 + i * 1024, 8 * i * D_ * 2 + (p / 3 * 3) * 128>((i & 1) ? (c.voff_x ^ 64u) : c.voff_x, c.sx, c.xo[p % 3],
                                                                                     c.w * (K1_XD * 4096));
}
template <int p, int j> __device__ __forceinline__ bf16x8 k1_read_x(const Ctx& c) {
  return *(LAS bf16x8*)(ldsp(c.xl0 ^ (32 * j)) + (p % K1_XD) * 4096);
}
template <int n> __device__ __forceinline__ bf16x8 k1_read_w(Ctx& c) {
  constexpr int sg = K1S[0].seg[n], t = K1S[0].tile[n], j = K1S[0].j[n];
  if constexpr (sg < 18) {
    if constexpr ((sg == 11 || sg == 13) && j == 0) {
      c.rd[t] += sg == 11 ? K1_XOFF : -K1_XOFF;
      asm volatile("" : "+v"(c.rd[t]));
    }
    LAS char* p = ldsp(c.rd[t]) + k1_wbase(sg) % K1_XOFF + (16 * j) * 128;
    return tr_frag(p, p + 4 * 128);
  } else if constexpr (sg < 20) {
    const int off = k1_wbase(sg) + (16 * j) * 32;
    return tr_frag(ldsp(c.rc0) + off, ldsp(c.rc1) + off);
  } else {
    const unsigned ex = (lane_of(c) >> 5) * 768u;
    const int off = k1_wbase(sg) + j * 2048;
    return tr_frag(ldsp(c.rc0 + ex) + off, ldsp(c.rc1 + ex) + off);
  }
}
#define GLD1(dst, voff, sb) asm volatile("global_load_dword %0, %1, %2" : "=v"(dst) : "v"(voff), "s"(sb) : "memory")
#define GLD4(dst, voff, sb) asm volatile("global_load_dwordx4 %0, %1, %2" : "=v"(dst) : "v"(voff), "s"(sb) : "memory")
template <int ctrl> __device__ __forceinline__ float dpp_add(float v) {
  return v + __builtin_bit_cast(float, __builtin_amdgcn_update_dpp(0, __builtin_bit_cast(int, v), ctrl, 0xf, 0xf, true));
}
__device__ __forceinline__ float rb1(float x) { return __builtin_bit_cast(float, pk2(0.f, x)); }
__device__ __forceinline__ f32x2 rb2(f32x2 v) { return (f32x2){rb1(v[0]), rb1(v[1])}; }
template <int k> __device__ __forceinline__ void k1_prep_unit(Ctx& c, K1Prep& P) {
  if constexpr (k == 0) {
    asm volatile("" : "+v"(P.sk), "+v"(P.ga0), "+v"(P.ga1), "+v"(P.gw0), "+v"(P.gw1), "+v"(c.ev)::"memory");
    P.gs = 0.f;
  }
  if constexpr (k < 4) {
    const u32x4 &a = k < 2 ? P.ga1 : P.ga0, &g = k < 2 ? P.gw1 : P.gw0;
#pragma unroll
    for (int i = 2 * (k & 1); i < 2 * (k & 1) + 2; ++i) P.gs = __builtin_fmaf(bfhi(a[i]), bfhi(g[i]), __builtin_fmaf(bflo(a[i]), bflo(g[i]), P.gs));
    if constexpr (k == 1) P.gs = lane_of(c) < 8 ? P.gs : 0.f;
    asm volatile("" : "+v"(P.gs));
  } else if constexpr (k == 4) {
    P.gs = dpp_add<0x111>(P.gs); P.gs = dpp_add<0x112>(P.gs);
    P.u[0] = *(LAS unsigned*)(ldsp(P.lp)); P.u[1] = *(LAS unsigned*)(ldsp(P.lp) + 144);
  } else if constexpr (k == 5) {
    P.gs = dpp_add<0x114>(P.gs); P.gs = dpp_add<0x118>(P.gs);
    P.u[2] = *(LAS unsigned*)(ldsp(P.lp) + 288); P.kn = *(LAS unsigned*)(ldsp(P.lp) + 432);
  } else if constexpr (k == 6) {
    int v = __builtin_bit_cast(int, P.gs);
    float a = __builtin_bit_cast(float, __builtin_amdgcn_readlane(v, 15)), b = __builtin_bit_cast(float, __builtin_amdgcn_readlane(v, 31));
    float d = __builtin_bit_cast(float, __builtin_amdgcn_readlane(v, 47)), e = __builtin_bit_cast(float, __builtin_amdgcn_readlane(v, 63));
    P.gs = __builtin_amdgcn_exp2f(rb1((a + b) + (d + e)) * -1.44269504088896f);
  } else if constexpr (k == 7) {
    float g = rb1(__builtin_amdgcn_rcpf(1.0f + P.gs));
    __builtin_amdgcn_raw_buffer_store_b32(__builtin_bit_cast(unsigned, g), c.rs, lane_of(c) == 0 ? 0u : OOB,
                                          WS_G + (256 * c.mt + 64 * c.fq + 8 * c.nl + c.w) * 4, AUX_SC1);
    P.s[0] = (f32x2){bflo(P.sk[0]), bflo(P.sk[2])};
  } else if constexpr (k == 8) {
    P.s[1] = (f32x2){bfhi(P.sk[0]), bfhi(P.sk[2])};
    P.s[2] = (f32x2){bflo(P.sk[1]), bflo(P.sk[3])}; P.s[3] = (f32x2){bfhi(P.sk[1]), bfhi(P.sk[3])};
    asm volatile("" : "+v"(P.s[0]), "+v"(P.s[1]), "+v"(P.s[2]), "+v"(P.s[3]));
  } else if constexpr (k == 9) {
    P.k2[0] = unpk2(P.u[0]); P.k2[1] = unpk2(P.u[1]); P.k2[2] = unpk2(P.u[2]);
    asm volatile("" : "+v"(P.k2[0]), "+v"(P.k2[1]), "+v"(P.k2[2]));
  } else if constexpr (k < 73) {
    constexpr int i = (k - 10) / 7, st = (k - 10) % 7;
    auto mul = [](f32x2 x, f32x2 y) { f32x2 r = {x[0] * y[0], x[1] * y[1]}; return r; };
    if constexpr (st == 0) { P.a = mul(P.k2[i & 3], P.s[3]); P.p = mul(P.k2[(i + 1) & 3], P.s[2]); P.vn = *(LAS unsigned*)(ldsp(P.lp) + K1_PV + 144 * i); }
    else if constexpr (st == 1) { P.a = rb2(P.a); P.p = rb2(P.p); }
    else if constexpr (st == 2) { P.a += P.p; P.p = mul(P.k2[(i + 2) & 3], P.s[1]); P.k2[(i + 3) & 3] = unpk2(P.kn); }
    else if constexpr (st == 3) {
      P.a = rb2(P.a); P.p = rb2(P.p);
      if constexpr (i < 8) P.kn = *(LAS unsigned*)(ldsp(P.lp) + 144 * (i + 4));
    }
    else if constexpr (st == 4) { P.a += P.p; P.p = mul(P.k2[(i + 3) & 3], P.s[0]); P.q = unpk2(P.vn); }
    else if constexpr (st == 5) { P.a = rb2(P.a); P.p = rb2(P.p); }
    else { P.a = mul(rb2(P.a + P.p), P.q); P.u[i] = pk2(P.a[0], P.a[1]); }
    if constexpr (st == 6) asm volatile("" : "+v"(P.u[i]));
    else if constexpr (st == 2) asm volatile("" : "+v"(P.a), "+v"(P.p), "+v"(P.k2[(i + 3) & 3]));
    else if constexpr (st == 4) asm volatile("" : "+v"(P.a), "+v"(P.p), "+v"(P.q));
    else asm volatile("" : "+v"(P.a), "+v"(P.p));
  } else if constexpr (k == 73) {
    const int l = lane_of(c);
    const int dp = 8 * c.w + (l >> 3);
    P.o0 = dp < 36 ? (unsigned)((72 * prep_dg(c.b) + 2 * dp) * 2 * S_ + (1984 - 64 * prep_tt(c.b)) + 8 * (l & 7)) * 2 : OOB;
  } else if constexpr (k >= 76) {
    constexpr int ch = (k - 76) >> 1, cp = (k - 76) & 1;
    constexpr unsigned sel = ch ? 0x07060302u : 0x05040100u;
    u32x4 o;
#pragma unroll
    for (int e = 0; e < 4; ++e) o[e] = __builtin_amdgcn_perm(P.u[7 + cp - 2 * e - 1], P.u[7 + cp - 2 * e], sel);
    st128(o, c.rs, P.o0, WS_UR + (ch * 2 + cp) * S_ * 2, AUX_SC1);
  }
}
template <int NT, int e> __device__ __forceinline__ void k1_epi_step(const Ctx& c, const f32x16* acc, K1Epi& E, int ft0) {
  constexpr int pr = e / 3, st = e % 3, t = pr / 4, i = 2 * (pr % 4);
  if constexpr (st == 0) {
    if constexpr (pr > 0) { E.h[0] = E.pq[0] * E.r[0]; E.h[1] = E.pq[1] * E.r[1]; }
    if constexpr (pr < 4 * NT) {
      E.m[0] = acc[t][i] * -1.44269504088896f; E.m[1] = acc[t][i + 1] * -1.44269504088896f;
      E.p[0] = acc[t][i] * acc[t][i + 8]; E.p[1] = acc[t][i + 1] * acc[t][i + 9];
      asm volatile("" : "+v"(E.p[0]), "+v"(E.p[1]));
    }
  } else if constexpr (st == 1) {
    if constexpr (pr < 4 * NT) { E.m[0] = __builtin_amdgcn_exp2f(E.m[0]); E.m[1] = __builtin_amdgcn_exp2f(E.m[1]); }
    if constexpr (pr > 0) {
      E.ho[(pr - 1) % 4] = pk2(E.h[0], E.h[1]);
      if constexpr (pr % 4 == 0) st128(E.ho, c.rs, c.lane16, c.hb + (ft0 + (pr - 1) / 4) * 1024, AUX_SC1);
    }
  } else {
    if constexpr (pr < 4 * NT) {
      float d0 = 1.0f + E.m[0], d1 = 1.0f + E.m[1];
      E.r[0] = __builtin_amdgcn_rcpf(d0); E.r[1] = __builtin_amdgcn_rcpf(d1);
      E.pq[0] = E.p[0]; E.pq[1] = E.p[1];
    }
  }
}

template <int ch, int n, int e = 0> __device__ __forceinline__ void k1_epi_at(const Ctx& c, const f32x16* acc, K1Epi& E) {
  if constexpr (n >= K1_EPI0[ch] && n <= k1_epi_slot(ch, 49)) {
    if constexpr (k1_epi_slot(ch, e) == n) k1_epi_step<4, e>(c, acc, E, 8 * c.nl + 4 * ch);
    if constexpr (e < 49 && k1_epi_slot(ch, e < 49 ? e + 1 : e) <= n) k1_epi_at<ch, n, e + 1>(c, acc, E);
  }
}
template <int i> __device__ __forceinline__ void warm_touch(Ctx& c, const u32x4& sw2, const u32x4& ssk) {
  constexpr unsigned long long m = i < 4 ? (0x3full << (6 * (i >> 1))) : (i < 8 ? (0x3full << (16 + 6 * (i - 4))) : (0x1full << (48 + 5 * (i - 8))));
  const u32x4& srd = i < 4 ? ((i & 1) ? c.sw3 : c.sw1) : (i < 8 ? sw2 : ssk);
  asm volatile("s_mov_b32 exec_lo, %3\n\ts_mov_b32 exec_hi, %4\n\tbuffer_load_dword %0, %1, %2, 0 offen\n\ts_mov_b64 exec, -1"
               : "+v"(c.touch) : "v"(c.warm), "s"(srd), "n"((int)(unsigned)m), "n"((int)(unsigned)(m >> 32)) : "memory");
}
__device__ __forceinline__ int g2_ft(int s, int i8) {
  return s >= 8 ? 64 + i8 : (s < 4 ? 16 * s + 8 * (i8 >> 2) + (i8 & 3) : 32 * (s & 1) + 8 * (i8 >> 1) + (i8 & 1) + 4 + (s & 2));
}
template <int s, int i> __device__ __forceinline__ void g2_issue_w(const Ctx& c) {
  const unsigned n16 = 64u * i + (s < 4 ? (unsigned)lane_of(c) : c.xl0);
  const unsigned vo = ((n16 * 171u) >> 11) * (D_ * 2 - 192) + n16 * 16u;
  dma16<K1_XOFF + (s % G2_NSLOT) * G2_SLOT + i * 1024, 0>(vo, c.s2, 16 * g2_ft(s, c.w) * (D_ * 2) + 192 * c.nl, c.w * 3072);
}
template <int i> __device__ __forceinline__ u32x4 g2_load_h(const Ctx& c) {
  return __builtin_amdgcn_raw_buffer_load_b128(c.rs, c.lane16, c.hb + g2_ft(i / 8, i % 8) * 1024, AUX_SC1);
}
__device__ __forceinline__ unsigned g2_out_base(const Ctx& c) {
  return WS_PART + g2_wg(c.mt, c.nl, c.w >> 1) * 49152 + (c.fq * 2 + (c.w & 1)) * 6144;
}
__device__ __forceinline__ unsigned cv_base(int ch) { return ch < 7 ? K1_XOFF + ch * CV_CH : (ch == 7 ? k1_wbase(17) : k1_wbase(18)); }
template <int i> __device__ __forceinline__ void cv_issue(const Ctx& c) {
  const int d = 9 * cv_id(c) + c.w;
  constexpr int lo = i < 4 ? CV_K + 64 + i * 1024 : (i < 8 ? CV_U0 + (i - 4) * 1024 : CV_U1 + (i - 8) * 1024);
  const unsigned lb = c.w < 7 ? K1_XOFF + c.w * CV_CH : LDS_BYTES - 1024 - lo;
  if constexpr (i < 4) dma16<lo, i * 1024>(c.lane16, c.s2, c.w < 7 ? d * (S_ * 2) : OOB, lb);
  else dma16<lo, WS_UR + (i - 4) * 1024>(c.lane16, c.sws, c.w < 7 ? d * (4 * S_) : OOB, lb);
}
template <int ch, int part> __device__ __forceinline__ void cv_issue_shared(const Ctx& c) {
  const int d = 9 * cv_id(c) + ch;
  if constexpr (part == 0) dma16<k1_wbase(10 + ch), WS_UR>(c.lane16, c.sws, d * (4 * S_) + c.w * 1024, c.w < 4 ? CV_U0 + c.w * 1024 : CV_U1 + (c.w - 4) * 1024);
  else dma16<k1_wbase(10 + ch) + CV_K + 64, 0>(c.lane16, c.s2, d * (S_ * 2) + (c.w >> 1) * 1024, (c.w >> 1) * 1024);
}
static_assert(K1_XOFF + 7 * CV_CH <= LDS_BYTES - 1024, "dummy target");
typedef unsigned u32x2a4 __attribute__((ext_vector_type(2), aligned(4)));
__device__ __forceinline__ bf16x8 cv_read_a(LAS char* p) {
  u32x2 lo = *(LAS u32x2a4*)p;
  u32x2 hi = *(LAS u32x2a4*)(p + 8);
  u32x4 v = {lo[0], lo[1], hi[0], hi[1]};
  return __builtin_bit_cast(bf16x8, v);
}
__device__ __forceinline__ int cv_zwg(const Ctx& c, int lane, int& slot) {
  const int cw = cv_id(c);
  const int n0 = (9 * cw) / 96, n1 = (9 * cw + 8) / 96;
  const int s0 = 16 + cw - (96 * n0) / 9, s1 = n1 != n0 ? 16 + cw - (96 * n1) / 9 : -1;
  slot = lane < 32 ? s0 : s1;
  return g2_wg((lane >> 2) & 7, lane < 32 ? n0 : n1, lane & 3);
}

template <int n, int ROLE>
__device__ __forceinline__ void k1_slot(Ctx& c, bf16x8 (&xf)[36], bf16x8 (&wf)[K1_RD + 1], f32x16 (&acc)[4], K1Epi& E, K1Prep& P, u32x4 (&hw)[24],
                                        CvRegs& V) {
  constexpr const K1Sched& S = K1S[ROLE];
  constexpr int sg = S.seg[n];
  if constexpr (n + K1_RD == 288) {
    const unsigned l = lane_of(c);
    unsigned mat = (l >> 4) & 1, h = l >> 5, q = (l & 15) >> 2, p = l & 3;
    unsigned base = mat * 8192 + (8 * h + q) * 32 + p * 8;
    c.rc0 = base + (mat ? 128 : 0);
    c.rc1 = base + (mat ? 0 : 128);
  }
  if constexpr (n + K1_RD < K1_NS) wf[(n + K1_RD) % (K1_RD + 1)] = k1_read_w<n + K1_RD>(c);
  SCHED_FENCE();
  if constexpr (n + 4 < 144) {
    constexpr int sn = S.seg[n + 4];
    if constexpr (S.tile[n + 4] == 0 && sn >= 1) xf[4 * sn + S.j[n + 4]] = k1_read_x<sn, S.j[n + 4]>(c);
  }
  {
    constexpr int t = S.tile[n];
    constexpr int xi = sg < 18 ? 4 * (sg % 9) + S.j[n] : n - 288;
    constexpr bool first = (sg < 18) ? (sg % 9 == 0 && S.j[n] == 0) : (n == 288);
    if constexpr (first) acc[t] = __builtin_amdgcn_mfma_f32_32x32x16_bf16(wf[n % (K1_RD + 1)], xf[xi], (f32x16){}, 0, 0, 0);
    else acc[t] = __builtin_amdgcn_mfma_f32_32x32x16_bf16(wf[n % (K1_RD + 1)], xf[xi], acc[t], 0, 0, 0);
  }
  SCHED_FENCE();
  {
    constexpr int ws0 = [] { for (int s = 0; s < K1_NSEG; ++s) if (K1S[0].wissue[s] == n) return s; return -1; }();
    constexpr int ws1 = [] { for (int s = 0; s < K1_NSEG; ++s) if (K1S[0].wissue[s] >= 0 && K1S[0].wissue[s] + k1_wgap(s) == n) return s; return -1; }();
    constexpr int xs = [] { for (int p = 0; p < 9; ++p) if (K1S[0].xissue[p] >= 0 && n >= K1S[0].xissue[p] && n < K1S[0].xissue[p] + 4 * k1_xstep(p) && (n - K1S[0].xissue[p]) % k1_xstep(p) == 0) return p; return -1; }();
    if constexpr (ws0 >= 0) k1_issue_w<ws0, 0>(c);
    if constexpr (ws1 >= 0) k1_issue_w<ws1, 1>(c);
    if constexpr (xs >= 0) k1_issue_x<xs, (n - S.xissue[xs]) / k1_xstep(xs)>(c);
  }
  if constexpr (n == SMALL_SLOT[0]) GLD4(P.ga0, c.lane16, P.gx);
  if constexpr (n == SMALL_SLOT[1]) GLD4(P.ga1, 1024 + (c.lane16 & 127), P.gx);
  if constexpr (n == SMALL_SLOT[2]) GLD4(P.gw0, c.lane16, P.gw);
  if constexpr (n == SMALL_SLOT[3]) GLD4(P.gw1, 1024 + (c.lane16 & 127), P.gw);
  if constexpr (n == SMALL_SLOT[4]) GLD4(P.sk, P.psk, P.skp);
  if constexpr (n == SMALL_SLOT[5]) {
    const unsigned so = WS_EPOCH;
    asm volatile("buffer_load_dword %0, %1, %2, %3 offen" : "=v"(c.ev) : "v"(P.pev), "s"(c.sws), "s"(so) : "memory");
  }
  if constexpr (n >= K1_PREP0 && n < K1_PREP0 + K1_PREP_UNITS) k1_prep_unit<n - K1_PREP0>(c, P);
  k1_epi_at<0, n>(c, acc, E);
  k1_epi_at<1, n>(c, acc, E);
  {
    constexpr int wi = [] { for (int i = 0; i < WARM_N; ++i) if (WARM_SLOT[i] == n) return i; return -1; }();
    if constexpr (wi >= 0) warm_touch<wi>(c, ROLE == 0 ? c.s2 : c.s3, ROLE == 0 ? c.s3 : c.s2);
    if constexpr (n == WARM_DONE + 1) asm volatile("" : "+v"(c.touch)::"memory");
  }
  if constexpr (n == EV_PUB_U) {
    if (c.w == 1) { const int l = lane_of(c); flag_store(c, l < 8, cv_wg(8 * prep_dg(c.b) + l), prep_tt(c.b), c.ev); }
  }
  if constexpr (n == EV_PUB_A) {
    if (c.w == 1) publish_h(c, 0);
  }
  if constexpr (n == EV_PUB_B1) {
    if (c.w == 1) publish_h(c, 8);
  }
  if constexpr (ROLE == 0) {
    if constexpr (n >= EV_SENT0 && n < EV_SENT0 + 6 * EV_SENT_STEP && (n - EV_SENT0) % EV_SENT_STEP == 0)
      st128(sent4(), c.rs, c.lane16, (c.w >> 1) == c.fq ? OOB : g2_out_base(c) + (n - EV_SENT0) / EV_SENT_STEP * 1024, AUX_SC1);
    if constexpr (n == EV_PUB_S) {
      if (c.w == 2) { const int l = lane_of(c); flag_store(c, (l & 60) == 16, g2_wg(c.mt, c.nl, l & 3), 32 + c.fq, c.ev); }
    }
    if constexpr (n >= EV_PRE0 && n < EV_PRE0 + 12 * EV_PRE_STEP && (n - EV_PRE0) % EV_PRE_STEP == 0)
      g2_issue_w<(n - EV_PRE0) / EV_PRE_STEP / 3, (n - EV_PRE0) / EV_PRE_STEP % 3>(c);
    if constexpr (n == EV_G2_POLL) poll_issue(c);
    if constexpr (n == EV_G2_CHECK) {
      POLL_LANDED(S.ops_after(EV_G2_POLL, EV_G2_CHECK));
      if (c.w == 0) poll_wait(c, lane_of(c) < 8);
    }
    if constexpr (n >= EV_G2_H0 && (n - EV_G2_H0) % 2 == 0 && (n - EV_G2_H0) / 2 < G2_WIN) hw[(n - EV_G2_H0) / 2] = g2_load_h<(n - EV_G2_H0) / 2>(c);
  } else {
    if constexpr (n == EV_CV_POLL) poll_issue(c);
    if constexpr (n == EV_CV_CHECK) {
      POLL_LANDED(K1S[1].ops_after(EV_CV_POLL, EV_CV_CHECK));
      if (c.w == 0) {
        poll_wait(c, lane_of(c) < 32);
        asm volatile("buffer_inv sc1" ::: "memory");
        __builtin_amdgcn_raw_buffer_store_b32(c.ev + 1u, c.rs, lane_of(c) == 14 ? 0u : OOB, WS_EPOCH + c.b * 4, AUX_SC1);
      }
    }
    if constexpr (n == EV_CV_INVW) { if (c.w == 0) wait_vm<S.ops_after(EV_CV_CHECK, EV_CV_INVW)>(); }
    if constexpr (n >= EV_PRE0 && n < EV_PRE0 + 12 * EV_PRE_STEP && (n - EV_PRE0) % EV_PRE_STEP == 0) {
      cv_issue<(n - EV_PRE0) / EV_PRE_STEP>(c);
    }
    if constexpr (n == EV_CV_PRE7 || n == EV_CV_PRE7 + 1) cv_issue_shared<7, n - EV_CV_PRE7>(c);
    if constexpr (n == EV_CV_PRE7 + 3) {
      const int l = lane_of(c);
      const unsigned cb = cv_base(c.w);
      if (l < 60) {
        int o = l < 4 ? CV_K + l * 16 : (l < 32 ? CV_U0 + 4096 + (l - 4) * 16 : CV_U1 + 4096 + (l - 32) * 16);
        *(LAS u32x4*)(ldsp(cb) + o) = zero4();
      }
    }
    if constexpr (n == EV_CV_PRE7 + 8) {
      c.dptr = karg(5);
      asm volatile("" : "+s"(c.dptr));
      const int l = lane_of(c);
      const unsigned cb = cv_base(c.w);
      const int rho = l & 15, q = l >> 4, pi = (l >> 3) & 1, ii = l & 7;
      V.bp = cb + CV_U0 + pi * 4640 + 2 * (2016 - 32 * ii + 8 * q) - 64 * 56;
#pragma unroll
      for (int i = 0; i < 4; ++i) { V.ap[i] = cb + CV_K + 4 + 4 * rho + 16 * q + 1024 * i; asm volatile("" : "+v"(V.ap[i])); }
    }
    if constexpr (n == EV_CV_PRE8 || n == EV_CV_PRE8 + 1) cv_issue_shared<8, n - EV_CV_PRE8>(c);
    if constexpr (n == EV_CV_PRE8 + 1) {
      if (c.w == 6) {
        const int l = lane_of(c);
        if (l < 60) {
          int o = l < 4 ? CV_K + l * 16 : (l < 32 ? CV_U0 + 4096 + (l - 4) * 16 : CV_U1 + 4096 + (l - 32) * 16);
          *(LAS u32x4*)(ldsp(k1_wbase(18)) + o) = zero4();
        }
      }
    }
    if constexpr (n >= 320) {
#pragma unroll
      for (int f = 2 * (n - 320); f < 2 * (n - 320) + 2; ++f) {
        V.b0[f] = *(LAS bf16x8*)(ldsp(V.bp) + 64 * (56 - 8 * f));
        V.a0[f] = cv_read_a(ldsp(V.ap[f >> 1]) + 512 * (f & 1));
      }
    }
  }
  SCHED_FENCE();
  if constexpr (S.bar[n]) {
    wait_vm_barrier<S.wait[n]>();
    SCHED_FENCE();
  }
  if constexpr (n + 1 < K1_NS) k1_slot<n + 1, ROLE>(c, xf, wf, acc, E, P, hw, V);
}

constexpr int g2_ks(int n) { return n < 168 ? n / 3 : 56 + (n - 168) % 16; }
constexpr int g2_tl(int n) { return n < 168 ? n % 3 : (n - 168) / 16; }
constexpr int G2_HW = 24;
template <int n> __device__ __forceinline__ bf16x8 g2_read_w(unsigned rd) {
  constexpr int i = g2_ks(n), t = g2_tl(n), s = i / 8, j = i % 8;
  LAS char* p = ldsp(rd) + (s % G2_NSLOT) * G2_SLOT + (16 * j) * 192 + t * 64;
  return tr_frag(p, p + 8 * 192);
}
static_assert(k1_epi_slot(1, 49) < K1_NS, "chunk B epilogue ends in phase 1");
constexpr int G2_TAIL0 = 2, G2_TAILST = G2_TAIL0 + 13;
constexpr int G2_PUBB = 19, G2_PUBC = 43;
constexpr int G2_CHECK0 = 39, G2_LEAD = 18;
constexpr bool g2_check(int n) { return n >= G2_CHECK0 && n <= G2_CHECK0 + 120 && (n - G2_CHECK0) % 24 == 0; }
constexpr int G2_PST = 186;
constexpr int G2_PRE3 = 206;
constexpr int g2_hslot(int i) {
  return i < 64 ? 3 * (i - G2_WIN) + 2 : 144 + (i - 64) + (i - 64) / 2;
}
constexpr int g2_hload_at(int n) { for (int i = G2_WIN; i < 72; ++i) if (g2_hslot(i) == n) return i; return -1; }
constexpr bool g2_bar(int n) { return (n % 24 == 24 - G2_PF - 1 && n / 24 < 7) || n == 176 - G2_PF - 1; }
constexpr int g2_ops_in_slot(int n) {
  int k = 0;
  for (int s = 1; s + 3 < 9; ++s) for (int i = 0; i < 3; ++i) if (n == 24 * s + 24 - G2_PF + 1 + 3 * i) ++k;
  if (g2_hload_at(n) >= 0) ++k;
  if (n == G2_TAILST) ++k;
  if (n == G2_PST || n == G2_PST + 2 || n == G2_PST + 16 || n == G2_PST + 18) ++k;
  return k;
}
constexpr int g2_ops_after(int a, int b) { int k = 0; for (int n = a + 1; n <= b; ++n) k += g2_ops_in_slot(n); return k; }
static_assert([] { for (int i = G2_WIN; i + 1 < 72; ++i) if (g2_hslot(i) >= g2_hslot(i + 1) || (i >= G2_HW && g2_hslot(i) < 3 * (i - G2_HW) + 2)) return false; return g2_hslot(64) > G2_CHECK0 + 100; }(), "h load slots");
static_assert(g2_bar(G2_PUBB) && g2_bar(G2_PUBC) && g2_bar(G2_CHECK0 + 4) && g2_hslot(32) > G2_CHECK0 + 4 && G2_PRE3 > G2_CHECK0 + 124 && G2_LEAD < 23, "events");
struct G2Fin { u32x4 zz[2], qq[2]; float g; };
__device__ __forceinline__ void g2_send(const Ctx& c, const f32x16& a, int t, int p2) {
  const bool own = (c.w >> 1) == c.fq;
  const u32x4 o = {pk2(a[8 * p2], a[8 * p2 + 1]), pk2(a[8 * p2 + 2], a[8 * p2 + 3]), pk2(a[8 * p2 + 4], a[8 * p2 + 5]), pk2(a[8 * p2 + 6], a[8 * p2 + 7])};
  st128(o, c.rs, c.lane16, own ? OOB : g2_out_base(c) + (t * 2 + p2) * 1024, AUX_SC1);
  if (own) {
#pragma unroll
    for (int qd = 2 * p2; qd < 2 * p2 + 2; ++qd)
      *(LAS f32x4*)(ldsp(c.lane16) + ((c.w & 1) * 12 + t * 4 + qd) * 1024) = (f32x4){a[4 * qd], a[4 * qd + 1], a[4 * qd + 2], a[4 * qd + 3]};
  }
}
__device__ __forceinline__ void g2_fin_load(const Ctx& c, G2Fin& Fn, bool all) {
  const int lane = lane_of(c);
  const int uh = (c.w / 3) & 1, ut = c.w % 3;
  const int t0 = 256 * c.mt + 64 * c.fq + 32 * uh, d0 = 96 * c.nl + 32 * ut;
#pragma unroll
  for (int i = 0; i < 2; ++i)
    Fn.zz[i] = __builtin_amdgcn_raw_buffer_load_b128(c.rs, ((16 * i + (lane >> 2)) * S_) * 2 + (lane & 3) * 16, WS_Z + (d0 * S_ + t0) * 2, AUX_SC1);
  if (all) {
    const char* qptr = karg(0);
#pragma unroll
    for (int i = 0; i < 2; ++i) Fn.qq[i] = *(const u32x4*)(qptr + ((size_t)(t0 + (lane & 31)) * D_ + d0 + 16 * (lane >> 5) + 8 * i) * 2);
    Fn.g = __builtin_bit_cast(float, __builtin_amdgcn_raw_buffer_load_b32(c.rs, (lane & 31) * 4, WS_G + t0 * 4, AUX_SC1));
  }
}
template <int n>
__device__ __forceinline__ void g2_slot(Ctx& c, unsigned rd, u32x4 (&hw)[24], bf16x8 (&wf)[G2_PF + 1], f32x16 (&acc)[3], const f32x16* tailacc, K1Epi& E,
                                        G2Fin& Fn) {
  constexpr int i = g2_ks(n), t = g2_tl(n), s = n / 24;
  if constexpr (n + G2_PF < G2_NS) wf[(n + G2_PF) % (G2_PF + 1)] = g2_read_w<n + G2_PF>(rd);
  if constexpr (i == 0) acc[t] = __builtin_amdgcn_mfma_f32_32x32x16_bf16(wf[n % (G2_PF + 1)], __builtin_bit_cast(bf16x8, hw[i % G2_HW]), (f32x16){}, 0, 0, 0);
  else acc[t] = __builtin_amdgcn_mfma_f32_32x32x16_bf16(wf[n % (G2_PF + 1)], __builtin_bit_cast(bf16x8, hw[i % G2_HW]), acc[t], 0, 0, 0);
  SCHED_FENCE();
  if constexpr (n >= G2_TAIL0 && n <= G2_TAILST) k1_epi_step<1, n - G2_TAIL0>(c, tailacc, E, 64 + c.nl);
  {
    constexpr int m = n - (24 - G2_PF + 1);
    if constexpr (m >= 24 && m % 24 < 9 && m % 3 == 0 && m / 24 + 3 < 9) g2_issue_w<m / 24 + 3, (m % 24) / 3>(c);
  }
  {
    constexpr int hl = g2_hload_at(n);
    if constexpr (hl >= 0) hw[hl % G2_HW] = g2_load_h<hl>(c);
  }
  if constexpr (n == G2_PST || n == G2_PST + 2) g2_send(c, acc[0], 0, (n - G2_PST) / 2);
  if constexpr (n == G2_PST + 16 || n == G2_PST + 18) g2_send(c, acc[1], 1, (n - G2_PST - 16) / 2);
  if constexpr (n == G2_PRE3) {
    g2_fin_load(c, Fn, true);
  }
  if constexpr (g2_check(n + G2_LEAD)) poll_issue(c);
  if constexpr (g2_check(n)) {
    constexpr int k = (n - G2_CHECK0) / 24;
    POLL_LANDED(g2_ops_after(n - G2_LEAD, n));
    if (c.w == 0) {
      const int l = lane_of(c);
      if constexpr (k == 0) poll_wait(c, (l & 60) == 8);
      if constexpr (k == 1) poll_wait(c, (l & 60) == 12);
      if constexpr (k == 2) poll_wait(c, (l & 60) == 48);
      if constexpr (k == 3) poll_wait(c, (l & 60) == 52);
      if constexpr (k == 4) poll_wait(c, (l & 56) == 40);
      if constexpr (k == 5) {
        poll_wait(c, (l >= 16 && l <= 16 + (96 * c.nl + 95) / 9 - (96 * c.nl) / 9) || (l & 60) == 32);
        __builtin_amdgcn_raw_buffer_store_b32(c.ev + 1u, c.rs, l == 14 ? 0u : OOB, WS_EPOCH + c.b * 4, AUX_SC1);
      }
    }
  }
  SCHED_FENCE();
  if constexpr (g2_bar(n)) {
    constexpr int wt = n == G2_PUBB ? K1S[0].ops_after(k1_epi_slot(1, 49), K1_NS - 1) + g2_ops_after(-1, n)
                                    : (n == G2_PUBC ? g2_ops_after(G2_TAILST, n) : (s + 1 < 4 ? 63 : g2_ops_after(24 * (s - 2) + 24 - G2_PF + 1 + 6, n)));
    wait_vm_barrier<cmin(63, wt)>();
    if constexpr (n == G2_PUBB) { if (c.w == 1) publish_h(c, 48); }
    if constexpr (n == G2_PUBC) { if (c.w == 1) publish_h(c, 40); }
    SCHED_FENCE();
  }
  if constexpr (n + 1 < G2_NS) g2_slot<n + 1>(c, rd, hw, wf, acc, tailacc, E, Fn);
}
__device__ __forceinline__ unsigned umax4(const u32x4& v) {
  unsigned a = v[0] > v[1] ? v[0] : v[1], b = v[2] > v[3] ? v[2] : v[3];
  return a > b ? a : b;
}

__device__ __forceinline__ void g2_phase(Ctx& c, u32x4 (&hw)[24], const f32x16* tailacc, K1Epi& E) {
  const int lane = lane_of(c);
  const int w = c.w, ks = c.fq, nt = c.nl, mt = c.mt;
  c.xl0 = lane;
  unsigned rd;
  {
    int gq = lane >> 4, chf = gq & 1, h = gq >> 1, q = (lane & 15) >> 2, p = lane & 3;
    rd = K1_XOFF + (4 * h + q) * 192 + chf * 32 + p * 8;
  }
  bf16x8 wf[G2_PF + 1];
  f32x16 acc[3];
  wf[0] = g2_read_w<0>(rd); wf[1] = g2_read_w<1>(rd); wf[2] = g2_read_w<2>(rd); wf[3] = g2_read_w<3>(rd);
  SCHED_FENCE();
  G2Fin Fn;
  g2_slot<0>(c, rd, hw, wf, acc, tailacc, E, Fn);
  g2_send(c, acc[2], 2, 0);
  g2_send(c, acc[2], 2, 1);
  const int uh = w / 3, ut = w % 3;
  const int qp = lane >> 5, cc = lane & 31;
  lds_barrier();
  if (w < 6) {
    char* optr = (char*)karg(11);
    u32x4 pp[3][2];
    float ga[16], og = 0.f;
    f32x4 own[2][2];
    bool zdone = false;
    unsigned spin = 0;
#pragma unroll 1
    for (; spin < SPIN_CAP; ++spin) {
      asm volatile("" ::: "memory");
      const bool zok = __all(umax4(Fn.zz[0]) != SENT && umax4(Fn.zz[1]) != SENT) || spin == SPIN_CAP - 1;
      if (!zok) g2_fin_load(c, Fn, false);
#pragma unroll
      for (int s = 0; s < 3; ++s) {
        const int src = s + (s >= ks ? 1 : 0);
#pragma unroll
        for (int h = 0; h < 2; ++h)
          pp[s][h] = __builtin_amdgcn_raw_buffer_load_b128(c.rs, qp * 1024 + (cc + 32 * h) * 16, WS_PART + c.b * 49152 + (src * 2 + uh) * 6144 + ut * 2048, AUX_SC1);
      }
      SCHED_FENCE();
      if (zok && !zdone) {
        zdone = true;
        LAS char* zt = ldsp(24576 + w * 2560);
#pragma unroll
        for (int i = 0; i < 2; ++i) *(LAS u32x4*)(zt + (16 * i + (lane >> 2)) * 80 + (lane & 3) * 16) = Fn.zz[i];
        unsigned zv[16];
#pragma unroll
        for (int j = 0; j < 16; ++j) zv[j] = *(LAS u16*)(zt + (16 * qp + j) * 80 + ((cc & 24) + 4 * (cc & 1) + ((cc & 7) >> 1)) * 2);
#pragma unroll
        for (int o = 0; o < 2; ++o)
#pragma unroll
          for (int h = 0; h < 2; ++h) own[o][h] = *(LAS f32x4*)(ldsp((cc + 32 * h) * 16) + (uh * 12 + ut * 4 + 2 * qp + o) * 1024);
        og = rbf(1.0f - Fn.g);
#pragma unroll
        for (int j = 0; j < 16; j += 2) {
          const unsigned qw = Fn.qq[j >> 3][(j >> 1) & 3];
          const unsigned a2 = pk2(bflo(qw) * bflo(zv[j]), bfhi(qw) * bflo(zv[j + 1]));
          ga[j] = Fn.g * bflo(a2); ga[j + 1] = Fn.g * bfhi(a2);
        }
      }
      SCHED_FENCE();
      u32x4 mx = {umax4(pp[0][0]), umax4(pp[0][1]), umax4(pp[1][0]), umax4(pp[1][1])};
      if (zdone && __all(umax4(mx) != SENT && umax4(pp[2][0]) != SENT && umax4(pp[2][1]) != SENT)) break;
    }
#pragma unroll
    for (int o = 0; o < 2; ++o) {
      float res[8];
#pragma unroll
      for (int h = 0; h < 2; ++h)
#pragma unroll
        for (int k = 0; k < 4; k += 2) {
          f32x2 bs = {own[o][h][k], own[o][h][k + 1]};
#pragma unroll
          for (int s = 0; s < 3; ++s) bs += unpk2(pp[s][h][2 * o + (k >> 1)]);
          const f32x2 b2 = unpk2(pk2(bs[0], bs[1]));
          res[4 * h + k] = radd(ga[8 * o + 4 * h + k], og * b2[0]);
          res[4 * h + k + 1] = radd(ga[8 * o + 4 * h + k + 1], og * b2[1]);
        }
      u32x4 ov = {pk2(res[0], res[1]), pk2(res[2], res[3]), pk2(res[4], res[5]), pk2(res[6], res[7])};
      *(u32x4*)(optr + ((size_t)(256 * mt + 64 * ks + 32 * uh + cc) * D_ + 96 * nt + 32 * ut + 16 * qp + 8 * o) * 2) = ov;
    }
  }
}

template <bool PREF, bool FIRST, class Hook>
__device__ __forceinline__ void cv_block(f32x4 (&acc)[8], const bf16x8 (&af)[8], const bf16x8 (&bf)[8], bf16x8 (&naf)[8], bf16x8 (&nbf)[8],
                                         const unsigned (&ap)[4], unsigned bp, int roff, Hook&& hook) {
  auto step = [&](auto K) {
    constexpr int k = decltype(K)::value;
    constexpr int p = [] { int r = k, q = 0; while (r >= 8 - q) { r -= 8 - q; ++q; } return q; }();
    constexpr int dl = [] { int r = k, q = 0; while (r >= 8 - q) { r -= 8 - q; ++q; } return r; }();
    if constexpr (PREF && (k & 1) == 0 && k < 32) {
      constexpr int f = k >> 2;
      if constexpr ((k & 2) != 0) naf[f] = cv_read_a(ldsp(ap[f >> 1]) + 512 * (f & 1) + roff);
      else nbf[f] = *(LAS bf16x8*)(ldsp(bp) + 64 * (56 - 8 * f) + roff);
    }
    if constexpr (FIRST && p == 0) asm volatile("v_mfma_f32_16x16x32_bf16 %0, %1, %2, 0" : "=&v"(acc[dl]) : "v"(af[p]), "v"(bf[dl]));
    else asm volatile("v_mfma_f32_16x16x32_bf16 %0, %1, %2, %0" : "+v"(acc[p + dl]) : "v"(af[p]), "v"(bf[dl]));
    if constexpr (k & 1) { SCHED_FENCE(); hook(K); SCHED_FENCE(); }
  };
  [&]<int... Ks>(std::integer_sequence<int, Ks...>) { (step(std::integral_constant<int, Ks>{}), ...); }(std::make_integer_sequence<int, 36>{});
}
__device__ __forceinline__ u32x4 cv_u(LAS char* cl, int g, int lane) {
  const int q = lane >> 4, pi = (lane >> 3) & 1, ii = lane & 7;
  return *(LAS u32x4*)(cl + (pi ? CV_U1 : CV_U0) + 2 * (2040 - (256 * g + 32 * ii + 8 * q)));
}
__device__ __forceinline__ f32x4 cv_ud(const u32x4& uu, float Dv) {
  return (f32x4){rb1(bflo(uu[3]) * Dv), rb1(bflo(uu[2]) * Dv), rb1(bflo(uu[1]) * Dv), rb1(bflo(uu[0]) * Dv)};
}
__device__ __forceinline__ u32x2 cv_z(const f32x4& y, const f32x4& ud) {
  return (u32x2){pk2(rb1(y[0]) + ud[0], rb1(y[1]) + ud[1]), pk2(rb1(y[2]) + ud[2], rb1(y[3]) + ud[3])};
}
__device__ __forceinline__ void cv_phase(Ctx& c, const f32x16* tailacc, K1Epi& E, CvRegs& V) {
  const int w = c.w;
  f32x4 acc[8], acc8[8];
  bf16x8 a1[8], b1[8];
  auto none = [](auto) {};
  cv_block<true, true>(acc, V.a0, V.b0, a1, b1, V.ap, V.bp, 64, [&](auto K) {
    constexpr int k = decltype(K)::value / 2;
    if constexpr (k >= G2_TAIL0 && k <= G2_TAILST) k1_epi_step<1, k - G2_TAIL0>(c, tailacc, E, 64 + c.nl);
  });
  wait_vm_barrier<1>();
  if (w == 1) publish_h(c, 48);
  const int lane = lane_of(c);
  const unsigned zb = WS_Z + 9 * cv_id(c) * (S_ * 2);
  const unsigned cb = cv_base(w), cb8 = cv_base(8);
  int zsl, zwg;
  unsigned zev, dd, ap8[4];
  SCHED_FENCE();
  cv_block<true, false>(acc, a1, b1, V.a0, V.b0, V.ap, V.bp, 128, [&](auto K) {
    constexpr int k = decltype(K)::value / 2;
    if constexpr (k == 1 || k == 5 || k == 9 || k == 13) st128(sent4(), c.rs, c.lane16, zb + w * (S_ * 2) + (k / 4) * 1024, AUX_SC1);
    if constexpr (k == 17) __builtin_amdgcn_raw_buffer_store_b64(sent2(), c.rs, lane * 8, zb + 8 * (S_ * 2) + 512 * w, AUX_SC1);
    if constexpr (k == 3) { zwg = cv_zwg(c, lane, zsl); zev = __builtin_amdgcn_raw_buffer_load_b32(c.rs, zwg * 4, WS_EPOCH, 0); }
    if constexpr (k == 7) dd = *(const u16*)(c.dptr + (9 * cv_id(c) + ((lane & 1) ? 8 : w)) * 2);
    if constexpr (k == 11) {
#pragma unroll
      for (int i = 0; i < 4; ++i) { ap8[i] = V.ap[i] + (cb8 - cb + 64 * w); asm volatile("" : "+v"(ap8[i])); }
    }
  });
  cv_block<true, false>(acc, V.a0, V.b0, a1, b1, ap8, V.bp + (cb8 - cb + 64 * w), 0, none);
  cv_block<true, true>(acc8, a1, b1, V.a0, V.b0, V.ap, V.bp, 192, none);
  LAS char* sc = ldsp(((w & 2) ? k1_wbase(20) : k1_wbase(19)) + (w & 1) * 8192 + lane * 16);
  cv_block<true, false>(acc, V.a0, V.b0, a1, b1, V.ap, V.bp, 256, [&](auto K) {
    constexpr int k = decltype(K)::value / 2;
    if constexpr (k >= 4 && k < 12) { if (w >= 4) *(LAS f32x4*)(sc + (k - 4) * 1024) = acc8[k - 4]; }
  });
  asm volatile("s_waitcnt vmcnt(0) lgkmcnt(0)\n\ts_barrier" ::: "memory");
  if (w == 5) publish_h(c, 40);
  if (w == 6) flag_store(c, zsl >= 0, zwg, zsl, zev);
  SCHED_FENCE();
  f32x4 o8[2];
  cv_block<true, false>(acc, a1, b1, V.a0, V.b0, V.ap, V.bp, 320, [&](auto K) {
    constexpr int k = decltype(K)::value / 2;
    if (w < 4) {
      if constexpr (k >= 2 && k < 18 && (k & 1) == 0) *(LAS f32x4*)(sc + (k / 2 - 1) * 1024) = acc8[k / 2 - 1] + o8[(k / 2 - 1) & 1];
      if constexpr (k < 16 && (k & 1) == 0) o8[(k / 2) & 1] = *(LAS f32x4*)(sc + (k / 2) * 1024);
    }
  });
  lds_barrier();
  const float Dv = bflo((unsigned)__builtin_amdgcn_readlane((int)dd, 0)), Dv8 = bflo((unsigned)__builtin_amdgcn_readlane((int)dd, 1));
  const int zo = (32 * (lane & 7) + 8 * (lane >> 4) + 4 * ((lane >> 3) & 1)) * 2;
  f32x4 y4[4], ud[8];
  u32x4 uu[2];
  SCHED_FENCE();
  cv_block<true, false>(acc, V.a0, V.b0, a1, b1, V.ap, V.bp, 384, [&](auto K) {
    constexpr int k = decltype(K)::value / 2;
    LAS char* s0 = ldsp(w * 1024 + lane * 16);
    if constexpr (k < 4) y4[k] = *(LAS f32x4*)(s0 + ((k & 2) ? k1_wbase(20) : k1_wbase(19)) + (k & 1) * 8192);
    if constexpr (k == 4) uu[0] = cv_u(ldsp(cb8), w, lane);
    if constexpr (k == 8) { y4[0] = (y4[0] + y4[1]) + (y4[2] + y4[3]); asm volatile("" : "+v"(y4[0])); }
    if constexpr (k == 12) __builtin_amdgcn_raw_buffer_store_b64(cv_z(y4[0], cv_ud(uu[0], Dv8)), c.rs, zo, zb + 8 * (S_ * 2) + 512 * w, AUX_SC1);
  });
  cv_block<true, false>(acc, a1, b1, V.a0, V.b0, V.ap, V.bp, 448, [&](auto K) {
    constexpr int k = decltype(K)::value / 2;
    if constexpr (k >= 2 && (k & 1) == 0) { ud[k / 2 - 1] = cv_ud(uu[(k / 2 - 1) & 1], Dv); asm volatile("" : "+v"(ud[k / 2 - 1])); }
    if constexpr (k < 16 && (k & 1) == 0) uu[(k / 2) & 1] = cv_u(ldsp(cb), k / 2, lane);
  });
  cv_block<false, false>(acc, V.a0, V.b0, a1, b1, V.ap, V.bp, 0, [&](auto K) {
    constexpr int k = decltype(K)::value;
    constexpr int g = k == 5 ? 0 : (k == 13 ? 1 : (k == 19 ? 2 : (k == 25 ? 3 : (k == 31 ? 4 : (k == 35 ? 5 : -1)))));
    if constexpr (g >= 0) {
      asm volatile("s_nop 3" : "+v"(acc[g]));
      __builtin_amdgcn_raw_buffer_store_b64(cv_z(acc[g], ud[g]), c.rs, zo, zb + w * (S_ * 2) + 512 * g, AUX_SC1);
    }
  });
  asm volatile("s_nop 7\n\ts_nop 1" : "+v"(acc[6]), "+v"(acc[7]));
#pragma unroll
  for (int g = 6; g < 8; ++g) __builtin_amdgcn_raw_buffer_store_b64(cv_z(acc[g], ud[g]), c.rs, zo, zb + w * (S_ * 2) + 512 * g, AUX_SC1);
}

template <int ROLE>
__device__ __forceinline__ void fused_body(const int w, const unsigned long long pc) {
  const int b = (blockIdx.x + 192) & 255, xcd = b & 7, r = b >> 3;
  const int ml = r & 3, nl = r >> 2, fq = xcd >> 1;
  const int mt = 4 * (xcd & 1) + ml;
  const int lane = __builtin_amdgcn_mbcnt_hi(~0u, __builtin_amdgcn_mbcnt_lo(~0u, 0u));
  Ctx c;
  c.w = w; c.nl = nl; c.ml = ml; c.b = b; c.mt = mt; c.fq = fq;
  const char* ka[11];
  {
    typedef const char* cptr;
    const __attribute__((address_space(4))) cptr* p = (const __attribute__((address_space(4))) cptr*)__builtin_amdgcn_kernarg_segment_ptr();
    asm volatile("" : "+s"(p));
#pragma unroll
    for (int i = 0; i < 11; ++i) ka[i] = p[i];
    SCHED_FENCE();
  }
  c.rot = (nl + ml) % 3;
  c.lane16 = lane * 16;
  asm volatile("" : "+v"(c.lane16));
#pragma unroll
  for (int i = 0; i < 3; ++i) { const int k = kslice(i, c.rot); c.wo[i] = k * (64 * F_ * 2); c.xo[i] = k * 128; }
  {
    int row = lane >> 3, pos = lane & 7;
    c.voff_w = (8 * w + row) * (F_ * 2) + ((pos ^ (2 * ((row >> 1) & 1))) * 16);
    c.voff_x = row * (D_ * 2) + ((pos ^ (lane >> 4)) * 16);
  }
  asm volatile("" : "+v"(c.voff_w), "+v"(c.voff_x), "+s"(c.wo[0]), "+s"(c.xo[0]), "+s"(c.wo[1]));
  SCHED_FENCE();
  asm volatile("" : "+s"(ka[0]), "+s"(ka[1]), "+s"(ka[2]), "+s"(ka[3]), "+s"(ka[4]), "+s"(ka[6]), "+s"(ka[7]), "+s"(ka[8]), "+s"(ka[9]), "+s"(ka[10]));
  c.sw1 = make_srd(ka[6], (1152 * fq + 128 * nl) * 2, D_ * F_ * 2);
  c.sw3 = make_srd(ka[7], (1152 * fq + 128 * nl) * 2, D_ * F_ * 2);
  c.sx = make_srd(ka[0], (256 * mt + 32 * w) * (D_ * 2), S_ * D_ * 2);
  k1_issue_w<0, 0>(c); k1_issue_w<0, 1>(c);
  k1_issue_x<0, 0>(c); k1_issue_x<0, 1>(c); k1_issue_x<0, 2>(c); k1_issue_x<0, 3>(c);
  {
    unsigned e0, e1;
    asm volatile("s_mov_b32 %0, .Lgm_code_end0-.Lgm_code_start\n\ts_mov_b32 %1, .Lgm_code_end1-.Lgm_code_start" : "=s"(e0), "=s"(e1));
    const u32x4 sc = make_srd((const char*)pc, 0, e0 > e1 ? e0 : e1);
    const unsigned vo = lane < 3 ? (unsigned)(8 * r + w + 256 * lane) * 128 : OOB;
    asm volatile("buffer_load_dword %0, %1, %2, 0 offen" : "=v"(c.touch) : "v"(vo), "s"(sc) : "memory");
  }
  k1_issue_w<1, 0>(c); k1_issue_w<1, 1>(c);
  SCHED_FENCE();
  c.sws = make_srd(ka[10], 0, WS_END);
  c.rs = __builtin_amdgcn_make_buffer_rsrc((void*)ka[10], 0, WS_END, 0x00020000);
  c.hb = WS_H + ((8 * mt + w) * 288 + 72 * fq) * 1024;
  {
    int gq = lane >> 4, mat = gq & 1, h = gq >> 1, q = (lane & 15) >> 2, p = lane & 3;
    int X = 2 * (q >> 1) + 4 * mat;
#pragma unroll
    for (int t = 0; t < 4; ++t) c.rd[t] = mat * 8192 + (8 * h + q) * 128 + (((2 * t) ^ X) + (p >> 1)) * 16 + (p & 1) * 8;
    int cc = lane & 31, hh = lane >> 5;
    c.xl0 = K1_XOFF + w * (K1_XD * 4096) + cc * 128 + ((hh ^ ((cc >> 1) & 7)) * 16);
  }
  {
    const u32x4 sw2 = make_srd(ka[8], 1152 * fq * D_ * 2, F_ * D_ * 2), ssk = make_srd(ka[4], 0, D_ * S_ * 2);
    c.s2 = ROLE == 0 ? sw2 : ssk;
    c.s3 = ROLE == 0 ? ssk : sw2;
    const unsigned mh = xcd & 1, i = 8 * r + w + 256 * (lane < 16 ? lane : (lane < 48 ? lane - 16 : lane - 48));
    const unsigned r1 = (i * 6554u) >> 16, c1 = i - 10 * r1;
    const int o1 = (int)((2 * r1 + mh) * (F_ * 2) + (c1 < 8 ? 2 * c1 + 1 : c1 + 8) * 128) - 256 * nl;
    const unsigned r2 = (i * 7282u) >> 16, o2 = (2 * r2 + mh) * (D_ * 2) + (i - 9 * r2) * 128;
    unsigned o = 72 * xcd * (S_ * 2) + i * 128;
    o = lane < 48 ? (i < 5184 ? o2 : OOB) : o;
    o = lane < 16 ? ((i < 2880 && o1 >= 0) ? (unsigned)o1 : OOB) : o;
    c.warm = lane < 57 ? o : OOB;
    asm volatile("" : "+v"(c.warm));
  }
  K1Prep P;
  {
    const u32x4 sk_ = make_srd(ka[1], 0, S_ * D_ * 2), sv_ = make_srd(ka[2], 0, S_ * D_ * 2);
    auto voff = [&](int blk, int halo, bool on) {
      const unsigned pc = (unsigned)(64 * blk + lane), row = (pc * 7282u) >> 16;
      const int t = 64 * prep_tt(b) - halo + (int)row;
      const unsigned o = (unsigned)t * (D_ * 2) + (pc - 9 * row) * 16 + 144 * prep_dg(b);
      return (on && t >= 0 && (int)row < 64 + halo) ? o : OOB;
    };
    const bool isv = (w & 2) != 0;
    const u32x4 sd = {isv ? sv_[0] : sk_[0], isv ? sv_[1] : sk_[1], sk_[2], sk_[3]};
    unsigned v0 = voff(w, 4, true), v1 = voff(w, 1, true), v2 = voff(8 + (w & 1), isv ? 1 : 4, w < 4);
    const int e0 = cv_wg(8 * prep_dg(b) + lane), e1 = g2_wg(mt, lane - 8, fq), e2 = g2_wg(mt, nl < 6 ? nl : 0, lane & 3);
    int ewg = b;
    ewg = (lane & 60) == 16 ? e2 : ewg;
    ewg = lane < 14 ? e1 : ewg;
    ewg = (lane < 8 ? e0 : ewg) * 4;
    asm volatile("" : "+v"(v0), "+v"(v1), "+v"(v2), "+v"(ewg), "+v"(c.rd[0]), "+v"(c.rd[1]), "+v"(c.rd[2]), "+v"(c.rd[3]), "+v"(c.xl0));
    SCHED_FENCE();
    dma16<K1_POFF, 0>(v0, sk_, 0u, w * 1024);
    dma16<K1_POFF + K1_PV, 0>(v1, sv_, 0u, w * 1024);
    dma16<K1_POFF, 0>(v2, sd, 0u, w < 4 ? (8 + (w & 1)) * 1024 + (isv ? K1_PV : 0) : 20480 + w * 1024);
    P.pev = ewg;
    const char* gx = ka[0] + (size_t)(256 * mt + 64 * fq + 8 * nl + w) * (D_ * 2);
    const int dp = 8 * w + (lane >> 3), dc = dp < 36 ? dp : 35;
    P.lp = K1_POFF + 8 * (7 - (lane & 7)) * 144 + dc * 4;
    asm volatile("" : "+v"(P.lp));
    P.psk = (unsigned)(72 * prep_dg(b) + 2 * dc) * 8; P.skp = ka[3]; P.gw = ka[9]; P.gx = gx;
  }
  bf16x8 xf[36];
  bf16x8 wf[K1_RD + 1];
  f32x16 acc[4];
  u32x4 hw[24];
  wait_vm_barrier<6>();
  k1_issue_x<1, 0>(c); k1_issue_x<1, 1>(c); k1_issue_x<1, 2>(c); k1_issue_x<1, 3>(c);
  xf[0] = k1_read_x<0, 0>(c); wf[0] = k1_read_w<0>(c);
  xf[1] = k1_read_x<0, 1>(c); wf[1] = k1_read_w<1>(c);
  xf[2] = k1_read_x<0, 2>(c); wf[2] = k1_read_w<2>(c);
  xf[3] = k1_read_x<0, 3>(c);
  static_assert(K1_RD == 3, "prologue reads");
  SCHED_FENCE();
  K1Epi E;
  CvRegs V;
  k1_slot<0, ROLE>(c, xf, wf, acc, E, P, hw, V);
  if constexpr (ROLE == 1) cv_phase(c, acc, E, V);
  else {
    g2_phase(c, hw, acc, E);
    __builtin_amdgcn_s_waitcnt(0);
  }
  if constexpr (ROLE == 0) asm volatile(".Lgm_code_end0:");
  else asm volatile(".Lgm_code_end1:");
}

__global__ __launch_bounds__(512, 2) void fused_kernel(const void* x, const void* kk, const void* vv, const void* shift_k, const void* ssm_k,
                                                       const void* ssm_D, const void* w1, const void* w3, const void* w2, const void* gate_w, void* ws,
                                                       void* out) {
  const int w = __builtin_amdgcn_readfirstlane(threadIdx.x >> 6);
  unsigned long long pc;
  asm volatile("s_getpc_b64 %0\n.Lgm_code_start:" : "=s"(pc));
  if (blockIdx.x < 64) fused_body<1>(w, pc);
  else fused_body<0>(w, pc);
}

extern "C" void kernel_launch(void* const* d_in, const int* in_sizes, int n_in, void* d_out, int out_size, void* d_ws, size_t ws_size,
                              hipStream_t stream) {
  (void)hipFuncSetAttribute((const void*)fused_kernel, hipFuncAttributeMaxDynamicSharedMemorySize, LDS_BYTES);
  fused_kernel<<<256, 512, LDS_BYTES, stream>>>(d_in[0], d_in[1], d_in[2], d_in[3], d_in[4], d_in[5], d_in[7], d_in[8], d_in[9], d_in[10], d_ws, d_out);
}
```
